```python
import torch

def _softmax(x, axis=-1):
    return torch.softmax(x, dim=axis)

def _gelu(x):
    return torch.nn.functional.gelu(x, approximate='tanh')

def _silu(x):
    return torch.nn.functional.silu(x)

def _sigmoid(x):
    return torch.sigmoid(x)

def _layernorm(x, axis=-1, eps=1e-05):
    m = x.mean(dim=axis, keepdim=True)
    v = x.var(dim=axis, keepdim=True, unbiased=False)
    return (x - m) / torch.sqrt(v + eps)

def _scatter_add(tgt, idx, src, axis=0):
    return tgt.clone().index_add_(axis, idx, src)

def _segment_sum(src, seg, n):
    out = torch.zeros((n,) + src.shape[1:], dtype=src.dtype)
    return out.index_add_(0, seg, src)

def _pad(x, pw):
    flat = tuple((v for pair in reversed(pw) for v in pair))
    return torch.nn.functional.pad(x, flat)
kernel_name = 'adaLN_cond__sliding_block_cached__glu_gated_reglu__decode'

def setup_inputs():

    _w = lambda a: (lambda t: t.to(torch.bfloat16) if t.is_floating_point() else t)(torch.as_tensor(a))

    def _A_setup():
        B, Sq, D = (4, 1, 640)
        Hq, Hkv, Sk, Dh = (32, 32, 4096, 192)
        assert Hq % Hkv == 0, (Hq, Hkv)
        return {'x': _w((lambda *s: torch.randn(*s, dtype=torch.bfloat16) * 0.1)(B, Sq, D)), 'k_cache': _w((lambda *s: torch.randn(*s, dtype=torch.bfloat16) * 0.8)(B, Hkv, Sk, Dh)), 'v_cache': _w((lambda *s: torch.randn(*s, dtype=torch.bfloat16) * 3.2)(B, Hkv, Sk, Dh)), 'wq': _w((lambda *s: torch.randn(*s, dtype=torch.bfloat16) * 0.8)(D, Hq * Dh) * D ** (-0.5)), 'wo': _w((lambda *s: torch.randn(*s, dtype=torch.bfloat16) * 3.2)(Hq * Dh, D) * (Hq * Dh) ** (-0.5))}
    _A_in = _A_setup()

    def _B_setup():
        B, S, D, F = (4, 1, 640, 1536)
        return {'x': _w((lambda *s: torch.randn(*s, dtype=torch.bfloat16) * 0.1)(B, S, D)), 'W_gate': _w((lambda *s: torch.randn(*s, dtype=torch.bfloat16) * 3.2)(D, F) * (2.0 / D) ** 0.5), 'W_up': _w((lambda *s: torch.randn(*s, dtype=torch.bfloat16) * 3.2)(D, F) * (2.0 / D) ** 0.5), 'W_down': _w((lambda *s: torch.randn(*s, dtype=torch.bfloat16) * 3.2)(F, D) * (1.0 / F) ** 0.5)}
    _B_in = _B_setup()
    return {'A_x': _A_in['x'], 'A_k_cache': _A_in['k_cache'], 'A_v_cache': _A_in['v_cache'], 'A_wq': _A_in['wq'], 'A_wo': _A_in['wo'], 'B_x': _B_in['x'], 'B_W_gate': _B_in['W_gate'], 'B_W_up': _B_in['W_up'], 'B_W_down': _B_in['W_down'], 'c': _w((lambda *s: torch.randn(*s, dtype=torch.bfloat16) * 0.4)(4, 640) * 8.0), 'w_mod': _w((lambda *s: torch.randn(*s, dtype=torch.bfloat16) * 0.4)(640, 6 * 640) * 640 ** (-0.5) * 8.0)}

def reference(A_x, A_k_cache, A_v_cache, A_wq, A_wo, B_x, B_W_gate, B_W_up, B_W_down, c, w_mod):

    def _A_ref(x, k_cache, v_cache, wq, wo):
        B, Sq, D = x.shape
        Hq, Dh = (32, 192)
        Hkv, Sk = (k_cache.shape[1], k_cache.shape[2])
        W = min(4096, max(256, Sk // 4))
        rep = Hq // Hkv
        q = (lambda x, *d: x.permute(*d))((x @ wq).reshape(B, Sq, Hq, Dh), 0, 2, 1, 3)
        k = (lambda x, n, axis: torch.repeat_interleave(x, n, dim=axis))(k_cache[:, :, Sk - W:, :], rep, axis=1)
        v = (lambda x, n, axis: torch.repeat_interleave(x, n, dim=axis))(v_cache[:, :, Sk - W:, :], rep, axis=1)
        scores = torch.einsum('bhqd,bhkd->bhqk', q, k) * Dh ** (-0.5)
        p = _softmax(scores, axis=-1)
        o = torch.einsum('bhqk,bhkd->bhqd', p, v)
        return (lambda x, *d: x.permute(*d))(o, 0, 2, 1, 3).reshape(B, Sq, Hq * Dh) @ wo

    def _B_ref(x, W_gate, W_up, W_down):
        gate = torch.einsum('bsd,df->bsf', x, W_gate)
        up = torch.einsum('bsd,df->bsf', x, W_up)
        gate = gate * (lambda x, dt: x.to(dt))(gate > 0, gate.dtype)
        h = gate * up
        return torch.einsum('bsf,fd->bsd', h, W_down)
    x = A_x
    mod = (_silu(c) @ w_mod).reshape(c.shape[0], 6, 1, -1)
    sa, ca, ga, sb, cb, gb = (mod[:, 0].clone(), mod[:, 1].clone(), mod[:, 2].clone(), mod[:, 3].clone(), mod[:, 4].clone(), mod[:, 5].clone())
    h = _layernorm(x, axis=-1) * (1 + ca) + sa
    x = x + ga * _A_ref(h, A_k_cache, A_v_cache, A_wq, A_wo)
    h = _layernorm(x, axis=-1) * (1 + cb) + sb
    x = x + gb * _B_ref(h, B_W_gate, B_W_up, B_W_down)
    return x
```

```cpp
#include <hip/hip_runtime.h>
#include <hip/hip_bf16.h>
#include <stdint.h>

typedef unsigned short u16;
constexpr int B = 4, D = 640, H = 32, SK = 4096, DH = 192, W = 1024, F = 1536, NQ = H * DH;

__device__ __forceinline__ float bf2f(u16 v) { return __uint_as_float(((unsigned)v) << 16); }
__device__ __forceinline__ u16 f2bf(float f) { __hip_bfloat16 h = __float2bfloat16(f); return *reinterpret_cast<u16*>(&h); }
__device__ __forceinline__ float rbf(float f) { return bf2f(f2bf(f)); }

__global__ void gemv_k(const float* __restrict__ a, const u16* __restrict__ w, float* __restrict__ out, int K, int N) {
    __shared__ float red[4][4][64];
    int n = blockIdx.x * 64 + (threadIdx.x & 63);
    int ks = threadIdx.x >> 6;
    float acc[4] = {0, 0, 0, 0};
    for (int k = ks; k < K; k += 4) {
        float wv = bf2f(w[(size_t)k * N + n]);
        for (int b = 0; b < 4; ++b) acc[b] += a[b * K + k] * wv;
    }
    for (int b = 0; b < 4; ++b) red[ks][b][threadIdx.x & 63] = acc[b];
    __syncthreads();
    if (ks == 0) for (int b = 0; b < 4; ++b) {
        int l = threadIdx.x & 63;
        out[b * N + n] = rbf(red[0][b][l] + red[1][b][l] + red[2][b][l] + red[3][b][l]);
    }
}
__global__ void silu_k(const u16* c, float* out) {
    int i = blockIdx.x * 256 + threadIdx.x;
    if (i < B * D) { float v = bf2f(c[i]); out[i] = rbf(v / (1.f + expf(-v))); }
}
__global__ void ln_k(const float* x, const float* mod, int sh, int sc, float* h) {
    __shared__ float red[640];
    int b = blockIdx.x, t = threadIdx.x;
    float v = x[b * D + t];
    red[t] = v; __syncthreads();
    float s = 0; for (int i = 0; i < D; ++i) s += red[i];
    float mean = s / D;
    float q = 0; for (int i = 0; i < D; ++i) { float d = red[i] - mean; q += d * d; }
    float m = rbf(mean), var = rbf(q / D);
    float den = rbf(sqrtf(rbf(var + 1e-5f)));
    float ln = rbf(rbf(v - m) / den);
    float ca = mod[b * 6 * D + sc * D + t], sa = mod[b * 6 * D + sh * D + t];
    h[b * D + t] = rbf(rbf(ln * rbf(1.f + ca)) + sa);
}
__global__ void cvt_k(const u16* x, float* o, int n) { int i = blockIdx.x * 256 + threadIdx.x; if (i < n) o[i] = bf2f(x[i]); }
__global__ void attn_k(const float* q, const u16* kc, const u16* vc, float* o) {
    __shared__ float s[W]; __shared__ float qs[DH]; __shared__ float red[256];
    int bh = blockIdx.x, t = threadIdx.x;
    if (t < DH) qs[t] = q[bh * DH + t];
    __syncthreads();
    const u16* kb = kc + ((size_t)bh * SK + (SK - W)) * DH;
    const u16* vb = vc + ((size_t)bh * SK + (SK - W)) * DH;
    float mx = -INFINITY;
    for (int k = t; k < W; k += 256) {
        float a = 0; for (int d = 0; d < DH; ++d) a += qs[d] * bf2f(kb[(size_t)k * DH + d]);
        a = rbf(rbf(a) * 0.07216878364870322f);
        s[k] = a; mx = fmaxf(mx, a);
    }
    red[t] = mx; __syncthreads();
    for (int i = 0; i < 256; ++i) mx = fmaxf(mx, red[i]);
    __syncthreads();
    float sum = 0;
    for (int k = t; k < W; k += 256) { float e = expf(s[k] - mx); s[k] = e; sum += e; }
    red[t] = sum; __syncthreads();
    sum = 0; for (int i = 0; i < 256; ++i) sum += red[i];
    if (t < DH) {
        float a = 0;
        for (int k = 0; k < W; ++k) a += rbf(s[k] / sum) * bf2f(vb[(size_t)k * DH + t]);
        o[bh * DH + t] = rbf(a);
    }
}
__global__ void resid_k(const float* x, const float* mod, int g, const float* y, float* xo, u16* out) {
    int i = blockIdx.x * 256 + threadIdx.x;
    if (i < B * D) { int b = i / D, t = i % D; float r = rbf(x[i] + rbf(mod[b * 6 * D + g * D + t] * y[i])); if (xo) xo[i] = r; if (out) out[i] = f2bf(r); }
}
__global__ void glu_k(const float* g, const float* u, float* h) {
    int i = blockIdx.x * 256 + threadIdx.x;
    if (i < B * F) { float gg = g[i]; gg = gg * (gg > 0 ? 1.f : 0.f); h[i] = rbf(gg * u[i]); }
}
extern "C" void kernel_launch(void* const* d_in, const int* in_sizes, int n_in, void* d_out, int out_size, void* d_ws, size_t ws_size, hipStream_t st) {
    const u16 *x = (const u16*)d_in[0], *kc = (const u16*)d_in[1], *vc = (const u16*)d_in[2], *wq = (const u16*)d_in[3], *wo = (const u16*)d_in[4];
    const u16 *wg = (const u16*)d_in[6], *wu = (const u16*)d_in[7], *wd = (const u16*)d_in[8], *c = (const u16*)d_in[9], *wm = (const u16*)d_in[10];
    float* ws = (float*)d_ws;
    float *sc = ws, *mod = ws + 4096, *xf = mod + 16384, *h = xf + 4096, *q = h + 4096, *o = q + 32768, *at = o + 32768, *x1 = at + 4096, *h2 = x1 + 4096, *g = h2 + 4096, *u = g + 8192, *hh = u + 8192, *ff = hh + 8192;
    silu_k<<<10, 256, 0, st>>>(c, sc);
    cvt_k<<<10, 256, 0, st>>>(x, xf, B * D);
    gemv_k<<<6 * D / 64, 256, 0, st>>>(sc, wm, mod, D, 6 * D);
    ln_k<<<B, D, 0, st>>>(xf, mod, 0, 1, h);
    gemv_k<<<NQ / 64, 256, 0, st>>>(h, wq, q, D, NQ);
    attn_k<<<B * H, 256, 0, st>>>(q, kc, vc, o);
    gemv_k<<<D / 64, 256, 0, st>>>(o, wo, at, NQ, D);
    resid_k<<<10, 256, 0, st>>>(xf, mod, 2, at, x1, nullptr);
    ln_k<<<B, D, 0, st>>>(x1, mod, 3, 4, h2);
    gemv_k<<<F / 64, 256, 0, st>>>(h2, wg, g, D, F);
    gemv_k<<<F / 64, 256, 0, st>>>(h2, wu, u, D, F);
    glu_k<<<24, 256, 0, st>>>(g, u, hh);
    gemv_k<<<D / 64, 256, 0, st>>>(hh, wd, ff, F, D);
    resid_k<<<10, 256, 0, st>>>(x1, mod, 5, ff, nullptr, (u16*)d_out);
}
```

```cpp
#include <hip/hip_runtime.h>
#include <stdint.h>
#pragma clang fp contract(off)

typedef unsigned short u16;
typedef unsigned u32;
typedef unsigned long long u64;
typedef u32 v4u __attribute__((ext_vector_type(4)));
typedef u32 v2u __attribute__((ext_vector_type(2)));
typedef float f4 __attribute__((ext_vector_type(4)));
typedef short v4s __attribute__((ext_vector_type(4)));
typedef __bf16 bf8 __attribute__((ext_vector_type(8)));
typedef __bf16 bf2 __attribute__((ext_vector_type(2)));
#define LAS __attribute__((address_space(3)))
#define GAS __attribute__((address_space(1)))
#define RLX_AGENT __ATOMIC_RELAXED, __HIP_MEMORY_SCOPE_AGENT

#define INIT_MODE 1

constexpr int DM = 640, NH = 32, SKV = 4096, DH = 192, WIN = 1024, FF = 1536, NQ = NH * DH;
constexpr u32 SENT = 0xFFFFFFFFu;
constexpr int NW = 64;
constexpr u32 WS_E = 0, WS_FLAG = 4096, WS_F1P = 8192, WS_MODF = WS_F1P + 5 * 4 * 1280 * 4, WS_QP = WS_MODF + 4 * 2560 * 2,
              WS_OP = WS_QP + 5 * 4 * NQ * 4, WS_ML = WS_OP + 128 * 2 * DH * 4, WS_AP = WS_ML + 32 * 4 * 16, WS_HID = WS_AP + 3 * 4 * DM * 4,
              WS_END = WS_HID + 4 * FF * 2;
constexpr u32 OOB = 0x7FFFFF00u;
constexpr u32 L_XS = 131072, L_CS = L_XS + 5120, L_HB = L_CS + 5120, L_RED = L_HB + 5120, L_ST = L_RED + 8192, L_GA = L_ST + 256, L_GB = L_GA + 1024,
              L_X1 = L_GB + 1024, L_MISC = L_X1 + 1024, L_STAMP = L_MISC + 64, L_END = L_STAMP + 256;
constexpr u32 K_QS = 0, K_PX = 1024, K_PART = K_PX + 8 * 256, K_ML = K_PART + 8 * 768, K_END = K_ML + 64;
static_assert(L_END <= 163840, "LDS");
constexpr int SPIN_G = 40000, SPIN_L = 2000000;

#define STAMP(i) do {} while (0)

struct Params { const u16 *x, *kc, *vc, *wq, *wo, *wg, *wu, *wd, *c, *wm; u16* out; unsigned char* ws; };

__device__ __forceinline__ v4u mk_srd(const void* p, u32 bytes) { const u64 a = (u64)p; return v4u{(u32)a, (u32)(a >> 32) & 0xffffu, bytes, 0x00020000u}; }
__device__ __forceinline__ float asf(u32 u) { return __builtin_bit_cast(float, u); }
__device__ __forceinline__ u32 asu(float f) { return __builtin_bit_cast(u32, f); }
__device__ __forceinline__ float bflo(u32 u) { return asf(u << 16); }
__device__ __forceinline__ float bfhi(u32 u) { return asf(u & 0xffff0000u); }
__device__ __forceinline__ u32 pack2(float lo, float hi) { bf2 v; v[0] = (__bf16)lo; v[1] = (__bf16)hi; return __builtin_bit_cast(u32, v); }
__device__ __forceinline__ float rbf(float f) { return (float)(__bf16)f; }
__device__ __forceinline__ u32 canon(u32 u) { return u == SENT ? 0x7FC07FC0u : u; }
__device__ __forceinline__ v4u canon4(f4 v) { return v4u{canon(asu(v[0])), canon(asu(v[1])), canon(asu(v[2])), canon(asu(v[3]))}; }
__device__ __forceinline__ u32 umax3(u32 a, u32 b, u32 c) { const u32 m = a > b ? a : b; return m > c ? m : c; }
__device__ __forceinline__ u32 vmax4(u32 m, v4u v) { return umax3(umax3(m, v[0], v[1]), v[2], v[3]); }
__device__ __forceinline__ bool all_valid(u32 m) { return __builtin_amdgcn_ballot_w64(m == SENT) == 0ull; }
__device__ __forceinline__ float dot2(u32 a, u32 b, float c) { return __builtin_amdgcn_fdot2_f32_bf16(__builtin_bit_cast(bf2, a), __builtin_bit_cast(bf2, b), c, false); }
__device__ __forceinline__ f4 mfma(v4u a, v4u b, f4 c) { return __builtin_amdgcn_mfma_f32_16x16x32_bf16(__builtin_bit_cast(bf8, a), __builtin_bit_cast(bf8, b), c, 0, 0, 0); }
template <int CTRL> __device__ __forceinline__ u32 dppu(u32 x) { return (u32)__builtin_amdgcn_mov_dpp((int)x, CTRL, 0xf, 0xf, true); }
template <int CTRL> __device__ __forceinline__ float dppf(float x) { return asf(dppu<CTRL>(asu(x))); }
template <int K> __device__ __forceinline__ v4u ror4k(v4u v) {
    if constexpr (K == 0) return v; else return v4u{dppu<0x120 + 4 * K>(v[0]), dppu<0x120 + 4 * K>(v[1]), dppu<0x120 + 4 * K>(v[2]), dppu<0x120 + 4 * K>(v[3])}; }
__device__ __forceinline__ float row_sum(float x) { x += dppf<0x128>(x); x += dppf<0x124>(x); x += dppf<0x122>(x); x += dppf<0x121>(x); return x; }
__device__ __forceinline__ float row_max(float x) { x = fmaxf(x, dppf<0x128>(x)); x = fmaxf(x, dppf<0x124>(x)); x = fmaxf(x, dppf<0x122>(x)); x = fmaxf(x, dppf<0x121>(x)); return x; }
__device__ __forceinline__ float xrow_sum(float x) {
    auto s = __builtin_amdgcn_permlane16_swap(asu(x), asu(x), false, false); x = asf(s[0]) + asf(s[1]);
    auto t = __builtin_amdgcn_permlane32_swap(asu(x), asu(x), false, false); return asf(t[0]) + asf(t[1]); }
__device__ __forceinline__ float xrow_max(float x) {
    auto s = __builtin_amdgcn_permlane16_swap(asu(x), asu(x), false, false); x = fmaxf(asf(s[0]), asf(s[1]));
    auto t = __builtin_amdgcn_permlane32_swap(asu(x), asu(x), false, false); return fmaxf(asf(t[0]), asf(t[1])); }
__device__ __forceinline__ float wave_sum(float x) { return xrow_sum(row_sum(x)); }
__device__ __forceinline__ float wave_max(float x) { return xrow_max(row_max(x)); }
__device__ __forceinline__ float fdiv(float a, float b) { const float r = __builtin_amdgcn_rcpf(b); const float q = a * r; return fmaf(fmaf(-q, b, a), r, q); }
__device__ __forceinline__ float silu_bf(float v) { return fdiv(v, 1.f + __builtin_amdgcn_exp2f(fminf(-v * 1.4426950408889634f, 126.f))); }
__device__ __forceinline__ v4u silu8(v4u c) {
    v4u r;
#pragma unroll
    for (int i = 0; i < 4; ++i) r[i] = pack2(silu_bf(bflo(c[i])), silu_bf(bfhi(c[i])));
    return r; }
#define BAR() asm volatile("s_waitcnt lgkmcnt(0)\n\ts_barrier" ::: "memory")
#define LGKM0() asm volatile("s_waitcnt lgkmcnt(0)" ::: "memory")
#define VMW(n) asm volatile("s_waitcnt vmcnt(%0)" :: "i"(n) : "memory")
#define MEMBAR() asm volatile("" ::: "memory")
typedef __amdgpu_buffer_rsrc_t rsrc_t;
__device__ __forceinline__ v4u ld_sc1(rsrc_t r, u32 off) { return __builtin_amdgcn_raw_buffer_load_b128(r, off, 0, 16); }
__device__ __forceinline__ v2u ld_sc1_2(rsrc_t r, u32 off) { return __builtin_amdgcn_raw_buffer_load_b64(r, off, 0, 16); }
__device__ __forceinline__ void st_sc1(rsrc_t r, u32 off, v4u v) { __builtin_amdgcn_raw_buffer_store_b128(v, r, off, 0, 16); }
__device__ __forceinline__ void st_sc1_2(rsrc_t r, u32 off, v2u v) { __builtin_amdgcn_raw_buffer_store_b64(v, r, off, 0, 16); }
__device__ __forceinline__ f4 asf4(v4u v) { return __builtin_bit_cast(f4, v); }

__device__ __forceinline__ void dma_unit(v4u srd, u32 v0, u32 v1, u32 sA, u32 sB, u32 lds) {
    u32 keep;
    asm volatile("s_nop 4\n\ts_mov_b32 %0, m0\n\ts_mov_b32 m0, %6\n\ts_nop 0\n\t"
                 "buffer_load_dwordx4 %1, %3, %4 offen nt lds\n\ts_add_u32 m0, m0, 0x400\n\ts_nop 0\n\t"
                 "buffer_load_dwordx4 %2, %3, %4 offen nt lds\n\ts_add_u32 m0, m0, 0x400\n\ts_nop 0\n\t"
                 "buffer_load_dwordx4 %1, %3, %5 offen nt lds\n\ts_add_u32 m0, m0, 0x400\n\ts_nop 0\n\t"
                 "buffer_load_dwordx4 %2, %3, %5 offen nt lds\n\ts_mov_b32 m0, %0"
                 : "=&s"(keep) : "v"(v0), "v"(v1), "s"(srd), "s"(sA), "s"(sB), "s"(lds) : "memory", "scc");
}
__device__ __forceinline__ void dma_1k(v4u srd, u32 voff, u32 soff, u32 lds) {
    u32 keep;
    asm volatile("s_nop 4\n\ts_mov_b32 %0, m0\n\ts_mov_b32 m0, %4\n\ts_nop 0\n\tbuffer_load_dwordx4 %1, %2, %3 offen lds\n\ts_mov_b32 m0, %0"
                 : "=&s"(keep) : "v"(voff), "s"(srd), "s"(soff), "s"(lds) : "memory");
}
struct WStream { v4u srd; u32 base, pitch, v0, v1; };
__device__ __forceinline__ WStream mk_stream(v4u srd, u32 base, u32 pitch, int l) {
    WStream s; s.srd = srd; s.base = base; s.pitch = pitch;
    const u32 sw = 2u * ((l >> 4) & 1);
    s.v0 = (l >> 3) * pitch + (((l & 7) ^ sw) << 4);
    s.v1 = (8 + (l >> 3)) * pitch + (((l & 7) ^ (sw | 4u)) << 4);
    return s; }
__device__ __forceinline__ void issue(const WStream& s, int n, u32 lds) { const u32 a = s.base + (u32)n * 32u * s.pitch; dma_unit(s.srd, s.v0, s.v1, a, a + 16u * s.pitch, lds); }
__device__ __forceinline__ void read_unit(LAS unsigned char* L, const u32 (&ta)[4], int slot, v4u (&f)[4]) {
#pragma unroll
    for (int T = 0; T < 4; ++T) {
        const v2u lo = __builtin_bit_cast(v2u, __builtin_amdgcn_ds_read_tr16_b64_v4i16((LAS v4s*)(L + ta[T] + slot * 4096)));
        const v2u hi = __builtin_bit_cast(v2u, __builtin_amdgcn_ds_read_tr16_b64_v4i16((LAS v4s*)(L + ta[T] + slot * 4096 + 512)));
        f[T] = v4u{lo[0], lo[1], hi[0], hi[1]};
    } }
__device__ __forceinline__ f4 sel_tile(const f4 (&a)[4], int t) { return t == 0 ? a[0] : t == 1 ? a[1] : t == 2 ? a[2] : a[3]; }

template <int NJ>
__device__ __forceinline__ void kv_round(LAS unsigned char* L, const v4u (&K)[3 * NJ], const v4u (&V)[3 * NJ], const v4u (&q)[3], int nj, int w, int l,
                                         float& m, float& lsum, float (&acc)[24], bool first) {
    const int c = l & 7;
    float x[8];
#pragma unroll
    for (int j = 0; j < 8; ++j) {
        float s = 0.f;
        if (j < NJ) {
#pragma unroll
            for (int i = 0; i < 3; ++i)
#pragma unroll
                for (int r = 0; r < 4; ++r) s = dot2(K[3 * j + i][r], q[i][r], s);
        }
        x[j] = s;
    }
#pragma unroll
    for (int h = 0; h < 4; ++h) { const float a = x[h] + dppf<0x141>(x[h]), b = x[h + 4] + dppf<0x141>(x[h + 4]); x[h] = (c & 4) ? b : a; }
#pragma unroll
    for (int h = 0; h < 2; ++h) { const float a = x[h] + dppf<0x4E>(x[h]), b = x[h + 2] + dppf<0x4E>(x[h + 2]); x[h] = (c & 2) ? b : a; }
    { const float a = x[0] + dppf<0xB1>(x[0]), b = x[1] + dppf<0xB1>(x[1]); x[0] = (c & 1) ? b : a; }
    float s = rbf(rbf(x[0]) * 0.07216878364870322f);
    if (NJ < 8) s = c < nj ? s : -INFINITY;
    const float mw = wave_max(s);
    const float mn = first ? mw : fmaxf(m, mw);
    const float e = __builtin_amdgcn_exp2f((s - mn) * 1.4426950408889634f);
    const float ls = wave_sum(e);
    if (!first) {
        const float al = __builtin_amdgcn_exp2f((m - mn) * 1.4426950408889634f);
        lsum = lsum * al + ls;
#pragma unroll
        for (int i = 0; i < 24; ++i) acc[i] *= al;
    } else {
        lsum = ls;
#pragma unroll
        for (int i = 0; i < 24; ++i) acc[i] = 0.f;
    }
    m = mn;
    *(LAS u32*)(L + K_PX + w * 256 + l * 4) = pack2(e, 0.f);
    LGKM0();
    const v4u p0 = *(LAS v4u*)(L + K_PX + w * 256 + (l >> 3) * 32), p1 = *(LAS v4u*)(L + K_PX + w * 256 + (l >> 3) * 32 + 16);
#pragma unroll
    for (int j = 0; j < NJ; ++j) {
        const u32 plo = j < 4 ? p0[j & 3] : p1[j & 3], phi = plo << 16;
#pragma unroll
        for (int i = 0; i < 3; ++i)
#pragma unroll
            for (int r = 0; r < 4; ++r) {
                acc[i * 8 + 2 * r] = dot2(V[3 * j + i][r], plo, acc[i * 8 + 2 * r]);
                acc[i * 8 + 2 * r + 1] = dot2(V[3 * j + i][r], phi, acc[i * 8 + 2 * r + 1]);
            }
    }
}

__device__ __forceinline__ void kv_path(const Params& p, LAS unsigned char* L, int j, int w, int l) {
    const rsrc_t rws = __builtin_amdgcn_make_buffer_rsrc((void*)p.ws, 0, WS_END, 0x00020000);
    const rsrc_t rk = __builtin_amdgcn_make_buffer_rsrc((void*)p.kc, 0, 4 * NH * SKV * DH * 2, 0x00020000);
    const rsrc_t rv = __builtin_amdgcn_make_buffer_rsrc((void*)p.vc, 0, 4 * NH * SKV * DH * 2, 0x00020000);
    volatile LAS u32* misc = (volatile LAS u32*)(L + L_MISC);
    const int t3 = j / 3, pos = j - 3 * t3;
    const int slot = pos == 1 ? (w & 1) : 0;
    const int bh = 2 * t3 + (pos == 2 ? 1 : (pos == 1 ? slot : 0));
    const int wi = pos == 1 ? (w >> 1) : w;
    const int ub = pos == 0 ? 0 : (pos == 2 ? 43 : (slot == 0 ? 85 : 0));
    const int u1 = ub + 8 * wi;
    const int u2 = ub + (pos == 1 ? 32 : 64) + (wi == 0 ? 0 : 1 + 5 * wi), n2 = wi == 0 ? 6 : 5;
    if (threadIdx.x == 0) { misc[0] = 0; misc[1] = 0; misc[2] = 0; }
    BAR();
    STAMP(0);
    if (w == 7) {
        const u64 T = __hip_atomic_load((GAS u64*)(p.ws + WS_E), RLX_AGENT) + 1ull;
        const int ns = pos == 1 ? 2 : 1;
        for (int s = 0; s < ns; ++s) {
            const int sbh = 2 * t3 + (pos == 2 ? 1 : (pos == 1 ? s : 0)), spart = pos == 0 ? 0 : (pos == 2 ? 1 : (s == 0 ? 1 : 0));
            st_sc1(rws, l < 48 ? WS_OP + (sbh * 2 + spart) * 768 + l * 16 : OOB, v4u{SENT, SENT, SENT, SENT});
            st_sc1_2(rws, l == 0 ? WS_ML + ((sbh & 31) * 4 + (sbh >> 5)) * 16 + spart * 8 : OOB, v2u{SENT, SENT});
        }
        VMW(0);
        if (l == 0) {
            __hip_atomic_store((GAS u64*)(p.ws + WS_FLAG) + blockIdx.x, T, RLX_AGENT);
            misc[4] = (u32)T; misc[5] = (u32)(T >> 32); LGKM0(); misc[2] = 1;
        }
    }
    const u32 vo = (l >> 3) * 384 + (l & 7) * 16;
    const u32 rowb = ((u32)bh * SKV + (SKV - WIN)) * 384u;
    float m, lsum, acc[24];
    v4u q[3];
    {
        v4u K[24], V[24];
        const u32 base = rowb + (u32)u1 * 3072u;
#pragma unroll
        for (int jj = 0; jj < 8; ++jj)
#pragma unroll
            for (int i = 0; i < 3; ++i) K[jj * 3 + i] = __builtin_amdgcn_raw_buffer_load_b128(rk, vo + i * 128, base + jj * 3072, 2);
#pragma unroll
        for (int jj = 0; jj < 8; ++jj)
#pragma unroll
            for (int i = 0; i < 3; ++i) V[jj * 3 + i] = __builtin_amdgcn_raw_buffer_load_b128(rv, vo + i * 128, base + jj * 3072, 2);
        __builtin_amdgcn_sched_barrier(0);
        if (w == 0) STAMP(1);
        if (w == 7) STAMP(2);
        if (w == 0 || (w == 1 && pos == 1)) {
            for (int sp = 0; misc[2] == 0 && sp < SPIN_L; ++sp) __builtin_amdgcn_s_sleep(1);
            const u32 tlo = misc[4], thi = misc[5];
            for (int sp = 0; sp < SPIN_G; ++sp) {
                MEMBAR();
                const v2u f = ld_sc1_2(rws, WS_FLAG + l * 8);
                if (__builtin_amdgcn_ballot_w64(f[0] != tlo || f[1] != thi) == 0ull) break;
            }
            const u32 qo = l < 48 ? WS_QP + (u32)((bh >> 5) * NQ + (bh & 31) * DH + 4 * l) * 4u : OOB;
            f4 qs;
            for (int sp = 0; sp < SPIN_G; ++sp) {
                MEMBAR();
                v4u a[5]; u32 mx = 0;
#pragma unroll
                for (int ks = 0; ks < 5; ++ks) a[ks] = ld_sc1(rws, qo + (l < 48 ? ks * (4 * NQ * 4) : 0));
#pragma unroll
                for (int ks = 0; ks < 5; ++ks) mx = vmax4(mx, a[ks]);
                qs = (((asf4(a[0]) + asf4(a[1])) + asf4(a[2])) + asf4(a[3])) + asf4(a[4]);
                if (all_valid(mx)) break;
            }
            if (l < 48) *(LAS v2u*)(L + K_QS + slot * 384 + l * 8) = v2u{pack2(qs[0], qs[1]), pack2(qs[2], qs[3])};
            LGKM0();
            misc[slot] = 1;
            if (w == 0) STAMP(3);
        } else {
            for (int sp = 0; misc[slot] == 0 && sp < SPIN_L; ++sp) __builtin_amdgcn_s_sleep(1);
        }
        MEMBAR();
#pragma unroll
        for (int i = 0; i < 3; ++i) q[i] = *(LAS v4u*)(L + K_QS + slot * 384 + ((l & 7) + 8 * i) * 16);
        kv_round<8>(L, K, V, q, 8, w, l, m, lsum, acc, true);
    }
    if (w == 0) STAMP(4);
    if (w < 4) {
        v4u K[18], V[18];
        const u32 base = rowb + (u32)u2 * 3072u;
#pragma unroll
        for (int jj = 0; jj < 5; ++jj)
#pragma unroll
            for (int i = 0; i < 3; ++i) K[jj * 3 + i] = __builtin_amdgcn_raw_buffer_load_b128(rk, vo + i * 128, base + jj * 3072, 2);
        const u32 vo5 = n2 == 6 ? vo : OOB;
#pragma unroll
        for (int i = 0; i < 3; ++i) K[15 + i] = __builtin_amdgcn_raw_buffer_load_b128(rk, vo5 + i * 128, base + 5 * 3072, 2);
#pragma unroll
        for (int jj = 0; jj < 5; ++jj)
#pragma unroll
            for (int i = 0; i < 3; ++i) V[jj * 3 + i] = __builtin_amdgcn_raw_buffer_load_b128(rv, vo + i * 128, base + jj * 3072, 2);
#pragma unroll
        for (int i = 0; i < 3; ++i) V[15 + i] = __builtin_amdgcn_raw_buffer_load_b128(rv, vo5 + i * 128, base + 5 * 3072, 2);
        __builtin_amdgcn_sched_barrier(0);
        kv_round<6>(L, K, V, q, n2, w, l, m, lsum, acc, false);
    }
    if (w == 0) STAMP(5);
    if (w == 7) STAMP(6);
    float y[12], z[6];
#pragma unroll
    for (int n = 0; n < 12; ++n) { auto s = __builtin_amdgcn_permlane32_swap(asu(acc[n]), asu(acc[n + 12]), false, false); y[n] = asf(s[0]) + asf(s[1]); }
#pragma unroll
    for (int n = 0; n < 6; ++n) { auto s = __builtin_amdgcn_permlane16_swap(asu(y[n]), asu(y[n + 6]), false, false); z[n] = asf(s[0]) + asf(s[1]); }
#pragma unroll
    for (int n = 0; n < 3; ++n) {
        const float a = z[n] + dppf<0x128>(z[n]), b = z[n + 3] + dppf<0x128>(z[n + 3]);
        const int ai = 3 * (l >> 3) + n;
        *(LAS float*)(L + K_PART + w * 768 + (64 * (ai >> 3) + 8 * (l & 7) + (ai & 7)) * 4) = (l & 8) ? b : a;
    }
    if (l == 0) *(LAS v2u*)(L + K_ML + w * 8) = v2u{asu(m), asu(lsum)};
    BAR();
    if (w == 0) STAMP(7);
    if (w == 0 || (w == 1 && pos == 1)) {
        const int part = pos == 0 ? 0 : (pos == 2 ? 1 : (slot == 0 ? 1 : 0));
        float mm[8], ll[8], M = -INFINITY;
#pragma unroll
        for (int i = 0; i < 8; ++i) {
            const v2u t = *(LAS v2u*)(L + K_ML + i * 8);
            const bool mine = pos != 1 || (i & 1) == slot;
            mm[i] = mine ? asf(t[0]) : -INFINITY; ll[i] = mine ? asf(t[1]) : 0.f; M = fmaxf(M, mm[i]);
        }
        f4 o = {0.f, 0.f, 0.f, 0.f}; float Ls = 0.f;
        const int lr = l < 48 ? l : 47;
#pragma unroll
        for (int i = 0; i < 8; ++i) {
            const float sc = __builtin_amdgcn_exp2f((mm[i] - M) * 1.4426950408889634f);
            const bool mine = pos != 1 || (i & 1) == slot;
            const f4 a = *(LAS f4*)(L + K_PART + i * 768 + lr * 16);
            if (mine) o += a * sc;
            Ls += ll[i] * sc;
        }
        st_sc1(rws, l < 48 ? WS_OP + (bh * 2 + part) * 768 + l * 16 : OOB, canon4(o));
        st_sc1_2(rws, l == 0 ? WS_ML + ((bh & 31) * 4 + (bh >> 5)) * 16 + part * 8 : OOB, v2u{canon(asu(M)), canon(asu(Ls))});
        if (w == 0) STAMP(8);
    }
}

enum { R_SA = 0, R_SB = 1, R_SC = 2 };
template <int ROLE>
__device__ __forceinline__ void w_path(const Params& p, LAS unsigned char* L, int u, int w, int l) {
    const rsrc_t rws = __builtin_amdgcn_make_buffer_rsrc((void*)p.ws, 0, WS_END, 0x00020000);
    const int tid = threadIdx.x, g = l >> 4, tq = (l & 15) >> 2, b4 = l & 3;
    const u32 lbase = (u32)(size_t)L, stg = lbase + w * 16384;
    const bool hasF1 = u < 50, hasF2 = u < 60;
    const int kf = u / 10, lp = u - 10 * kf, kf2 = u / 12, cs = u - 12 * kf2;
    const int ri = ROLE == R_SA ? u : (ROLE == R_SB ? u - 30 : u - 54);
    const WStream sF1 = mk_stream(mk_srd(p.wm, hasF1 ? DM * 3840 * 2 : 0), (u32)((128 * kf + 32 * (w >> 1)) * 7680 + (128 * lp + 64 * (w & 1)) * 2), 7680, l);
    const WStream sF2 = mk_stream(mk_srd(p.wq, hasF2 ? DM * NQ * 2 : 0), (u32)(128 * kf2 * 12288 + (512 * cs + 64 * w) * 2), 12288, l);
    const bool hasM = ROLE == R_SC || (ROLE == R_SB && ri < 20 && w < 4);
    const int mcol = ROLE == R_SC ? ((w < 4 ? 1280 : 3200) + 64 * ri) : 1920 + 64 * ri;
    const WStream sM = mk_stream(mk_srd(p.wm, hasM ? DM * 3840 * 2 : 0), (u32)(160 * (w & 3) * 7680 + mcol * 2), 7680, l);
    const int ra = ri / 10, ca = ri - 10 * ra;
    const WStream sT = ROLE == R_SA ? mk_stream(mk_srd(p.wo, NQ * DM * 2), (u32)((2048 * ra + 256 * w) * 1280 + ca * 128), 1280, l)
                     : ROLE == R_SB ? mk_stream(mk_srd((w & 1) ? p.wu : p.wg, DM * FF * 2), (u32)(160 * (w >> 1) * 3072 + ri * 128), 3072, l)
                                    : mk_stream(mk_srd(p.wd, FF * DM * 2), (u32)(192 * w * 1280 + ri * 128), 1280, l);
    constexpr int NM = ROLE == R_SA ? 0 : 5, NT = ROLE == R_SA ? 8 : (ROLE == R_SB ? 5 : 6), NA = NM + NT;
    auto issueA = [&](int n) { if (n < NM) issue(sM, n, stg + ((n + 1) & 3) * 4096); else issue(sT, n - NM, stg + ((n + 1) & 3) * 4096); };
    u32 ta[4];
    { const u32 f = 2u * (tq >> 1) | 4u * (g & 1);
#pragma unroll
      for (int T = 0; T < 4; ++T) ta[T] = w * 16384 + (8 * g + tq) * 128 + ((((u32)(2 * T)) ^ f) + (b4 >> 1)) * 16 + (b4 & 1) * 8; }
    const u32 dcol = 16 * tq + 4 * g;
    STAMP(0);
    u64 T;
    asm volatile("s_nop 4\n\tglobal_load_dwordx2 %0, %1, %2 sc1" : "=v"(T) : "v"(0u), "s"(p.ws + WS_E) : "memory");
    { const v4u s4 = {SENT, SENT, SENT, SENT};
      st_sc1(rws, hasF2 ? WS_QP + (u32)((kf2 * 4 + b4) * NQ + 512 * cs + 64 * w + dcol) * 4u : OOB, s4);
      if (w < 2) st_sc1(rws, hasF1 ? WS_F1P + (u32)((kf * 4 + b4) * 1280 + 128 * lp + 64 * w + dcol) * 4u : OOB, s4);
      if (w == 0) {
          if (ROLE == R_SA) st_sc1(rws, WS_AP + (u32)((ra * 4 + b4) * DM + 64 * ca + dcol) * 4u, s4);
          if (ROLE == R_SB) st_sc1_2(rws, WS_HID + (u32)(b4 * FF + 64 * ri + dcol) * 2u, v2u{SENT, SENT});
          if (ROLE != R_SA) st_sc1_2(rws, hasM ? WS_MODF + (u32)(b4 * 2560 + mcol - 1280 + dcol) * 2u : OOB, v2u{SENT, SENT});
      } }
    if (w < 5) { dma_1k(mk_srd(p.c, 5120), l * 16, w * 1024, lbase + L_CS + w * 1024); dma_1k(mk_srd(p.x, 5120), l * 16, w * 1024, lbase + L_XS + w * 1024); }
    issue(sF1, 0, stg);
    issue(sF2, 0, stg + 4096); issue(sF2, 1, stg + 8192); issue(sF2, 2, stg + 12288);
    VMW(12);
    BAR();
    STAMP(1);
    asm volatile("" : "+v"(T)); T += 1ull;
    if (tid == 0) __hip_atomic_store((GAS u64*)(p.ws + WS_FLAG) + blockIdx.x, T, RLX_AGENT);
    {
        v4u f[4]; read_unit(L, ta, 0, f);
        const v4u a = silu8(*(LAS v4u*)(L + L_CS + b4 * 1280 + (128 * kf + 32 * (w >> 1) + 8 * g) * 2));
        f4 d[4];
#pragma unroll
        for (int T2 = 0; T2 < 4; ++T2) d[T2] = mfma(f[T2], a, f4{0.f, 0.f, 0.f, 0.f});
        *(LAS f4*)(L + L_RED + w * 1024 + l * 16) = sel_tile(d, tq);
        if (w >= 4) {
            float xv[10], s = 0.f;
#pragma unroll
            for (int i = 0; i < 5; ++i) { const u32 t = *(LAS u32*)(L + L_XS + (w - 4) * 1280 + (l + 64 * i) * 4); xv[2 * i] = bflo(t); xv[2 * i + 1] = bfhi(t); s += xv[2 * i] + xv[2 * i + 1]; }
            const float mean = wave_sum(s) * (1.f / DM);
            float v = 0.f;
#pragma unroll
            for (int i = 0; i < 10; ++i) v += (xv[i] - mean) * (xv[i] - mean);
            const float var = wave_sum(v) * (1.f / DM);
            if (l == 0) *(LAS v2u*)(L + L_ST + (w - 4) * 8) = v2u{asu(rbf(mean)), asu(rbf(__builtin_sqrtf(rbf(rbf(var) + 1e-5f))))};
        }
        LGKM0();
        issue(sF2, 3, stg);
        BAR();
        if (w < 2) {
            f4 s = *(LAS f4*)(L + L_RED + w * 1024 + l * 16);
#pragma unroll
            for (int i = 1; i < 4; ++i) s += *(LAS f4*)(L + L_RED + (2 * i + w) * 1024 + l * 16);
            st_sc1(rws, hasF1 ? WS_F1P + (u32)((kf * 4 + b4) * 1280 + 128 * lp + 64 * w + dcol) * 4u : OOB, canon4(s));
        }
        STAMP(2);
    }
    v4u fq[4][4];
#pragma unroll
    for (int n = 0; n < 4; ++n) { VMW(12); read_unit(L, ta, (n + 1) & 3, fq[n]); LGKM0(); issueA(n); }
    STAMP(3);
    if (w < 2) {
        if (w == 0) {
            const u32 tlo = (u32)T, thi = (u32)(T >> 32);
            for (int sp = 0; sp < SPIN_G; ++sp) {
                MEMBAR();
                const v4u f0 = ld_sc1(rws, WS_FLAG + l * 16), f1 = ld_sc1(rws, WS_FLAG + 1024 + l * 16);
                const bool bad = f0[0] != tlo || f0[1] != thi || f0[2] != tlo || f0[3] != thi || f1[0] != tlo || f1[1] != thi || f1[2] != tlo || f1[3] != thi;
                if (__builtin_amdgcn_ballot_w64(bad) == 0ull) break;
            }
            if (u == 0 && l == 0) __hip_atomic_store((GAS u64*)(p.ws + WS_E), T, RLX_AGENT);
        }
    }
    BAR();
    if (w < 2) {
        const int hb = tid >> 5, hi = tid & 31, col = 128 * kf2 + 4 * hi;
        const u32 o = hasF2 ? WS_F1P + (u32)(hb * 1280 + col) * 4u : OOB;
        f4 sa, sc;
        for (int sp = 0; sp < SPIN_G; ++sp) {
            MEMBAR();
            v4u a[5], c2[5]; u32 mx = 0;
#pragma unroll
            for (int ks = 0; ks < 5; ++ks) { a[ks] = ld_sc1(rws, o + (hasF2 ? ks * 20480 : 0)); c2[ks] = ld_sc1(rws, o + (hasF2 ? ks * 20480 + 2560 : 0)); }
#pragma unroll
            for (int ks = 0; ks < 5; ++ks) mx = vmax4(vmax4(mx, a[ks]), c2[ks]);
            sa = (((asf4(a[0]) + asf4(a[1])) + asf4(a[2])) + asf4(a[3])) + asf4(a[4]);
            sc = (((asf4(c2[0]) + asf4(c2[1])) + asf4(c2[2])) + asf4(c2[3])) + asf4(c2[4]);
            if (all_valid(mx)) break;
        }
        const v2u st = *(LAS v2u*)(L + L_ST + hb * 8), xr = *(LAS v2u*)(L + L_XS + hb * 1280 + (hasF2 ? col : 0) * 2);
        const float xv[4] = {bflo(xr[0]), bfhi(xr[0]), bflo(xr[1]), bfhi(xr[1])};
        float h[4];
#pragma unroll
        for (int i = 0; i < 4; ++i) h[i] = rbf(rbf(fdiv(rbf(xv[i] - asf(st[0])), asf(st[1]))) * rbf(1.f + rbf(sc[i]))) + rbf(sa[i]);
        *(LAS v2u*)(L + L_HB + hb * 256 + hi * 8) = v2u{pack2(h[0], h[1]), pack2(h[2], h[3])};
    }
    BAR();
    STAMP(4);
    {
        f4 d[4] = {};
#pragma unroll
        for (int n = 0; n < 4; ++n) {
            const v4u a = *(LAS v4u*)(L + L_HB + b4 * 256 + (32 * n + 8 * g) * 2);
#pragma unroll
            for (int T2 = 0; T2 < 4; ++T2) d[T2] = mfma(fq[n][T2], a, d[T2]);
        }
        st_sc1(rws, hasF2 ? WS_QP + (u32)((kf2 * 4 + b4) * NQ + 512 * cs + 64 * w + dcol) * 4u : OOB, canon4(sel_tile(d, tq)));
    }
    STAMP(5);
    if (NM) {
        f4 d[4] = {};
#pragma unroll
        for (int n = 0; n < NM; ++n) {
            VMW(12);
            v4u f[4]; read_unit(L, ta, (n + 1) & 3, f);
            const v4u a = silu8(*(LAS v4u*)(L + L_CS + b4 * 1280 + (160 * (w & 3) + 32 * n + 8 * g) * 2));
            LGKM0();
            issueA(n + 4);
#pragma unroll
            for (int T2 = 0; T2 < 4; ++T2) d[T2] = mfma(f[T2], a, d[T2]);
        }
        *(LAS f4*)(L + L_RED + w * 1024 + l * 16) = sel_tile(d, tq);
        BAR();
        if ((w & 3) == 0) {
            f4 s = *(LAS f4*)(L + L_RED + w * 1024 + l * 16);
#pragma unroll
            for (int i = 1; i < 4; ++i) s += *(LAS f4*)(L + L_RED + (w + i) * 1024 + l * 16);
            const v2u pk = {canon(pack2(s[0], s[1])), canon(pack2(s[2], s[3]))};
            if (w == 0) st_sc1_2(rws, hasM ? WS_MODF + (u32)(b4 * 2560 + mcol - 1280 + dcol) * 2u : OOB, pk);
            if (ROLE == R_SC) *(LAS v2u*)(L + (w == 0 ? L_GA : L_GB) + l * 8) = pk;
        }
        BAR();
    }
    v4u ft[NT][4];
#pragma unroll
    for (int n = 0; n < NT; ++n) {
        const int a = NM + n, younger = (NA - 1 - a) < 3 ? (NA - 1 - a) : 3;
        if (younger == 3) VMW(12); else if (younger == 2) VMW(8); else if (younger == 1) VMW(4); else VMW(0);
        read_unit(L, ta, (a + 1) & 3, ft[n]);
        if (a + 4 < NA) { LGKM0(); issueA(a + 4); }
    }
    LGKM0();
    STAMP(6);
    if (ROLE == R_SA) {
        f4 d[2][4] = {};
        u32 oo[2], mo[2];
#pragma unroll
        for (int r = 0; r < 2; ++r) {
            const int row = 2048 * ra + 256 * w + 32 * (4 * r + tq) + 8 * g, hd = row / DH, dd = row - hd * DH;
            oo[r] = WS_OP + (u32)((b4 * 32 + hd) * 2) * 768u + dd * 4; mo[r] = WS_ML + (u32)(hd * 4 + b4) * 16u;
        }
        bool done0 = false, done1 = false;
        for (int sp = 0; sp < SPIN_G && !(done0 && done1); ++sp) {
            MEMBAR();
            v4u a[2][5];
#pragma unroll
            for (int r = 0; r < 2; ++r) if (!(r ? done1 : done0)) {
                a[r][0] = ld_sc1(rws, oo[r]); a[r][1] = ld_sc1(rws, oo[r] + 16); a[r][2] = ld_sc1(rws, oo[r] + 768); a[r][3] = ld_sc1(rws, oo[r] + 784); a[r][4] = ld_sc1(rws, mo[r]);
            }
#pragma unroll
            for (int r = 0; r < 2; ++r) if (!(r ? done1 : done0)) {
                u32 mx = 0;
#pragma unroll
                for (int i = 0; i < 5; ++i) mx = vmax4(mx, a[r][i]);
                if (all_valid(mx) || sp == SPIN_G - 1) {
                    const float m0 = asf(a[r][4][0]), l0 = asf(a[r][4][1]), m1 = asf(a[r][4][2]), l1 = asf(a[r][4][3]), M = fmaxf(m0, m1);
                    const float e0 = __builtin_amdgcn_exp2f((m0 - M) * 1.4426950408889634f), e1 = __builtin_amdgcn_exp2f((m1 - M) * 1.4426950408889634f);
                    const float inv = fdiv(1.f, e0 * l0 + e1 * l1), w0 = e0 * inv, w1 = e1 * inv;
                    const f4 x0 = asf4(a[r][0]) * w0 + asf4(a[r][2]) * w1, x1 = asf4(a[r][1]) * w0 + asf4(a[r][3]) * w1;
                    const v4u bq = {pack2(x0[0], x0[1]), pack2(x0[2], x0[3]), pack2(x1[0], x1[1]), pack2(x1[2], x1[3])};
                    const v4u bk[4] = {bq, ror4k<1>(bq), ror4k<2>(bq), ror4k<3>(bq)};
#pragma unroll
                    for (int s = 0; s < 4; ++s)
#pragma unroll
                        for (int T2 = 0; T2 < 4; ++T2) d[r][T2] = mfma(ft[4 * r + s][T2], bk[(T2 - s) & 3], d[r][T2]);
                    if (r) done1 = true; else done0 = true;
                }
            }
        }
        if (w == 0) STAMP(7);
        *(LAS f4*)(L + L_RED + w * 1024 + l * 16) = sel_tile(d[0], tq) + sel_tile(d[1], tq);
        BAR();
        if (w == 0) {
            STAMP(8);
            f4 s = *(LAS f4*)(L + L_RED + l * 16);
#pragma unroll
            for (int i = 1; i < 8; ++i) s += *(LAS f4*)(L + L_RED + i * 1024 + l * 16);
            st_sc1(rws, WS_AP + (u32)((ra * 4 + b4) * DM + 64 * ca + dcol) * 4u, canon4(s));
            STAMP(9);
        }
    } else if (ROLE == R_SB) {
        const int i16 = l & 15, col = 128 * w + 8 * i16;
        float x1[8];
        v4u sbv, cbv;
        if (w < 5) {
            v4u gav;
            const u32 mo = WS_MODF + (u32)(g * 2560 + col) * 2u;
            for (int sp = 0; sp < SPIN_G; ++sp) {
                MEMBAR();
                gav = ld_sc1(rws, mo); sbv = ld_sc1(rws, mo + 1280); cbv = ld_sc1(rws, mo + 2560);
                if (all_valid(vmax4(vmax4(vmax4(0u, gav), sbv), cbv))) break;
            }
            const v4u xr = *(LAS v4u*)(L + L_XS + g * 1280 + col * 2);
            if (w == 0) STAMP(10);
            const u32 ao = WS_AP + (u32)(g * DM + col) * 4u;
            f4 s0, s1;
            for (int sp = 0; sp < SPIN_G; ++sp) {
                MEMBAR();
                v4u a[6]; u32 mx = 0;
#pragma unroll
                for (int r = 0; r < 3; ++r) { a[2 * r] = ld_sc1(rws, ao + r * 10240); a[2 * r + 1] = ld_sc1(rws, ao + r * 10240 + 16); }
#pragma unroll
                for (int r = 0; r < 6; ++r) mx = vmax4(mx, a[r]);
                s0 = (asf4(a[0]) + asf4(a[2])) + asf4(a[4]); s1 = (asf4(a[1]) + asf4(a[3])) + asf4(a[5]);
                if (all_valid(mx)) break;
            }
            if (w == 0) STAMP(11);
            float s = 0.f;
#pragma unroll
            for (int i = 0; i < 4; ++i) {
                const float a0 = i < 2 ? s0[2 * i] : s1[2 * i - 4], a1 = i < 2 ? s0[2 * i + 1] : s1[2 * i - 3];
                x1[2 * i] = rbf(bflo(xr[i]) + rbf(bflo(gav[i]) * rbf(a0))); x1[2 * i + 1] = rbf(bfhi(xr[i]) + rbf(bfhi(gav[i]) * rbf(a1)));
                s += x1[2 * i] + x1[2 * i + 1];
            }
            const float mw = row_sum(s) * (1.f / 128.f);
            float v = 0.f;
#pragma unroll
            for (int i = 0; i < 8; ++i) v += (x1[i] - mw) * (x1[i] - mw);
            v = row_sum(v);
            if (i16 == 0) *(LAS v2u*)(L + L_ST + 64 + (g * 5 + w) * 8) = v2u{asu(mw), asu(v)};
        }
        BAR();
        if (w < 5) {
            float mm[5], M2 = 0.f, mean = 0.f;
#pragma unroll
            for (int i = 0; i < 5; ++i) { const v2u t = *(LAS v2u*)(L + L_ST + 64 + (g * 5 + i) * 8); mm[i] = asf(t[0]); M2 += asf(t[1]); mean += mm[i]; }
            mean *= 0.2f;
#pragma unroll
            for (int i = 0; i < 5; ++i) M2 += 128.f * (mm[i] - mean) * (mm[i] - mean);
            const float mb = rbf(mean), den = rbf(__builtin_sqrtf(rbf(rbf(M2 * (1.f / DM)) + 1e-5f)));
            v4u hv;
#pragma unroll
            for (int i = 0; i < 4; ++i) {
                const float h0 = rbf(rbf(fdiv(rbf(x1[2 * i] - mb), den)) * rbf(1.f + bflo(cbv[i]))) + bflo(sbv[i]);
                const float h1 = rbf(rbf(fdiv(rbf(x1[2 * i + 1] - mb), den)) * rbf(1.f + bfhi(cbv[i]))) + bfhi(sbv[i]);
                hv[i] = pack2(h0, h1);
            }
            *(LAS v4u*)(L + L_HB + g * 1280 + col * 2) = hv;
        }
        BAR();
        if (w == 0) STAMP(12);
        f4 d[4] = {};
#pragma unroll
        for (int n = 0; n < 5; ++n) {
            const v4u a = *(LAS v4u*)(L + L_HB + b4 * 1280 + (160 * (w >> 1) + 32 * n + 8 * g) * 2);
#pragma unroll
            for (int T2 = 0; T2 < 4; ++T2) d[T2] = mfma(ft[n][T2], a, d[T2]);
        }
        *(LAS f4*)(L + L_RED + w * 1024 + l * 16) = sel_tile(d, tq);
        BAR();
        if (w == 0) {
            STAMP(13);
            f4 gt = *(LAS f4*)(L + L_RED + l * 16), up = *(LAS f4*)(L + L_RED + 1024 + l * 16);
#pragma unroll
            for (int i = 1; i < 4; ++i) { gt += *(LAS f4*)(L + L_RED + 2 * i * 1024 + l * 16); up += *(LAS f4*)(L + L_RED + (2 * i + 1) * 1024 + l * 16); }
            float hh[4];
#pragma unroll
            for (int i = 0; i < 4; ++i) { const float gg = rbf(gt[i]); hh[i] = (gg * (gg > 0.f ? 1.f : 0.f)) * rbf(up[i]); }
            st_sc1_2(rws, WS_HID + (u32)(b4 * FF + 64 * ri + dcol) * 2u, v2u{canon(pack2(hh[0], hh[1])), canon(pack2(hh[2], hh[3]))});
            STAMP(14);
        }
    } else {
        f4 x1 = {0.f, 0.f, 0.f, 0.f};
        if (w == 0) {
            const u32 ao = WS_AP + (u32)(b4 * DM + 64 * ri + dcol) * 4u;
            f4 s;
            for (int sp = 0; sp < SPIN_G; ++sp) {
                MEMBAR();
                const v4u a0 = ld_sc1(rws, ao), a1 = ld_sc1(rws, ao + 10240), a2 = ld_sc1(rws, ao + 20480);
                s = (asf4(a0) + asf4(a1)) + asf4(a2);
                if (all_valid(vmax4(vmax4(vmax4(0u, a0), a1), a2))) break;
            }
            const v2u xr = *(LAS v2u*)(L + L_XS + b4 * 1280 + (64 * ri + dcol) * 2), ga = *(LAS v2u*)(L + L_GA + l * 8);
            x1[0] = rbf(bflo(xr[0]) + rbf(bflo(ga[0]) * rbf(s[0]))); x1[1] = rbf(bfhi(xr[0]) + rbf(bfhi(ga[0]) * rbf(s[1])));
            x1[2] = rbf(bflo(xr[1]) + rbf(bflo(ga[1]) * rbf(s[2]))); x1[3] = rbf(bfhi(xr[1]) + rbf(bfhi(ga[1]) * rbf(s[3])));
            STAMP(15);
        }
        const u32 h0 = WS_HID + (u32)(b4 * FF + 192 * w + 32 * tq + 8 * g) * 2u, h1 = tq < 2 ? h0 + 256 : OOB;
        v4u a0, a1;
        for (int sp = 0; sp < SPIN_G; ++sp) {
            MEMBAR();
            a0 = ld_sc1(rws, h0); a1 = ld_sc1(rws, h1);
            if (all_valid(vmax4(vmax4(0u, a0), a1))) break;
        }
        if (w == 0) STAMP(16);
        const v4u bk[4] = {a0, ror4k<1>(a0), ror4k<2>(a0), ror4k<3>(a0)}, bl[4] = {a1, ror4k<1>(a1), ror4k<2>(a1), ror4k<3>(a1)};
        f4 d[4] = {};
#pragma unroll
        for (int s = 0; s < 6; ++s)
#pragma unroll
            for (int T2 = 0; T2 < 4; ++T2) d[T2] = mfma(ft[s][T2], s < 4 ? bk[(T2 - s) & 3] : bl[(T2 - s) & 3], d[T2]);
        *(LAS f4*)(L + L_RED + w * 1024 + l * 16) = sel_tile(d, tq);
        BAR();
        if (w == 0) {
            STAMP(17);
            f4 s = *(LAS f4*)(L + L_RED + l * 16);
#pragma unroll
            for (int i = 1; i < 8; ++i) s += *(LAS f4*)(L + L_RED + i * 1024 + l * 16);
            const v2u gb = *(LAS v2u*)(L + L_GB + l * 8);
            const v2u o = {pack2(x1[0] + rbf(bflo(gb[0]) * rbf(s[0])), x1[1] + rbf(bfhi(gb[0]) * rbf(s[1]))),
                           pack2(x1[2] + rbf(bflo(gb[1]) * rbf(s[2])), x1[3] + rbf(bfhi(gb[1]) * rbf(s[3])))};
            *(GAS v2u*)(p.out + b4 * DM + 64 * ri + dcol) = o;
            STAMP(18);
        }
    }
}

__global__ void __launch_bounds__(512, 2) fused_block(Params p) {
    extern __shared__ __attribute__((aligned(16))) unsigned char smem[];
    LAS unsigned char* L = (LAS unsigned char*)smem;
    const int l = threadIdx.x & 63, w = __builtin_amdgcn_readfirstlane(threadIdx.x >> 6), c = blockIdx.x;
    if (c >= NW) kv_path(p, L, c - NW, w, l);
    else if (c < 30) w_path<R_SA>(p, L, c, w, l);
    else if (c < 54) w_path<R_SB>(p, L, c, w, l);
    else w_path<R_SC>(p, L, c, w, l);
}

extern "C" void kernel_launch(void* const* d_in, const int* in_sizes, int n_in, void* d_out, int out_size, void* d_ws, size_t ws_size, hipStream_t stream) {
    Params p;
    p.x = (const u16*)d_in[0]; p.kc = (const u16*)d_in[1]; p.vc = (const u16*)d_in[2]; p.wq = (const u16*)d_in[3]; p.wo = (const u16*)d_in[4];
    p.wg = (const u16*)d_in[6]; p.wu = (const u16*)d_in[7]; p.wd = (const u16*)d_in[8]; p.c = (const u16*)d_in[9]; p.wm = (const u16*)d_in[10];
    p.out = (u16*)d_out; p.ws = (unsigned char*)d_ws;
    (void)hipFuncSetAttribute((const void*)fused_block, hipFuncAttributeMaxDynamicSharedMemorySize, (int)L_END);
    hipLaunchKernelGGL(fused_block, dim3(256), dim3(512), L_END, stream, p);
}
```

```cpp
#include <hip/hip_runtime.h>
#include <stdint.h>
#pragma clang fp contract(off)

typedef unsigned short u16;
typedef unsigned u32;
typedef unsigned long long u64;
typedef u32 v4u __attribute__((ext_vector_type(4)));
typedef u32 v2u __attribute__((ext_vector_type(2)));
typedef float f4 __attribute__((ext_vector_type(4)));
typedef short v4s __attribute__((ext_vector_type(4)));
typedef __bf16 bf8 __attribute__((ext_vector_type(8)));
typedef __bf16 bf2 __attribute__((ext_vector_type(2)));
#define LAS __attribute__((address_space(3)))
#define GAS __attribute__((address_space(1)))
#define RLX_AGENT __ATOMIC_RELAXED, __HIP_MEMORY_SCOPE_AGENT

#define INIT_MODE 1

constexpr int DM = 640, NH = 32, SKV = 4096, DH = 192, WIN = 1024, FF = 1536, NQ = NH * DH;
constexpr u32 SENT = 0xFFFFFFFFu;
constexpr int NW = 64;
constexpr u32 WS_E = 0, WS_FLAG = 4096, WS_F1P = 8192, WS_MODF = WS_F1P + 5 * 4 * 1280 * 4, WS_QP = WS_MODF + 4 * 2560 * 2,
              WS_OP = WS_QP + 5 * 4 * NQ * 4, WS_ML = WS_OP + 128 * 2 * DH * 4, WS_AP = WS_ML + 32 * 4 * 16, WS_HID = WS_AP + 3 * 4 * DM * 4,
              WS_END = WS_HID + 4 * FF * 2;
constexpr u32 OOB = 0x7FFFFF00u;
constexpr u32 L_XS = 131072, L_CS = L_XS + 5120, L_HB = L_CS + 5120, L_RED = L_HB + 5120, L_ST = L_RED + 8192, L_GA = L_ST + 256, L_GB = L_GA + 1024,
              L_X1 = L_GB + 1024, L_MISC = L_X1 + 1024, L_STAMP = L_MISC + 64, L_END = L_STAMP + 256;
constexpr u32 K_QS = 0, K_PX = 1024, K_PART = K_PX + 8 * 256, K_ML = K_PART + 8 * 768, K_END = K_ML + 64;
constexpr float LOG2E = 1.4426950408889634f;
static_assert(L_END <= 163840, "LDS");
constexpr int SPIN_G = 40000, SPIN_L = 2000000;

#define STAMP(i) do {} while (0)

struct Params { const u16 *x, *kc, *vc, *wq, *wo, *wg, *wu, *wd, *c, *wm; u16* out; unsigned char* ws; };

__device__ __forceinline__ v4u mk_srd(const void* p, u32 bytes) { const u64 a = (u64)p; return v4u{(u32)a, (u32)(a >> 32) & 0xffffu, bytes, 0x00020000u}; }
__device__ __forceinline__ float asf(u32 u) { return __builtin_bit_cast(float, u); }
__device__ __forceinline__ u32 asu(float f) { return __builtin_bit_cast(u32, f); }
__device__ __forceinline__ float bflo(u32 u) { return asf(u << 16); }
__device__ __forceinline__ float bfhi(u32 u) { return asf(u & 0xffff0000u); }
__device__ __forceinline__ u32 pack2(float lo, float hi) { bf2 v; v[0] = (__bf16)lo; v[1] = (__bf16)hi; return __builtin_bit_cast(u32, v); }
__device__ __forceinline__ float rbf(float f) { return (float)(__bf16)f; }
typedef float f2 __attribute__((ext_vector_type(2)));
__device__ __forceinline__ f2 rb2(f2 v) { return f2{rbf(v[0]), rbf(v[1])}; }
__device__ __forceinline__ f4 rb4(f4 v) {
    float a = v[0], b = v[1], c = v[2], d = v[3];
    asm("v_cvt_pk_bf16_f32 %0, 0, %0\n\tv_cvt_pk_bf16_f32 %1, 0, %1\n\tv_cvt_pk_bf16_f32 %2, 0, %2\n\tv_cvt_pk_bf16_f32 %3, 0, %3" : "+v"(a), "+v"(b), "+v"(c), "+v"(d));
    return f4{a, b, c, d}; }
__device__ __forceinline__ f4 unpk(v2u v) { return f4{bflo(v[0]), bfhi(v[0]), bflo(v[1]), bfhi(v[1])}; }
__device__ __forceinline__ f4 fma4(f4 a, f4 b, f4 c) { return __builtin_elementwise_fma(a, b, c); }
__device__ __forceinline__ f4 bc4(float x) { return f4{x, x, x, x}; }
__device__ __forceinline__ u32 canon(u32 u) { return u < 0xFFFFFFFEu ? u : 0xFFFFFFFEu; }
__device__ __forceinline__ v4u canon4(f4 v) { return v4u{canon(asu(v[0])), canon(asu(v[1])), canon(asu(v[2])), canon(asu(v[3]))}; }
__device__ __forceinline__ u32 umax3(u32 a, u32 b, u32 c) { const u32 m = a > b ? a : b; return m > c ? m : c; }
__device__ __forceinline__ u32 vmax4(u32 m, v4u v) { return umax3(umax3(m, v[0], v[1]), v[2], v[3]); }
__device__ __forceinline__ bool all_valid(u32 m) { return __builtin_amdgcn_ballot_w64(m == SENT) == 0ull; }
__device__ __forceinline__ float dot2(u32 a, u32 b, float c) { return __builtin_amdgcn_fdot2_f32_bf16(__builtin_bit_cast(bf2, a), __builtin_bit_cast(bf2, b), c, false); }
__device__ __forceinline__ f4 mfma(v4u a, v4u b, f4 c) { return __builtin_amdgcn_mfma_f32_16x16x32_bf16(__builtin_bit_cast(bf8, a), __builtin_bit_cast(bf8, b), c, 0, 0, 0); }
template <int CTRL> __device__ __forceinline__ u32 dppu(u32 x) { return (u32)__builtin_amdgcn_mov_dpp((int)x, CTRL, 0xf, 0xf, true); }
template <int CTRL> __device__ __forceinline__ float dppf(float x) { return asf(dppu<CTRL>(asu(x))); }
template <int K> __device__ __forceinline__ v4u ror4k(v4u v) {
    if constexpr (K == 0) return v; else return v4u{dppu<0x120 + 4 * K>(v[0]), dppu<0x120 + 4 * K>(v[1]), dppu<0x120 + 4 * K>(v[2]), dppu<0x120 + 4 * K>(v[3])}; }
__device__ __forceinline__ float row_sum(float x) { x += dppf<0x128>(x); x += dppf<0x124>(x); x += dppf<0x122>(x); x += dppf<0x121>(x); return x; }
__device__ __forceinline__ float row_max(float x) { x = fmaxf(x, dppf<0x128>(x)); x = fmaxf(x, dppf<0x124>(x)); x = fmaxf(x, dppf<0x122>(x)); x = fmaxf(x, dppf<0x121>(x)); return x; }
__device__ __forceinline__ float xrow_sum(float x) {
    auto s = __builtin_amdgcn_permlane16_swap(asu(x), asu(x), false, false); x = asf(s[0]) + asf(s[1]);
    auto t = __builtin_amdgcn_permlane32_swap(asu(x), asu(x), false, false); return asf(t[0]) + asf(t[1]); }
__device__ __forceinline__ float xrow_max(float x) {
    auto s = __builtin_amdgcn_permlane16_swap(asu(x), asu(x), false, false); x = fmaxf(asf(s[0]), asf(s[1]));
    auto t = __builtin_amdgcn_permlane32_swap(asu(x), asu(x), false, false); return fmaxf(asf(t[0]), asf(t[1])); }
__device__ __forceinline__ float wave_sum(float x) { return xrow_sum(row_sum(x)); }
__device__ __forceinline__ float wave_max(float x) { return xrow_max(row_max(x)); }
__device__ __forceinline__ float fdiv(float a, float b) { const float r = __builtin_amdgcn_rcpf(b); const float q = a * r; return fmaf(fmaf(-q, b, a), r, q); }
__device__ __forceinline__ float silu_bf(float v) { return fdiv(v, 1.f + __builtin_amdgcn_exp2f(fminf(-v * 1.4426950408889634f, 126.f))); }
__device__ __forceinline__ v4u silu8(v4u c) {
    v4u r;
#pragma unroll
    for (int i = 0; i < 4; ++i) r[i] = pack2(silu_bf(bflo(c[i])), silu_bf(bfhi(c[i])));
    return r; }
#define BAR() asm volatile("s_waitcnt lgkmcnt(0)\n\ts_barrier" ::: "memory")
#define LGKM0() asm volatile("s_waitcnt lgkmcnt(0)" ::: "memory")
#define VMW(n) asm volatile("s_waitcnt vmcnt(%0)" :: "i"(n) : "memory")
#define MEMBAR() asm volatile("" ::: "memory")
typedef __amdgpu_buffer_rsrc_t rsrc_t;
__device__ __forceinline__ v4u ld_sc1(rsrc_t r, u32 off) { return __builtin_amdgcn_raw_buffer_load_b128(r, off, 0, 16); }
__device__ __forceinline__ v2u ld_sc1_2(rsrc_t r, u32 off) { return __builtin_amdgcn_raw_buffer_load_b64(r, off, 0, 16); }
__device__ __forceinline__ void st_sc1(rsrc_t r, u32 off, v4u v) { __builtin_amdgcn_raw_buffer_store_b128(v, r, off, 0, 16); }
__device__ __forceinline__ void st_sc1_2(rsrc_t r, u32 off, v2u v) { __builtin_amdgcn_raw_buffer_store_b64(v, r, off, 0, 16); }
__device__ __forceinline__ f4 asf4(v4u v) { return __builtin_bit_cast(f4, v); }

__device__ __forceinline__ void dma_unit(v4u srd, u32 v0, u32 v1, u32 sA, u32 sB, u32 lds) {
    u32 keep;
    asm volatile("s_nop 4\n\ts_mov_b32 %0, m0\n\ts_mov_b32 m0, %6\n\ts_nop 0\n\t"
                 "buffer_load_dwordx4 %1, %3, %4 offen nt lds\n\ts_add_u32 m0, m0, 0x400\n\ts_nop 0\n\t"
                 "buffer_load_dwordx4 %2, %3, %4 offen nt lds\n\ts_add_u32 m0, m0, 0x400\n\ts_nop 0\n\t"
                 "buffer_load_dwordx4 %1, %3, %5 offen nt lds\n\ts_add_u32 m0, m0, 0x400\n\ts_nop 0\n\t"
                 "buffer_load_dwordx4 %2, %3, %5 offen nt lds\n\ts_mov_b32 m0, %0"
                 : "=&s"(keep) : "v"(v0), "v"(v1), "s"(srd), "s"(sA), "s"(sB), "s"(lds) : "memory", "scc");
}
__device__ __forceinline__ void dma_1k(v4u srd, u32 voff, u32 soff, u32 lds) {
    u32 keep;
    asm volatile("s_nop 4\n\ts_mov_b32 %0, m0\n\ts_mov_b32 m0, %4\n\ts_nop 0\n\tbuffer_load_dwordx4 %1, %2, %3 offen lds\n\ts_mov_b32 m0, %0"
                 : "=&s"(keep) : "v"(voff), "s"(srd), "s"(soff), "s"(lds) : "memory");
}
struct WStream { v4u srd; u32 base, pitch, v0, v1; };
__device__ __forceinline__ WStream mk_stream(v4u srd, u32 base, u32 pitch, int l) {
    WStream s; s.srd = srd; s.base = base; s.pitch = pitch;
    const u32 sw = 2u * ((l >> 4) & 1);
    s.v0 = (l >> 3) * pitch + (((l & 7) ^ sw) << 4);
    s.v1 = (8 + (l >> 3)) * pitch + (((l & 7) ^ (sw | 4u)) << 4);
    return s; }
__device__ __forceinline__ void issue(const WStream& s, int n, u32 lds) { const u32 a = s.base + (u32)n * 32u * s.pitch; dma_unit(s.srd, s.v0, s.v1, a, a + 16u * s.pitch, lds); }
__device__ __forceinline__ void read_unit(LAS unsigned char* L, const u32 (&ta)[4], int slot, v4u (&f)[4]) {
#pragma unroll
    for (int T = 0; T < 4; ++T) {
        const v2u lo = __builtin_bit_cast(v2u, __builtin_amdgcn_ds_read_tr16_b64_v4i16((LAS v4s*)(L + ta[T] + slot * 4096)));
        const v2u hi = __builtin_bit_cast(v2u, __builtin_amdgcn_ds_read_tr16_b64_v4i16((LAS v4s*)(L + ta[T] + slot * 4096 + 512)));
        f[T] = v4u{lo[0], lo[1], hi[0], hi[1]};
    } }
__device__ __forceinline__ f4 sel_tile(const f4 (&a)[4], int t) { return t == 0 ? a[0] : t == 1 ? a[1] : t == 2 ? a[2] : a[3]; }

template <int NJ>
__device__ __forceinline__ void kv_qk(LAS unsigned char* L, const v4u (&K)[3 * NJ], const v4u (&q)[3], int nj, int w, int l,
                                      float& m, float& lsum, float (&acc)[24], bool first, v4u& p0, v4u& p1) {
    const int c = l & 7;
    float x[8];
#pragma unroll
    for (int j = 0; j < 8; ++j) {
        float s = 0.f;
        if (j < NJ) {
#pragma unroll
            for (int i = 0; i < 3; ++i)
#pragma unroll
                for (int r = 0; r < 4; ++r) s = dot2(K[3 * j + i][r], q[i][r], s);
        }
        x[j] = s;
    }
#pragma unroll
    for (int h = 0; h < 4; ++h) { const float a = x[h] + dppf<0x141>(x[h]), b = x[h + 4] + dppf<0x141>(x[h + 4]); x[h] = (c & 4) ? b : a; }
#pragma unroll
    for (int h = 0; h < 2; ++h) { const float a = x[h] + dppf<0x4E>(x[h]), b = x[h + 2] + dppf<0x4E>(x[h + 2]); x[h] = (c & 2) ? b : a; }
    { const float a = x[0] + dppf<0xB1>(x[0]), b = x[1] + dppf<0xB1>(x[1]); x[0] = (c & 1) ? b : a; }
    float s = rbf(rbf(x[0]) * 0.07216878364870322f);
    if (NJ < 8) s = c < nj ? s : -INFINITY;
    const float mw = wave_max(s);
    const float mn = first ? mw : fmaxf(m, mw);
    const float e = __builtin_amdgcn_exp2f((s - mn) * LOG2E);
    *(LAS u32*)(L + K_PX + w * 256 + l * 4) = pack2(e, 0.f);
    const float ls = wave_sum(e);
    if (!first) {
        const float al = __builtin_amdgcn_exp2f((m - mn) * LOG2E);
        lsum = fmaf(lsum, al, ls);
#pragma unroll
        for (int i = 0; i < 24; ++i) acc[i] *= al;
    } else {
        lsum = ls;
#pragma unroll
        for (int i = 0; i < 24; ++i) acc[i] = 0.f;
    }
    m = mn;
    LGKM0();
    p0 = *(LAS v4u*)(L + K_PX + w * 256 + (l >> 3) * 32); p1 = *(LAS v4u*)(L + K_PX + w * 256 + (l >> 3) * 32 + 16);
}
template <int NJ, int J0, int J1>
__device__ __forceinline__ void kv_pv(const v4u (&V)[3 * NJ], v4u p0, v4u p1, float (&acc)[24]) {
#pragma unroll
    for (int j = J0; j < J1; ++j) {
        const u32 plo = j < 4 ? p0[j & 3] : p1[j & 3], phi = plo << 16;
#pragma unroll
        for (int i = 0; i < 3; ++i)
#pragma unroll
            for (int r = 0; r < 4; ++r) {
                acc[i * 8 + 2 * r] = dot2(V[3 * j + i][r], plo, acc[i * 8 + 2 * r]);
                acc[i * 8 + 2 * r + 1] = dot2(V[3 * j + i][r], phi, acc[i * 8 + 2 * r + 1]);
            }
    }
}

__device__ __forceinline__ void kv_path(const Params& p, LAS unsigned char* L, int j, int w, int l) {
    const rsrc_t rws = __builtin_amdgcn_make_buffer_rsrc((void*)p.ws, 0, WS_END, 0x00020000);
    const rsrc_t rk = __builtin_amdgcn_make_buffer_rsrc((void*)p.kc, 0, 4 * NH * SKV * DH * 2, 0x00020000);
    const rsrc_t rv = __builtin_amdgcn_make_buffer_rsrc((void*)p.vc, 0, 4 * NH * SKV * DH * 2, 0x00020000);
    volatile LAS u32* misc = (volatile LAS u32*)(L + L_MISC);
    const int t3 = j / 3, pos = j - 3 * t3;
    const int slot = pos == 1 ? (w & 1) : 0;
    const int bh = 2 * t3 + (pos == 2 ? 1 : (pos == 1 ? slot : 0));
    const int part = pos == 0 ? 0 : (pos == 2 ? 1 : (slot == 0 ? 1 : 0));
    const int wi = pos == 1 ? (w >> 1) : w;
    const int ub = pos == 0 ? 0 : (pos == 2 ? 43 : (slot == 0 ? 85 : 0));
    const int u1 = ub + 8 * wi;
    const int u2 = ub + (pos == 1 ? 32 : 64) + (wi == 0 ? 0 : 1 + 5 * wi), n2 = wi == 0 ? 6 : 5;
    const bool lead = w == 0 || (w == 1 && pos == 1);
    const u32 opb = WS_OP + (bh * 2 + part) * 768, mlb = WS_ML + ((bh & 31) * 4 + (bh >> 5)) * 16 + part * 8;
    if (threadIdx.x == 0) { misc[0] = 0; misc[1] = 0; misc[2] = 0; misc[3] = 0; }
    BAR();
    STAMP(0);
    if (w == 0) {
        const u64 T = __hip_atomic_load((GAS u64*)(p.ws + WS_E), RLX_AGENT) + 1ull;
        st_sc1(rws, l < 48 ? opb + l * 16 : OOB, v4u{SENT, SENT, SENT, SENT});
        st_sc1_2(rws, l == 0 ? mlb : OOB, v2u{SENT, SENT});
        const int bh1 = 2 * t3 + 1;
        st_sc1(rws, (pos == 1 && l < 48) ? WS_OP + bh1 * 2 * 768 + l * 16 : OOB, v4u{SENT, SENT, SENT, SENT});
        st_sc1_2(rws, (pos == 1 && l == 0) ? WS_ML + ((bh1 & 31) * 4 + (bh1 >> 5)) * 16 : OOB, v2u{SENT, SENT});
        VMW(0);
        if (l == 0) {
            __hip_atomic_store((GAS u64*)(p.ws + WS_FLAG) + blockIdx.x, T, RLX_AGENT);
            misc[4] = (u32)T; misc[5] = (u32)(T >> 32); LGKM0(); misc[2] = 1;
        }
    }
    const u32 vo = (l >> 3) * 384 + (l & 7) * 16;
    const u32 rowb = ((u32)bh * SKV + (SKV - WIN)) * 384u;
    float m, lsum, acc[24], fsc;
    v4u q[3], p0, p1;
    auto post = [&]() {
        if (l == 0) { *(LAS v2u*)(L + K_ML + w * 8) = v2u{asu(m), asu(lsum)}; LGKM0(); __hip_atomic_fetch_add((LAS u32*)(L + L_MISC + 12), 1u, __ATOMIC_RELAXED, __HIP_MEMORY_SCOPE_WORKGROUP); }
    };
    auto scale = [&]() {
        for (int sp = 0; misc[3] < 8u && sp < SPIN_L; ++sp) __builtin_amdgcn_s_sleep(1);
        MEMBAR();
        const v2u t = *(LAS v2u*)(L + K_ML + (l & 7) * 8);
        const bool mine = pos != 1 || (l & 1) == slot;
        const float mi = mine ? asf(t[0]) : -INFINITY;
        float M = fmaxf(mi, dppf<0xB1>(mi)); M = fmaxf(M, dppf<0x4E>(M)); M = fmaxf(M, dppf<0x141>(M));
        fsc = __builtin_amdgcn_exp2f((m - M) * LOG2E);
        if (lead) {
            float e = mine ? asf(t[1]) * __builtin_amdgcn_exp2f((mi - M) * LOG2E) : 0.f;
            e += dppf<0xB1>(e); e += dppf<0x4E>(e); e += dppf<0x141>(e);
            st_sc1_2(rws, l == 0 ? mlb : OOB, v2u{canon(asu(M)), canon(asu(e))});
        }
    };
    {
        v4u K[24], V[24];
        const u32 base = rowb + (u32)u1 * 3072u;
#pragma unroll
        for (int jj = 0; jj < 8; ++jj)
#pragma unroll
            for (int i = 0; i < 3; ++i) K[jj * 3 + i] = __builtin_amdgcn_raw_buffer_load_b128(rk, vo + i * 128, base + jj * 3072, 2);
#pragma unroll
        for (int jj = 0; jj < 8; ++jj)
#pragma unroll
            for (int i = 0; i < 3; ++i) V[jj * 3 + i] = __builtin_amdgcn_raw_buffer_load_b128(rv, vo + i * 128, base + jj * 3072, 2);
        __builtin_amdgcn_sched_barrier(0);
        if (w == 0) STAMP(1);
        if (w == 7) STAMP(2);
        if (lead) {
            for (int sp = 0; misc[2] == 0 && sp < SPIN_L; ++sp) __builtin_amdgcn_s_sleep(1);
            const u32 tlo = misc[4], thi = misc[5];
            for (int sp = 0; sp < SPIN_G; ++sp) {
                MEMBAR();
                const v2u f = ld_sc1_2(rws, WS_FLAG + l * 8);
                if (__builtin_amdgcn_ballot_w64(f[0] != tlo || f[1] != thi) == 0ull) break;
            }
            const u32 qo = WS_QP + (u32)((bh >> 5) * NQ + (bh & 31) * DH + 4 * l) * 4u;
            v4u a[5] = {};
            bool need = l < 48;
            for (int sp = 0; sp < SPIN_G; ++sp) {
                MEMBAR();
                if (need) {
                    u32 mx = 0;
#pragma unroll
                    for (int ks = 0; ks < 5; ++ks) a[ks] = ld_sc1(rws, qo + ks * (4 * NQ * 4));
#pragma unroll
                    for (int ks = 0; ks < 5; ++ks) mx = vmax4(mx, a[ks]);
                    need = mx == SENT;
                }
                if (__builtin_amdgcn_ballot_w64(need) == 0ull) break;
            }
            const f4 qs = (((asf4(a[0]) + asf4(a[1])) + asf4(a[2])) + asf4(a[3])) + asf4(a[4]);
            if (l < 48) *(LAS v2u*)(L + K_QS + slot * 384 + l * 8) = v2u{pack2(qs[0], qs[1]), pack2(qs[2], qs[3])};
            LGKM0();
            misc[slot] = 1;
            if (w == 0) STAMP(3);
        } else {
            for (int sp = 0; misc[slot] == 0 && sp < SPIN_L; ++sp) __builtin_amdgcn_s_sleep(1);
        }
        MEMBAR();
#pragma unroll
        for (int i = 0; i < 3; ++i) q[i] = *(LAS v4u*)(L + K_QS + slot * 384 + ((l & 7) + 8 * i) * 16);
        kv_qk<8>(L, K, q, 8, w, l, m, lsum, acc, true, p0, p1);
        if (w >= 4) post();
        kv_pv<8, 0, 7>(V, p0, p1, acc);
        if (w >= 4) scale();
        kv_pv<8, 7, 8>(V, p0, p1, acc);
    }
    if (w == 0) STAMP(4);
    if (w < 4) {
        v4u K[18], V[18];
        const u32 base = rowb + (u32)u2 * 3072u;
#pragma unroll
        for (int jj = 0; jj < 5; ++jj)
#pragma unroll
            for (int i = 0; i < 3; ++i) K[jj * 3 + i] = __builtin_amdgcn_raw_buffer_load_b128(rk, vo + i * 128, base + jj * 3072, 2);
        const u32 vo5 = n2 == 6 ? vo : OOB;
#pragma unroll
        for (int i = 0; i < 3; ++i) K[15 + i] = __builtin_amdgcn_raw_buffer_load_b128(rk, vo5 + i * 128, base + 5 * 3072, 2);
#pragma unroll
        for (int i = 0; i < 3; ++i) V[15 + i] = __builtin_amdgcn_raw_buffer_load_b128(rv, vo5 + i * 128, base + 5 * 3072, 2);
#pragma unroll
        for (int jj = 0; jj < 5; ++jj)
#pragma unroll
            for (int i = 0; i < 3; ++i) V[jj * 3 + i] = __builtin_amdgcn_raw_buffer_load_b128(rv, vo + i * 128, base + jj * 3072, 2);
        __builtin_amdgcn_sched_barrier(0);
        kv_qk<6>(L, K, q, n2, w, l, m, lsum, acc, false, p0, p1);
        post();
        kv_pv<6, 5, 6>(V, p0, p1, acc);
        kv_pv<6, 0, 3>(V, p0, p1, acc);
        scale();
        kv_pv<6, 3, 5>(V, p0, p1, acc);
    }
    if (w == 0) STAMP(5);
    if (w == 7) STAMP(6);
    float y[12], z[6];
#pragma unroll
    for (int n = 0; n < 12; ++n) { auto s = __builtin_amdgcn_permlane32_swap(asu(acc[n]), asu(acc[n + 12]), false, false); y[n] = asf(s[0]) + asf(s[1]); }
#pragma unroll
    for (int n = 0; n < 6; ++n) { auto s = __builtin_amdgcn_permlane16_swap(asu(y[n]), asu(y[n + 6]), false, false); z[n] = asf(s[0]) + asf(s[1]); }
#pragma unroll
    for (int n = 0; n < 3; ++n) {
        const float a = z[n] + dppf<0x128>(z[n]), b = z[n + 3] + dppf<0x128>(z[n + 3]);
        const int ai = 3 * (l >> 3) + n;
        *(LAS float*)(L + K_PART + w * 768 + (64 * (ai >> 3) + 8 * (l & 7) + (ai & 7)) * 4) = ((l & 8) ? b : a) * fsc;
    }
    BAR();
    if (w == 0) STAMP(7);
    if (lead) {
        const u32 pb = K_PART + slot * 768 + (l < 48 ? l : 47) * 16;
        f4 o = (*(LAS f4*)(L + pb) + *(LAS f4*)(L + pb + 1536)) + (*(LAS f4*)(L + pb + 3072) + *(LAS f4*)(L + pb + 4608));
        if (pos != 1) o += (*(LAS f4*)(L + pb + 768) + *(LAS f4*)(L + pb + 2304)) + (*(LAS f4*)(L + pb + 3840) + *(LAS f4*)(L + pb + 5376));
        st_sc1(rws, l < 48 ? opb + l * 16 : OOB, canon4(o));
        if (w == 0) STAMP(8);
    }
}

enum { R_SA = 0, R_SB = 1, R_SC = 2 };
#define UNDEF4(v) asm volatile("" : "=v"(v))
template <int ROLE>
__device__ __forceinline__ void w_path(const Params& p, LAS unsigned char* L, int u, int w, int l) {
    const rsrc_t rws = __builtin_amdgcn_make_buffer_rsrc((void*)p.ws, 0, WS_END, 0x00020000);
    const int tid = threadIdx.x, g = l >> 4, tq = (l & 15) >> 2, b4 = l & 3;
    const u32 lbase = (u32)(size_t)L, stg = lbase + w * 16384;
    const bool hasF1 = u < 50, hasF2 = u < 60;
    const int kf = u / 10, lp = u - 10 * kf, kf2 = u / 12, cs = u - 12 * kf2;
    const int ri = ROLE == R_SA ? u : (ROLE == R_SB ? u - 30 : u - 54);
    const WStream sF1 = mk_stream(mk_srd(p.wm, hasF1 ? DM * 3840 * 2 : 0), (u32)((128 * kf + 32 * (w >> 1)) * 7680 + (128 * lp + 64 * (w & 1)) * 2), 7680, l);
    const WStream sF2 = mk_stream(mk_srd(p.wq, hasF2 ? DM * NQ * 2 : 0), (u32)(128 * kf2 * 12288 + (512 * cs + 64 * w) * 2), 12288, l);
    const int mid = ROLE == R_SC ? (w < 4 ? (ri >= 6 ? ri : -1) : 30 + ri) : (w < 4 ? (ri < 6 ? ri : ri + 4) : (ri == 20 ? 28 : (ri == 21 ? 29 : -1)));
    const bool hasM = ROLE != R_SA && mid >= 0;
    const WStream sM = mk_stream(mk_srd(p.wm, hasM ? DM * 3840 * 2 : 0), (u32)(160 * (w & 3) * 7680 + (1280 + 64 * mid) * 2), 7680, l);
    const int ra = ri / 10, ca = ri - 10 * ra;
    const WStream sT = ROLE == R_SA ? mk_stream(mk_srd(p.wo, NQ * DM * 2), (u32)((2048 * ra + 256 * w) * 1280 + ca * 128), 1280, l)
                     : ROLE == R_SB ? mk_stream(mk_srd((w & 1) ? p.wu : p.wg, DM * FF * 2), (u32)(160 * (w >> 1) * 3072 + ri * 128), 3072, l)
                                    : mk_stream(mk_srd(p.wd, FF * DM * 2), (u32)(192 * w * 1280 + ri * 128), 1280, l);
    constexpr int NM = ROLE == R_SA ? 0 : 5, NT = ROLE == R_SA ? 8 : (ROLE == R_SB ? 5 : 6), NA = NM + NT;
    auto issueA = [&](int n) { if (n < NM) issue(sM, n, stg + (n & 3) * 4096); else issue(sT, n - NM, stg + (n & 3) * 4096); };
    u32 ta[4];
    { const u32 f = 2u * (tq >> 1) | 4u * (g & 1);
#pragma unroll
      for (int T = 0; T < 4; ++T) ta[T] = w * 16384 + (8 * g + tq) * 128 + ((((u32)(2 * T)) ^ f) + (b4 >> 1)) * 16 + (b4 & 1) * 8; }
    const u32 dcol = 16 * tq + 4 * g;
    const u32 f1o = hasF1 ? WS_F1P + (u32)((kf * 4 + b4) * 1280 + 128 * lp + 64 * w + dcol) * 4u : OOB;
    const u32 qpo = hasF2 ? WS_QP + (u32)((kf2 * 4 + b4) * NQ + 512 * cs + 64 * w + dcol) * 4u : OOB;
    const u32 mfo = hasM ? WS_MODF + (u32)(b4 * 2560 + 64 * mid + dcol) * 2u : OOB;
    const u32 outo = ROLE == R_SA ? WS_AP + (u32)((ra * 4 + b4) * DM + 64 * ca + dcol) * 4u : WS_HID + (u32)(b4 * FF + 64 * ri + dcol) * 2u;
    STAMP(0);
    if (w < 5) { dma_1k(mk_srd(p.c, 5120), l * 16, w * 1024, lbase + L_CS + w * 1024); dma_1k(mk_srd(p.x, 5120), l * 16, w * 1024, lbase + L_XS + w * 1024); }
    issue(sF1, 0, stg);
    if (w == 0) {
        u32 keep;
        asm volatile("s_nop 4\n\ts_mov_b32 %0, m0\n\ts_mov_b32 m0, %3\n\ts_nop 0\n\tbuffer_load_dword %1, %2, 0 offen sc1 lds\n\ts_mov_b32 m0, %0"
                     : "=&s"(keep) : "v"(l < 2 ? WS_E + l * 4 : OOB), "s"(mk_srd(p.ws, WS_END)), "s"(lbase + L_GA) : "memory");
    }
    { const v4u s4 = {SENT, SENT, SENT, SENT};
      st_sc1(rws, qpo, s4);
      if (w < 2) st_sc1(rws, f1o, s4);
      if (ROLE != R_SA && (w & 3) == 0) st_sc1_2(rws, mfo, v2u{SENT, SENT});
      if (w == 0) {
          if (ROLE == R_SA) st_sc1(rws, outo, s4);
          if (ROLE == R_SB) st_sc1_2(rws, outo, v2u{SENT, SENT});
      } }
    asm volatile("s_barrier" ::: "memory");
    issue(sF2, 0, stg + 4096); issue(sF2, 1, stg + 8192); issue(sF2, 2, stg + 12288);
    {
        v4u ft[NT][4];
        {
            VMW(12);
            BAR();
            STAMP(1);
            const u64 T = *(LAS u64*)(L + L_GA) + 1ull;
            if (tid == 0) __hip_atomic_store((GAS u64*)(p.ws + WS_FLAG) + blockIdx.x, T, RLX_AGENT);
            {
                v4u f[4]; read_unit(L, ta, 0, f);
                const v4u a = silu8(*(LAS v4u*)(L + L_CS + b4 * 1280 + (128 * kf + 32 * (w >> 1) + 8 * g) * 2));
                f4 d[4];
#pragma unroll
                for (int T2 = 0; T2 < 4; ++T2) d[T2] = mfma(f[T2], a, f4{0.f, 0.f, 0.f, 0.f});
                *(LAS f4*)(L + L_RED + w * 1024 + l * 16) = sel_tile(d, tq);
                LGKM0();
                issue(sF2, 3, stg);
                BAR();
                if (w < 2) {
                    f4 s = *(LAS f4*)(L + L_RED + w * 1024 + l * 16);
#pragma unroll
                    for (int i = 1; i < 4; ++i) s += *(LAS f4*)(L + L_RED + (2 * i + w) * 1024 + l * 16);
                    st_sc1(rws, f1o, canon4(s));
                }
                if (w >= 4) {
                    float xv[10], s = 0.f;
#pragma unroll
                    for (int i = 0; i < 5; ++i) { const u32 t = *(LAS u32*)(L + L_XS + (w - 4) * 1280 + (l + 64 * i) * 4); xv[2 * i] = bflo(t); xv[2 * i + 1] = bfhi(t); s += xv[2 * i] + xv[2 * i + 1]; }
                    const float mean = wave_sum(s) * (1.f / DM);
                    float v = 0.f;
#pragma unroll
                    for (int i = 0; i < 10; ++i) v = fmaf(xv[i] - mean, xv[i] - mean, v);
                    const float var = wave_sum(v) * (1.f / DM);
                    if (l == 0) *(LAS v2u*)(L + L_ST + (w - 4) * 8) = v2u{asu(rbf(mean)), asu(rbf(__builtin_sqrtf(rbf(rbf(var) + 1e-5f))))};
                }
                if (w == 0) STAMP(2);
            }
            v4u fq[4][4];
            VMW(12); read_unit(L, ta, 1, fq[0]);
            VMW(8); read_unit(L, ta, 2, fq[1]);
            VMW(4); read_unit(L, ta, 3, fq[2]);
            VMW(0); read_unit(L, ta, 0, fq[3]);
            BAR();
            STAMP(3);
            if (w < 2) {
                const u32 tlo = (u32)T, thi = (u32)(T >> 32);
                for (int sp = 0; sp < SPIN_G; ++sp) {
                    MEMBAR();
                    const v4u f0 = ld_sc1(rws, WS_FLAG + l * 16), f1 = ld_sc1(rws, WS_FLAG + 1024 + l * 16);
                    const bool bad = f0[0] != tlo || f0[1] != thi || f0[2] != tlo || f0[3] != thi || f1[0] != tlo || f1[1] != thi || f1[2] != tlo || f1[3] != thi;
                    if (__builtin_amdgcn_ballot_w64(bad) == 0ull) break;
                }
                if (u == 0 && tid == 0) __hip_atomic_store((GAS u64*)(p.ws + WS_E), T, RLX_AGENT);
                const int hb = tid >> 5, hi = tid & 31, col = 128 * kf2 + 4 * hi;
                const u32 o = WS_F1P + (u32)(hb * 1280 + col) * 4u;
                v4u a[5] = {}, c2[5] = {};
                bool need = hasF2;
                for (int sp = 0; sp < SPIN_G; ++sp) {
                    MEMBAR();
                    if (need) {
                        u32 mx = 0;
#pragma unroll
                        for (int ks = 0; ks < 5; ++ks) { a[ks] = ld_sc1(rws, o + ks * 20480); c2[ks] = ld_sc1(rws, o + ks * 20480 + 2560); }
#pragma unroll
                        for (int ks = 0; ks < 5; ++ks) mx = vmax4(vmax4(mx, a[ks]), c2[ks]);
                        need = mx == SENT;
                    }
                    if (__builtin_amdgcn_ballot_w64(need) == 0ull) break;
                }
                const f4 sa = (((asf4(a[0]) + asf4(a[1])) + asf4(a[2])) + asf4(a[3])) + asf4(a[4]);
                const f4 sc = (((asf4(c2[0]) + asf4(c2[1])) + asf4(c2[2])) + asf4(c2[3])) + asf4(c2[4]);
                const v2u st = *(LAS v2u*)(L + L_ST + hb * 8), xr = *(LAS v2u*)(L + L_XS + hb * 1280 + (hasF2 ? col : 0) * 2);
                const float den = asf(st[1]), rc = __builtin_amdgcn_rcpf(den);
                const f4 e = rb4(unpk(xr) - asf(st[0])), q1 = e * rc;
                const f4 h = rb4(rb4(fma4(fma4(-q1, bc4(den), e), bc4(rc), q1)) * rb4(rb4(sc) + 1.f)) + rb4(sa);
                *(LAS v2u*)(L + L_HB + hb * 256 + hi * 8) = v2u{pack2(h[0], h[1]), pack2(h[2], h[3])};
            }
            BAR();
            STAMP(4);
            {
                f4 d[4] = {};
#pragma unroll
                for (int n = 0; n < 4; ++n) {
                    const v4u a = *(LAS v4u*)(L + L_HB + b4 * 256 + (32 * n + 8 * g) * 2);
#pragma unroll
                    for (int T2 = 0; T2 < 4; ++T2) d[T2] = mfma(fq[n][T2], a, d[T2]);
                }
                st_sc1(rws, qpo, canon4(sel_tile(d, tq)));
            }
            asm volatile("s_barrier" ::: "memory");
            STAMP(5);
#pragma unroll
            for (int n = 0; n < 4; ++n) issueA(n);
            if (NM) {
                f4 d[4] = {};
#pragma unroll
                for (int n = 0; n < NM; ++n) {
                    VMW(12);
                    v4u f[4]; read_unit(L, ta, n & 3, f);
                    const v4u a = silu8(*(LAS v4u*)(L + L_CS + b4 * 1280 + (160 * (w & 3) + 32 * n + 8 * g) * 2));
                    LGKM0();
                    issueA(n + 4);
#pragma unroll
                    for (int T2 = 0; T2 < 4; ++T2) d[T2] = mfma(f[T2], a, d[T2]);
                }
                *(LAS f4*)(L + L_RED + w * 1024 + l * 16) = sel_tile(d, tq);
                BAR();
                if ((w & 3) == 0) {
                    f4 s = *(LAS f4*)(L + L_RED + w * 1024 + l * 16);
#pragma unroll
                    for (int i = 1; i < 4; ++i) s += *(LAS f4*)(L + L_RED + (w + i) * 1024 + l * 16);
                    st_sc1_2(rws, mfo, v2u{canon(pack2(s[0], s[1])), canon(pack2(s[2], s[3]))});
                }
                BAR();
            }
#pragma unroll
            for (int n = 0; n < NT; ++n) {
                const int a = NM + n, younger = (NA - 1 - a) < 3 ? (NA - 1 - a) : 3;
                if (younger == 3) VMW(12); else if (younger == 2) VMW(8); else if (younger == 1) VMW(4); else VMW(0);
                read_unit(L, ta, a & 3, ft[n]);
                if (a + 4 < NA) { LGKM0(); issueA(a + 4); }
            }
            LGKM0();
            STAMP(6);
        }
        if (ROLE == R_SA) {
            f4 res[2];
            UNDEF4(res[0]); UNDEF4(res[1]);
            u32 oo[2], mo[2];
#pragma unroll
            for (int r = 0; r < 2; ++r) {
                const int row = 2048 * ra + 256 * w + 32 * (4 * r + tq) + 8 * g, hd = row / DH, dd = row - hd * DH;
                oo[r] = WS_OP + (u32)((b4 * 32 + hd) * 2) * 768u + dd * 4; mo[r] = WS_ML + (u32)(hd * 4 + b4) * 16u;
            }
            bool done[2] = {false, false}, wdone[2] = {false, false}, need[2] = {true, true}, needm[2] = {true, true};
            v4u a[2][4], ml[2];
            float w0[2], w1[2];
#pragma unroll
            for (int r = 0; r < 2; ++r) {
                UNDEF4(ml[r]); UNDEF4(w0[r]); UNDEF4(w1[r]);
#pragma unroll
                for (int i = 0; i < 4; ++i) UNDEF4(a[r][i]);
            }
            for (int sp = 0; sp < SPIN_G && !(done[0] && done[1]); ++sp) {
                MEMBAR();
#pragma unroll
                for (int r = 0; r < 2; ++r) {
                    if (needm[r]) ml[r] = ld_sc1(rws, mo[r]);
                    if (need[r]) { a[r][0] = ld_sc1(rws, oo[r]); a[r][1] = ld_sc1(rws, oo[r] + 16); a[r][2] = ld_sc1(rws, oo[r] + 768); a[r][3] = ld_sc1(rws, oo[r] + 784); }
                }
                const bool last = sp == SPIN_G - 1;
#pragma unroll
                for (int r = 0; r < 2; ++r) {
                    if (!wdone[r]) {
                        needm[r] = vmax4(0u, ml[r]) == SENT;
                        if (__builtin_amdgcn_ballot_w64(needm[r]) == 0ull || last) {
                            const float m0 = asf(ml[r][0]), l0 = asf(ml[r][1]), m1 = asf(ml[r][2]), l1 = asf(ml[r][3]), M = fmaxf(m0, m1);
                            const float e0 = __builtin_amdgcn_exp2f((m0 - M) * LOG2E), e1 = __builtin_amdgcn_exp2f((m1 - M) * LOG2E);
                            const float inv = fdiv(1.f, fmaf(e0, l0, e1 * l1));
                            w0[r] = e0 * inv; w1[r] = e1 * inv; wdone[r] = true;
                        }
                    }
                    if (!done[r]) {
                        need[r] = vmax4(vmax4(vmax4(vmax4(0u, a[r][0]), a[r][1]), a[r][2]), a[r][3]) == SENT;
                        if ((__builtin_amdgcn_ballot_w64(need[r]) == 0ull && wdone[r]) || last) {
                            const f4 x0 = fma4(asf4(a[r][0]), bc4(w0[r]), asf4(a[r][2]) * w1[r]), x1 = fma4(asf4(a[r][1]), bc4(w0[r]), asf4(a[r][3]) * w1[r]);
                            const v4u bq = {pack2(x0[0], x0[1]), pack2(x0[2], x0[3]), pack2(x1[0], x1[1]), pack2(x1[2], x1[3])};
                            const v4u bk[4] = {bq, ror4k<1>(bq), ror4k<2>(bq), ror4k<3>(bq)};
                            f4 d[4] = {};
#pragma unroll
                            for (int s = 0; s < 4; ++s)
#pragma unroll
                                for (int T2 = 0; T2 < 4; ++T2) d[(T2 + s) & 3] = mfma(ft[4 * r + s][(T2 + s) & 3], bk[T2], d[(T2 + s) & 3]);
                            res[r] = sel_tile(d, tq);
                            done[r] = true;
                        }
                    }
                }
            }
            if (w == 0) STAMP(7);
            *(LAS f4*)(L + L_RED + w * 1024 + l * 16) = res[0] + res[1];
            BAR();
            if (w == 0) {
                STAMP(8);
                LAS f4* R = (LAS f4*)(L + L_RED + l * 16);
                st_sc1(rws, outo, canon4(((R[0] + R[64]) + (R[128] + R[192])) + ((R[256] + R[320]) + (R[384] + R[448]))));
                STAMP(9);
            }
        } else if (ROLE == R_SB) {
            const int i16 = l & 15, col = 128 * w + 8 * i16;
            f4 xa, xb, c1a, c1b, sba, sbb;
            if (w < 5) {
                v4u gav, sbv, cbv, a[6];
                const u32 mo = WS_MODF + (u32)(g * 2560 + col) * 2u;
                for (int sp = 0; sp < SPIN_G; ++sp) {
                    MEMBAR();
                    gav = ld_sc1(rws, mo); sbv = ld_sc1(rws, mo + 1280); cbv = ld_sc1(rws, mo + 2560);
                    if (all_valid(vmax4(vmax4(vmax4(0u, gav), sbv), cbv))) break;
                }
                const v4u xr = *(LAS v4u*)(L + L_XS + g * 1280 + col * 2);
                xa = unpk(v2u{xr[0], xr[1]}); xb = unpk(v2u{xr[2], xr[3]});
                f4 gaa = unpk(v2u{gav[0], gav[1]}), gab = unpk(v2u{gav[2], gav[3]});
                c1a = rb4(unpk(v2u{cbv[0], cbv[1]}) + 1.f); c1b = rb4(unpk(v2u{cbv[2], cbv[3]}) + 1.f);
                sba = unpk(v2u{sbv[0], sbv[1]}); sbb = unpk(v2u{sbv[2], sbv[3]});
                asm volatile("" : "+v"(xa), "+v"(xb), "+v"(gaa), "+v"(gab), "+v"(c1a), "+v"(c1b), "+v"(sba), "+v"(sbb));
                if (w == 0) STAMP(10);
                const u32 ao = WS_AP + (u32)(g * DM + col) * 4u;
#pragma unroll
                for (int r = 0; r < 6; ++r) UNDEF4(a[r]);
                bool need = true;
                for (int sp = 0; sp < SPIN_G; ++sp) {
                    MEMBAR();
                    if (need) {
                        u32 mx = 0;
#pragma unroll
                        for (int r = 0; r < 3; ++r) { a[2 * r] = ld_sc1(rws, ao + r * 10240); a[2 * r + 1] = ld_sc1(rws, ao + r * 10240 + 16); }
#pragma unroll
                        for (int r = 0; r < 6; ++r) mx = vmax4(mx, a[r]);
                        need = mx == SENT;
                    }
                    if (__builtin_amdgcn_ballot_w64(need) == 0ull) break;
                }
                if (w == 0) STAMP(11);
                xa = rb4(xa + rb4(gaa * rb4((asf4(a[0]) + asf4(a[2])) + asf4(a[4]))));
                xb = rb4(xb + rb4(gab * rb4((asf4(a[1]) + asf4(a[3])) + asf4(a[5]))));
                const f4 t = xa + xb;
                const float mw = row_sum((t[0] + t[1]) + (t[2] + t[3])) * (1.f / 128.f);
                const f4 da = xa - mw, db = xb - mw, q2 = fma4(da, da, db * db);
                const float v = row_sum((q2[0] + q2[1]) + (q2[2] + q2[3]));
                *(LAS v2u*)(L + L_ST + 64 + g * 48 + w * 8) = v2u{asu(mw), asu(v)};
            }
            BAR();
            if (w < 5) {
                const f4 s0 = *(LAS f4*)(L + L_ST + 64 + g * 48), s1 = *(LAS f4*)(L + L_ST + 64 + g * 48 + 16);
                const f2 s2 = *(LAS f2*)(L + L_ST + 64 + g * 48 + 32);
                const float mean = (((s0[0] + s0[2]) + (s1[0] + s1[2])) + s2[0]) * 0.2f;
                const f4 dm = f4{s0[0], s0[2], s1[0], s1[2]} - mean, dq = dm * dm; const float d4 = s2[0] - mean;
                const float M2 = fmaf(128.f, fmaf(d4, d4, (dq[0] + dq[1]) + (dq[2] + dq[3])), ((s0[1] + s0[3]) + (s1[1] + s1[3])) + s2[1]);
                const float mb = rbf(mean), den = rbf(__builtin_amdgcn_sqrtf(rbf(rbf(M2 * (1.f / DM)) + 1e-5f))), rc = __builtin_amdgcn_rcpf(den);
                const f4 ea = rb4(xa - mb), eb = rb4(xb - mb), qa = ea * rc, qb = eb * rc;
                const f4 ha = rb4(rb4(fma4(fma4(-qa, bc4(den), ea), bc4(rc), qa)) * c1a) + sba, hb = rb4(rb4(fma4(fma4(-qb, bc4(den), eb), bc4(rc), qb)) * c1b) + sbb;
                *(LAS v4u*)(L + L_HB + g * 1280 + col * 2) = v4u{pack2(ha[0], ha[1]), pack2(ha[2], ha[3]), pack2(hb[0], hb[1]), pack2(hb[2], hb[3])};
            }
            BAR();
            if (w == 0) STAMP(12);
            f4 d[4] = {};
#pragma unroll
            for (int n = 0; n < 5; ++n) {
                const v4u a = *(LAS v4u*)(L + L_HB + b4 * 1280 + (160 * (w >> 1) + 32 * n + 8 * g) * 2);
#pragma unroll
                for (int T2 = 0; T2 < 4; ++T2) d[T2] = mfma(ft[n][T2], a, d[T2]);
            }
            *(LAS f4*)(L + L_RED + w * 1024 + l * 16) = sel_tile(d, tq);
            BAR();
            if (w == 0) {
                STAMP(13);
                LAS f4* R = (LAS f4*)(L + L_RED + l * 16);
                const f4 gt = rb4((R[0] + R[128]) + (R[256] + R[384])), up = rb4((R[64] + R[192]) + (R[320] + R[448]));
                const f4 mk = {gt[0] > 0.f ? 1.f : 0.f, gt[1] > 0.f ? 1.f : 0.f, gt[2] > 0.f ? 1.f : 0.f, gt[3] > 0.f ? 1.f : 0.f};
                const f4 hh = (gt * mk) * up;
                st_sc1_2(rws, outo, v2u{canon(pack2(hh[0], hh[1])), canon(pack2(hh[2], hh[3]))});
                STAMP(14);
            }
        } else {
            f4 x1, gbf;
            UNDEF4(x1); UNDEF4(gbf);
            if (w == 0) {
                v2u ga, gb;
                const u32 mo = WS_MODF + (u32)(b4 * 2560 + 64 * ri + dcol) * 2u;
                for (int sp = 0; sp < SPIN_G; ++sp) {
                    MEMBAR();
                    ga = ld_sc1_2(rws, mo); gb = ld_sc1_2(rws, mo + 3840);
                    if (all_valid(umax3(umax3(ga[0], ga[1], gb[0]), gb[1], 0u))) break;
                }
                const u32 ao = WS_AP + (u32)(b4 * DM + 64 * ri + dcol) * 4u;
                f4 s;
                for (int sp = 0; sp < SPIN_G; ++sp) {
                    MEMBAR();
                    const v4u p0 = ld_sc1(rws, ao), p1 = ld_sc1(rws, ao + 10240), p2 = ld_sc1(rws, ao + 20480);
                    s = (asf4(p0) + asf4(p1)) + asf4(p2);
                    if (all_valid(vmax4(vmax4(vmax4(0u, p0), p1), p2))) break;
                }
                x1 = rb4(unpk(*(LAS v2u*)(L + L_XS + b4 * 1280 + (64 * ri + dcol) * 2)) + rb4(unpk(ga) * rb4(s)));
                gbf = unpk(gb);
                asm volatile("" : "+v"(x1), "+v"(gbf));
                STAMP(15);
            }
            v4u a0, a1;
            UNDEF4(a0); UNDEF4(a1);
            const u32 h0 = WS_HID + (u32)(b4 * FF + 192 * w + 32 * tq + 8 * g) * 2u, h1 = tq < 2 ? h0 + 256 : OOB;
            bool need = true;
            for (int sp = 0; sp < SPIN_G; ++sp) {
                MEMBAR();
                if (need) { a0 = ld_sc1(rws, h0); a1 = ld_sc1(rws, h1); need = vmax4(vmax4(0u, a0), a1) == SENT; }
                if (__builtin_amdgcn_ballot_w64(need) == 0ull) break;
            }
            if (w == 0) STAMP(16);
            const v4u bk[4] = {a0, ror4k<1>(a0), ror4k<2>(a0), ror4k<3>(a0)}, bl[4] = {a1, ror4k<1>(a1), ror4k<2>(a1), ror4k<3>(a1)};
            f4 d[4] = {};
#pragma unroll
            for (int k = 0; k < 4; ++k)
#pragma unroll
                for (int s = 0; s < 6; ++s) d[(s + k) & 3] = mfma(ft[s][(s + k) & 3], s < 4 ? bk[k] : bl[k], d[(s + k) & 3]);
            *(LAS f4*)(L + L_RED + w * 1024 + l * 16) = sel_tile(d, tq);
            BAR();
            if (w == 0) {
                STAMP(17);
                LAS f4* R = (LAS f4*)(L + L_RED + l * 16);
                const f4 o = x1 + rb4(gbf * rb4(((R[0] + R[64]) + (R[128] + R[192])) + ((R[256] + R[320]) + (R[384] + R[448]))));
                *(GAS v2u*)(p.out + b4 * DM + 64 * ri + dcol) = v2u{pack2(o[0], o[1]), pack2(o[2], o[3])};
                STAMP(18);
            }
        }
    }
}

__global__ void __launch_bounds__(512, 2) fused_block(Params p) {
    extern __shared__ __attribute__((aligned(16))) unsigned char smem[];
    LAS unsigned char* L = (LAS unsigned char*)smem;
    const int l = threadIdx.x & 63, w = __builtin_amdgcn_readfirstlane(threadIdx.x >> 6), c = blockIdx.x;
    if (c >= NW) kv_path(p, L, c - NW, w, l);
    else if (c < 30) w_path<R_SA>(p, L, c, w, l);
    else if (c < 54) w_path<R_SB>(p, L, c, w, l);
    else w_path<R_SC>(p, L, c, w, l);
}

extern "C" void kernel_launch(void* const* d_in, const int* in_sizes, int n_in, void* d_out, int out_size, void* d_ws, size_t ws_size, hipStream_t stream) {
    Params p;
    p.x = (const u16*)d_in[0]; p.kc = (const u16*)d_in[1]; p.vc = (const u16*)d_in[2]; p.wq = (const u16*)d_in[3]; p.wo = (const u16*)d_in[4];
    p.wg = (const u16*)d_in[6]; p.wu = (const u16*)d_in[7]; p.wd = (const u16*)d_in[8]; p.c = (const u16*)d_in[9]; p.wm = (const u16*)d_in[10];
    p.out = (u16*)d_out; p.ws = (unsigned char*)d_ws;
    (void)hipFuncSetAttribute((const void*)fused_block, hipFuncAttributeMaxDynamicSharedMemorySize, (int)L_END);
    hipLaunchKernelGGL(fused_block, dim3(256), dim3(512), L_END, stream, p);
}
```

```cpp
#include <hip/hip_runtime.h>
#include <stdint.h>
#pragma clang fp contract(off)

typedef unsigned short u16;
typedef unsigned u32;
typedef unsigned long long u64;
typedef u32 v4u __attribute__((ext_vector_type(4)));
typedef u32 v2u __attribute__((ext_vector_type(2)));
typedef float f4 __attribute__((ext_vector_type(4)));
typedef short v4s __attribute__((ext_vector_type(4)));
typedef __bf16 bf8 __attribute__((ext_vector_type(8)));
typedef __bf16 bf2 __attribute__((ext_vector_type(2)));
#define LAS __attribute__((address_space(3)))
#define GAS __attribute__((address_space(1)))
#define RLX_AGENT __ATOMIC_RELAXED, __HIP_MEMORY_SCOPE_AGENT

#define PRETOUCH 1
#define POLL_SLEEP 2
#define INIT_MODE 1

constexpr int DM = 640, NH = 32, SKV = 4096, DH = 192, WIN = 1024, FF = 1536, NQ = NH * DH;
constexpr u32 SENT = 0xFFFFFFFFu;
constexpr int NW = 64;
constexpr u32 WS_E = 0, WS_FLAG = 4096, WS_F1P = 8192, WS_MODF = WS_F1P + 5 * 4 * 1280 * 4, WS_QP = WS_MODF + 4 * 2560 * 2,
              WS_OP = WS_QP + 5 * 4 * NQ * 4, WS_ML = WS_OP + 128 * 2 * DH * 4, WS_AP = WS_ML + 32 * 4 * 16, WS_HID = WS_AP + 3 * 4 * DM * 4,
              WS_END = WS_HID + 4 * FF * 2;
constexpr u32 OOB = 0x7FFFFF00u;
constexpr u32 L_XS = 131072, L_CS = L_XS + 5120, L_HB = L_CS + 5120, L_RED = L_HB + 5120, L_ST = L_RED + 8192, L_GA = L_ST + 256, L_GB = L_GA + 1024,
              L_X1 = L_GB + 1024, L_MISC = L_X1 + 1024, L_STAMP = L_MISC + 64, L_END = L_STAMP + 256;
constexpr u32 K_W = 18432, K_PX = 1024, K_PX2 = 1536, K_V = 9216, K_QS = 8 * K_W, K_ML = K_QS + 768, K_END = K_ML + 64;
static_assert(K_END <= L_MISC, "LDS");
constexpr float LOG2E = 1.4426950408889634f;
static_assert(L_END <= 163840, "LDS");
constexpr int SPIN_G = 40000, SPIN_L = 2000000;

#define STAMP(i) do {} while (0)

struct Params { const u16 *x, *kc, *vc, *wq, *wo, *wg, *wu, *wd, *c, *wm; u16* out; unsigned char* ws; };

__device__ __forceinline__ v4u mk_srd(const void* p, u32 bytes) { const u64 a = (u64)p; return v4u{(u32)a, (u32)(a >> 32) & 0xffffu, bytes, 0x00020000u}; }
__device__ __forceinline__ float asf(u32 u) { return __builtin_bit_cast(float, u); }
__device__ __forceinline__ u32 asu(float f) { return __builtin_bit_cast(u32, f); }
__device__ __forceinline__ float bflo(u32 u) { return asf(u << 16); }
__device__ __forceinline__ float bfhi(u32 u) { return asf(u & 0xffff0000u); }
__device__ __forceinline__ u32 pack2(float lo, float hi) { bf2 v; v[0] = (__bf16)lo; v[1] = (__bf16)hi; return __builtin_bit_cast(u32, v); }
__device__ __forceinline__ float rbf(float f) { return (float)(__bf16)f; }
typedef float f2 __attribute__((ext_vector_type(2)));
__device__ __forceinline__ f2 rb2(f2 v) { return f2{rbf(v[0]), rbf(v[1])}; }
__device__ __forceinline__ f4 rb4(f4 v) {
    float a = v[0], b = v[1], c = v[2], d = v[3];
    asm("v_cvt_pk_bf16_f32 %0, 0, %0\n\tv_cvt_pk_bf16_f32 %1, 0, %1\n\tv_cvt_pk_bf16_f32 %2, 0, %2\n\tv_cvt_pk_bf16_f32 %3, 0, %3" : "+v"(a), "+v"(b), "+v"(c), "+v"(d));
    return f4{a, b, c, d}; }
__device__ __forceinline__ f4 unpk(v2u v) { return f4{bflo(v[0]), bfhi(v[0]), bflo(v[1]), bfhi(v[1])}; }
__device__ __forceinline__ f4 fma4(f4 a, f4 b, f4 c) { return __builtin_elementwise_fma(a, b, c); }
__device__ __forceinline__ f4 bc4(float x) { return f4{x, x, x, x}; }
__device__ __forceinline__ u32 canon(u32 u) { return u < 0xFFFFFFFEu ? u : 0xFFFFFFFEu; }
__device__ __forceinline__ v4u canon4(f4 v) { return v4u{canon(asu(v[0])), canon(asu(v[1])), canon(asu(v[2])), canon(asu(v[3]))}; }
__device__ __forceinline__ u32 umax3(u32 a, u32 b, u32 c) { const u32 m = a > b ? a : b; return m > c ? m : c; }
__device__ __forceinline__ u32 vmax4(u32 m, v4u v) { return umax3(umax3(m, v[0], v[1]), v[2], v[3]); }
__device__ __forceinline__ bool all_valid(u32 m) { return __builtin_amdgcn_ballot_w64(m == SENT) == 0ull; }
__device__ __forceinline__ float dot2(u32 a, u32 b, float c) { return __builtin_amdgcn_fdot2_f32_bf16(__builtin_bit_cast(bf2, a), __builtin_bit_cast(bf2, b), c, false); }
__device__ __forceinline__ f4 mfma(v4u a, v4u b, f4 c) { return __builtin_amdgcn_mfma_f32_16x16x32_bf16(__builtin_bit_cast(bf8, a), __builtin_bit_cast(bf8, b), c, 0, 0, 0); }
template <int CTRL> __device__ __forceinline__ u32 dppu(u32 x) { return (u32)__builtin_amdgcn_mov_dpp((int)x, CTRL, 0xf, 0xf, true); }
template <int CTRL> __device__ __forceinline__ float dppf(float x) { return asf(dppu<CTRL>(asu(x))); }
template <int K> __device__ __forceinline__ v4u ror4k(v4u v) {
    if constexpr (K == 0) return v; else return v4u{dppu<0x120 + 4 * K>(v[0]), dppu<0x120 + 4 * K>(v[1]), dppu<0x120 + 4 * K>(v[2]), dppu<0x120 + 4 * K>(v[3])}; }
__device__ __forceinline__ float row_sum(float x) { x += dppf<0x128>(x); x += dppf<0x124>(x); x += dppf<0x122>(x); x += dppf<0x121>(x); return x; }
__device__ __forceinline__ float row_max(float x) { x = fmaxf(x, dppf<0x128>(x)); x = fmaxf(x, dppf<0x124>(x)); x = fmaxf(x, dppf<0x122>(x)); x = fmaxf(x, dppf<0x121>(x)); return x; }
__device__ __forceinline__ float xrow_sum(float x) {
    auto s = __builtin_amdgcn_permlane16_swap(asu(x), asu(x), false, false); x = asf(s[0]) + asf(s[1]);
    auto t = __builtin_amdgcn_permlane32_swap(asu(x), asu(x), false, false); return asf(t[0]) + asf(t[1]); }
__device__ __forceinline__ float xrow_max(float x) {
    auto s = __builtin_amdgcn_permlane16_swap(asu(x), asu(x), false, false); x = fmaxf(asf(s[0]), asf(s[1]));
    auto t = __builtin_amdgcn_permlane32_swap(asu(x), asu(x), false, false); return fmaxf(asf(t[0]), asf(t[1])); }
__device__ __forceinline__ float wave_sum(float x) { return xrow_sum(row_sum(x)); }
__device__ __forceinline__ float wave_max(float x) { return xrow_max(row_max(x)); }
__device__ __forceinline__ float fdiv(float a, float b) { const float r = __builtin_amdgcn_rcpf(b); const float q = a * r; return fmaf(fmaf(-q, b, a), r, q); }
__device__ __forceinline__ float silu_bf(float v) { return fdiv(v, 1.f + __builtin_amdgcn_exp2f(fminf(-v * 1.4426950408889634f, 126.f))); }
__device__ __forceinline__ v4u silu8(v4u c) {
    v4u r;
#pragma unroll
    for (int i = 0; i < 4; ++i) r[i] = pack2(silu_bf(bflo(c[i])), silu_bf(bfhi(c[i])));
    return r; }
#define BAR() asm volatile("s_waitcnt lgkmcnt(0)\n\ts_barrier" ::: "memory")
#define LGKM0() asm volatile("s_waitcnt lgkmcnt(0)" ::: "memory")
#define VMW(n) asm volatile("s_waitcnt vmcnt(%0)" :: "i"(n) : "memory")
#define MEMBAR() asm volatile("" ::: "memory")
typedef __amdgpu_buffer_rsrc_t rsrc_t;
__device__ __forceinline__ v4u ld_sc1(rsrc_t r, u32 off) { return __builtin_amdgcn_raw_buffer_load_b128(r, off, 0, 16); }
__device__ __forceinline__ v2u ld_sc1_2(rsrc_t r, u32 off) { return __builtin_amdgcn_raw_buffer_load_b64(r, off, 0, 16); }
__device__ __forceinline__ void st_sc1(rsrc_t r, u32 off, v4u v) { __builtin_amdgcn_raw_buffer_store_b128(v, r, off, 0, 16); }
__device__ __forceinline__ void st_sc1_2(rsrc_t r, u32 off, v2u v) { __builtin_amdgcn_raw_buffer_store_b64(v, r, off, 0, 16); }
__device__ __forceinline__ f4 asf4(v4u v) { return __builtin_bit_cast(f4, v); }

__device__ __forceinline__ void dma_unit(v4u srd, u32 v0, u32 v1, u32 sA, u32 sB, u32 lds) {
    u32 keep;
    asm volatile("s_nop 4\n\ts_mov_b32 %0, m0\n\ts_mov_b32 m0, %6\n\ts_nop 0\n\t"
                 "buffer_load_dwordx4 %1, %3, %4 offen nt lds\n\ts_add_u32 m0, m0, 0x400\n\ts_nop 0\n\t"
                 "buffer_load_dwordx4 %2, %3, %4 offen nt lds\n\ts_add_u32 m0, m0, 0x400\n\ts_nop 0\n\t"
                 "buffer_load_dwordx4 %1, %3, %5 offen nt lds\n\ts_add_u32 m0, m0, 0x400\n\ts_nop 0\n\t"
                 "buffer_load_dwordx4 %2, %3, %5 offen nt lds\n\ts_mov_b32 m0, %0"
                 : "=&s"(keep) : "v"(v0), "v"(v1), "s"(srd), "s"(sA), "s"(sB), "s"(lds) : "memory", "scc");
}
__device__ __forceinline__ void dma_1k(v4u srd, u32 voff, u32 soff, u32 lds) {
    u32 keep;
    asm volatile("s_nop 4\n\ts_mov_b32 %0, m0\n\ts_mov_b32 m0, %4\n\ts_nop 0\n\tbuffer_load_dwordx4 %1, %2, %3 offen lds\n\ts_mov_b32 m0, %0"
                 : "=&s"(keep) : "v"(voff), "s"(srd), "s"(soff), "s"(lds) : "memory");
}
struct WStream { v4u srd; u32 base, pitch, v0, v1; };
__device__ __forceinline__ WStream mk_stream(v4u srd, u32 base, u32 pitch, int l) {
    WStream s; s.srd = srd; s.base = base; s.pitch = pitch;
    const u32 sw = 2u * ((l >> 4) & 1);
    s.v0 = (l >> 3) * pitch + (((l & 7) ^ sw) << 4);
    s.v1 = (8 + (l >> 3)) * pitch + (((l & 7) ^ (sw | 4u)) << 4);
    return s; }
__device__ __forceinline__ void issue(const WStream& s, int n, u32 lds) { const u32 a = s.base + (u32)n * 32u * s.pitch; dma_unit(s.srd, s.v0, s.v1, a, a + 16u * s.pitch, lds); }
__device__ __forceinline__ void read_unit(LAS unsigned char* L, const u32 (&ta)[4], int slot, v4u (&f)[4]) {
#pragma unroll
    for (int T = 0; T < 4; ++T) {
        const v2u lo = __builtin_bit_cast(v2u, __builtin_amdgcn_ds_read_tr16_b64_v4i16((LAS v4s*)(L + ta[T] + slot * 4096)));
        const v2u hi = __builtin_bit_cast(v2u, __builtin_amdgcn_ds_read_tr16_b64_v4i16((LAS v4s*)(L + ta[T] + slot * 4096 + 512)));
        f[T] = v4u{lo[0], lo[1], hi[0], hi[1]};
    } }
__device__ __forceinline__ f4 sel_tile(const f4 (&a)[4], int t) { return t == 0 ? a[0] : t == 1 ? a[1] : t == 2 ? a[2] : a[3]; }

__device__ __forceinline__ void dma_kv(v4u srd, u32 vo, u32 so, u32 lds) {
    u32 keep;
    asm volatile("s_nop 4\n\ts_mov_b32 %0, m0\n\ts_mov_b32 m0, %4\n\ts_nop 0\n\t"
                 "buffer_load_dwordx4 %1, %2, %3 offen nt lds\n\ts_add_u32 m0, m0, 0x380\n\ts_nop 0\n\t"
                 "buffer_load_dwordx4 %1, %2, %3 offen offset:128 nt lds\n\ts_add_u32 m0, m0, 0x380\n\ts_nop 0\n\t"
                 "buffer_load_dwordx4 %1, %2, %3 offen offset:256 nt lds\n\ts_mov_b32 m0, %0"
                 : "=&s"(keep) : "v"(vo), "s"(srd), "s"(so), "s"(lds) : "memory", "scc");
}
__device__ __forceinline__ float qk_unit(v4u k0, v4u k1, v4u k2, const v4u (&q)[3]) {
    float s = 0.f, t = 0.f;
#pragma unroll
    for (int r = 0; r < 4; r += 2) { s = dot2(k0[r], q[0][r], s); t = dot2(k0[r + 1], q[0][r + 1], t); }
#pragma unroll
    for (int r = 0; r < 4; r += 2) { s = dot2(k1[r], q[1][r], s); t = dot2(k1[r + 1], q[1][r + 1], t); }
#pragma unroll
    for (int r = 0; r < 4; r += 2) { s = dot2(k2[r], q[2][r], s); t = dot2(k2[r + 1], q[2][r + 1], t); }
    return s + t;
}
__device__ __forceinline__ void pv_unit(v4u v0, v4u v1, v4u v2, u32 plo, float (&acc)[24]) {
    const u32 phi = plo << 16;
#pragma unroll
    for (int r = 0; r < 4; ++r) {
        acc[2 * r] = dot2(v0[r], plo, acc[2 * r]); acc[2 * r + 1] = dot2(v0[r], phi, acc[2 * r + 1]);
        acc[8 + 2 * r] = dot2(v1[r], plo, acc[8 + 2 * r]); acc[8 + 2 * r + 1] = dot2(v1[r], phi, acc[8 + 2 * r + 1]);
        acc[16 + 2 * r] = dot2(v2[r], plo, acc[16 + 2 * r]); acc[16 + 2 * r + 1] = dot2(v2[r], phi, acc[16 + 2 * r + 1]);
    }
}

__device__ __forceinline__ void kv_path(const Params& p, LAS unsigned char* L, int j, int w, int l) {
    const rsrc_t rws = __builtin_amdgcn_make_buffer_rsrc((void*)p.ws, 0, WS_END, 0x00020000);
    const rsrc_t rk = __builtin_amdgcn_make_buffer_rsrc((void*)p.kc, 0, 4 * NH * SKV * DH * 2, 0x00020000);
    const rsrc_t rv = __builtin_amdgcn_make_buffer_rsrc((void*)p.vc, 0, 4 * NH * SKV * DH * 2, 0x00020000);
    volatile LAS u32* misc = (volatile LAS u32*)(L + L_MISC);
    const int rk3 = (j >> 3) / 3, t3 = (j & 7) * 8 + rk3, pos = (j >> 3) - 3 * rk3;
    const int slot = pos == 1 ? (w & 1) : 0;
    const int bh = 2 * t3 + (pos == 2 ? 1 : (pos == 1 ? slot : 0));
    const int part = pos == 0 ? 0 : (pos == 2 ? 1 : (slot == 0 ? 1 : 0));
    const int wi = pos == 1 ? (w >> 1) : w;
    const int ub = pos == 0 ? 0 : (pos == 2 ? 43 : (slot == 0 ? 85 : 0));
    const int u1 = ub + 11 * wi - (wi > 5 ? wi - 5 : 0);
    const int nl = (pos == 1 ? wi == 3 : wi >= 5) ? 2 : 3;
    const bool lead = w == 0 || (w == 1 && pos == 1);
    const u32 opb = WS_OP + (bh * 2 + part) * 768, mlb = WS_ML + ((bh & 31) * 4 + (bh >> 5)) * 16 + part * 8;
    if (threadIdx.x == 0) { misc[0] = 0; misc[1] = 0; misc[2] = 0; misc[3] = 0; misc[6] = 0; }
    BAR();
    STAMP(0);
    if (w == 4) {
        __builtin_amdgcn_s_setprio(3);
        const u64 T = __hip_atomic_load((GAS u64*)(p.ws + WS_E), RLX_AGENT) + 1ull;
        st_sc1(rws, l < 48 ? opb + l * 16 : OOB, v4u{SENT, SENT, SENT, SENT});
        st_sc1_2(rws, l == 0 ? mlb : OOB, v2u{SENT, SENT});
        const int bh1 = 2 * t3 + 1;
        st_sc1(rws, (pos == 1 && l < 48) ? WS_OP + bh1 * 2 * 768 + l * 16 : OOB, v4u{SENT, SENT, SENT, SENT});
        st_sc1_2(rws, (pos == 1 && l == 0) ? WS_ML + ((bh1 & 31) * 4 + (bh1 >> 5)) * 16 : OOB, v2u{SENT, SENT});
        VMW(0);
        if (l == 0) {
            __hip_atomic_store((GAS u64*)(p.ws + WS_FLAG) + blockIdx.x, T, RLX_AGENT);
            misc[4] = (u32)T; misc[5] = (u32)(T >> 32); LGKM0(); misc[2] = 1;
        }
        STAMP(9);
        __builtin_amdgcn_s_setprio(0);
    }
    const u32 vo = (l >> 3) * 384 + (l & 7) * 16;
    const u32 base = ((u32)bh * SKV + (SKV - WIN)) * 384u + (u32)u1 * 3072u;
    const u32 wl = w * K_W;
    {
        const v4u sk = mk_srd(p.kc, 4 * NH * SKV * DH * 2), sv = mk_srd(p.vc, 4 * NH * SKV * DH * 2);
        const u32 lb = (u32)(size_t)L + wl;
        dma_kv(sk, vo, base + 8 * 3072, lb); dma_kv(sk, vo, base + 9 * 3072, lb + 3072);
        if (nl == 3) dma_kv(sk, vo, base + 10 * 3072, lb + 6144);
        dma_kv(sv, vo, base + 8 * 3072, lb + K_V); dma_kv(sv, vo, base + 9 * 3072, lb + K_V + 3072);
        if (nl == 3) dma_kv(sv, vo, base + 10 * 3072, lb + K_V + 6144);
    }
    v4u K[24], V[24];
#pragma unroll
    for (int jj = 0; jj < 8; ++jj)
#pragma unroll
        for (int i = 0; i < 3; ++i) K[jj * 3 + i] = __builtin_amdgcn_raw_buffer_load_b128(rk, vo + i * 128, base + jj * 3072, 2);
#pragma unroll
    for (int jj = 0; jj < 8; ++jj)
#pragma unroll
        for (int i = 0; i < 3; ++i) V[jj * 3 + i] = __builtin_amdgcn_raw_buffer_load_b128(rv, vo + i * 128, base + jj * 3072, 2);
    __builtin_amdgcn_sched_barrier(0);
    if (w == 0) STAMP(1);
    if (w == 7) STAMP(2);
    if (w < 4) {
        for (int sp = 0; misc[2] == 0 && sp < SPIN_L; ++sp) __builtin_amdgcn_s_sleep(1);
        const u32 tlo = misc[4], thi = misc[5];
        for (int sp = 0; sp < SPIN_G && misc[6] == 0; ++sp) {
            MEMBAR();
            const v2u f = ld_sc1_2(rws, WS_FLAG + l * 8);
            if (__builtin_amdgcn_ballot_w64(f[0] != tlo || f[1] != thi) == 0ull) misc[6] = 1;
        }
        const u32 qo = WS_QP + (u32)((bh >> 5) * NQ + (bh & 31) * DH + 4 * l) * 4u;
        v4u a[5] = {};
        bool need = l < 48;
        for (int sp = 0; sp < SPIN_G; ++sp) {
            MEMBAR();
            if (__builtin_amdgcn_readfirstlane(misc[slot]) != 0u) break;
            if (need) {
                u32 mx = 0;
#pragma unroll
                for (int ks = 0; ks < 5; ++ks) a[ks] = ld_sc1(rws, qo + ks * (4 * NQ * 4));
#pragma unroll
                for (int ks = 0; ks < 5; ++ks) mx = vmax4(mx, a[ks]);
                need = mx == SENT;
            }
            if (__builtin_amdgcn_ballot_w64(need) == 0ull || sp == SPIN_G - 1) {
                const f4 qs = (((asf4(a[0]) + asf4(a[1])) + asf4(a[2])) + asf4(a[3])) + asf4(a[4]);
                if (l < 48) *(LAS v2u*)(L + K_QS + slot * 384 + l * 8) = v2u{pack2(qs[0], qs[1]), pack2(qs[2], qs[3])};
                LGKM0();
                misc[slot] = 1;
                break;
            }
        }
        if (w == 0) STAMP(3);
    } else {
        for (int sp = 0; misc[slot] == 0 && sp < SPIN_L; ++sp) __builtin_amdgcn_s_sleep(1);
    }
    VMW(48);
    const int c = l & 7;
    float m, lsum;
    v4u p0, p1, p2;
    {
        v4u q[3];
#pragma unroll
        for (int i = 0; i < 3; ++i) q[i] = *(LAS v4u*)(L + K_QS + slot * 384 + (c + 8 * i) * 16);
        float y[4], x[8];
        LAS v4u* KL = (LAS v4u*)(L + wl + l * 16);
#pragma unroll
        for (int jj = 0; jj < 3; ++jj) { y[jj] = qk_unit(KL[jj * 192], KL[jj * 192 + 64], KL[jj * 192 + 128], q); __builtin_amdgcn_sched_barrier(0); }
#pragma unroll
        for (int jj = 0; jj < 8; ++jj) x[jj] = qk_unit(K[3 * jj], K[3 * jj + 1], K[3 * jj + 2], q);
#pragma unroll
        for (int h = 0; h < 4; ++h) { const float a = x[h] + dppf<0x141>(x[h]), b = x[h + 4] + dppf<0x141>(x[h + 4]); x[h] = (c & 4) ? b : a; }
        { const float a = y[0] + dppf<0x141>(y[0]), b = y[2] + dppf<0x141>(y[2]), a1 = y[1] + dppf<0x141>(y[1]); y[0] = (c & 4) ? b : a; y[1] = a1; }
#pragma unroll
        for (int h = 0; h < 2; ++h) { const float a = x[h] + dppf<0x4E>(x[h]), b = x[h + 2] + dppf<0x4E>(x[h + 2]); x[h] = (c & 2) ? b : a; }
        { const float a = y[0] + dppf<0x4E>(y[0]), b = y[1] + dppf<0x4E>(y[1]); y[0] = (c & 2) ? b : a; }
        { const float a = x[0] + dppf<0xB1>(x[0]), b = x[1] + dppf<0xB1>(x[1]); x[0] = (c & 1) ? b : a; }
        y[0] += dppf<0xB1>(y[0]);
        const float s1 = rbf(rbf(x[0]) * 0.07216878364870322f);
        const float s2 = (c >> 1) < nl ? rbf(rbf(y[0]) * 0.07216878364870322f) : -INFINITY;
        m = wave_max(fmaxf(s1, s2));
        const float e1 = __builtin_amdgcn_exp2f((s1 - m) * LOG2E), e2 = __builtin_amdgcn_exp2f((s2 - m) * LOG2E);
        *(LAS u32*)(L + wl + K_PX + l * 4) = pack2(e1, 0.f);
        *(LAS u32*)(L + wl + K_PX2 + (l >> 1) * 4) = pack2(e2, 0.f);
        lsum = wave_sum(e1 + ((c & 1) ? 0.f : e2));
        if (l == 0) { *(LAS v2u*)(L + K_ML + w * 8) = v2u{asu(m), asu(lsum)}; LGKM0(); __hip_atomic_fetch_add((LAS u32*)(L + L_MISC + 12), 1u, __ATOMIC_RELAXED, __HIP_MEMORY_SCOPE_WORKGROUP); }
        LGKM0();
        p0 = *(LAS v4u*)(L + wl + K_PX + (l >> 3) * 32); p1 = *(LAS v4u*)(L + wl + K_PX + (l >> 3) * 32 + 16); p2 = *(LAS v4u*)(L + wl + K_PX2 + (l >> 3) * 16);
    }
    if (w == 0) STAMP(4);
    float acc[24];
#pragma unroll
    for (int i = 0; i < 24; ++i) acc[i] = 0.f;
    {
        LAS v4u* VL = (LAS v4u*)(L + wl + K_V + l * 16);
        pv_unit(VL[0], VL[64], VL[128], p2[0], acc);
        pv_unit(VL[192], VL[256], VL[320], p2[1], acc);
        if (nl == 3) pv_unit(VL[384], VL[448], VL[512], p2[2], acc);
    }
#pragma unroll
    for (int jj = 0; jj < 6; ++jj) pv_unit(V[3 * jj], V[3 * jj + 1], V[3 * jj + 2], jj < 4 ? p0[jj & 3] : p1[jj & 3], acc);
    float fsc;
    {
        for (int sp = 0; misc[3] < 8u && sp < SPIN_L; ++sp) __builtin_amdgcn_s_sleep(1);
        MEMBAR();
        const v2u t = *(LAS v2u*)(L + K_ML + c * 8);
        const bool mine = pos != 1 || (l & 1) == slot;
        const float mi = mine ? asf(t[0]) : -INFINITY;
        float M = fmaxf(mi, dppf<0xB1>(mi)); M = fmaxf(M, dppf<0x4E>(M)); M = fmaxf(M, dppf<0x141>(M));
        fsc = __builtin_amdgcn_exp2f((m - M) * LOG2E);
        if (lead) {
            float e = mine ? asf(t[1]) * __builtin_amdgcn_exp2f((mi - M) * LOG2E) : 0.f;
            e += dppf<0xB1>(e); e += dppf<0x4E>(e); e += dppf<0x141>(e);
            st_sc1_2(rws, l == 0 ? mlb : OOB, v2u{canon(asu(M)), canon(asu(e))});
            if (w == 0) STAMP(5);
        }
    }
#pragma unroll
    for (int jj = 6; jj < 8; ++jj) pv_unit(V[3 * jj], V[3 * jj + 1], V[3 * jj + 2], p1[jj & 3], acc);
    if (w == 7) STAMP(6);
    float y[12], z[6];
#pragma unroll
    for (int n = 0; n < 12; ++n) { auto s = __builtin_amdgcn_permlane32_swap(asu(acc[n]), asu(acc[n + 12]), false, false); y[n] = asf(s[0]) + asf(s[1]); }
#pragma unroll
    for (int n = 0; n < 6; ++n) { auto s = __builtin_amdgcn_permlane16_swap(asu(y[n]), asu(y[n + 6]), false, false); z[n] = asf(s[0]) + asf(s[1]); }
#pragma unroll
    for (int n = 0; n < 3; ++n) {
        const float a = z[n] + dppf<0x128>(z[n]), b = z[n + 3] + dppf<0x128>(z[n + 3]);
        const int ai = 3 * (l >> 3) + n;
        *(LAS float*)(L + wl + (64 * (ai >> 3) + 8 * c + (ai & 7)) * 4) = ((l & 8) ? b : a) * fsc;
    }
    BAR();
    if (w == 0) STAMP(7);
    if (lead) {
        const u32 pb = slot * K_W + (l < 48 ? l : 47) * 16;
        f4 o = (*(LAS f4*)(L + pb) + *(LAS f4*)(L + pb + 2 * K_W)) + (*(LAS f4*)(L + pb + 4 * K_W) + *(LAS f4*)(L + pb + 6 * K_W));
        if (pos != 1) o += (*(LAS f4*)(L + pb + K_W) + *(LAS f4*)(L + pb + 3 * K_W)) + (*(LAS f4*)(L + pb + 5 * K_W) + *(LAS f4*)(L + pb + 7 * K_W));
        st_sc1(rws, l < 48 ? opb + l * 16 : OOB, canon4(o));
        if (w == 0) STAMP(8);
    }
}

enum { R_SA = 0, R_SB = 1, R_SC = 2 };
#define UNDEF4(v) asm volatile("" : "=v"(v))
template <int ROLE>
__device__ __forceinline__ void w_path(const Params& p, LAS unsigned char* L, int u, int w, int l) {
    const rsrc_t rws = __builtin_amdgcn_make_buffer_rsrc((void*)p.ws, 0, WS_END, 0x00020000);
    const int tid = threadIdx.x, g = l >> 4, tq = (l & 15) >> 2, b4 = l & 3;
    const u32 lbase = (u32)(size_t)L, stg = lbase + w * 16384;
    const bool hasF1 = u < 50, hasF2 = u < 60;
    const int kf = u / 10, lp = u - 10 * kf, kf2 = u / 12, cs = u - 12 * kf2;
    const int ri = ROLE == R_SA ? u : (ROLE == R_SB ? u - 30 : u - 54);
    const WStream sF1 = mk_stream(mk_srd(p.wm, hasF1 ? DM * 3840 * 2 : 0), (u32)((128 * kf + 32 * (w >> 1)) * 7680 + (128 * lp + 64 * (w & 1)) * 2), 7680, l);
    const WStream sF2 = mk_stream(mk_srd(p.wq, hasF2 ? DM * NQ * 2 : 0), (u32)(128 * kf2 * 12288 + (512 * cs + 64 * w) * 2), 12288, l);
    const int mid = ROLE == R_SC ? (w < 4 ? (ri >= 6 ? ri : -1) : 30 + ri) : (w < 4 ? (ri < 6 ? ri : ri + 4) : (ri == 20 ? 28 : (ri == 21 ? 29 : -1)));
    const bool hasM = ROLE != R_SA && mid >= 0;
    const WStream sM = mk_stream(mk_srd(p.wm, hasM ? DM * 3840 * 2 : 0), (u32)(160 * (w & 3) * 7680 + (1280 + 64 * mid) * 2), 7680, l);
    const int ra = ri / 10, ca = ri - 10 * ra;
    const WStream sT = ROLE == R_SA ? mk_stream(mk_srd(p.wo, NQ * DM * 2), (u32)((2048 * ra + 256 * w) * 1280 + ca * 128), 1280, l)
                     : ROLE == R_SB ? mk_stream(mk_srd((w & 1) ? p.wu : p.wg, DM * FF * 2), (u32)(160 * (w >> 1) * 3072 + ri * 128), 3072, l)
                                    : mk_stream(mk_srd(p.wd, FF * DM * 2), (u32)(192 * w * 1280 + ri * 128), 1280, l);
    constexpr int NM = ROLE == R_SA ? 0 : 5, NT = ROLE == R_SA ? 8 : (ROLE == R_SB ? 5 : 6), NA = NM + NT;
    u32 ta[4];
    { const u32 f = 2u * (tq >> 1) | 4u * (g & 1);
#pragma unroll
      for (int T = 0; T < 4; ++T) ta[T] = w * 16384 + (8 * g + tq) * 128 + ((((u32)(2 * T)) ^ f) + (b4 >> 1)) * 16 + (b4 & 1) * 8; }
    const u32 dcol = 16 * tq + 4 * g;
    const u32 f1o = hasF1 ? WS_F1P + (u32)((kf * 4 + b4) * 1280 + 128 * lp + 64 * w + dcol) * 4u : OOB;
    const u32 qpo = hasF2 ? WS_QP + (u32)((kf2 * 4 + b4) * NQ + 512 * cs + 64 * w + dcol) * 4u : OOB;
    const u32 mfo = hasM ? WS_MODF + (u32)(b4 * 2560 + 64 * mid + dcol) * 2u : OOB;
    const u32 outo = ROLE == R_SA ? WS_AP + (u32)((ra * 4 + b4) * DM + 64 * ca + dcol) * 4u : WS_HID + (u32)(b4 * FF + 64 * ri + dcol) * 2u;
    volatile LAS u32* misc = (volatile LAS u32*)(L + L_MISC);
    STAMP(0);
    if (w < 5) { dma_1k(mk_srd(p.c, 5120), l * 16, w * 1024, lbase + L_CS + w * 1024); dma_1k(mk_srd(p.x, 5120), l * 16, w * 1024, lbase + L_XS + w * 1024); }
    issue(sF1, 0, stg);
    { u32 keep;
      asm volatile("s_nop 4\n\ts_mov_b32 %0, m0\n\ts_mov_b32 m0, %5\n\ts_nop 0\n\tbuffer_load_dword %1, %2, %3 offen nt lds\n\tbuffer_load_dword %1, %4, 0 offen nt lds\n\ts_mov_b32 m0, %0"
                   : "=&s"(keep) : "v"(l == 0 ? 0u : OOB), "s"(sT.srd), "s"(sT.base), "s"(mk_srd(p.out, 5120)), "s"(lbase + L_GB) : "memory"); }
    if (w == 0) {
        u32 keep;
        asm volatile("s_nop 4\n\ts_mov_b32 %0, m0\n\ts_mov_b32 m0, %3\n\ts_nop 0\n\tbuffer_load_dword %1, %2, 0 offen sc1 lds\n\ts_mov_b32 m0, %0"
                     : "=&s"(keep) : "v"(l < 2 ? WS_E + l * 4 : OOB), "s"(mk_srd(p.ws, WS_END)), "s"(lbase + L_GA) : "memory");
    }
    { const v4u s4 = {SENT, SENT, SENT, SENT};
      st_sc1(rws, qpo, s4);
      if (w < 2) st_sc1(rws, f1o, s4);
      if (ROLE != R_SA && (w & 3) == 0) st_sc1_2(rws, mfo, v2u{SENT, SENT});
      if (w == 0) {
          if (ROLE == R_SA) st_sc1(rws, outo, s4);
          if (ROLE == R_SB) st_sc1_2(rws, outo, v2u{SENT, SENT});
      } }
    if (tid == 0) { misc[0] = 0; misc[1] = 0; }
    BAR();
    issue(sF2, 0, stg + 4096);
    auto issueB = [&](int n) { if (n < NM) issue(sM, n, stg + ((n + 1) & 3) * 4096); else issue(sT, n - NM, stg + ((n + 1) & 3) * 4096); };
    {
        v4u ft[NT][4];
        {
            VMW(4);
            BAR();
            STAMP(1);
            const u64 T = *(LAS u64*)(L + L_GA) + 1ull;
            const u32 tlo = (u32)T, thi = (u32)(T >> 32);
            if (tid == 0) __hip_atomic_store((GAS u64*)(p.ws + WS_FLAG) + blockIdx.x, T, RLX_AGENT);
            {
                v4u f[4]; read_unit(L, ta, 0, f);
                const v4u a = silu8(*(LAS v4u*)(L + L_CS + b4 * 1280 + (128 * kf + 32 * (w >> 1) + 8 * g) * 2));
                f4 d[4];
#pragma unroll
                for (int T2 = 0; T2 < 4; ++T2) d[T2] = mfma(f[T2], a, f4{0.f, 0.f, 0.f, 0.f});
                *(LAS f4*)(L + L_RED + w * 1024 + l * 16) = sel_tile(d, tq);
                if (w >= 4) {
                    float xv[10], s = 0.f;
#pragma unroll
                    for (int i = 0; i < 5; ++i) { const u32 t = *(LAS u32*)(L + L_XS + (w - 4) * 1280 + (l + 64 * i) * 4); xv[2 * i] = bflo(t); xv[2 * i + 1] = bfhi(t); s += xv[2 * i] + xv[2 * i + 1]; }
                    const float mean = wave_sum(s) * (1.f / DM);
                    float v = 0.f;
#pragma unroll
                    for (int i = 0; i < 10; ++i) v = fmaf(xv[i] - mean, xv[i] - mean, v);
                    const float var = wave_sum(v) * (1.f / DM);
                    if (l == 0) *(LAS v2u*)(L + L_ST + (w - 4) * 8) = v2u{asu(rbf(mean)), asu(rbf(__builtin_amdgcn_sqrtf(rbf(rbf(var) + 1e-5f))))};
                }
                BAR();
                if (w < 2) {
                    LAS f4* R = (LAS f4*)(L + L_RED + w * 1024 + l * 16);
                    st_sc1(rws, f1o, canon4((R[0] + R[128]) + (R[256] + R[384])));
                }
                if (w == 0) STAMP(2);
                issue(sF2, 1, stg + 8192); issue(sF2, 2, stg + 12288); issue(sF2, 3, stg);
            }
            if (w < 2) {
                for (int sp = 0; sp < SPIN_G; ++sp) {
                    MEMBAR();
                    const v2u f = ld_sc1_2(rws, WS_FLAG + l * 8);
                    if (__builtin_amdgcn_ballot_w64(f[0] != tlo || f[1] != thi) == 0ull) break;
                }
                if (w == 0) STAMP(19);
                const int hb = tid >> 5, hi = tid & 31, col = 128 * kf2 + 4 * hi;
                const u32 o = WS_F1P + (u32)(hb * 1280 + col) * 4u;
                v4u a[5] = {}, c2[5] = {};
                bool need = hasF2;
                for (int sp = 0; sp < SPIN_G; ++sp) {
                    MEMBAR();
                    if (need) {
                        u32 mx = 0;
#pragma unroll
                        for (int ks = 0; ks < 5; ++ks) { a[ks] = ld_sc1(rws, o + ks * 20480); c2[ks] = ld_sc1(rws, o + ks * 20480 + 2560); }
#pragma unroll
                        for (int ks = 0; ks < 5; ++ks) mx = vmax4(vmax4(mx, a[ks]), c2[ks]);
                        need = mx == SENT;
                    }
                    if (__builtin_amdgcn_ballot_w64(need) == 0ull) break;
                }
                const f4 sa = (((asf4(a[0]) + asf4(a[1])) + asf4(a[2])) + asf4(a[3])) + asf4(a[4]);
                const f4 sc = (((asf4(c2[0]) + asf4(c2[1])) + asf4(c2[2])) + asf4(c2[3])) + asf4(c2[4]);
                const v2u st = *(LAS v2u*)(L + L_ST + hb * 8), xr = *(LAS v2u*)(L + L_XS + hb * 1280 + (hasF2 ? col : 0) * 2);
                const float den = asf(st[1]), rc = __builtin_amdgcn_rcpf(den);
                const f4 e = rb4(unpk(xr) - asf(st[0])), q1 = e * rc;
                const f4 h = rb4(rb4(fma4(fma4(-q1, bc4(den), e), bc4(rc), q1)) * rb4(rb4(sc) + 1.f)) + rb4(sa);
                *(LAS v2u*)(L + L_HB + hb * 256 + hi * 8) = v2u{pack2(h[0], h[1]), pack2(h[2], h[3])};
                LGKM0();
                if (l == 0) __hip_atomic_fetch_add((LAS u32*)(L + L_MISC), 1u, __ATOMIC_RELAXED, __HIP_MEMORY_SCOPE_WORKGROUP);
                if (w == 0) STAMP(4);
            }
            {
                v4u fq[4][4];
                VMW(12); read_unit(L, ta, 1, fq[0]); LGKM0();
                VMW(8); read_unit(L, ta, 2, fq[1]);
                VMW(4); read_unit(L, ta, 3, fq[2]);
                issueB(0);
                VMW(4); read_unit(L, ta, 0, fq[3]);
                if (w == 7) STAMP(3);
                for (int sp = 0; misc[0] < 2u && sp < SPIN_L; ++sp) __builtin_amdgcn_s_sleep(1);
                MEMBAR();
                f4 d[4] = {};
#pragma unroll
                for (int n = 0; n < 4; ++n) {
                    const v4u a = *(LAS v4u*)(L + L_HB + b4 * 256 + (32 * n + 8 * g) * 2);
#pragma unroll
                    for (int T2 = 0; T2 < 4; ++T2) d[T2] = mfma(fq[n][T2], a, d[T2]);
                }
                st_sc1(rws, qpo, canon4(sel_tile(d, tq)));
                BAR();
                if (w == 7) STAMP(5);
                issueB(1); issueB(2); issueB(3);
            }
            if (NM) {
                f4 d[4] = {};
#pragma unroll
                for (int n = 0; n < NM; ++n) {
                    VMW(12);
                    v4u f[4]; read_unit(L, ta, (n + 1) & 3, f);
                    const v4u a = silu8(*(LAS v4u*)(L + L_CS + b4 * 1280 + (160 * (w & 3) + 32 * n + 8 * g) * 2));
                    LGKM0();
                    issueB(n + 4);
#pragma unroll
                    for (int T2 = 0; T2 < 4; ++T2) d[T2] = mfma(f[T2], a, d[T2]);
                }
                *(LAS f4*)(L + L_RED + w * 1024 + l * 16) = sel_tile(d, tq);
                BAR();
                if ((w & 3) == 0) {
                    LAS f4* R = (LAS f4*)(L + L_RED + w * 1024 + l * 16);
                    const f4 s = (R[0] + R[64]) + (R[128] + R[192]);
                    st_sc1_2(rws, mfo, v2u{canon(pack2(s[0], s[1])), canon(pack2(s[2], s[3]))});
                }
                BAR();
            }
#pragma unroll
            for (int n = 0; n < NT; ++n) {
                const int a = NM + n, younger = (NA - 1 - a) < 3 ? (NA - 1 - a) : 3;
                if (younger == 3) VMW(12); else if (younger == 2) VMW(8); else if (younger == 1) VMW(4); else VMW(0);
                read_unit(L, ta, (a + 1) & 3, ft[n]);
                if (a + 4 < NA) { LGKM0(); issueB(a + 4); }
                if (ROLE == R_SA && n == 1 && w == 0) {
                    for (int sp = 0; sp < SPIN_G; ++sp) {
                        MEMBAR();
                        const v4u f0 = ld_sc1(rws, WS_FLAG + l * 16), f1 = ld_sc1(rws, WS_FLAG + 1024 + l * 16);
                        const bool bad = f0[0] != tlo || f0[1] != thi || f0[2] != tlo || f0[3] != thi || f1[0] != tlo || f1[1] != thi || f1[2] != tlo || f1[3] != thi;
                        if (__builtin_amdgcn_ballot_w64(bad) == 0ull) break;
                    }
                    if (u == 0 && l == 0) __hip_atomic_store((GAS u64*)(p.ws + WS_E), T, RLX_AGENT);
                }
            }
            LGKM0();
            STAMP(20 + w);
            BAR();
            STAMP(6);
        }
        if (ROLE == R_SA) {
            f4 res[2];
            UNDEF4(res[0]); UNDEF4(res[1]);
            u32 oo[2], mo[2];
#pragma unroll
            for (int r = 0; r < 2; ++r) {
                const int row = 2048 * ra + 256 * w + 32 * (4 * r + tq) + 8 * g, hd = row / DH, dd = row - hd * DH;
                oo[r] = WS_OP + (u32)((b4 * 32 + hd) * 2) * 768u + dd * 4; mo[r] = WS_ML + (u32)(hd * 4 + b4) * 16u;
            }
            bool done[2] = {false, false}, wdone[2] = {false, false}, need[2] = {true, true}, needm[2] = {true, true};
            v4u a[2][4], ml[2];
            float w0[2], w1[2];
#pragma unroll
            for (int r = 0; r < 2; ++r) {
                UNDEF4(ml[r]); UNDEF4(w0[r]); UNDEF4(w1[r]);
#pragma unroll
                for (int i = 0; i < 4; ++i) UNDEF4(a[r][i]);
            }
            for (int sp = 0; sp < SPIN_G && !(done[0] && done[1]); ++sp) {
                MEMBAR();
#pragma unroll
                for (int r = 0; r < 2; ++r) {
                    if (needm[r]) ml[r] = ld_sc1(rws, mo[r]);
                    else if (need[r]) { a[r][0] = ld_sc1(rws, oo[r]); a[r][1] = ld_sc1(rws, oo[r] + 16); a[r][2] = ld_sc1(rws, oo[r] + 768); a[r][3] = ld_sc1(rws, oo[r] + 784); }
                }
                const bool last = sp == SPIN_G - 1;
                if (!(wdone[0] && wdone[1])) __builtin_amdgcn_s_sleep(POLL_SLEEP);
#pragma unroll
                for (int r = 0; r < 2; ++r) {
                    const bool hadm = !needm[r];
                    if (!wdone[r]) {
                        needm[r] = vmax4(0u, ml[r]) == SENT;
                        if (__builtin_amdgcn_ballot_w64(needm[r]) == 0ull || last) {
                            const float m0 = asf(ml[r][0]), l0 = asf(ml[r][1]), m1 = asf(ml[r][2]), l1 = asf(ml[r][3]), M = fmaxf(m0, m1);
                            const float e0 = __builtin_amdgcn_exp2f((m0 - M) * LOG2E), e1 = __builtin_amdgcn_exp2f((m1 - M) * LOG2E);
                            const float inv = fdiv(1.f, fmaf(e0, l0, e1 * l1));
                            w0[r] = e0 * inv; w1[r] = e1 * inv; wdone[r] = true;
                        }
                    }
                    if (!done[r]) {
                        need[r] = !hadm || vmax4(vmax4(vmax4(vmax4(0u, a[r][0]), a[r][1]), a[r][2]), a[r][3]) == SENT;
                        if ((__builtin_amdgcn_ballot_w64(need[r]) == 0ull && wdone[r]) || last) {
                            const f4 x0 = fma4(asf4(a[r][0]), bc4(w0[r]), asf4(a[r][2]) * w1[r]), x1 = fma4(asf4(a[r][1]), bc4(w0[r]), asf4(a[r][3]) * w1[r]);
                            const v4u bq = {pack2(x0[0], x0[1]), pack2(x0[2], x0[3]), pack2(x1[0], x1[1]), pack2(x1[2], x1[3])};
                            const v4u bk[4] = {bq, ror4k<1>(bq), ror4k<2>(bq), ror4k<3>(bq)};
                            f4 d[4] = {};
#pragma unroll
                            for (int s = 0; s < 4; ++s)
#pragma unroll
                                for (int T2 = 0; T2 < 4; ++T2) d[(T2 + s) & 3] = mfma(ft[4 * r + s][(T2 + s) & 3], bk[T2], d[(T2 + s) & 3]);
                            res[r] = sel_tile(d, tq);
                            done[r] = true;
                        }
                    }
                }
            }
            if (w == 0) STAMP(7);
            *(LAS f4*)(L + L_RED + w * 1024 + l * 16) = res[0] + res[1];
            BAR();
            if (w == 0) {
                STAMP(8);
                LAS f4* R = (LAS f4*)(L + L_RED + l * 16);
                st_sc1(rws, outo, canon4(((R[0] + R[64]) + (R[128] + R[192])) + ((R[256] + R[320]) + (R[384] + R[448]))));
                STAMP(9);
            }
        } else if (ROLE == R_SB) {
            const int i16 = l & 15, col = 128 * w + 8 * i16;
            f4 xa, xb, c1a, c1b, sba, sbb;
            if (w < 5) {
                v4u gav, sbv, cbv, a[6];
                const u32 mo = WS_MODF + (u32)(g * 2560 + col) * 2u;
                for (int sp = 0; sp < SPIN_G; ++sp) {
                    MEMBAR();
                    gav = ld_sc1(rws, mo); sbv = ld_sc1(rws, mo + 1280); cbv = ld_sc1(rws, mo + 2560);
                    if (all_valid(vmax4(vmax4(vmax4(0u, gav), sbv), cbv))) break;
                }
                const v4u xr = *(LAS v4u*)(L + L_XS + g * 1280 + col * 2);
                xa = unpk(v2u{xr[0], xr[1]}); xb = unpk(v2u{xr[2], xr[3]});
                f4 gaa = unpk(v2u{gav[0], gav[1]}), gab = unpk(v2u{gav[2], gav[3]});
                c1a = rb4(unpk(v2u{cbv[0], cbv[1]}) + 1.f); c1b = rb4(unpk(v2u{cbv[2], cbv[3]}) + 1.f);
                sba = unpk(v2u{sbv[0], sbv[1]}); sbb = unpk(v2u{sbv[2], sbv[3]});
                asm volatile("" : "+v"(xa), "+v"(xb), "+v"(gaa), "+v"(gab), "+v"(c1a), "+v"(c1b), "+v"(sba), "+v"(sbb));
                if (w == 0) STAMP(10);
                const u32 ao = WS_AP + (u32)(g * DM + col) * 4u;
#pragma unroll
                for (int r = 0; r < 6; ++r) UNDEF4(a[r]);
                bool need = true;
                for (int sp = 0; sp < SPIN_G; ++sp) {
                    MEMBAR();
                    if (need) {
                        u32 mx = 0;
#pragma unroll
                        for (int r = 0; r < 3; ++r) { a[2 * r] = ld_sc1(rws, ao + r * 10240); a[2 * r + 1] = ld_sc1(rws, ao + r * 10240 + 16); }
#pragma unroll
                        for (int r = 0; r < 6; ++r) mx = vmax4(mx, a[r]);
                        need = mx == SENT;
                    }
                    if (__builtin_amdgcn_ballot_w64(need) == 0ull) break;
                }
                if (w == 0) STAMP(11);
                xa = rb4(xa + rb4(gaa * rb4((asf4(a[0]) + asf4(a[2])) + asf4(a[4]))));
                xb = rb4(xb + rb4(gab * rb4((asf4(a[1]) + asf4(a[3])) + asf4(a[5]))));
                const f4 t = xa + xb;
                const float mw = row_sum((t[0] + t[1]) + (t[2] + t[3])) * (1.f / 128.f);
                const f4 da = xa - mw, db = xb - mw, q2 = fma4(da, da, db * db);
                const float v = row_sum((q2[0] + q2[1]) + (q2[2] + q2[3]));
                *(LAS v2u*)(L + L_ST + 64 + g * 48 + w * 8) = v2u{asu(mw), asu(v)};
            }
            BAR();
            if (w < 5) {
                const f4 s0 = *(LAS f4*)(L + L_ST + 64 + g * 48), s1 = *(LAS f4*)(L + L_ST + 64 + g * 48 + 16);
                const f2 s2 = *(LAS f2*)(L + L_ST + 64 + g * 48 + 32);
                const float mean = (((s0[0] + s0[2]) + (s1[0] + s1[2])) + s2[0]) * 0.2f;
                const f4 dm = f4{s0[0], s0[2], s1[0], s1[2]} - mean, dq = dm * dm; const float d4 = s2[0] - mean;
                const float M2 = fmaf(128.f, fmaf(d4, d4, (dq[0] + dq[1]) + (dq[2] + dq[3])), ((s0[1] + s0[3]) + (s1[1] + s1[3])) + s2[1]);
                const float mb = rbf(mean), den = rbf(__builtin_amdgcn_sqrtf(rbf(rbf(M2 * (1.f / DM)) + 1e-5f))), rc = __builtin_amdgcn_rcpf(den);
                const f4 ea = rb4(xa - mb), eb = rb4(xb - mb), qa = ea * rc, qb = eb * rc;
                const f4 ha = rb4(rb4(fma4(fma4(-qa, bc4(den), ea), bc4(rc), qa)) * c1a) + sba, hb = rb4(rb4(fma4(fma4(-qb, bc4(den), eb), bc4(rc), qb)) * c1b) + sbb;
                *(LAS v4u*)(L + L_HB + g * 1280 + col * 2) = v4u{pack2(ha[0], ha[1]), pack2(ha[2], ha[3]), pack2(hb[0], hb[1]), pack2(hb[2], hb[3])};
            }
            BAR();
            if (w == 0) STAMP(12);
            f4 d[4] = {};
#pragma unroll
            for (int n = 0; n < 5; ++n) {
                const v4u a = *(LAS v4u*)(L + L_HB + b4 * 1280 + (160 * (w >> 1) + 32 * n + 8 * g) * 2);
#pragma unroll
                for (int T2 = 0; T2 < 4; ++T2) d[T2] = mfma(ft[n][T2], a, d[T2]);
            }
            *(LAS f4*)(L + L_RED + w * 1024 + l * 16) = sel_tile(d, tq);
            BAR();
            if (w == 0) {
                STAMP(13);
                LAS f4* R = (LAS f4*)(L + L_RED + l * 16);
                const f4 gt = rb4((R[0] + R[128]) + (R[256] + R[384])), up = rb4((R[64] + R[192]) + (R[320] + R[448]));
                const f4 mk = {gt[0] > 0.f ? 1.f : 0.f, gt[1] > 0.f ? 1.f : 0.f, gt[2] > 0.f ? 1.f : 0.f, gt[3] > 0.f ? 1.f : 0.f};
                const f4 hh = (gt * mk) * up;
                st_sc1_2(rws, outo, v2u{canon(pack2(hh[0], hh[1])), canon(pack2(hh[2], hh[3]))});
                STAMP(14);
            }
        } else {
            f4 x1, gbf;
            UNDEF4(x1); UNDEF4(gbf);
            if (w == 0) {
                v2u ga, gb;
                const u32 mo = WS_MODF + (u32)(b4 * 2560 + 64 * ri + dcol) * 2u;
                for (int sp = 0; sp < SPIN_G; ++sp) {
                    MEMBAR();
                    ga = ld_sc1_2(rws, mo); gb = ld_sc1_2(rws, mo + 3840);
                    if (all_valid(umax3(umax3(ga[0], ga[1], gb[0]), gb[1], 0u))) break;
                }
                const u32 ao = WS_AP + (u32)(b4 * DM + 64 * ri + dcol) * 4u;
                f4 s;
                for (int sp = 0; sp < SPIN_G; ++sp) {
                    MEMBAR();
                    const v4u p0 = ld_sc1(rws, ao), p1 = ld_sc1(rws, ao + 10240), p2 = ld_sc1(rws, ao + 20480);
                    s = (asf4(p0) + asf4(p1)) + asf4(p2);
                    if (all_valid(vmax4(vmax4(vmax4(0u, p0), p1), p2))) break;
                }
                x1 = rb4(unpk(*(LAS v2u*)(L + L_XS + b4 * 1280 + (64 * ri + dcol) * 2)) + rb4(unpk(ga) * rb4(s)));
                gbf = unpk(gb);
                asm volatile("" : "+v"(x1), "+v"(gbf));
                STAMP(15);
            }
            v4u a0, a1;
            UNDEF4(a0); UNDEF4(a1);
            const u32 h0 = WS_HID + (u32)(b4 * FF + 192 * w + 32 * tq + 8 * g) * 2u, h1 = tq < 2 ? h0 + 256 : OOB;
            bool need = true;
            for (int sp = 0; sp < SPIN_G; ++sp) {
                MEMBAR();
                if (need) { a0 = ld_sc1(rws, h0); a1 = ld_sc1(rws, h1); need = vmax4(vmax4(0u, a0), a1) == SENT; }
                if (__builtin_amdgcn_ballot_w64(need) == 0ull) break;
            }
            if (w == 0) STAMP(16);
            const v4u bk[4] = {a0, ror4k<1>(a0), ror4k<2>(a0), ror4k<3>(a0)}, bl[4] = {a1, ror4k<1>(a1), ror4k<2>(a1), ror4k<3>(a1)};
            f4 d[4] = {};
#pragma unroll
            for (int k = 0; k < 4; ++k)
#pragma unroll
                for (int s = 0; s < 6; ++s) d[(s + k) & 3] = mfma(ft[s][(s + k) & 3], s < 4 ? bk[k] : bl[k], d[(s + k) & 3]);
            *(LAS f4*)(L + L_RED + w * 1024 + l * 16) = sel_tile(d, tq);
            BAR();
            if (w == 0) {
                STAMP(17);
                LAS f4* R = (LAS f4*)(L + L_RED + l * 16);
                const f4 o = x1 + rb4(gbf * rb4(((R[0] + R[64]) + (R[128] + R[192])) + ((R[256] + R[320]) + (R[384] + R[448]))));
                *(GAS v2u*)(p.out + b4 * DM + 64 * ri + dcol) = v2u{pack2(o[0], o[1]), pack2(o[2], o[3])};
                STAMP(18);
            }
        }
    }
}

__global__ void __launch_bounds__(512, 2) fused_block(Params p) {
    extern __shared__ __attribute__((aligned(16))) unsigned char smem[];
    LAS unsigned char* L = (LAS unsigned char*)smem;
    const int l = threadIdx.x & 63, w = __builtin_amdgcn_readfirstlane(threadIdx.x >> 6), c = blockIdx.x;
    if (c >= NW) kv_path(p, L, c - NW, w, l);
    else if (c < 30) w_path<R_SA>(p, L, c, w, l);
    else if (c < 54) w_path<R_SB>(p, L, c, w, l);
    else w_path<R_SC>(p, L, c, w, l);
}

extern "C" void kernel_launch(void* const* d_in, const int* in_sizes, int n_in, void* d_out, int out_size, void* d_ws, size_t ws_size, hipStream_t stream) {
    Params p;
    p.x = (const u16*)d_in[0]; p.kc = (const u16*)d_in[1]; p.vc = (const u16*)d_in[2]; p.wq = (const u16*)d_in[3]; p.wo = (const u16*)d_in[4];
    p.wg = (const u16*)d_in[6]; p.wu = (const u16*)d_in[7]; p.wd = (const u16*)d_in[8]; p.c = (const u16*)d_in[9]; p.wm = (const u16*)d_in[10];
    p.out = (u16*)d_out; p.ws = (unsigned char*)d_ws;
    (void)hipFuncSetAttribute((const void*)fused_block, hipFuncAttributeMaxDynamicSharedMemorySize, (int)L_END);
    hipLaunchKernelGGL(fused_block, dim3(256), dim3(512), L_END, stream, p);
}
```

```cpp
#include <hip/hip_runtime.h>
#include <stdint.h>
#pragma clang fp contract(off)

typedef unsigned short u16;
typedef unsigned u32;
typedef unsigned long long u64;
typedef u32 v4u __attribute__((ext_vector_type(4)));
typedef u32 v2u __attribute__((ext_vector_type(2)));
typedef float f4 __attribute__((ext_vector_type(4)));
typedef short v4s __attribute__((ext_vector_type(4)));
typedef __bf16 bf8 __attribute__((ext_vector_type(8)));
typedef __bf16 bf2 __attribute__((ext_vector_type(2)));
#define LAS __attribute__((address_space(3)))
#define GAS __attribute__((address_space(1)))
#define RLX_AGENT __ATOMIC_RELAXED, __HIP_MEMORY_SCOPE_AGENT

#define PRETOUCH 1
#define POLL_SLEEP 2
#define INIT_MODE 1

constexpr int DM = 640, NH = 32, SKV = 4096, DH = 192, WIN = 1024, FF = 1536, NQ = NH * DH;
constexpr u32 SENT = 0xFFFFFFFFu;
constexpr int NW = 64;
constexpr u32 WS_E = 0, WS_FLAG = 4096, WS_F1P = 8192, WS_MODF = WS_F1P + 5 * 4 * 1280 * 4, WS_QP = WS_MODF + 4 * 2560 * 2,
              WS_OP = WS_QP + 5 * 4 * NQ * 4, WS_ML = WS_OP + 128 * 2 * DH * 4, WS_AP = WS_ML + 32 * 4 * 16, WS_HID = WS_AP + 3 * 4 * DM * 4,
              WS_END = WS_HID + 4 * FF * 2;
constexpr u32 OOB = 0x7FFFFF00u;
constexpr u32 L_XS = 131072, L_CS = L_XS + 5120, L_HB = L_CS + 5120, L_RED = L_HB + 5120, L_ST = L_RED + 8192, L_GA = L_ST + 256, L_GB = L_GA + 1024,
              L_X1 = L_GB + 1024, L_MISC = L_X1 + 1024, L_STAMP = L_MISC + 64, L_END = L_STAMP + 256;
constexpr u32 K_W = 18432, K_PX = 1024, K_PX2 = 1536, K_V = 9216, K_QS = 8 * K_W, K_ML = K_QS + 768, K_END = K_ML + 64;
static_assert(K_END <= L_MISC, "LDS");
constexpr float LOG2E = 1.4426950408889634f;
static_assert(L_END <= 163840, "LDS");
constexpr int SPIN_G = 40000, SPIN_L = 2000000;

#define STAMP(i) do {} while (0)

struct Params { const u16 *x, *kc, *vc, *wq, *wo, *wg, *wu, *wd, *c, *wm; u16* out; unsigned char* ws; };

__device__ __forceinline__ v4u mk_srd(const void* p, u32 bytes) { const u64 a = (u64)p; return v4u{(u32)a, (u32)(a >> 32) & 0xffffu, bytes, 0x00020000u}; }
__device__ __forceinline__ float asf(u32 u) { return __builtin_bit_cast(float, u); }
__device__ __forceinline__ u32 asu(float f) { return __builtin_bit_cast(u32, f); }
__device__ __forceinline__ float bflo(u32 u) { return asf(u << 16); }
__device__ __forceinline__ float bfhi(u32 u) { return asf(u & 0xffff0000u); }
__device__ __forceinline__ u32 pack2(float lo, float hi) { bf2 v; v[0] = (__bf16)lo; v[1] = (__bf16)hi; return __builtin_bit_cast(u32, v); }
__device__ __forceinline__ float rbf(float f) { return (float)(__bf16)f; }
typedef float f2 __attribute__((ext_vector_type(2)));
__device__ __forceinline__ f2 rb2(f2 v) { return f2{rbf(v[0]), rbf(v[1])}; }
__device__ __forceinline__ f4 rb4(f4 v) {
    float a = v[0], b = v[1], c = v[2], d = v[3];
    asm("v_cvt_pk_bf16_f32 %0, 0, %0\n\tv_cvt_pk_bf16_f32 %1, 0, %1\n\tv_cvt_pk_bf16_f32 %2, 0, %2\n\tv_cvt_pk_bf16_f32 %3, 0, %3" : "+v"(a), "+v"(b), "+v"(c), "+v"(d));
    return f4{a, b, c, d}; }
__device__ __forceinline__ f4 unpk(v2u v) { return f4{bflo(v[0]), bfhi(v[0]), bflo(v[1]), bfhi(v[1])}; }
__device__ __forceinline__ f4 fma4(f4 a, f4 b, f4 c) { return __builtin_elementwise_fma(a, b, c); }
__device__ __forceinline__ f4 bc4(float x) { return f4{x, x, x, x}; }
__device__ __forceinline__ u32 canon(u32 u) { return u < 0xFFFFFFFEu ? u : 0xFFFFFFFEu; }
__device__ __forceinline__ v4u canon4(f4 v) { return v4u{canon(asu(v[0])), canon(asu(v[1])), canon(asu(v[2])), canon(asu(v[3]))}; }
__device__ __forceinline__ u32 umax3(u32 a, u32 b, u32 c) { const u32 m = a > b ? a : b; return m > c ? m : c; }
__device__ __forceinline__ u32 vmax4(u32 m, v4u v) { return umax3(umax3(m, v[0], v[1]), v[2], v[3]); }
__device__ __forceinline__ bool all_valid(u32 m) { return __builtin_amdgcn_ballot_w64(m == SENT) == 0ull; }
__device__ __forceinline__ float dot2(u32 a, u32 b, float c) { return __builtin_amdgcn_fdot2_f32_bf16(__builtin_bit_cast(bf2, a), __builtin_bit_cast(bf2, b), c, false); }
__device__ __forceinline__ f4 mfma(v4u a, v4u b, f4 c) { return __builtin_amdgcn_mfma_f32_16x16x32_bf16(__builtin_bit_cast(bf8, a), __builtin_bit_cast(bf8, b), c, 0, 0, 0); }
template <int CTRL> __device__ __forceinline__ u32 dppu(u32 x) { return (u32)__builtin_amdgcn_mov_dpp((int)x, CTRL, 0xf, 0xf, true); }
template <int CTRL> __device__ __forceinline__ float dppf(float x) { return asf(dppu<CTRL>(asu(x))); }
template <int K> __device__ __forceinline__ v4u ror4k(v4u v) {
    if constexpr (K == 0) return v; else return v4u{dppu<0x120 + 4 * K>(v[0]), dppu<0x120 + 4 * K>(v[1]), dppu<0x120 + 4 * K>(v[2]), dppu<0x120 + 4 * K>(v[3])}; }
__device__ __forceinline__ float row_sum(float x) { x += dppf<0x128>(x); x += dppf<0x124>(x); x += dppf<0x122>(x); x += dppf<0x121>(x); return x; }
__device__ __forceinline__ float row_max(float x) { x = fmaxf(x, dppf<0x128>(x)); x = fmaxf(x, dppf<0x124>(x)); x = fmaxf(x, dppf<0x122>(x)); x = fmaxf(x, dppf<0x121>(x)); return x; }
__device__ __forceinline__ float xrow_sum(float x) {
    auto s = __builtin_amdgcn_permlane16_swap(asu(x), asu(x), false, false); x = asf(s[0]) + asf(s[1]);
    auto t = __builtin_amdgcn_permlane32_swap(asu(x), asu(x), false, false); return asf(t[0]) + asf(t[1]); }
__device__ __forceinline__ float xrow_max(float x) {
    auto s = __builtin_amdgcn_permlane16_swap(asu(x), asu(x), false, false); x = fmaxf(asf(s[0]), asf(s[1]));
    auto t = __builtin_amdgcn_permlane32_swap(asu(x), asu(x), false, false); return fmaxf(asf(t[0]), asf(t[1])); }
__device__ __forceinline__ float wave_sum(float x) { return xrow_sum(row_sum(x)); }
__device__ __forceinline__ float wave_max(float x) { return xrow_max(row_max(x)); }
__device__ __forceinline__ float fdiv(float a, float b) { const float r = __builtin_amdgcn_rcpf(b); const float q = a * r; return fmaf(fmaf(-q, b, a), r, q); }
__device__ __forceinline__ float silu_bf(float v) { return fdiv(v, 1.f + __builtin_amdgcn_exp2f(fminf(-v * 1.4426950408889634f, 126.f))); }
__device__ __forceinline__ v4u silu8(v4u c) {
    v4u r;
#pragma unroll
    for (int i = 0; i < 4; ++i) r[i] = pack2(silu_bf(bflo(c[i])), silu_bf(bfhi(c[i])));
    return r; }
#define BAR() asm volatile("s_waitcnt lgkmcnt(0)\n\ts_barrier" ::: "memory")
#define LGKM0() asm volatile("s_waitcnt lgkmcnt(0)" ::: "memory")
#define VMW(n) asm volatile("s_waitcnt vmcnt(%0)" :: "i"(n) : "memory")
#define MEMBAR() asm volatile("" ::: "memory")
typedef __amdgpu_buffer_rsrc_t rsrc_t;
__device__ __forceinline__ v4u ld_sc1(rsrc_t r, u32 off) { return __builtin_amdgcn_raw_buffer_load_b128(r, off, 0, 16); }
__device__ __forceinline__ v2u ld_sc1_2(rsrc_t r, u32 off) { return __builtin_amdgcn_raw_buffer_load_b64(r, off, 0, 16); }
__device__ __forceinline__ void st_sc1(rsrc_t r, u32 off, v4u v) { __builtin_amdgcn_raw_buffer_store_b128(v, r, off, 0, 16); }
__device__ __forceinline__ void st_sc1_2(rsrc_t r, u32 off, v2u v) { __builtin_amdgcn_raw_buffer_store_b64(v, r, off, 0, 16); }
__device__ __forceinline__ f4 asf4(v4u v) { return __builtin_bit_cast(f4, v); }

__device__ __forceinline__ void dma_unit(v4u srd, u32 v0, u32 v1, u32 sA, u32 sB, u32 lds) {
    u32 keep;
    asm volatile("s_nop 4\n\ts_mov_b32 %0, m0\n\ts_mov_b32 m0, %6\n\ts_nop 0\n\t"
                 "buffer_load_dwordx4 %1, %3, %4 offen nt lds\n\ts_add_u32 m0, m0, 0x400\n\ts_nop 0\n\t"
                 "buffer_load_dwordx4 %2, %3, %4 offen nt lds\n\ts_add_u32 m0, m0, 0x400\n\ts_nop 0\n\t"
                 "buffer_load_dwordx4 %1, %3, %5 offen nt lds\n\ts_add_u32 m0, m0, 0x400\n\ts_nop 0\n\t"
                 "buffer_load_dwordx4 %2, %3, %5 offen nt lds\n\ts_mov_b32 m0, %0"
                 : "=&s"(keep) : "v"(v0), "v"(v1), "s"(srd), "s"(sA), "s"(sB), "s"(lds) : "memory", "scc");
}
__device__ __forceinline__ void dma_1k(v4u srd, u32 voff, u32 soff, u32 lds) {
    u32 keep;
    asm volatile("s_nop 4\n\ts_mov_b32 %0, m0\n\ts_mov_b32 m0, %4\n\ts_nop 0\n\tbuffer_load_dwordx4 %1, %2, %3 offen lds\n\ts_mov_b32 m0, %0"
                 : "=&s"(keep) : "v"(voff), "s"(srd), "s"(soff), "s"(lds) : "memory");
}
struct WStream { v4u srd; u32 base, pitch, v0, v1; };
__device__ __forceinline__ WStream mk_stream(v4u srd, u32 base, u32 pitch, int l) {
    WStream s; s.srd = srd; s.base = base; s.pitch = pitch;
    const u32 sw = 2u * ((l >> 4) & 1);
    s.v0 = (l >> 3) * pitch + (((l & 7) ^ sw) << 4);
    s.v1 = (8 + (l >> 3)) * pitch + (((l & 7) ^ (sw | 4u)) << 4);
    return s; }
__device__ __forceinline__ void issue(const WStream& s, int n, u32 lds) { const u32 a = s.base + (u32)n * 32u * s.pitch; dma_unit(s.srd, s.v0, s.v1, a, a + 16u * s.pitch, lds); }
__device__ __forceinline__ void read_unit(LAS unsigned char* L, const u32 (&ta)[4], int slot, v4u (&f)[4]) {
#pragma unroll
    for (int T = 0; T < 4; ++T) {
        const v2u lo = __builtin_bit_cast(v2u, __builtin_amdgcn_ds_read_tr16_b64_v4i16((LAS v4s*)(L + ta[T] + slot * 4096)));
        const v2u hi = __builtin_bit_cast(v2u, __builtin_amdgcn_ds_read_tr16_b64_v4i16((LAS v4s*)(L + ta[T] + slot * 4096 + 512)));
        f[T] = v4u{lo[0], lo[1], hi[0], hi[1]};
    } }
__device__ __forceinline__ f4 sel_tile(const f4 (&a)[4], int t) { return t == 0 ? a[0] : t == 1 ? a[1] : t == 2 ? a[2] : a[3]; }

__device__ __forceinline__ void dma_kv(v4u srd, u32 vo, u32 so, u32 lds) {
    u32 keep;
    asm volatile("s_nop 4\n\ts_mov_b32 %0, m0\n\ts_mov_b32 m0, %4\n\ts_nop 0\n\t"
                 "buffer_load_dwordx4 %1, %2, %3 offen nt lds\n\ts_add_u32 m0, m0, 0x380\n\ts_nop 0\n\t"
                 "buffer_load_dwordx4 %1, %2, %3 offen offset:128 nt lds\n\ts_add_u32 m0, m0, 0x380\n\ts_nop 0\n\t"
                 "buffer_load_dwordx4 %1, %2, %3 offen offset:256 nt lds\n\ts_mov_b32 m0, %0"
                 : "=&s"(keep) : "v"(vo), "s"(srd), "s"(so), "s"(lds) : "memory", "scc");
}
__device__ __forceinline__ float qk_unit(v4u k0, v4u k1, v4u k2, const v4u (&q)[3]) {
    float s = 0.f, t = 0.f;
#pragma unroll
    for (int r = 0; r < 4; r += 2) { s = dot2(k0[r], q[0][r], s); t = dot2(k0[r + 1], q[0][r + 1], t); }
#pragma unroll
    for (int r = 0; r < 4; r += 2) { s = dot2(k1[r], q[1][r], s); t = dot2(k1[r + 1], q[1][r + 1], t); }
#pragma unroll
    for (int r = 0; r < 4; r += 2) { s = dot2(k2[r], q[2][r], s); t = dot2(k2[r + 1], q[2][r + 1], t); }
    return s + t;
}
__device__ __forceinline__ void pv_unit(v4u v0, v4u v1, v4u v2, u32 plo, float (&acc)[24]) {
    const u32 phi = plo << 16;
#pragma unroll
    for (int r = 0; r < 4; ++r) {
        acc[2 * r] = dot2(v0[r], plo, acc[2 * r]); acc[2 * r + 1] = dot2(v0[r], phi, acc[2 * r + 1]);
        acc[8 + 2 * r] = dot2(v1[r], plo, acc[8 + 2 * r]); acc[8 + 2 * r + 1] = dot2(v1[r], phi, acc[8 + 2 * r + 1]);
        acc[16 + 2 * r] = dot2(v2[r], plo, acc[16 + 2 * r]); acc[16 + 2 * r + 1] = dot2(v2[r], phi, acc[16 + 2 * r + 1]);
    }
}

__device__ __forceinline__ void kv_path(const Params& p, LAS unsigned char* L, int j, int w, int l) {
    const rsrc_t rws = __builtin_amdgcn_make_buffer_rsrc((void*)p.ws, 0, WS_END, 0x00020000);
    const rsrc_t rk = __builtin_amdgcn_make_buffer_rsrc((void*)p.kc, 0, 4 * NH * SKV * DH * 2, 0x00020000);
    const rsrc_t rv = __builtin_amdgcn_make_buffer_rsrc((void*)p.vc, 0, 4 * NH * SKV * DH * 2, 0x00020000);
    volatile LAS u32* misc = (volatile LAS u32*)(L + L_MISC);
    const int rk3 = (j >> 3) / 3, t3 = (j & 7) * 8 + rk3, pos = (j >> 3) - 3 * rk3;
    const int slot = pos == 1 ? (w & 1) : 0;
    const int bh = 2 * t3 + (pos == 2 ? 1 : (pos == 1 ? slot : 0));
    const int part = pos == 0 ? 0 : (pos == 2 ? 1 : (slot == 0 ? 1 : 0));
    const int wi = pos == 1 ? (w >> 1) : w;
    const int ub = pos == 0 ? 0 : (pos == 2 ? 43 : (slot == 0 ? 85 : 0));
    const int u1 = ub + 11 * wi - (wi > 5 ? wi - 5 : 0);
    const int nl = (pos == 1 ? wi == 3 : wi >= 5) ? 2 : 3;
    const bool lead = w == 0 || (w == 1 && pos == 1);
    const u32 opb = WS_OP + (bh * 2 + part) * 768, mlb = WS_ML + ((bh & 31) * 4 + (bh >> 5)) * 16 + part * 8;
    if (threadIdx.x < 12) misc[threadIdx.x] = 0;
    BAR();
    STAMP(0);
    if (w == 4) {
        __builtin_amdgcn_s_setprio(3);
        const u64 T = __hip_atomic_load((GAS u64*)(p.ws + WS_E), RLX_AGENT) + 1ull;
        st_sc1(rws, l < 48 ? opb + l * 16 : OOB, v4u{SENT, SENT, SENT, SENT});
        st_sc1_2(rws, l == 0 ? mlb : OOB, v2u{SENT, SENT});
        const int bh1 = 2 * t3 + 1;
        st_sc1(rws, (pos == 1 && l < 48) ? WS_OP + bh1 * 2 * 768 + l * 16 : OOB, v4u{SENT, SENT, SENT, SENT});
        st_sc1_2(rws, (pos == 1 && l == 0) ? WS_ML + ((bh1 & 31) * 4 + (bh1 >> 5)) * 16 : OOB, v2u{SENT, SENT});
        VMW(0);
        if (l == 0) {
            __hip_atomic_store((GAS u64*)(p.ws + WS_FLAG) + blockIdx.x, T, RLX_AGENT);
            misc[4] = (u32)T; misc[5] = (u32)(T >> 32); LGKM0(); misc[2] = 1;
        }
        STAMP(9);
        __builtin_amdgcn_s_setprio(0);
    }
    const u32 vo = (l >> 3) * 384 + (l & 7) * 16;
    const u32 base = ((u32)bh * SKV + (SKV - WIN)) * 384u + (u32)u1 * 3072u;
    const u32 wl = w * K_W;
    const int c = l & 7;
    float m, lsum, fsc;
    const u32 nslot = pos == 1 ? 4u : 8u;
    auto slot_stats = [&](float& M, float& mi, float& li) {
        const v2u t = *(LAS v2u*)(L + K_ML + c * 8);
        const bool mine = pos != 1 || (l & 1) == slot;
        mi = mine ? asf(t[0]) : -INFINITY; li = mine ? asf(t[1]) : 0.f;
        M = fmaxf(mi, dppf<0xB1>(mi)); M = fmaxf(M, dppf<0x4E>(M)); M = fmaxf(M, dppf<0x141>(M));
    };
    auto post = [&]() {
        u32 old = 0;
        if (l == 0) { *(LAS v2u*)(L + K_ML + w * 8) = v2u{asu(m), asu(lsum)}; LGKM0(); old = __hip_atomic_fetch_add((LAS u32*)(L + L_MISC + 32 + slot * 4), 1u, __ATOMIC_RELAXED, __HIP_MEMORY_SCOPE_WORKGROUP); }
        if (__builtin_amdgcn_readfirstlane(old) == nslot - 1u) {
            MEMBAR();
            float M, mi, li; slot_stats(M, mi, li);
            float e = li * __builtin_amdgcn_exp2f((mi - M) * LOG2E);
            e += dppf<0xB1>(e); e += dppf<0x4E>(e); e += dppf<0x141>(e);
            st_sc1_2(rws, l == 0 ? mlb : OOB, v2u{canon(asu(M)), canon(asu(e))});
            STAMP(5);
        }
    };
    auto scale = [&]() {
        for (int sp = 0; misc[8 + slot] < nslot && sp < SPIN_L; ++sp) __builtin_amdgcn_s_sleep(1);
        MEMBAR();
        float M, mi, li; slot_stats(M, mi, li);
        fsc = __builtin_amdgcn_exp2f((m - M) * LOG2E);
    };
    {
        const v4u sk = mk_srd(p.kc, 4 * NH * SKV * DH * 2), sv = mk_srd(p.vc, 4 * NH * SKV * DH * 2);
        const u32 lb = (u32)(size_t)L + wl;
        dma_kv(sk, vo, base + 8 * 3072, lb); dma_kv(sk, vo, base + 9 * 3072, lb + 3072);
        if (nl == 3) dma_kv(sk, vo, base + 10 * 3072, lb + 6144);
        dma_kv(sv, vo, base + 8 * 3072, lb + K_V); dma_kv(sv, vo, base + 9 * 3072, lb + K_V + 3072);
        if (nl == 3) dma_kv(sv, vo, base + 10 * 3072, lb + K_V + 6144);
    }
    v4u K[24], V[24];
#pragma unroll
    for (int jj = 0; jj < 8; ++jj)
#pragma unroll
        for (int i = 0; i < 3; ++i) K[jj * 3 + i] = __builtin_amdgcn_raw_buffer_load_b128(rk, vo + i * 128, base + jj * 3072, 2);
#pragma unroll
    for (int jj = 0; jj < 8; ++jj)
#pragma unroll
        for (int i = 0; i < 3; ++i) V[jj * 3 + i] = __builtin_amdgcn_raw_buffer_load_b128(rv, vo + i * 128, base + jj * 3072, 2);
    __builtin_amdgcn_sched_barrier(0);
    if (w == 0) STAMP(1);
    if (w == 7) STAMP(2);
    if (w < 4) {
        for (int sp = 0; misc[2] == 0 && sp < SPIN_L; ++sp) __builtin_amdgcn_s_sleep(1);
        const u32 tlo = misc[4], thi = misc[5];
        for (int sp = 0; sp < SPIN_G && misc[6] == 0; ++sp) {
            MEMBAR();
            const v2u f = ld_sc1_2(rws, WS_FLAG + l * 8);
            if (__builtin_amdgcn_ballot_w64(f[0] != tlo || f[1] != thi) == 0ull) misc[6] = 1;
        }
        const u32 qo = WS_QP + (u32)((bh >> 5) * NQ + (bh & 31) * DH + 4 * l) * 4u;
        v4u a[5] = {};
        bool need = l < 48;
        for (int sp = 0; sp < SPIN_G; ++sp) {
            MEMBAR();
            if (__builtin_amdgcn_readfirstlane(misc[slot]) != 0u) break;
            if (need) {
                u32 mx = 0;
#pragma unroll
                for (int ks = 0; ks < 5; ++ks) a[ks] = ld_sc1(rws, qo + ks * (4 * NQ * 4));
#pragma unroll
                for (int ks = 0; ks < 5; ++ks) mx = vmax4(mx, a[ks]);
                need = mx == SENT;
            }
            if (__builtin_amdgcn_ballot_w64(need) == 0ull || sp == SPIN_G - 1) {
                const f4 qs = (((asf4(a[0]) + asf4(a[1])) + asf4(a[2])) + asf4(a[3])) + asf4(a[4]);
                if (l < 48) *(LAS v2u*)(L + K_QS + slot * 384 + l * 8) = v2u{pack2(qs[0], qs[1]), pack2(qs[2], qs[3])};
                LGKM0();
                misc[slot] = 1;
                break;
            }
        }
        if (w == 0) STAMP(3);
    } else {
        for (int sp = 0; misc[slot] == 0 && sp < SPIN_L; ++sp) __builtin_amdgcn_s_sleep(1);
    }
    VMW(48);
    v4u p0, p1, p2;
    {
        v4u q[3];
#pragma unroll
        for (int i = 0; i < 3; ++i) q[i] = *(LAS v4u*)(L + K_QS + slot * 384 + (c + 8 * i) * 16);
        float y[4], x[8];
        LAS v4u* KL = (LAS v4u*)(L + wl + l * 16);
#pragma unroll
        for (int jj = 0; jj < 3; ++jj) { y[jj] = qk_unit(KL[jj * 192], KL[jj * 192 + 64], KL[jj * 192 + 128], q); __builtin_amdgcn_sched_barrier(0); }
#pragma unroll
        for (int jj = 0; jj < 8; ++jj) x[jj] = qk_unit(K[3 * jj], K[3 * jj + 1], K[3 * jj + 2], q);
#pragma unroll
        for (int h = 0; h < 4; ++h) { const float a = x[h] + dppf<0x141>(x[h]), b = x[h + 4] + dppf<0x141>(x[h + 4]); x[h] = (c & 4) ? b : a; }
        { const float a = y[0] + dppf<0x141>(y[0]), b = y[2] + dppf<0x141>(y[2]), a1 = y[1] + dppf<0x141>(y[1]); y[0] = (c & 4) ? b : a; y[1] = a1; }
#pragma unroll
        for (int h = 0; h < 2; ++h) { const float a = x[h] + dppf<0x4E>(x[h]), b = x[h + 2] + dppf<0x4E>(x[h + 2]); x[h] = (c & 2) ? b : a; }
        { const float a = y[0] + dppf<0x4E>(y[0]), b = y[1] + dppf<0x4E>(y[1]); y[0] = (c & 2) ? b : a; }
        { const float a = x[0] + dppf<0xB1>(x[0]), b = x[1] + dppf<0xB1>(x[1]); x[0] = (c & 1) ? b : a; }
        y[0] += dppf<0xB1>(y[0]);
        const float s1 = rbf(rbf(x[0]) * 0.07216878364870322f);
        const float s2 = (c >> 1) < nl ? rbf(rbf(y[0]) * 0.07216878364870322f) : -INFINITY;
        m = wave_max(fmaxf(s1, s2));
        const float e1 = __builtin_amdgcn_exp2f((s1 - m) * LOG2E), e2 = __builtin_amdgcn_exp2f((s2 - m) * LOG2E);
        *(LAS u32*)(L + wl + K_PX + l * 4) = pack2(e1, 0.f);
        *(LAS u32*)(L + wl + K_PX2 + (l >> 1) * 4) = pack2(e2, 0.f);
        lsum = wave_sum(e1 + ((c & 1) ? 0.f : e2));
        post();
        LGKM0();
        p0 = *(LAS v4u*)(L + wl + K_PX + (l >> 3) * 32); p1 = *(LAS v4u*)(L + wl + K_PX + (l >> 3) * 32 + 16); p2 = *(LAS v4u*)(L + wl + K_PX2 + (l >> 3) * 16);
    }
    if (w == 0) STAMP(4);
    float acc[24];
#pragma unroll
    for (int i = 0; i < 24; ++i) acc[i] = 0.f;
    {
        LAS v4u* VL = (LAS v4u*)(L + wl + K_V + l * 16);
        pv_unit(VL[0], VL[64], VL[128], p2[0], acc);
        pv_unit(VL[192], VL[256], VL[320], p2[1], acc);
        if (nl == 3) pv_unit(VL[384], VL[448], VL[512], p2[2], acc);
    }
#pragma unroll
    for (int jj = 0; jj < 6; ++jj) pv_unit(V[3 * jj], V[3 * jj + 1], V[3 * jj + 2], jj < 4 ? p0[jj & 3] : p1[jj & 3], acc);
    scale();
#pragma unroll
    for (int jj = 6; jj < 8; ++jj) pv_unit(V[3 * jj], V[3 * jj + 1], V[3 * jj + 2], p1[jj & 3], acc);
    if (w == 7) STAMP(6);
    float y[12], z[6];
#pragma unroll
    for (int n = 0; n < 12; ++n) { auto s = __builtin_amdgcn_permlane32_swap(asu(acc[n]), asu(acc[n + 12]), false, false); y[n] = asf(s[0]) + asf(s[1]); }
#pragma unroll
    for (int n = 0; n < 6; ++n) { auto s = __builtin_amdgcn_permlane16_swap(asu(y[n]), asu(y[n + 6]), false, false); z[n] = asf(s[0]) + asf(s[1]); }
#pragma unroll
    for (int n = 0; n < 3; ++n) {
        const float a = z[n] + dppf<0x128>(z[n]), b = z[n + 3] + dppf<0x128>(z[n + 3]);
        const int ai = 3 * (l >> 3) + n;
        *(LAS float*)(L + wl + (64 * (ai >> 3) + 8 * c + (ai & 7)) * 4) = ((l & 8) ? b : a) * fsc;
    }
    BAR();
    if (w == 0) STAMP(7);
    if (lead) {
        const u32 pb = slot * K_W + (l < 48 ? l : 47) * 16;
        f4 o = (*(LAS f4*)(L + pb) + *(LAS f4*)(L + pb + 2 * K_W)) + (*(LAS f4*)(L + pb + 4 * K_W) + *(LAS f4*)(L + pb + 6 * K_W));
        if (pos != 1) o += (*(LAS f4*)(L + pb + K_W) + *(LAS f4*)(L + pb + 3 * K_W)) + (*(LAS f4*)(L + pb + 5 * K_W) + *(LAS f4*)(L + pb + 7 * K_W));
        st_sc1(rws, l < 48 ? opb + l * 16 : OOB, canon4(o));
        if (w == 0) STAMP(8);
    }
}

enum { R_SA = 0, R_SB = 1, R_SC = 2 };
#define UNDEF4(v) asm volatile("" : "=v"(v))
template <int ROLE>
__device__ __forceinline__ void w_path(const Params& p, LAS unsigned char* L, int u, int w, int l) {
    const rsrc_t rws = __builtin_amdgcn_make_buffer_rsrc((void*)p.ws, 0, WS_END, 0x00020000);
    const int tid = threadIdx.x, g = l >> 4, tq = (l & 15) >> 2, b4 = l & 3;
    const u32 lbase = (u32)(size_t)L, stg = lbase + w * 16384;
    const bool hasF1 = u < 50, hasF2 = u < 60;
    const int kf = u / 10, lp = u - 10 * kf, kf2 = u / 12, cs = u - 12 * kf2;
    const int ri = ROLE == R_SA ? u : (ROLE == R_SB ? u - 30 : u - 54);
    const WStream sF1 = mk_stream(mk_srd(p.wm, hasF1 ? DM * 3840 * 2 : 0), (u32)((128 * kf + 32 * (w >> 1)) * 7680 + (128 * lp + 64 * (w & 1)) * 2), 7680, l);
    const WStream sF2 = mk_stream(mk_srd(p.wq, hasF2 ? DM * NQ * 2 : 0), (u32)(128 * kf2 * 12288 + (512 * cs + 64 * w) * 2), 12288, l);
    const int mid = ROLE == R_SC ? (w < 4 ? (ri >= 6 ? ri : -1) : 30 + ri) : (w < 4 ? (ri < 6 ? ri : ri + 4) : (ri == 20 ? 28 : (ri == 21 ? 29 : -1)));
    const bool hasM = ROLE != R_SA && mid >= 0;
    const WStream sM = mk_stream(mk_srd(p.wm, hasM ? DM * 3840 * 2 : 0), (u32)(160 * (w & 3) * 7680 + (1280 + 64 * mid) * 2), 7680, l);
    const int ra = ri / 10, ca = ri - 10 * ra;
    const WStream sT = ROLE == R_SA ? mk_stream(mk_srd(p.wo, NQ * DM * 2), (u32)((2048 * ra + 256 * w) * 1280 + ca * 128), 1280, l)
                     : ROLE == R_SB ? mk_stream(mk_srd((w & 1) ? p.wu : p.wg, DM * FF * 2), (u32)(160 * (w >> 1) * 3072 + ri * 128), 3072, l)
                                    : mk_stream(mk_srd(p.wd, FF * DM * 2), (u32)(192 * w * 1280 + ri * 128), 1280, l);
    constexpr int NM = ROLE == R_SA ? 0 : 5, NT = ROLE == R_SA ? 8 : (ROLE == R_SB ? 5 : 6), NA = NM + NT;
    u32 ta[4];
    { const u32 f = 2u * (tq >> 1) | 4u * (g & 1);
#pragma unroll
      for (int T = 0; T < 4; ++T) ta[T] = w * 16384 + (8 * g + tq) * 128 + ((((u32)(2 * T)) ^ f) + (b4 >> 1)) * 16 + (b4 & 1) * 8; }
    const u32 dcol = 16 * tq + 4 * g;
    const u32 f1o = hasF1 ? WS_F1P + (u32)((kf * 4 + b4) * 1280 + 128 * lp + 64 * w + dcol) * 4u : OOB;
    const u32 qpo = hasF2 ? WS_QP + (u32)((kf2 * 4 + b4) * NQ + 512 * cs + 64 * w + dcol) * 4u : OOB;
    const u32 mfo = hasM ? WS_MODF + (u32)(b4 * 2560 + 64 * mid + dcol) * 2u : OOB;
    const u32 outo = ROLE == R_SA ? WS_AP + (u32)((ra * 4 + b4) * DM + 64 * ca + dcol) * 4u : WS_HID + (u32)(b4 * FF + 64 * ri + dcol) * 2u;
    volatile LAS u32* misc = (volatile LAS u32*)(L + L_MISC);
    STAMP(0);
    if (w < 5) { dma_1k(mk_srd(p.c, 5120), l * 16, w * 1024, lbase + L_CS + w * 1024); dma_1k(mk_srd(p.x, 5120), l * 16, w * 1024, lbase + L_XS + w * 1024); }
    issue(sF1, 0, stg);
    { u32 keep;
      asm volatile("s_nop 4\n\ts_mov_b32 %0, m0\n\ts_mov_b32 m0, %5\n\ts_nop 0\n\tbuffer_load_dword %1, %2, %3 offen nt lds\n\tbuffer_load_dword %1, %4, 0 offen nt lds\n\ts_mov_b32 m0, %0"
                   : "=&s"(keep) : "v"(l == 0 ? 0u : OOB), "s"(sT.srd), "s"(sT.base), "s"(mk_srd(p.out, 5120)), "s"(lbase + L_GB) : "memory"); }
    if (w == 0) {
        u32 keep;
        asm volatile("s_nop 4\n\ts_mov_b32 %0, m0\n\ts_mov_b32 m0, %3\n\ts_nop 0\n\tbuffer_load_dword %1, %2, 0 offen sc1 lds\n\ts_mov_b32 m0, %0"
                     : "=&s"(keep) : "v"(l < 2 ? WS_E + l * 4 : OOB), "s"(mk_srd(p.ws, WS_END)), "s"(lbase + L_GA) : "memory");
    }
    { const v4u s4 = {SENT, SENT, SENT, SENT};
      st_sc1(rws, qpo, s4);
      if (w < 2) st_sc1(rws, f1o, s4);
      if (ROLE != R_SA && (w & 3) == 0) st_sc1_2(rws, mfo, v2u{SENT, SENT});
      if (w == 0) {
          if (ROLE == R_SA) st_sc1(rws, outo, s4);
          if (ROLE == R_SB) st_sc1_2(rws, outo, v2u{SENT, SENT});
      } }
    if (tid == 0) { misc[0] = 0; misc[1] = 0; }
    BAR();
    issue(sF2, 0, stg + 4096);
    auto issueB = [&](int n) { if (n < NM) issue(sM, n, stg + ((n + 1) & 3) * 4096); else issue(sT, n - NM, stg + ((n + 1) & 3) * 4096); };
    {
        v4u ft[NT][4];
        {
            VMW(4);
            BAR();
            STAMP(1);
            const u64 T = *(LAS u64*)(L + L_GA) + 1ull;
            const u32 tlo = (u32)T, thi = (u32)(T >> 32);
            if (tid == 0) __hip_atomic_store((GAS u64*)(p.ws + WS_FLAG) + blockIdx.x, T, RLX_AGENT);
            {
                v4u f[4]; read_unit(L, ta, 0, f);
                const v4u a = silu8(*(LAS v4u*)(L + L_CS + b4 * 1280 + (128 * kf + 32 * (w >> 1) + 8 * g) * 2));
                f4 d[4];
#pragma unroll
                for (int T2 = 0; T2 < 4; ++T2) d[T2] = mfma(f[T2], a, f4{0.f, 0.f, 0.f, 0.f});
                *(LAS f4*)(L + L_RED + w * 1024 + l * 16) = sel_tile(d, tq);
                if (w >= 4) {
                    float xv[10], s = 0.f;
#pragma unroll
                    for (int i = 0; i < 5; ++i) { const u32 t = *(LAS u32*)(L + L_XS + (w - 4) * 1280 + (l + 64 * i) * 4); xv[2 * i] = bflo(t); xv[2 * i + 1] = bfhi(t); s += xv[2 * i] + xv[2 * i + 1]; }
                    const float mean = wave_sum(s) * (1.f / DM);
                    float v = 0.f;
#pragma unroll
                    for (int i = 0; i < 10; ++i) v = fmaf(xv[i] - mean, xv[i] - mean, v);
                    const float var = wave_sum(v) * (1.f / DM);
                    if (l == 0) *(LAS v2u*)(L + L_ST + (w - 4) * 8) = v2u{asu(rbf(mean)), asu(rbf(__builtin_amdgcn_sqrtf(rbf(rbf(var) + 1e-5f))))};
                }
                BAR();
                if (w < 2) {
                    LAS f4* R = (LAS f4*)(L + L_RED + w * 1024 + l * 16);
                    st_sc1(rws, f1o, canon4((R[0] + R[128]) + (R[256] + R[384])));
                }
                if (w == 0) STAMP(2);
                issue(sF2, 1, stg + 8192); issue(sF2, 2, stg + 12288); issue(sF2, 3, stg);
            }
            if (w < 2) {
                for (int sp = 0; sp < SPIN_G; ++sp) {
                    MEMBAR();
                    const v2u f = ld_sc1_2(rws, WS_FLAG + l * 8);
                    if (__builtin_amdgcn_ballot_w64(f[0] != tlo || f[1] != thi) == 0ull) break;
                }
                if (w == 0) STAMP(19);
                const int hb = tid >> 5, hi = tid & 31, col = 128 * kf2 + 4 * hi;
                const u32 o = WS_F1P + (u32)(hb * 1280 + col) * 4u;
                v4u a[5] = {}, c2[5] = {};
                bool need = hasF2;
                for (int sp = 0; sp < SPIN_G; ++sp) {
                    MEMBAR();
                    if (need) {
                        u32 mx = 0;
#pragma unroll
                        for (int ks = 0; ks < 5; ++ks) { a[ks] = ld_sc1(rws, o + ks * 20480); c2[ks] = ld_sc1(rws, o + ks * 20480 + 2560); }
#pragma unroll
                        for (int ks = 0; ks < 5; ++ks) mx = vmax4(vmax4(mx, a[ks]), c2[ks]);
                        need = mx == SENT;
                    }
                    if (__builtin_amdgcn_ballot_w64(need) == 0ull) break;
                }
                const f4 sa = (((asf4(a[0]) + asf4(a[1])) + asf4(a[2])) + asf4(a[3])) + asf4(a[4]);
                const f4 sc = (((asf4(c2[0]) + asf4(c2[1])) + asf4(c2[2])) + asf4(c2[3])) + asf4(c2[4]);
                const v2u st = *(LAS v2u*)(L + L_ST + hb * 8), xr = *(LAS v2u*)(L + L_XS + hb * 1280 + (hasF2 ? col : 0) * 2);
                const float den = asf(st[1]), rc = __builtin_amdgcn_rcpf(den);
                const f4 e = rb4(unpk(xr) - asf(st[0])), q1 = e * rc;
                const f4 h = rb4(rb4(fma4(fma4(-q1, bc4(den), e), bc4(rc), q1)) * rb4(rb4(sc) + 1.f)) + rb4(sa);
                *(LAS v2u*)(L + L_HB + hb * 256 + hi * 8) = v2u{pack2(h[0], h[1]), pack2(h[2], h[3])};
                LGKM0();
                if (l == 0) __hip_atomic_fetch_add((LAS u32*)(L + L_MISC), 1u, __ATOMIC_RELAXED, __HIP_MEMORY_SCOPE_WORKGROUP);
                if (w == 0) STAMP(4);
            }
            {
                v4u fq[4][4];
                VMW(12); read_unit(L, ta, 1, fq[0]); LGKM0();
                VMW(8); read_unit(L, ta, 2, fq[1]);
                VMW(4); read_unit(L, ta, 3, fq[2]);
                issueB(0);
                VMW(4); read_unit(L, ta, 0, fq[3]);
                if (w == 7) STAMP(3);
                for (int sp = 0; misc[0] < 2u && sp < SPIN_L; ++sp) __builtin_amdgcn_s_sleep(1);
                MEMBAR();
                f4 d[4] = {};
#pragma unroll
                for (int n = 0; n < 4; ++n) {
                    const v4u a = *(LAS v4u*)(L + L_HB + b4 * 256 + (32 * n + 8 * g) * 2);
#pragma unroll
                    for (int T2 = 0; T2 < 4; ++T2) d[T2] = mfma(fq[n][T2], a, d[T2]);
                }
                st_sc1(rws, qpo, canon4(sel_tile(d, tq)));
                BAR();
                if (w == 7) STAMP(5);
                issueB(1); issueB(2); issueB(3);
            }
            if (NM) {
                f4 d[4] = {};
#pragma unroll
                for (int n = 0; n < NM; ++n) {
                    VMW(12);
                    v4u f[4]; read_unit(L, ta, (n + 1) & 3, f);
                    const v4u a = silu8(*(LAS v4u*)(L + L_CS + b4 * 1280 + (160 * (w & 3) + 32 * n + 8 * g) * 2));
                    LGKM0();
                    issueB(n + 4);
#pragma unroll
                    for (int T2 = 0; T2 < 4; ++T2) d[T2] = mfma(f[T2], a, d[T2]);
                }
                *(LAS f4*)(L + L_RED + w * 1024 + l * 16) = sel_tile(d, tq);
                BAR();
                if ((w & 3) == 0) {
                    LAS f4* R = (LAS f4*)(L + L_RED + w * 1024 + l * 16);
                    const f4 s = (R[0] + R[64]) + (R[128] + R[192]);
                    st_sc1_2(rws, mfo, v2u{canon(pack2(s[0], s[1])), canon(pack2(s[2], s[3]))});
                }
                BAR();
            }
#pragma unroll
            for (int n = 0; n < NT; ++n) {
                const int a = NM + n, younger = (NA - 1 - a) < 3 ? (NA - 1 - a) : 3;
                if (younger == 3) VMW(12); else if (younger == 2) VMW(8); else if (younger == 1) VMW(4); else VMW(0);
                read_unit(L, ta, (a + 1) & 3, ft[n]);
                if (a + 4 < NA) { LGKM0(); issueB(a + 4); }
                if (ROLE == R_SA && n == 1 && w == 0) {
                    for (int sp = 0; sp < SPIN_G; ++sp) {
                        MEMBAR();
                        const v4u f0 = ld_sc1(rws, WS_FLAG + l * 16), f1 = ld_sc1(rws, WS_FLAG + 1024 + l * 16);
                        const bool bad = f0[0] != tlo || f0[1] != thi || f0[2] != tlo || f0[3] != thi || f1[0] != tlo || f1[1] != thi || f1[2] != tlo || f1[3] != thi;
                        if (__builtin_amdgcn_ballot_w64(bad) == 0ull) break;
                    }
                    if (u == 0 && l == 0) __hip_atomic_store((GAS u64*)(p.ws + WS_E), T, RLX_AGENT);
                }
            }
            LGKM0();
            STAMP(20 + w);
            BAR();
            STAMP(6);
        }
        if (ROLE == R_SA) {
            f4 res[2];
            UNDEF4(res[0]); UNDEF4(res[1]);
            u32 oo[2], mo[2];
#pragma unroll
            for (int r = 0; r < 2; ++r) {
                const int row = 2048 * ra + 256 * w + 32 * (4 * r + tq) + 8 * g, hd = row / DH, dd = row - hd * DH;
                oo[r] = WS_OP + (u32)((b4 * 32 + hd) * 2) * 768u + dd * 4; mo[r] = WS_ML + (u32)(hd * 4 + b4) * 16u;
            }
            bool done[2] = {false, false}, wdone[2] = {false, false}, need[2] = {true, true}, needm[2] = {true, true};
            v4u a[2][4], ml[2];
            float w0[2], w1[2];
#pragma unroll
            for (int r = 0; r < 2; ++r) {
                UNDEF4(ml[r]); UNDEF4(w0[r]); UNDEF4(w1[r]);
#pragma unroll
                for (int i = 0; i < 4; ++i) UNDEF4(a[r][i]);
            }
            for (int sp = 0; sp < SPIN_G && !(done[0] && done[1]); ++sp) {
                MEMBAR();
#pragma unroll
                for (int r = 0; r < 2; ++r) {
                    if (needm[r]) ml[r] = ld_sc1(rws, mo[r]);
                    else if (need[r]) { a[r][0] = ld_sc1(rws, oo[r]); a[r][1] = ld_sc1(rws, oo[r] + 16); a[r][2] = ld_sc1(rws, oo[r] + 768); a[r][3] = ld_sc1(rws, oo[r] + 784); }
                }
                const bool last = sp == SPIN_G - 1;
                if (!(wdone[0] && wdone[1])) __builtin_amdgcn_s_sleep(POLL_SLEEP);
#pragma unroll
                for (int r = 0; r < 2; ++r) {
                    const bool hadm = !needm[r];
                    if (!wdone[r]) {
                        needm[r] = vmax4(0u, ml[r]) == SENT;
                        if (__builtin_amdgcn_ballot_w64(needm[r]) == 0ull || last) {
                            const float m0 = asf(ml[r][0]), l0 = asf(ml[r][1]), m1 = asf(ml[r][2]), l1 = asf(ml[r][3]), M = fmaxf(m0, m1);
                            const float e0 = __builtin_amdgcn_exp2f((m0 - M) * LOG2E), e1 = __builtin_amdgcn_exp2f((m1 - M) * LOG2E);
                            const float inv = fdiv(1.f, fmaf(e0, l0, e1 * l1));
                            w0[r] = e0 * inv; w1[r] = e1 * inv; wdone[r] = true;
                        }
                    }
                    if (!done[r]) {
                        need[r] = !hadm || vmax4(vmax4(vmax4(vmax4(0u, a[r][0]), a[r][1]), a[r][2]), a[r][3]) == SENT;
                        if ((__builtin_amdgcn_ballot_w64(need[r]) == 0ull && wdone[r]) || last) {
                            const f4 x0 = fma4(asf4(a[r][0]), bc4(w0[r]), asf4(a[r][2]) * w1[r]), x1 = fma4(asf4(a[r][1]), bc4(w0[r]), asf4(a[r][3]) * w1[r]);
                            const v4u bq = {pack2(x0[0], x0[1]), pack2(x0[2], x0[3]), pack2(x1[0], x1[1]), pack2(x1[2], x1[3])};
                            const v4u bk[4] = {bq, ror4k<1>(bq), ror4k<2>(bq), ror4k<3>(bq)};
                            f4 d[4] = {};
#pragma unroll
                            for (int s = 0; s < 4; ++s)
#pragma unroll
                                for (int T2 = 0; T2 < 4; ++T2) d[(T2 + s) & 3] = mfma(ft[4 * r + s][(T2 + s) & 3], bk[T2], d[(T2 + s) & 3]);
                            res[r] = sel_tile(d, tq);
                            done[r] = true;
                        }
                    }
                }
            }
            if (w == 0) STAMP(7);
            *(LAS f4*)(L + L_RED + w * 1024 + l * 16) = res[0] + res[1];
            BAR();
            if (w == 0) {
                STAMP(8);
                LAS f4* R = (LAS f4*)(L + L_RED + l * 16);
                st_sc1(rws, outo, canon4(((R[0] + R[64]) + (R[128] + R[192])) + ((R[256] + R[320]) + (R[384] + R[448]))));
                STAMP(9);
            }
        } else if (ROLE == R_SB) {
            const int i16 = l & 15, col = 128 * w + 8 * i16;
            f4 xa, xb, c1a, c1b, sba, sbb;
            if (w < 5) {
                v4u gav, sbv, cbv, a[6];
                const u32 mo = WS_MODF + (u32)(g * 2560 + col) * 2u;
                for (int sp = 0; sp < SPIN_G; ++sp) {
                    MEMBAR();
                    gav = ld_sc1(rws, mo); sbv = ld_sc1(rws, mo + 1280); cbv = ld_sc1(rws, mo + 2560);
                    if (all_valid(vmax4(vmax4(vmax4(0u, gav), sbv), cbv))) break;
                }
                const v4u xr = *(LAS v4u*)(L + L_XS + g * 1280 + col * 2);
                xa = unpk(v2u{xr[0], xr[1]}); xb = unpk(v2u{xr[2], xr[3]});
                f4 gaa = unpk(v2u{gav[0], gav[1]}), gab = unpk(v2u{gav[2], gav[3]});
                c1a = rb4(unpk(v2u{cbv[0], cbv[1]}) + 1.f); c1b = rb4(unpk(v2u{cbv[2], cbv[3]}) + 1.f);
                sba = unpk(v2u{sbv[0], sbv[1]}); sbb = unpk(v2u{sbv[2], sbv[3]});
                asm volatile("" : "+v"(xa), "+v"(xb), "+v"(gaa), "+v"(gab), "+v"(c1a), "+v"(c1b), "+v"(sba), "+v"(sbb));
                if (w == 0) STAMP(10);
                const u32 ao = WS_AP + (u32)(g * DM + col) * 4u;
#pragma unroll
                for (int r = 0; r < 6; ++r) UNDEF4(a[r]);
                bool need = true;
                for (int sp = 0; sp < SPIN_G; ++sp) {
                    MEMBAR();
                    if (need) {
                        u32 mx = 0;
#pragma unroll
                        for (int r = 0; r < 3; ++r) { a[2 * r] = ld_sc1(rws, ao + r * 10240); a[2 * r + 1] = ld_sc1(rws, ao + r * 10240 + 16); }
#pragma unroll
                        for (int r = 0; r < 6; ++r) mx = vmax4(mx, a[r]);
                        need = mx == SENT;
                    }
                    if (__builtin_amdgcn_ballot_w64(need) == 0ull) break;
                }
                if (w == 0) STAMP(11);
                xa = rb4(xa + rb4(gaa * rb4((asf4(a[0]) + asf4(a[2])) + asf4(a[4]))));
                xb = rb4(xb + rb4(gab * rb4((asf4(a[1]) + asf4(a[3])) + asf4(a[5]))));
                const f4 t = xa + xb;
                const float mw = row_sum((t[0] + t[1]) + (t[2] + t[3])) * (1.f / 128.f);
                const f4 da = xa - mw, db = xb - mw, q2 = fma4(da, da, db * db);
                const float v = row_sum((q2[0] + q2[1]) + (q2[2] + q2[3]));
                *(LAS v2u*)(L + L_ST + 64 + g * 48 + w * 8) = v2u{asu(mw), asu(v)};
            }
            BAR();
            if (w < 5) {
                const f4 s0 = *(LAS f4*)(L + L_ST + 64 + g * 48), s1 = *(LAS f4*)(L + L_ST + 64 + g * 48 + 16);
                const f2 s2 = *(LAS f2*)(L + L_ST + 64 + g * 48 + 32);
                const float mean = (((s0[0] + s0[2]) + (s1[0] + s1[2])) + s2[0]) * 0.2f;
                const f4 dm = f4{s0[0], s0[2], s1[0], s1[2]} - mean, dq = dm * dm; const float d4 = s2[0] - mean;
                const float M2 = fmaf(128.f, fmaf(d4, d4, (dq[0] + dq[1]) + (dq[2] + dq[3])), ((s0[1] + s0[3]) + (s1[1] + s1[3])) + s2[1]);
                const float mb = rbf(mean), den = rbf(__builtin_amdgcn_sqrtf(rbf(rbf(M2 * (1.f / DM)) + 1e-5f))), rc = __builtin_amdgcn_rcpf(den);
                const f4 ea = rb4(xa - mb), eb = rb4(xb - mb), qa = ea * rc, qb = eb * rc;
                const f4 ha = rb4(rb4(fma4(fma4(-qa, bc4(den), ea), bc4(rc), qa)) * c1a) + sba, hb = rb4(rb4(fma4(fma4(-qb, bc4(den), eb), bc4(rc), qb)) * c1b) + sbb;
                *(LAS v4u*)(L + L_HB + g * 1280 + col * 2) = v4u{pack2(ha[0], ha[1]), pack2(ha[2], ha[3]), pack2(hb[0], hb[1]), pack2(hb[2], hb[3])};
            }
            BAR();
            if (w == 0) STAMP(12);
            f4 d[4] = {};
#pragma unroll
            for (int n = 0; n < 5; ++n) {
                const v4u a = *(LAS v4u*)(L + L_HB + b4 * 1280 + (160 * (w >> 1) + 32 * n + 8 * g) * 2);
#pragma unroll
                for (int T2 = 0; T2 < 4; ++T2) d[T2] = mfma(ft[n][T2], a, d[T2]);
            }
            *(LAS f4*)(L + L_RED + w * 1024 + l * 16) = sel_tile(d, tq);
            BAR();
            if (w == 0) {
                STAMP(13);
                LAS f4* R = (LAS f4*)(L + L_RED + l * 16);
                const f4 gt = rb4((R[0] + R[128]) + (R[256] + R[384])), up = rb4((R[64] + R[192]) + (R[320] + R[448]));
                const f4 mk = {gt[0] > 0.f ? 1.f : 0.f, gt[1] > 0.f ? 1.f : 0.f, gt[2] > 0.f ? 1.f : 0.f, gt[3] > 0.f ? 1.f : 0.f};
                const f4 hh = (gt * mk) * up;
                st_sc1_2(rws, outo, v2u{canon(pack2(hh[0], hh[1])), canon(pack2(hh[2], hh[3]))});
                STAMP(14);
            }
        } else {
            f4 x1, gbf;
            UNDEF4(x1); UNDEF4(gbf);
            if (w == 0) {
                v2u ga, gb;
                const u32 mo = WS_MODF + (u32)(b4 * 2560 + 64 * ri + dcol) * 2u;
                for (int sp = 0; sp < SPIN_G; ++sp) {
                    MEMBAR();
                    ga = ld_sc1_2(rws, mo); gb = ld_sc1_2(rws, mo + 3840);
                    if (all_valid(umax3(umax3(ga[0], ga[1], gb[0]), gb[1], 0u))) break;
                }
                const u32 ao = WS_AP + (u32)(b4 * DM + 64 * ri + dcol) * 4u;
                f4 s;
                for (int sp = 0; sp < SPIN_G; ++sp) {
                    MEMBAR();
                    const v4u p0 = ld_sc1(rws, ao), p1 = ld_sc1(rws, ao + 10240), p2 = ld_sc1(rws, ao + 20480);
                    s = (asf4(p0) + asf4(p1)) + asf4(p2);
                    if (all_valid(vmax4(vmax4(vmax4(0u, p0), p1), p2))) break;
                }
                x1 = rb4(unpk(*(LAS v2u*)(L + L_XS + b4 * 1280 + (64 * ri + dcol) * 2)) + rb4(unpk(ga) * rb4(s)));
                gbf = unpk(gb);
                asm volatile("" : "+v"(x1), "+v"(gbf));
                STAMP(15);
            }
            v4u a0, a1;
            UNDEF4(a0); UNDEF4(a1);
            const u32 h0 = WS_HID + (u32)(b4 * FF + 192 * w + 32 * tq + 8 * g) * 2u, h1 = tq < 2 ? h0 + 256 : OOB;
            bool need = true;
            for (int sp = 0; sp < SPIN_G; ++sp) {
                MEMBAR();
                if (need) { a0 = ld_sc1(rws, h0); a1 = ld_sc1(rws, h1); need = vmax4(vmax4(0u, a0), a1) == SENT; }
                if (__builtin_amdgcn_ballot_w64(need) == 0ull) break;
            }
            if (w == 0) STAMP(16);
            const v4u bk[4] = {a0, ror4k<1>(a0), ror4k<2>(a0), ror4k<3>(a0)}, bl[4] = {a1, ror4k<1>(a1), ror4k<2>(a1), ror4k<3>(a1)};
            f4 d[4] = {};
#pragma unroll
            for (int k = 0; k < 4; ++k)
#pragma unroll
                for (int s = 0; s < 6; ++s) d[(s + k) & 3] = mfma(ft[s][(s + k) & 3], s < 4 ? bk[k] : bl[k], d[(s + k) & 3]);
            *(LAS f4*)(L + L_RED + w * 1024 + l * 16) = sel_tile(d, tq);
            BAR();
            if (w == 0) {
                STAMP(17);
                LAS f4* R = (LAS f4*)(L + L_RED + l * 16);
                const f4 o = x1 + rb4(gbf * rb4(((R[0] + R[64]) + (R[128] + R[192])) + ((R[256] + R[320]) + (R[384] + R[448]))));
                *(GAS v2u*)(p.out + b4 * DM + 64 * ri + dcol) = v2u{pack2(o[0], o[1]), pack2(o[2], o[3])};
                STAMP(18);
            }
        }
    }
}

typedef const __attribute__((address_space(4))) Params* kargp;
#define GET_PARAMS() kargp pp_ = kp; asm volatile("" : "+s"(pp_)); \
    const Params q_ = {pp_->x, pp_->kc, pp_->vc, pp_->wq, pp_->wo, pp_->wg, pp_->wu, pp_->wd, pp_->c, pp_->wm, pp_->out, pp_->ws};
__global__ void __launch_bounds__(512, 2) fused_block(Params p) {
    extern __shared__ __attribute__((aligned(16))) unsigned char smem[];
    LAS unsigned char* L = (LAS unsigned char*)smem;
    const kargp kp = (kargp)__builtin_amdgcn_kernarg_segment_ptr();
    asm volatile("s_load_dword s98, %0, 0x0\n\ts_load_dword s99, %0, 0x40" :: "s"(kp) : "s98", "s99");
    const int l = threadIdx.x & 63, w = __builtin_amdgcn_readfirstlane(threadIdx.x >> 6), c = blockIdx.x;
    if (__builtin_expect(c >= NW, 1)) { GET_PARAMS(); kv_path(q_, L, c - NW, w, l); }
    else if (c < 30) { GET_PARAMS(); w_path<R_SA>(q_, L, c, w, l); }
    else if (c < 54) { GET_PARAMS(); w_path<R_SB>(q_, L, c, w, l); }
    else { GET_PARAMS(); w_path<R_SC>(q_, L, c, w, l); }
}

extern "C" void kernel_launch(void* const* d_in, const int* in_sizes, int n_in, void* d_out, int out_size, void* d_ws, size_t ws_size, hipStream_t stream) {
    Params p;
    p.x = (const u16*)d_in[0]; p.kc = (const u16*)d_in[1]; p.vc = (const u16*)d_in[2]; p.wq = (const u16*)d_in[3]; p.wo = (const u16*)d_in[4];
    p.wg = (const u16*)d_in[6]; p.wu = (const u16*)d_in[7]; p.wd = (const u16*)d_in[8]; p.c = (const u16*)d_in[9]; p.wm = (const u16*)d_in[10];
    p.out = (u16*)d_out; p.ws = (unsigned char*)d_ws;
    (void)hipFuncSetAttribute((const void*)fused_block, hipFuncAttributeMaxDynamicSharedMemorySize, (int)L_END);
    hipLaunchKernelGGL(fused_block, dim3(256), dim3(512), L_END, stream, p);
}
```

```cpp
#include <hip/hip_runtime.h>
#include <stdint.h>
#pragma clang fp contract(off)

typedef unsigned short u16;
typedef unsigned u32;
typedef unsigned long long u64;
typedef u32 v4u __attribute__((ext_vector_type(4)));
typedef u32 v2u __attribute__((ext_vector_type(2)));
typedef float f4 __attribute__((ext_vector_type(4)));
typedef short v4s __attribute__((ext_vector_type(4)));
typedef __bf16 bf8 __attribute__((ext_vector_type(8)));
typedef __bf16 bf2 __attribute__((ext_vector_type(2)));
#define LAS __attribute__((address_space(3)))
#define GAS __attribute__((address_space(1)))
#define RLX_AGENT __ATOMIC_RELAXED, __HIP_MEMORY_SCOPE_AGENT

#define PRETOUCH 1
#define POLL_SLEEP 2
#define INIT_MODE 1

constexpr int DM = 640, NH = 32, SKV = 4096, DH = 192, WIN = 1024, FF = 1536, NQ = NH * DH;
constexpr u32 SENT = 0xFFFFFFFFu;
constexpr int NW = 64;
constexpr u32 WS_E = 0, WS_FLAG = 4096, WS_F1P = 8192, WS_MODF = WS_F1P + 5 * 4 * 1280 * 4, WS_QP = WS_MODF + 4 * 2560 * 2,
              WS_OP = WS_QP + 5 * 4 * NQ * 4, WS_ML = WS_OP + 128 * 2 * DH * 4, WS_AP = WS_ML + 32 * 4 * 16, WS_HID = WS_AP + 3 * 4 * DM * 4,
              WS_END = WS_HID + 4 * FF * 2;
constexpr u32 OOB = 0x7FFFFF00u;
constexpr u32 L_XS = 131072, L_CS = L_XS + 5120, L_HB = L_CS + 5120, L_RED = L_HB + 5120, L_ST = L_RED + 8192, L_GA = L_ST + 256, L_GB = L_GA + 1024,
              L_X1 = L_GB + 1024, L_MISC = L_X1 + 1024, L_STAMP = L_MISC + 64, L_END = L_STAMP + 256;
constexpr u32 K_W = 18432, K_PX = 1024, K_PX2 = 1536, K_V = 9216, K_QS = 8 * K_W, K_ML = K_QS + 768, K_END = K_ML + 64;
static_assert(K_END <= L_MISC, "LDS");
constexpr float LOG2E = 1.4426950408889634f;
static_assert(L_END <= 163840, "LDS");
constexpr int SPIN_G = 40000, SPIN_L = 2000000;

#define STAMP(i) do {} while (0)

struct Params { const u16 *x, *kc, *vc, *wq, *wo, *wg, *wu, *wd, *c, *wm; u16* out; unsigned char* ws; };

__device__ __forceinline__ v4u mk_srd(const void* p, u32 bytes) { const u64 a = (u64)p; return v4u{(u32)a, (u32)(a >> 32) & 0xffffu, bytes, 0x00020000u}; }
__device__ __forceinline__ float asf(u32 u) { return __builtin_bit_cast(float, u); }
__device__ __forceinline__ u32 asu(float f) { return __builtin_bit_cast(u32, f); }
__device__ __forceinline__ float bflo(u32 u) { return asf(u << 16); }
__device__ __forceinline__ float bfhi(u32 u) { return asf(u & 0xffff0000u); }
__device__ __forceinline__ u32 pack2(float lo, float hi) { bf2 v; v[0] = (__bf16)lo; v[1] = (__bf16)hi; return __builtin_bit_cast(u32, v); }
__device__ __forceinline__ float rbf(float f) { return (float)(__bf16)f; }
typedef float f2 __attribute__((ext_vector_type(2)));
__device__ __forceinline__ f2 rb2(f2 v) { return f2{rbf(v[0]), rbf(v[1])}; }
__device__ __forceinline__ f4 rb4(f4 v) {
    float a = v[0], b = v[1], c = v[2], d = v[3];
    asm("v_cvt_pk_bf16_f32 %0, 0, %0\n\tv_cvt_pk_bf16_f32 %1, 0, %1\n\tv_cvt_pk_bf16_f32 %2, 0, %2\n\tv_cvt_pk_bf16_f32 %3, 0, %3" : "+v"(a), "+v"(b), "+v"(c), "+v"(d));
    return f4{a, b, c, d}; }
__device__ __forceinline__ f4 unpk(v2u v) { return f4{bflo(v[0]), bfhi(v[0]), bflo(v[1]), bfhi(v[1])}; }
__device__ __forceinline__ f4 fma4(f4 a, f4 b, f4 c) { return __builtin_elementwise_fma(a, b, c); }
__device__ __forceinline__ f4 bc4(float x) { return f4{x, x, x, x}; }
__device__ __forceinline__ u32 canon(u32 u) { return u < 0xFFFFFFFEu ? u : 0xFFFFFFFEu; }
__device__ __forceinline__ v4u canon4(f4 v) { return v4u{canon(asu(v[0])), canon(asu(v[1])), canon(asu(v[2])), canon(asu(v[3]))}; }
__device__ __forceinline__ u32 umax3(u32 a, u32 b, u32 c) { const u32 m = a > b ? a : b; return m > c ? m : c; }
__device__ __forceinline__ u32 vmax4(u32 m, v4u v) { return umax3(umax3(m, v[0], v[1]), v[2], v[3]); }
__device__ __forceinline__ bool all_valid(u32 m) { return __builtin_amdgcn_ballot_w64(m == SENT) == 0ull; }
__device__ __forceinline__ float dot2(u32 a, u32 b, float c) { return __builtin_amdgcn_fdot2_f32_bf16(__builtin_bit_cast(bf2, a), __builtin_bit_cast(bf2, b), c, false); }
__device__ __forceinline__ f4 mfma(v4u a, v4u b, f4 c) { return __builtin_amdgcn_mfma_f32_16x16x32_bf16(__builtin_bit_cast(bf8, a), __builtin_bit_cast(bf8, b), c, 0, 0, 0); }
template <int CTRL> __device__ __forceinline__ u32 dppu(u32 x) { return (u32)__builtin_amdgcn_mov_dpp((int)x, CTRL, 0xf, 0xf, true); }
template <int CTRL> __device__ __forceinline__ float dppf(float x) { return asf(dppu<CTRL>(asu(x))); }
template <int K> __device__ __forceinline__ v4u ror4k(v4u v) {
    if constexpr (K == 0) return v; else return v4u{dppu<0x120 + 4 * K>(v[0]), dppu<0x120 + 4 * K>(v[1]), dppu<0x120 + 4 * K>(v[2]), dppu<0x120 + 4 * K>(v[3])}; }
__device__ __forceinline__ float row_sum(float x) { x += dppf<0x128>(x); x += dppf<0x124>(x); x += dppf<0x122>(x); x += dppf<0x121>(x); return x; }
__device__ __forceinline__ float row_max(float x) { x = fmaxf(x, dppf<0x128>(x)); x = fmaxf(x, dppf<0x124>(x)); x = fmaxf(x, dppf<0x122>(x)); x = fmaxf(x, dppf<0x121>(x)); return x; }
__device__ __forceinline__ float xrow_sum(float x) {
    auto s = __builtin_amdgcn_permlane16_swap(asu(x), asu(x), false, false); x = asf(s[0]) + asf(s[1]);
    auto t = __builtin_amdgcn_permlane32_swap(asu(x), asu(x), false, false); return asf(t[0]) + asf(t[1]); }
__device__ __forceinline__ float xrow_max(float x) {
    auto s = __builtin_amdgcn_permlane16_swap(asu(x), asu(x), false, false); x = fmaxf(asf(s[0]), asf(s[1]));
    auto t = __builtin_amdgcn_permlane32_swap(asu(x), asu(x), false, false); return fmaxf(asf(t[0]), asf(t[1])); }
__device__ __forceinline__ float wave_sum(float x) { return xrow_sum(row_sum(x)); }
__device__ __forceinline__ float wave_max(float x) { return xrow_max(row_max(x)); }
__device__ __forceinline__ float fdiv(float a, float b) { const float r = __builtin_amdgcn_rcpf(b); const float q = a * r; return fmaf(fmaf(-q, b, a), r, q); }
__device__ __forceinline__ float silu_bf(float v) { return fdiv(v, 1.f + __builtin_amdgcn_exp2f(fminf(-v * 1.4426950408889634f, 126.f))); }
__device__ __forceinline__ v4u silu8(v4u c) {
    v4u r;
#pragma unroll
    for (int i = 0; i < 4; ++i) r[i] = pack2(silu_bf(bflo(c[i])), silu_bf(bfhi(c[i])));
    return r; }
#define BAR() asm volatile("s_waitcnt lgkmcnt(0)\n\ts_barrier" ::: "memory")
#define LGKM0() asm volatile("s_waitcnt lgkmcnt(0)" ::: "memory")
#define VMW(n) asm volatile("s_waitcnt vmcnt(%0)" :: "i"(n) : "memory")
#define MEMBAR() asm volatile("" ::: "memory")
typedef __amdgpu_buffer_rsrc_t rsrc_t;
__device__ __forceinline__ v4u ld_sc1(rsrc_t r, u32 off) { return __builtin_amdgcn_raw_buffer_load_b128(r, off, 0, 16); }
__device__ __forceinline__ v2u ld_sc1_2(rsrc_t r, u32 off) { return __builtin_amdgcn_raw_buffer_load_b64(r, off, 0, 16); }
__device__ __forceinline__ void st_sc1(rsrc_t r, u32 off, v4u v) { __builtin_amdgcn_raw_buffer_store_b128(v, r, off, 0, 16); }
__device__ __forceinline__ void st_sc1_2(rsrc_t r, u32 off, v2u v) { __builtin_amdgcn_raw_buffer_store_b64(v, r, off, 0, 16); }
__device__ __forceinline__ f4 asf4(v4u v) { return __builtin_bit_cast(f4, v); }

__device__ __forceinline__ void dma_unit(v4u srd, u32 v0, u32 v1, u32 sA, u32 sB, u32 lds) {
    u32 keep;
    asm volatile("s_nop 4\n\ts_mov_b32 %0, m0\n\ts_mov_b32 m0, %6\n\ts_nop 0\n\t"
                 "buffer_load_dwordx4 %1, %3, %4 offen lds\n\ts_add_u32 m0, m0, 0x400\n\ts_nop 0\n\t"
                 "buffer_load_dwordx4 %2, %3, %4 offen lds\n\ts_add_u32 m0, m0, 0x400\n\ts_nop 0\n\t"
                 "buffer_load_dwordx4 %1, %3, %5 offen lds\n\ts_add_u32 m0, m0, 0x400\n\ts_nop 0\n\t"
                 "buffer_load_dwordx4 %2, %3, %5 offen lds\n\ts_mov_b32 m0, %0"
                 : "=&s"(keep) : "v"(v0), "v"(v1), "s"(srd), "s"(sA), "s"(sB), "s"(lds) : "memory", "scc");
}
__device__ __forceinline__ void dma_1k(v4u srd, u32 voff, u32 soff, u32 lds) {
    u32 keep;
    asm volatile("s_nop 4\n\ts_mov_b32 %0, m0\n\ts_mov_b32 m0, %4\n\ts_nop 0\n\tbuffer_load_dwordx4 %1, %2, %3 offen lds\n\ts_mov_b32 m0, %0"
                 : "=&s"(keep) : "v"(voff), "s"(srd), "s"(soff), "s"(lds) : "memory");
}
struct WStream { v4u srd; u32 base, pitch, v0, v1; };
__device__ __forceinline__ WStream mk_stream(v4u srd, u32 base, u32 pitch, int l) {
    WStream s; s.srd = srd; s.base = base; s.pitch = pitch;
    const u32 sw = 2u * ((l >> 4) & 1);
    s.v0 = (l >> 3) * pitch + (((l & 7) ^ sw) << 4);
    s.v1 = (8 + (l >> 3)) * pitch + (((l & 7) ^ (sw | 4u)) << 4);
    return s; }
__device__ __forceinline__ void issue(const WStream& s, int n, u32 lds) { const u32 a = s.base + (u32)n * 32u * s.pitch; dma_unit(s.srd, s.v0, s.v1, a, a + 16u * s.pitch, lds); }
__device__ __forceinline__ void read_unit(LAS unsigned char* L, const u32 (&ta)[4], int slot, v4u (&f)[4]) {
#pragma unroll
    for (int T = 0; T < 4; ++T) {
        const v2u lo = __builtin_bit_cast(v2u, __builtin_amdgcn_ds_read_tr16_b64_v4i16((LAS v4s*)(L + ta[T] + slot * 4096)));
        const v2u hi = __builtin_bit_cast(v2u, __builtin_amdgcn_ds_read_tr16_b64_v4i16((LAS v4s*)(L + ta[T] + slot * 4096 + 512)));
        f[T] = v4u{lo[0], lo[1], hi[0], hi[1]};
    } }
__device__ __forceinline__ f4 sel_tile(const f4 (&a)[4], int t) { return t == 0 ? a[0] : t == 1 ? a[1] : t == 2 ? a[2] : a[3]; }

__device__ __forceinline__ void dma_kv(v4u srd, u32 vo, u32 so, u32 lds) {
    u32 keep;
    asm volatile("s_nop 4\n\ts_mov_b32 %0, m0\n\ts_mov_b32 m0, %4\n\ts_nop 0\n\t"
                 "buffer_load_dwordx4 %1, %2, %3 offen nt lds\n\ts_add_u32 m0, m0, 0x380\n\ts_nop 0\n\t"
                 "buffer_load_dwordx4 %1, %2, %3 offen offset:128 nt lds\n\ts_add_u32 m0, m0, 0x380\n\ts_nop 0\n\t"
                 "buffer_load_dwordx4 %1, %2, %3 offen offset:256 nt lds\n\ts_mov_b32 m0, %0"
                 : "=&s"(keep) : "v"(vo), "s"(srd), "s"(so), "s"(lds) : "memory", "scc");
}
__device__ __forceinline__ float qk_unit(v4u k0, v4u k1, v4u k2, const v4u (&q)[3]) {
    float s = 0.f, t = 0.f;
#pragma unroll
    for (int r = 0; r < 4; r += 2) { s = dot2(k0[r], q[0][r], s); t = dot2(k0[r + 1], q[0][r + 1], t); }
#pragma unroll
    for (int r = 0; r < 4; r += 2) { s = dot2(k1[r], q[1][r], s); t = dot2(k1[r + 1], q[1][r + 1], t); }
#pragma unroll
    for (int r = 0; r < 4; r += 2) { s = dot2(k2[r], q[2][r], s); t = dot2(k2[r + 1], q[2][r + 1], t); }
    return s + t;
}
__device__ __forceinline__ void pv_unit(v4u v0, v4u v1, v4u v2, u32 plo, float (&acc)[24]) {
    const u32 phi = plo << 16;
#pragma unroll
    for (int r = 0; r < 4; ++r) {
        acc[2 * r] = dot2(v0[r], plo, acc[2 * r]); acc[2 * r + 1] = dot2(v0[r], phi, acc[2 * r + 1]);
        acc[8 + 2 * r] = dot2(v1[r], plo, acc[8 + 2 * r]); acc[8 + 2 * r + 1] = dot2(v1[r], phi, acc[8 + 2 * r + 1]);
        acc[16 + 2 * r] = dot2(v2[r], plo, acc[16 + 2 * r]); acc[16 + 2 * r + 1] = dot2(v2[r], phi, acc[16 + 2 * r + 1]);
    }
}

__device__ __forceinline__ void kv_path(const Params& p, LAS unsigned char* L, int j, int w, int l) {
    const rsrc_t rws = __builtin_amdgcn_make_buffer_rsrc((void*)p.ws, 0, WS_END, 0x00020000);
    const rsrc_t rk = __builtin_amdgcn_make_buffer_rsrc((void*)p.kc, 0, 4 * NH * SKV * DH * 2, 0x00020000);
    const rsrc_t rv = __builtin_amdgcn_make_buffer_rsrc((void*)p.vc, 0, 4 * NH * SKV * DH * 2, 0x00020000);
    volatile LAS u32* misc = (volatile LAS u32*)(L + L_MISC);
    const int rk3 = (j >> 3) / 3, t3 = (j & 7) * 8 + rk3, pos = (j >> 3) - 3 * rk3;
    const int slot = pos == 1 ? (w & 1) : 0;
    const int bh = 2 * t3 + (pos == 2 ? 1 : (pos == 1 ? slot : 0));
    const int part = pos == 0 ? 0 : (pos == 2 ? 1 : (slot == 0 ? 1 : 0));
    const int wi = pos == 1 ? (w >> 1) : w;
    const int ub = pos == 0 ? 0 : (pos == 2 ? 43 : (slot == 0 ? 85 : 0));
    const int u1 = ub + 11 * wi - (wi > 5 ? wi - 5 : 0);
    const int nl = (pos == 1 ? wi == 3 : wi >= 5) ? 2 : 3;
    const bool lead = w == 0 || (w == 1 && pos == 1);
    const u32 opb = WS_OP + (bh * 2 + part) * 768, mlb = WS_ML + ((bh & 31) * 4 + (bh >> 5)) * 16 + part * 8;
    if (threadIdx.x < 12) misc[threadIdx.x] = 0;
    BAR();
    STAMP(0);
    if (w == 4) {
        __builtin_amdgcn_s_setprio(3);
        const u64 T = __hip_atomic_load((GAS u64*)(p.ws + WS_E), RLX_AGENT) + 1ull;
        st_sc1(rws, l < 48 ? opb + l * 16 : OOB, v4u{SENT, SENT, SENT, SENT});
        st_sc1_2(rws, l == 0 ? mlb : OOB, v2u{SENT, SENT});
        const int bh1 = 2 * t3 + 1;
        st_sc1(rws, (pos == 1 && l < 48) ? WS_OP + bh1 * 2 * 768 + l * 16 : OOB, v4u{SENT, SENT, SENT, SENT});
        st_sc1_2(rws, (pos == 1 && l == 0) ? WS_ML + ((bh1 & 31) * 4 + (bh1 >> 5)) * 16 : OOB, v2u{SENT, SENT});
        VMW(0);
        if (l == 0) {
            __hip_atomic_store((GAS u64*)(p.ws + WS_FLAG) + blockIdx.x, T, RLX_AGENT);
            misc[4] = (u32)T; misc[5] = (u32)(T >> 32); LGKM0(); misc[2] = 1;
        }
        STAMP(9);
        __builtin_amdgcn_s_setprio(0);
    }
    const u32 vo = (l >> 3) * 384 + (l & 7) * 16;
    const u32 base = ((u32)bh * SKV + (SKV - WIN)) * 384u + (u32)u1 * 3072u;
    const u32 wl = w * K_W;
    const int c = l & 7;
    float m, lsum, fsc;
    const u32 nslot = pos == 1 ? 4u : 8u;
    auto slot_stats = [&](float& M, float& mi, float& li) {
        const v2u t = *(LAS v2u*)(L + K_ML + c * 8);
        const bool mine = pos != 1 || (l & 1) == slot;
        mi = mine ? asf(t[0]) : -INFINITY; li = mine ? asf(t[1]) : 0.f;
        M = fmaxf(mi, dppf<0xB1>(mi)); M = fmaxf(M, dppf<0x4E>(M)); M = fmaxf(M, dppf<0x141>(M));
    };
    auto post = [&]() {
        u32 old = 0;
        if (l == 0) { *(LAS v2u*)(L + K_ML + w * 8) = v2u{asu(m), asu(lsum)}; LGKM0(); old = __hip_atomic_fetch_add((LAS u32*)(L + L_MISC + 32 + slot * 4), 1u, __ATOMIC_RELAXED, __HIP_MEMORY_SCOPE_WORKGROUP); }
        if (__builtin_amdgcn_readfirstlane(old) == nslot - 1u) {
            MEMBAR();
            float M, mi, li; slot_stats(M, mi, li);
            float e = li * __builtin_amdgcn_exp2f((mi - M) * LOG2E);
            e += dppf<0xB1>(e); e += dppf<0x4E>(e); e += dppf<0x141>(e);
            st_sc1_2(rws, l == 0 ? mlb : OOB, v2u{canon(asu(M)), canon(asu(e))});
            STAMP(5);
        }
    };
    auto scale = [&]() {
        for (int sp = 0; misc[8 + slot] < nslot && sp < SPIN_L; ++sp) __builtin_amdgcn_s_sleep(1);
        MEMBAR();
        float M, mi, li; slot_stats(M, mi, li);
        fsc = __builtin_amdgcn_exp2f((m - M) * LOG2E);
    };
    {
        const v4u sk = mk_srd(p.kc, 4 * NH * SKV * DH * 2), sv = mk_srd(p.vc, 4 * NH * SKV * DH * 2);
        const u32 lb = (u32)(size_t)L + wl;
        dma_kv(sk, vo, base + 8 * 3072, lb); dma_kv(sk, vo, base + 9 * 3072, lb + 3072);
        if (nl == 3) dma_kv(sk, vo, base + 10 * 3072, lb + 6144);
        dma_kv(sv, vo, base + 8 * 3072, lb + K_V); dma_kv(sv, vo, base + 9 * 3072, lb + K_V + 3072);
        if (nl == 3) dma_kv(sv, vo, base + 10 * 3072, lb + K_V + 6144);
    }
    v4u K[24], V[24];
#pragma unroll
    for (int jj = 0; jj < 8; ++jj)
#pragma unroll
        for (int i = 0; i < 3; ++i) K[jj * 3 + i] = __builtin_amdgcn_raw_buffer_load_b128(rk, vo + i * 128, base + jj * 3072, 2);
#pragma unroll
    for (int jj = 0; jj < 8; ++jj)
#pragma unroll
        for (int i = 0; i < 3; ++i) V[jj * 3 + i] = __builtin_amdgcn_raw_buffer_load_b128(rv, vo + i * 128, base + jj * 3072, 2);
    __builtin_amdgcn_sched_barrier(0);
    if (w == 0) STAMP(1);
    if (w == 7) STAMP(2);
    if (w < 4) {
        for (int sp = 0; misc[2] == 0 && sp < SPIN_L; ++sp) __builtin_amdgcn_s_sleep(1);
        const u32 tlo = misc[4], thi = misc[5];
        for (int sp = 0; sp < SPIN_G && misc[6] == 0; ++sp) {
            MEMBAR();
            const v2u f = ld_sc1_2(rws, WS_FLAG + l * 8);
            if (__builtin_amdgcn_ballot_w64(f[0] != tlo || f[1] != thi) == 0ull) misc[6] = 1;
        }
        const u32 qo = WS_QP + (u32)((bh >> 5) * NQ + (bh & 31) * DH + 4 * l) * 4u;
        v4u a[5] = {};
        bool need = l < 48;
        for (int sp = 0; sp < SPIN_G; ++sp) {
            MEMBAR();
            if (__builtin_amdgcn_readfirstlane(misc[slot]) != 0u) break;
            if (need) {
                u32 mx = 0;
#pragma unroll
                for (int ks = 0; ks < 5; ++ks) a[ks] = ld_sc1(rws, qo + ks * (4 * NQ * 4));
#pragma unroll
                for (int ks = 0; ks < 5; ++ks) mx = vmax4(mx, a[ks]);
                need = mx == SENT;
            }
            if (__builtin_amdgcn_ballot_w64(need) == 0ull || sp == SPIN_G - 1) {
                const f4 qs = (((asf4(a[0]) + asf4(a[1])) + asf4(a[2])) + asf4(a[3])) + asf4(a[4]);
                if (l < 48) *(LAS v2u*)(L + K_QS + slot * 384 + l * 8) = v2u{pack2(qs[0], qs[1]), pack2(qs[2], qs[3])};
                LGKM0();
                misc[slot] = 1;
                break;
            }
        }
        if (w == 0) STAMP(3);
    } else {
        for (int sp = 0; misc[slot] == 0 && sp < SPIN_L; ++sp) __builtin_amdgcn_s_sleep(1);
    }
    VMW(48);
    v4u p0, p1, p2;
    {
        v4u q[3];
#pragma unroll
        for (int i = 0; i < 3; ++i) q[i] = *(LAS v4u*)(L + K_QS + slot * 384 + (c + 8 * i) * 16);
        float y[4], x[8];
        LAS v4u* KL = (LAS v4u*)(L + wl + l * 16);
#pragma unroll
        for (int jj = 0; jj < 3; ++jj) { y[jj] = qk_unit(KL[jj * 192], KL[jj * 192 + 64], KL[jj * 192 + 128], q); __builtin_amdgcn_sched_barrier(0); }
#pragma unroll
        for (int jj = 0; jj < 8; ++jj) x[jj] = qk_unit(K[3 * jj], K[3 * jj + 1], K[3 * jj + 2], q);
#pragma unroll
        for (int h = 0; h < 4; ++h) { const float a = x[h] + dppf<0x141>(x[h]), b = x[h + 4] + dppf<0x141>(x[h + 4]); x[h] = (c & 4) ? b : a; }
        { const float a = y[0] + dppf<0x141>(y[0]), b = y[2] + dppf<0x141>(y[2]), a1 = y[1] + dppf<0x141>(y[1]); y[0] = (c & 4) ? b : a; y[1] = a1; }
#pragma unroll
        for (int h = 0; h < 2; ++h) { const float a = x[h] + dppf<0x4E>(x[h]), b = x[h + 2] + dppf<0x4E>(x[h + 2]); x[h] = (c & 2) ? b : a; }
        { const float a = y[0] + dppf<0x4E>(y[0]), b = y[1] + dppf<0x4E>(y[1]); y[0] = (c & 2) ? b : a; }
        { const float a = x[0] + dppf<0xB1>(x[0]), b = x[1] + dppf<0xB1>(x[1]); x[0] = (c & 1) ? b : a; }
        y[0] += dppf<0xB1>(y[0]);
        const float s1 = rbf(rbf(x[0]) * 0.07216878364870322f);
        const float s2 = (c >> 1) < nl ? rbf(rbf(y[0]) * 0.07216878364870322f) : -INFINITY;
        m = wave_max(fmaxf(s1, s2));
        const float e1 = __builtin_amdgcn_exp2f((s1 - m) * LOG2E), e2 = __builtin_amdgcn_exp2f((s2 - m) * LOG2E);
        *(LAS u32*)(L + wl + K_PX + l * 4) = pack2(e1, 0.f);
        *(LAS u32*)(L + wl + K_PX2 + (l >> 1) * 4) = pack2(e2, 0.f);
        lsum = wave_sum(e1 + ((c & 1) ? 0.f : e2));
        post();
        LGKM0();
        p0 = *(LAS v4u*)(L + wl + K_PX + (l >> 3) * 32); p1 = *(LAS v4u*)(L + wl + K_PX + (l >> 3) * 32 + 16); p2 = *(LAS v4u*)(L + wl + K_PX2 + (l >> 3) * 16);
    }
    if (w == 0) STAMP(4);
    float acc[24];
#pragma unroll
    for (int i = 0; i < 24; ++i) acc[i] = 0.f;
    {
        LAS v4u* VL = (LAS v4u*)(L + wl + K_V + l * 16);
        pv_unit(VL[0], VL[64], VL[128], p2[0], acc);
        pv_unit(VL[192], VL[256], VL[320], p2[1], acc);
        if (nl == 3) pv_unit(VL[384], VL[448], VL[512], p2[2], acc);
    }
#pragma unroll
    for (int jj = 0; jj < 6; ++jj) pv_unit(V[3 * jj], V[3 * jj + 1], V[3 * jj + 2], jj < 4 ? p0[jj & 3] : p1[jj & 3], acc);
    scale();
#pragma unroll
    for (int jj = 6; jj < 8; ++jj) pv_unit(V[3 * jj], V[3 * jj + 1], V[3 * jj + 2], p1[jj & 3], acc);
    if (w == 7) STAMP(6);
    float y[12], z[6];
#pragma unroll
    for (int n = 0; n < 12; ++n) { auto s = __builtin_amdgcn_permlane32_swap(asu(acc[n]), asu(acc[n + 12]), false, false); y[n] = asf(s[0]) + asf(s[1]); }
#pragma unroll
    for (int n = 0; n < 6; ++n) { auto s = __builtin_amdgcn_permlane16_swap(asu(y[n]), asu(y[n + 6]), false, false); z[n] = asf(s[0]) + asf(s[1]); }
#pragma unroll
    for (int n = 0; n < 3; ++n) {
        const float a = z[n] + dppf<0x128>(z[n]), b = z[n + 3] + dppf<0x128>(z[n + 3]);
        const int ai = 3 * (l >> 3) + n;
        *(LAS float*)(L + wl + (64 * (ai >> 3) + 8 * c + (ai & 7)) * 4) = ((l & 8) ? b : a) * fsc;
    }
    BAR();
    if (w == 0) STAMP(7);
    if (lead) {
        const u32 pb = slot * K_W + (l < 48 ? l : 47) * 16;
        f4 o = (*(LAS f4*)(L + pb) + *(LAS f4*)(L + pb + 2 * K_W)) + (*(LAS f4*)(L + pb + 4 * K_W) + *(LAS f4*)(L + pb + 6 * K_W));
        if (pos != 1) o += (*(LAS f4*)(L + pb + K_W) + *(LAS f4*)(L + pb + 3 * K_W)) + (*(LAS f4*)(L + pb + 5 * K_W) + *(LAS f4*)(L + pb + 7 * K_W));
        st_sc1(rws, l < 48 ? opb + l * 16 : OOB, canon4(o));
        if (w == 0) STAMP(8);
    }
}

enum { R_SA = 0, R_SB = 1, R_SC = 2 };
#define UNDEF4(v) asm volatile("" : "=v"(v))
template <int ROLE>
__device__ __forceinline__ void w_path(const Params& p, LAS unsigned char* L, int u, int w, int l) {
    const rsrc_t rws = __builtin_amdgcn_make_buffer_rsrc((void*)p.ws, 0, WS_END, 0x00020000);
    const int tid = threadIdx.x, g = l >> 4, tq = (l & 15) >> 2, b4 = l & 3;
    const u32 lbase = (u32)(size_t)L, stg = lbase + w * 16384;
    const bool hasF1 = u < 50, hasF2 = u < 60;
    const int kf = u / 10, lp = u - 10 * kf, kf2 = u / 12, cs = u - 12 * kf2;
    const int ri = ROLE == R_SA ? u : (ROLE == R_SB ? u - 30 : u - 54);
    const WStream sF1 = mk_stream(mk_srd(p.wm, hasF1 ? DM * 3840 * 2 : 0), (u32)((128 * kf + 32 * (w >> 1)) * 7680 + (128 * lp + 64 * (w & 1)) * 2), 7680, l);
    const WStream sF2 = mk_stream(mk_srd(p.wq, hasF2 ? DM * NQ * 2 : 0), (u32)(128 * kf2 * 12288 + (512 * cs + 64 * w) * 2), 12288, l);
    const int mid = ROLE == R_SC ? (w < 4 ? (ri >= 6 ? ri : -1) : 30 + ri) : (w < 4 ? (ri < 6 ? ri : ri + 4) : (ri == 20 ? 28 : (ri == 21 ? 29 : -1)));
    const bool hasM = ROLE != R_SA && mid >= 0;
    const WStream sM = mk_stream(mk_srd(p.wm, hasM ? DM * 3840 * 2 : 0), (u32)(160 * (w & 3) * 7680 + (1280 + 64 * mid) * 2), 7680, l);
    const int ra = ri / 10, ca = ri - 10 * ra;
    const WStream sT = ROLE == R_SA ? mk_stream(mk_srd(p.wo, NQ * DM * 2), (u32)((2048 * ra + 256 * w) * 1280 + ca * 128), 1280, l)
                     : ROLE == R_SB ? mk_stream(mk_srd((w & 1) ? p.wu : p.wg, DM * FF * 2), (u32)(160 * (w >> 1) * 3072 + ri * 128), 3072, l)
                                    : mk_stream(mk_srd(p.wd, FF * DM * 2), (u32)(192 * w * 1280 + ri * 128), 1280, l);
    constexpr int NM = ROLE == R_SA ? 0 : 5, NT = ROLE == R_SA ? 8 : (ROLE == R_SB ? 5 : 6), NA = NM + NT;
    u32 ta[4];
    { const u32 f = 2u * (tq >> 1) | 4u * (g & 1);
#pragma unroll
      for (int T = 0; T < 4; ++T) ta[T] = w * 16384 + (8 * g + tq) * 128 + ((((u32)(2 * T)) ^ f) + (b4 >> 1)) * 16 + (b4 & 1) * 8; }
    const u32 dcol = 16 * tq + 4 * g;
    const u32 f1o = hasF1 ? WS_F1P + (u32)((kf * 4 + b4) * 1280 + 128 * lp + 64 * w + dcol) * 4u : OOB;
    const u32 qpo = hasF2 ? WS_QP + (u32)((kf2 * 4 + b4) * NQ + 512 * cs + 64 * w + dcol) * 4u : OOB;
    const u32 mfo = hasM ? WS_MODF + (u32)(b4 * 2560 + 64 * mid + dcol) * 2u : OOB;
    const u32 outo = ROLE == R_SA ? WS_AP + (u32)((ra * 4 + b4) * DM + 64 * ca + dcol) * 4u : WS_HID + (u32)(b4 * FF + 64 * ri + dcol) * 2u;
    volatile LAS u32* misc = (volatile LAS u32*)(L + L_MISC);
    STAMP(0);
    if (w < 5) { dma_1k(mk_srd(p.c, 5120), l * 16, w * 1024, lbase + L_CS + w * 1024); dma_1k(mk_srd(p.x, 5120), l * 16, w * 1024, lbase + L_XS + w * 1024); }
    issue(sF1, 0, stg);
    { u32 keep;
      asm volatile("s_nop 4\n\ts_mov_b32 %0, m0\n\ts_mov_b32 m0, %5\n\ts_nop 0\n\tbuffer_load_dword %1, %2, %3 offen nt lds\n\tbuffer_load_dword %1, %4, 0 offen nt lds\n\ts_mov_b32 m0, %0"
                   : "=&s"(keep) : "v"(l == 0 ? 0u : OOB), "s"(sT.srd), "s"(sT.base), "s"(mk_srd(p.out, 5120)), "s"(lbase + L_GB) : "memory"); }
    if (w == 0) {
        u32 keep;
        asm volatile("s_nop 4\n\ts_mov_b32 %0, m0\n\ts_mov_b32 m0, %3\n\ts_nop 0\n\tbuffer_load_dword %1, %2, 0 offen sc1 lds\n\ts_mov_b32 m0, %0"
                     : "=&s"(keep) : "v"(l < 2 ? WS_E + l * 4 : OOB), "s"(mk_srd(p.ws, WS_END)), "s"(lbase + L_GA) : "memory");
    }
    { const v4u s4 = {SENT, SENT, SENT, SENT};
      st_sc1(rws, qpo, s4);
      if (w < 2) st_sc1(rws, f1o, s4);
      if (ROLE != R_SA && (w & 3) == 0) st_sc1_2(rws, mfo, v2u{SENT, SENT});
      if (w == 0) {
          if (ROLE == R_SA) st_sc1(rws, outo, s4);
          if (ROLE == R_SB) st_sc1_2(rws, outo, v2u{SENT, SENT});
      } }
    if (tid == 0) { misc[0] = 0; misc[1] = 0; }
    BAR();
    issue(sF2, 0, stg + 4096);
    auto issueB = [&](int n) { if (n < NM) issue(sM, n, stg + ((n + 1) & 3) * 4096); else issue(sT, n - NM, stg + ((n + 1) & 3) * 4096); };
    {
        v4u ft[NT][4];
        {
            VMW(4);
            BAR();
            STAMP(1);
            const u64 T = *(LAS u64*)(L + L_GA) + 1ull;
            const u32 tlo = (u32)T, thi = (u32)(T >> 32);
            if (tid == 0) __hip_atomic_store((GAS u64*)(p.ws + WS_FLAG) + blockIdx.x, T, RLX_AGENT);
            {
                v4u f[4]; read_unit(L, ta, 0, f);
                const v4u a = silu8(*(LAS v4u*)(L + L_CS + b4 * 1280 + (128 * kf + 32 * (w >> 1) + 8 * g) * 2));
                f4 d[4];
#pragma unroll
                for (int T2 = 0; T2 < 4; ++T2) d[T2] = mfma(f[T2], a, f4{0.f, 0.f, 0.f, 0.f});
                *(LAS f4*)(L + L_RED + w * 1024 + l * 16) = sel_tile(d, tq);
                if (w >= 4) {
                    float xv[10], s = 0.f;
#pragma unroll
                    for (int i = 0; i < 5; ++i) { const u32 t = *(LAS u32*)(L + L_XS + (w - 4) * 1280 + (l + 64 * i) * 4); xv[2 * i] = bflo(t); xv[2 * i + 1] = bfhi(t); s += xv[2 * i] + xv[2 * i + 1]; }
                    const float mean = wave_sum(s) * (1.f / DM);
                    float v = 0.f;
#pragma unroll
                    for (int i = 0; i < 10; ++i) v = fmaf(xv[i] - mean, xv[i] - mean, v);
                    const float var = wave_sum(v) * (1.f / DM);
                    if (l == 0) *(LAS v2u*)(L + L_ST + (w - 4) * 8) = v2u{asu(rbf(mean)), asu(rbf(__builtin_amdgcn_sqrtf(rbf(rbf(var) + 1e-5f))))};
                }
                BAR();
                if (w < 2) {
                    LAS f4* R = (LAS f4*)(L + L_RED + w * 1024 + l * 16);
                    st_sc1(rws, f1o, canon4((R[0] + R[128]) + (R[256] + R[384])));
                }
                if (w == 0) STAMP(2);
                issue(sF2, 1, stg + 8192); issue(sF2, 2, stg + 12288); issue(sF2, 3, stg);
            }
            if (w < 2) {
                for (int sp = 0; sp < SPIN_G; ++sp) {
                    MEMBAR();
                    const v2u f = ld_sc1_2(rws, WS_FLAG + l * 8);
                    if (__builtin_amdgcn_ballot_w64(f[0] != tlo || f[1] != thi) == 0ull) break;
                }
                if (w == 0) STAMP(19);
                const int hb = tid >> 5, hi = tid & 31, col = 128 * kf2 + 4 * hi;
                const u32 o = WS_F1P + (u32)(hb * 1280 + col) * 4u;
                v4u a[5] = {}, c2[5] = {};
                bool need = hasF2;
                for (int sp = 0; sp < SPIN_G; ++sp) {
                    MEMBAR();
                    if (need) {
                        u32 mx = 0;
#pragma unroll
                        for (int ks = 0; ks < 5; ++ks) { a[ks] = ld_sc1(rws, o + ks * 20480); c2[ks] = ld_sc1(rws, o + ks * 20480 + 2560); }
#pragma unroll
                        for (int ks = 0; ks < 5; ++ks) mx = vmax4(vmax4(mx, a[ks]), c2[ks]);
                        need = mx == SENT;
                    }
                    if (__builtin_amdgcn_ballot_w64(need) == 0ull) break;
                }
                const f4 sa = (((asf4(a[0]) + asf4(a[1])) + asf4(a[2])) + asf4(a[3])) + asf4(a[4]);
                const f4 sc = (((asf4(c2[0]) + asf4(c2[1])) + asf4(c2[2])) + asf4(c2[3])) + asf4(c2[4]);
                const v2u st = *(LAS v2u*)(L + L_ST + hb * 8), xr = *(LAS v2u*)(L + L_XS + hb * 1280 + (hasF2 ? col : 0) * 2);
                const float den = asf(st[1]), rc = __builtin_amdgcn_rcpf(den);
                const f4 e = rb4(unpk(xr) - asf(st[0])), q1 = e * rc;
                const f4 h = rb4(rb4(fma4(fma4(-q1, bc4(den), e), bc4(rc), q1)) * rb4(rb4(sc) + 1.f)) + rb4(sa);
                *(LAS v2u*)(L + L_HB + hb * 256 + hi * 8) = v2u{pack2(h[0], h[1]), pack2(h[2], h[3])};
                LGKM0();
                if (l == 0) __hip_atomic_fetch_add((LAS u32*)(L + L_MISC), 1u, __ATOMIC_RELAXED, __HIP_MEMORY_SCOPE_WORKGROUP);
                if (w == 0) STAMP(4);
            }
            {
                v4u fq[4][4];
                VMW(12); read_unit(L, ta, 1, fq[0]); LGKM0();
                VMW(8); read_unit(L, ta, 2, fq[1]);
                VMW(4); read_unit(L, ta, 3, fq[2]);
                issueB(0);
                VMW(4); read_unit(L, ta, 0, fq[3]);
                if (w == 7) STAMP(3);
                for (int sp = 0; misc[0] < 2u && sp < SPIN_L; ++sp) __builtin_amdgcn_s_sleep(1);
                MEMBAR();
                f4 d[4] = {};
#pragma unroll
                for (int n = 0; n < 4; ++n) {
                    const v4u a = *(LAS v4u*)(L + L_HB + b4 * 256 + (32 * n + 8 * g) * 2);
#pragma unroll
                    for (int T2 = 0; T2 < 4; ++T2) d[T2] = mfma(fq[n][T2], a, d[T2]);
                }
                st_sc1(rws, qpo, canon4(sel_tile(d, tq)));
                BAR();
                if (w == 7) STAMP(5);
                issueB(1); issueB(2); issueB(3);
            }
            if (NM) {
                f4 d[4] = {};
#pragma unroll
                for (int n = 0; n < NM; ++n) {
                    VMW(12);
                    v4u f[4]; read_unit(L, ta, (n + 1) & 3, f);
                    const v4u a = silu8(*(LAS v4u*)(L + L_CS + b4 * 1280 + (160 * (w & 3) + 32 * n + 8 * g) * 2));
                    LGKM0();
                    issueB(n + 4);
#pragma unroll
                    for (int T2 = 0; T2 < 4; ++T2) d[T2] = mfma(f[T2], a, d[T2]);
                }
                *(LAS f4*)(L + L_RED + w * 1024 + l * 16) = sel_tile(d, tq);
                BAR();
                if ((w & 3) == 0) {
                    LAS f4* R = (LAS f4*)(L + L_RED + w * 1024 + l * 16);
                    const f4 s = (R[0] + R[64]) + (R[128] + R[192]);
                    st_sc1_2(rws, mfo, v2u{canon(pack2(s[0], s[1])), canon(pack2(s[2], s[3]))});
                }
                BAR();
            }
#pragma unroll
            for (int n = 0; n < NT; ++n) {
                const int a = NM + n, younger = (NA - 1 - a) < 3 ? (NA - 1 - a) : 3;
                if (younger == 3) VMW(12); else if (younger == 2) VMW(8); else if (younger == 1) VMW(4); else VMW(0);
                read_unit(L, ta, (a + 1) & 3, ft[n]);
                if (a + 4 < NA) { LGKM0(); issueB(a + 4); }
                if (ROLE == R_SA && n == 1 && w == 0) {
                    for (int sp = 0; sp < SPIN_G; ++sp) {
                        MEMBAR();
                        const v4u f0 = ld_sc1(rws, WS_FLAG + l * 16), f1 = ld_sc1(rws, WS_FLAG + 1024 + l * 16);
                        const bool bad = f0[0] != tlo || f0[1] != thi || f0[2] != tlo || f0[3] != thi || f1[0] != tlo || f1[1] != thi || f1[2] != tlo || f1[3] != thi;
                        if (__builtin_amdgcn_ballot_w64(bad) == 0ull) break;
                    }
                    if (u == 0 && l == 0) __hip_atomic_store((GAS u64*)(p.ws + WS_E), T, RLX_AGENT);
                }
            }
            LGKM0();
            STAMP(20 + w);
            BAR();
            STAMP(6);
        }
        if (ROLE == R_SA) {
            f4 res[2];
            UNDEF4(res[0]); UNDEF4(res[1]);
            u32 oo[2], mo[2];
#pragma unroll
            for (int r = 0; r < 2; ++r) {
                const int row = 2048 * ra + 256 * w + 32 * (4 * r + tq) + 8 * g, hd = row / DH, dd = row - hd * DH;
                oo[r] = WS_OP + (u32)((b4 * 32 + hd) * 2) * 768u + dd * 4; mo[r] = WS_ML + (u32)(hd * 4 + b4) * 16u;
            }
            bool done[2] = {false, false}, wdone[2] = {false, false}, need[2] = {true, true}, needm[2] = {true, true};
            v4u a[2][4], ml[2];
            float w0[2], w1[2];
#pragma unroll
            for (int r = 0; r < 2; ++r) {
                UNDEF4(ml[r]); UNDEF4(w0[r]); UNDEF4(w1[r]);
#pragma unroll
                for (int i = 0; i < 4; ++i) UNDEF4(a[r][i]);
            }
            for (int sp = 0; sp < SPIN_G && !(done[0] && done[1]); ++sp) {
                MEMBAR();
#pragma unroll
                for (int r = 0; r < 2; ++r) {
                    if (needm[r]) ml[r] = ld_sc1(rws, mo[r]);
                    else if (need[r]) { a[r][0] = ld_sc1(rws, oo[r]); a[r][1] = ld_sc1(rws, oo[r] + 16); a[r][2] = ld_sc1(rws, oo[r] + 768); a[r][3] = ld_sc1(rws, oo[r] + 784); }
                }
                const bool last = sp == SPIN_G - 1;
                if (!(wdone[0] && wdone[1])) __builtin_amdgcn_s_sleep(POLL_SLEEP);
#pragma unroll
                for (int r = 0; r < 2; ++r) {
                    const bool hadm = !needm[r];
                    if (!wdone[r]) {
                        needm[r] = vmax4(0u, ml[r]) == SENT;
                        if (__builtin_amdgcn_ballot_w64(needm[r]) == 0ull || last) {
                            const float m0 = asf(ml[r][0]), l0 = asf(ml[r][1]), m1 = asf(ml[r][2]), l1 = asf(ml[r][3]), M = fmaxf(m0, m1);
                            const float e0 = __builtin_amdgcn_exp2f((m0 - M) * LOG2E), e1 = __builtin_amdgcn_exp2f((m1 - M) * LOG2E);
                            const float inv = fdiv(1.f, fmaf(e0, l0, e1 * l1));
                            w0[r] = e0 * inv; w1[r] = e1 * inv; wdone[r] = true;
                        }
                    }
                    if (!done[r]) {
                        need[r] = !hadm || vmax4(vmax4(vmax4(vmax4(0u, a[r][0]), a[r][1]), a[r][2]), a[r][3]) == SENT;
                        if ((__builtin_amdgcn_ballot_w64(need[r]) == 0ull && wdone[r]) || last) {
                            const f4 x0 = fma4(asf4(a[r][0]), bc4(w0[r]), asf4(a[r][2]) * w1[r]), x1 = fma4(asf4(a[r][1]), bc4(w0[r]), asf4(a[r][3]) * w1[r]);
                            const v4u bq = {pack2(x0[0], x0[1]), pack2(x0[2], x0[3]), pack2(x1[0], x1[1]), pack2(x1[2], x1[3])};
                            const v4u bk[4] = {bq, ror4k<1>(bq), ror4k<2>(bq), ror4k<3>(bq)};
                            f4 d[4] = {};
#pragma unroll
                            for (int s = 0; s < 4; ++s)
#pragma unroll
                                for (int T2 = 0; T2 < 4; ++T2) d[(T2 + s) & 3] = mfma(ft[4 * r + s][(T2 + s) & 3], bk[T2], d[(T2 + s) & 3]);
                            res[r] = sel_tile(d, tq);
                            done[r] = true;
                        }
                    }
                }
            }
            if (w == 0) STAMP(7);
            *(LAS f4*)(L + L_RED + w * 1024 + l * 16) = res[0] + res[1];
            BAR();
            if (w == 0) {
                STAMP(8);
                LAS f4* R = (LAS f4*)(L + L_RED + l * 16);
                st_sc1(rws, outo, canon4(((R[0] + R[64]) + (R[128] + R[192])) + ((R[256] + R[320]) + (R[384] + R[448]))));
                STAMP(9);
            }
        } else if (ROLE == R_SB) {
            const int i16 = l & 15, col = 128 * w + 8 * i16;
            f4 xa, xb, c1a, c1b, sba, sbb;
            if (w < 5) {
                v4u gav, sbv, cbv, a[6];
                const u32 mo = WS_MODF + (u32)(g * 2560 + col) * 2u;
                for (int sp = 0; sp < SPIN_G; ++sp) {
                    MEMBAR();
                    gav = ld_sc1(rws, mo); sbv = ld_sc1(rws, mo + 1280); cbv = ld_sc1(rws, mo + 2560);
                    if (all_valid(vmax4(vmax4(vmax4(0u, gav), sbv), cbv))) break;
                }
                const v4u xr = *(LAS v4u*)(L + L_XS + g * 1280 + col * 2);
                xa = unpk(v2u{xr[0], xr[1]}); xb = unpk(v2u{xr[2], xr[3]});
                f4 gaa = unpk(v2u{gav[0], gav[1]}), gab = unpk(v2u{gav[2], gav[3]});
                c1a = rb4(unpk(v2u{cbv[0], cbv[1]}) + 1.f); c1b = rb4(unpk(v2u{cbv[2], cbv[3]}) + 1.f);
                sba = unpk(v2u{sbv[0], sbv[1]}); sbb = unpk(v2u{sbv[2], sbv[3]});
                asm volatile("" : "+v"(xa), "+v"(xb), "+v"(gaa), "+v"(gab), "+v"(c1a), "+v"(c1b), "+v"(sba), "+v"(sbb));
                if (w == 0) STAMP(10);
                const u32 ao = WS_AP + (u32)(g * DM + col) * 4u;
#pragma unroll
                for (int r = 0; r < 6; ++r) UNDEF4(a[r]);
                bool need = true;
                for (int sp = 0; sp < SPIN_G; ++sp) {
                    MEMBAR();
                    if (need) {
                        u32 mx = 0;
#pragma unroll
                        for (int r = 0; r < 3; ++r) { a[2 * r] = ld_sc1(rws, ao + r * 10240); a[2 * r + 1] = ld_sc1(rws, ao + r * 10240 + 16); }
#pragma unroll
                        for (int r = 0; r < 6; ++r) mx = vmax4(mx, a[r]);
                        need = mx == SENT;
                    }
                    if (__builtin_amdgcn_ballot_w64(need) == 0ull) break;
                }
                if (w == 0) STAMP(11);
                xa = rb4(xa + rb4(gaa * rb4((asf4(a[0]) + asf4(a[2])) + asf4(a[4]))));
                xb = rb4(xb + rb4(gab * rb4((asf4(a[1]) + asf4(a[3])) + asf4(a[5]))));
                const f4 t = xa + xb;
                const float mw = row_sum((t[0] + t[1]) + (t[2] + t[3])) * (1.f / 128.f);
                const f4 da = xa - mw, db = xb - mw, q2 = fma4(da, da, db * db);
                const float v = row_sum((q2[0] + q2[1]) + (q2[2] + q2[3]));
                *(LAS v2u*)(L + L_ST + 64 + g * 48 + w * 8) = v2u{asu(mw), asu(v)};
            }
            BAR();
            if (w < 5) {
                const f4 s0 = *(LAS f4*)(L + L_ST + 64 + g * 48), s1 = *(LAS f4*)(L + L_ST + 64 + g * 48 + 16);
                const f2 s2 = *(LAS f2*)(L + L_ST + 64 + g * 48 + 32);
                const float mean = (((s0[0] + s0[2]) + (s1[0] + s1[2])) + s2[0]) * 0.2f;
                const f4 dm = f4{s0[0], s0[2], s1[0], s1[2]} - mean, dq = dm * dm; const float d4 = s2[0] - mean;
                const float M2 = fmaf(128.f, fmaf(d4, d4, (dq[0] + dq[1]) + (dq[2] + dq[3])), ((s0[1] + s0[3]) + (s1[1] + s1[3])) + s2[1]);
                const float mb = rbf(mean), den = rbf(__builtin_amdgcn_sqrtf(rbf(rbf(M2 * (1.f / DM)) + 1e-5f))), rc = __builtin_amdgcn_rcpf(den);
                const f4 ea = rb4(xa - mb), eb = rb4(xb - mb), qa = ea * rc, qb = eb * rc;
                const f4 ha = rb4(rb4(fma4(fma4(-qa, bc4(den), ea), bc4(rc), qa)) * c1a) + sba, hb = rb4(rb4(fma4(fma4(-qb, bc4(den), eb), bc4(rc), qb)) * c1b) + sbb;
                *(LAS v4u*)(L + L_HB + g * 1280 + col * 2) = v4u{pack2(ha[0], ha[1]), pack2(ha[2], ha[3]), pack2(hb[0], hb[1]), pack2(hb[2], hb[3])};
            }
            BAR();
            if (w == 0) STAMP(12);
            f4 d[4] = {};
#pragma unroll
            for (int n = 0; n < 5; ++n) {
                const v4u a = *(LAS v4u*)(L + L_HB + b4 * 1280 + (160 * (w >> 1) + 32 * n + 8 * g) * 2);
#pragma unroll
                for (int T2 = 0; T2 < 4; ++T2) d[T2] = mfma(ft[n][T2], a, d[T2]);
            }
            *(LAS f4*)(L + L_RED + w * 1024 + l * 16) = sel_tile(d, tq);
            BAR();
            if (w == 0) {
                STAMP(13);
                LAS f4* R = (LAS f4*)(L + L_RED + l * 16);
                const f4 gt = rb4((R[0] + R[128]) + (R[256] + R[384])), up = rb4((R[64] + R[192]) + (R[320] + R[448]));
                const f4 mk = {gt[0] > 0.f ? 1.f : 0.f, gt[1] > 0.f ? 1.f : 0.f, gt[2] > 0.f ? 1.f : 0.f, gt[3] > 0.f ? 1.f : 0.f};
                const f4 hh = (gt * mk) * up;
                st_sc1_2(rws, outo, v2u{canon(pack2(hh[0], hh[1])), canon(pack2(hh[2], hh[3]))});
                STAMP(14);
            }
        } else {
            f4 x1, gbf;
            UNDEF4(x1); UNDEF4(gbf);
            if (w == 0) {
                v2u ga, gb;
                const u32 mo = WS_MODF + (u32)(b4 * 2560 + 64 * ri + dcol) * 2u;
                for (int sp = 0; sp < SPIN_G; ++sp) {
                    MEMBAR();
                    ga = ld_sc1_2(rws, mo); gb = ld_sc1_2(rws, mo + 3840);
                    if (all_valid(umax3(umax3(ga[0], ga[1], gb[0]), gb[1], 0u))) break;
                }
                const u32 ao = WS_AP + (u32)(b4 * DM + 64 * ri + dcol) * 4u;
                f4 s;
                for (int sp = 0; sp < SPIN_G; ++sp) {
                    MEMBAR();
                    const v4u p0 = ld_sc1(rws, ao), p1 = ld_sc1(rws, ao + 10240), p2 = ld_sc1(rws, ao + 20480);
                    s = (asf4(p0) + asf4(p1)) + asf4(p2);
                    if (all_valid(vmax4(vmax4(vmax4(0u, p0), p1), p2))) break;
                }
                x1 = rb4(unpk(*(LAS v2u*)(L + L_XS + b4 * 1280 + (64 * ri + dcol) * 2)) + rb4(unpk(ga) * rb4(s)));
                gbf = unpk(gb);
                asm volatile("" : "+v"(x1), "+v"(gbf));
                STAMP(15);
            }
            v4u a0, a1;
            UNDEF4(a0); UNDEF4(a1);
            const u32 h0 = WS_HID + (u32)(b4 * FF + 192 * w + 32 * tq + 8 * g) * 2u, h1 = tq < 2 ? h0 + 256 : OOB;
            bool need = true;
            for (int sp = 0; sp < SPIN_G; ++sp) {
                MEMBAR();
                if (need) { a0 = ld_sc1(rws, h0); a1 = ld_sc1(rws, h1); need = vmax4(vmax4(0u, a0), a1) == SENT; }
                if (__builtin_amdgcn_ballot_w64(need) == 0ull) break;
            }
            if (w == 0) STAMP(16);
            const v4u bk[4] = {a0, ror4k<1>(a0), ror4k<2>(a0), ror4k<3>(a0)}, bl[4] = {a1, ror4k<1>(a1), ror4k<2>(a1), ror4k<3>(a1)};
            f4 d[4] = {};
#pragma unroll
            for (int k = 0; k < 4; ++k)
#pragma unroll
                for (int s = 0; s < 6; ++s) d[(s + k) & 3] = mfma(ft[s][(s + k) & 3], s < 4 ? bk[k] : bl[k], d[(s + k) & 3]);
            *(LAS f4*)(L + L_RED + w * 1024 + l * 16) = sel_tile(d, tq);
            BAR();
            if (w == 0) {
                STAMP(17);
                LAS f4* R = (LAS f4*)(L + L_RED + l * 16);
                const f4 o = x1 + rb4(gbf * rb4(((R[0] + R[64]) + (R[128] + R[192])) + ((R[256] + R[320]) + (R[384] + R[448]))));
                *(GAS v2u*)(p.out + b4 * DM + 64 * ri + dcol) = v2u{pack2(o[0], o[1]), pack2(o[2], o[3])};
                STAMP(18);
            }
        }
    }
}

typedef const __attribute__((address_space(4))) Params* kargp;
#define GET_PARAMS() kargp pp_ = kp; asm volatile("" : "+s"(pp_)); \
    const Params q_ = {pp_->x, pp_->kc, pp_->vc, pp_->wq, pp_->wo, pp_->wg, pp_->wu, pp_->wd, pp_->c, pp_->wm, pp_->out, pp_->ws};
__global__ void __launch_bounds__(512, 2) fused_block(Params p) {
    extern __shared__ __attribute__((aligned(16))) unsigned char smem[];
    LAS unsigned char* L = (LAS unsigned char*)smem;
    const kargp kp = (kargp)__builtin_amdgcn_kernarg_segment_ptr();
    asm volatile("s_load_dword s98, %0, 0x0\n\ts_load_dword s99, %0, 0x40" :: "s"(kp) : "s98", "s99");
    const int l = threadIdx.x & 63, w = __builtin_amdgcn_readfirstlane(threadIdx.x >> 6), c = blockIdx.x;
    if (__builtin_expect(c >= NW, 1)) { GET_PARAMS(); kv_path(q_, L, c - NW, w, l); }
    else if (c < 30) { GET_PARAMS(); w_path<R_SA>(q_, L, c, w, l); }
    else if (c < 54) { GET_PARAMS(); w_path<R_SB>(q_, L, c, w, l); }
    else { GET_PARAMS(); w_path<R_SC>(q_, L, c, w, l); }
}

extern "C" void kernel_launch(void* const* d_in, const int* in_sizes, int n_in, void* d_out, int out_size, void* d_ws, size_t ws_size, hipStream_t stream) {
    Params p;
    p.x = (const u16*)d_in[0]; p.kc = (const u16*)d_in[1]; p.vc = (const u16*)d_in[2]; p.wq = (const u16*)d_in[3]; p.wo = (const u16*)d_in[4];
    p.wg = (const u16*)d_in[6]; p.wu = (const u16*)d_in[7]; p.wd = (const u16*)d_in[8]; p.c = (const u16*)d_in[9]; p.wm = (const u16*)d_in[10];
    p.out = (u16*)d_out; p.ws = (unsigned char*)d_ws;
    (void)hipFuncSetAttribute((const void*)fused_block, hipFuncAttributeMaxDynamicSharedMemorySize, (int)L_END);
    hipLaunchKernelGGL(fused_block, dim3(256), dim3(512), L_END, stream, p);
}
```

```cpp
#define OUTAUX 16
#include <hip/hip_runtime.h>
#include <stdint.h>
#pragma clang fp contract(off)

typedef unsigned short u16;
typedef unsigned u32;
typedef unsigned long long u64;
typedef u32 v4u __attribute__((ext_vector_type(4)));
typedef u32 v2u __attribute__((ext_vector_type(2)));
typedef float f4 __attribute__((ext_vector_type(4)));
typedef short v4s __attribute__((ext_vector_type(4)));
typedef __bf16 bf8 __attribute__((ext_vector_type(8)));
typedef __bf16 bf2 __attribute__((ext_vector_type(2)));
#define LAS __attribute__((address_space(3)))
#define GAS __attribute__((address_space(1)))
#define RLX_AGENT __ATOMIC_RELAXED, __HIP_MEMORY_SCOPE_AGENT

#define PRETOUCH 1
#define POLL_SLEEP 2
#define INIT_MODE 1

constexpr int DM = 640, NH = 32, SKV = 4096, DH = 192, WIN = 1024, FF = 1536, NQ = NH * DH;
constexpr u32 SENT = 0xFFFFFFFFu;
constexpr int NW = 64;
constexpr u32 WS_E = 0, WS_FLAG = 4096, WS_F1P = 8192, WS_MODF = WS_F1P + 5 * 4 * 1280 * 4, WS_QP = WS_MODF + 4 * 2560 * 2,
              WS_OP = WS_QP + 5 * 4 * NQ * 4, WS_ML = WS_OP + 128 * 2 * DH * 4, WS_AP = WS_ML + 32 * 4 * 16, WS_HID = WS_AP + 3 * 4 * DM * 4,
              WS_END = WS_HID + 4 * FF * 2;
constexpr u32 OOB = 0x7FFFFF00u;
constexpr u32 L_XS = 131072, L_CS = L_XS + 5120, L_HB = L_CS + 5120, L_RED = L_HB + 5120, L_ST = L_RED + 8192, L_GA = L_ST + 256, L_GB = L_GA + 1024,
              L_X1 = L_GB + 1024, L_MISC = L_X1 + 1024, L_STAMP = L_MISC + 64, L_END = L_STAMP + 256;
constexpr u32 K_W = 18432, K_PX = 1024, K_PX2 = 1536, K_V = 9216, K_QS = 8 * K_W, K_ML = K_QS + 768, K_END = K_ML + 64;
static_assert(K_END <= L_MISC, "LDS");
constexpr float LOG2E = 1.4426950408889634f;
static_assert(L_END <= 163840, "LDS");
constexpr int SPIN_G = 40000, SPIN_L = 2000000;

#define STAMP(i) do {} while (0)

struct Params { const u16 *x, *kc, *vc, *wq, *wo, *wg, *wu, *wd, *c, *wm; u16* out; unsigned char* ws; };

__device__ __forceinline__ v4u mk_srd(const void* p, u32 bytes) { const u64 a = (u64)p; return v4u{(u32)a, (u32)(a >> 32) & 0xffffu, bytes, 0x00020000u}; }
__device__ __forceinline__ float asf(u32 u) { return __builtin_bit_cast(float, u); }
__device__ __forceinline__ u32 asu(float f) { return __builtin_bit_cast(u32, f); }
__device__ __forceinline__ float bflo(u32 u) { return asf(u << 16); }
__device__ __forceinline__ float bfhi(u32 u) { return asf(u & 0xffff0000u); }
__device__ __forceinline__ u32 pack2(float lo, float hi) { bf2 v; v[0] = (__bf16)lo; v[1] = (__bf16)hi; return __builtin_bit_cast(u32, v); }
__device__ __forceinline__ float rbf(float f) { return (float)(__bf16)f; }
typedef float f2 __attribute__((ext_vector_type(2)));
__device__ __forceinline__ f2 rb2(f2 v) { return f2{rbf(v[0]), rbf(v[1])}; }
__device__ __forceinline__ f4 rb4(f4 v) {
    float a = v[0], b = v[1], c = v[2], d = v[3];
    asm("v_cvt_pk_bf16_f32 %0, 0, %0\n\tv_cvt_pk_bf16_f32 %1, 0, %1\n\tv_cvt_pk_bf16_f32 %2, 0, %2\n\tv_cvt_pk_bf16_f32 %3, 0, %3" : "+v"(a), "+v"(b), "+v"(c), "+v"(d));
    return f4{a, b, c, d}; }
__device__ __forceinline__ f4 unpk(v2u v) { return f4{bflo(v[0]), bfhi(v[0]), bflo(v[1]), bfhi(v[1])}; }
__device__ __forceinline__ f4 fma4(f4 a, f4 b, f4 c) { return __builtin_elementwise_fma(a, b, c); }
__device__ __forceinline__ f4 bc4(float x) { return f4{x, x, x, x}; }
__device__ __forceinline__ u32 canon(u32 u) { return u < 0xFFFFFFFEu ? u : 0xFFFFFFFEu; }
__device__ __forceinline__ v4u canon4(f4 v) { return v4u{canon(asu(v[0])), canon(asu(v[1])), canon(asu(v[2])), canon(asu(v[3]))}; }
__device__ __forceinline__ u32 umax3(u32 a, u32 b, u32 c) { const u32 m = a > b ? a : b; return m > c ? m : c; }
__device__ __forceinline__ u32 vmax4(u32 m, v4u v) { return umax3(umax3(m, v[0], v[1]), v[2], v[3]); }
__device__ __forceinline__ bool all_valid(u32 m) { return __builtin_amdgcn_ballot_w64(m == SENT) == 0ull; }
__device__ __forceinline__ float dot2(u32 a, u32 b, float c) { return __builtin_amdgcn_fdot2_f32_bf16(__builtin_bit_cast(bf2, a), __builtin_bit_cast(bf2, b), c, false); }
__device__ __forceinline__ f4 mfma(v4u a, v4u b, f4 c) { return __builtin_amdgcn_mfma_f32_16x16x32_bf16(__builtin_bit_cast(bf8, a), __builtin_bit_cast(bf8, b), c, 0, 0, 0); }
template <int CTRL> __device__ __forceinline__ u32 dppu(u32 x) { return (u32)__builtin_amdgcn_mov_dpp((int)x, CTRL, 0xf, 0xf, true); }
template <int CTRL> __device__ __forceinline__ float dppf(float x) { return asf(dppu<CTRL>(asu(x))); }
template <int K> __device__ __forceinline__ v4u ror4k(v4u v) {
    if constexpr (K == 0) return v; else return v4u{dppu<0x120 + 4 * K>(v[0]), dppu<0x120 + 4 * K>(v[1]), dppu<0x120 + 4 * K>(v[2]), dppu<0x120 + 4 * K>(v[3])}; }
__device__ __forceinline__ float row_sum(float x) { x += dppf<0x128>(x); x += dppf<0x124>(x); x += dppf<0x122>(x); x += dppf<0x121>(x); return x; }
__device__ __forceinline__ float row_max(float x) { x = fmaxf(x, dppf<0x128>(x)); x = fmaxf(x, dppf<0x124>(x)); x = fmaxf(x, dppf<0x122>(x)); x = fmaxf(x, dppf<0x121>(x)); return x; }
__device__ __forceinline__ float xrow_sum(float x) {
    auto s = __builtin_amdgcn_permlane16_swap(asu(x), asu(x), false, false); x = asf(s[0]) + asf(s[1]);
    auto t = __builtin_amdgcn_permlane32_swap(asu(x), asu(x), false, false); return asf(t[0]) + asf(t[1]); }
__device__ __forceinline__ float xrow_max(float x) {
    auto s = __builtin_amdgcn_permlane16_swap(asu(x), asu(x), false, false); x = fmaxf(asf(s[0]), asf(s[1]));
    auto t = __builtin_amdgcn_permlane32_swap(asu(x), asu(x), false, false); return fmaxf(asf(t[0]), asf(t[1])); }
__device__ __forceinline__ float wave_sum(float x) { return xrow_sum(row_sum(x)); }
__device__ __forceinline__ float wave_max(float x) { return xrow_max(row_max(x)); }
__device__ __forceinline__ float fdiv(float a, float b) { const float r = __builtin_amdgcn_rcpf(b); const float q = a * r; return fmaf(fmaf(-q, b, a), r, q); }
__device__ __forceinline__ float silu_bf(float v) { return fdiv(v, 1.f + __builtin_amdgcn_exp2f(fminf(-v * 1.4426950408889634f, 126.f))); }
__device__ __forceinline__ v4u silu8(v4u c) {
    v4u r;
#pragma unroll
    for (int i = 0; i < 4; ++i) r[i] = pack2(silu_bf(bflo(c[i])), silu_bf(bfhi(c[i])));
    return r; }
#define BAR() asm volatile("s_waitcnt lgkmcnt(0)\n\ts_barrier" ::: "memory")
#define LGKM0() asm volatile("s_waitcnt lgkmcnt(0)" ::: "memory")
#define VMW(n) asm volatile("s_waitcnt vmcnt(%0)" :: "i"(n) : "memory")
#define MEMBAR() asm volatile("" ::: "memory")
typedef __amdgpu_buffer_rsrc_t rsrc_t;
__device__ __forceinline__ v4u ld_sc1(rsrc_t r, u32 off) { return __builtin_amdgcn_raw_buffer_load_b128(r, off, 0, 16); }
__device__ __forceinline__ v2u ld_sc1_2(rsrc_t r, u32 off) { return __builtin_amdgcn_raw_buffer_load_b64(r, off, 0, 16); }
__device__ __forceinline__ void st_sc1(rsrc_t r, u32 off, v4u v) { __builtin_amdgcn_raw_buffer_store_b128(v, r, off, 0, 16); }
__device__ __forceinline__ void st_sc1_2(rsrc_t r, u32 off, v2u v) { __builtin_amdgcn_raw_buffer_store_b64(v, r, off, 0, 16); }
__device__ __forceinline__ f4 asf4(v4u v) { return __builtin_bit_cast(f4, v); }

__device__ __forceinline__ void dma_unit(v4u srd, u32 v0, u32 v1, u32 sA, u32 sB, u32 lds) {
    u32 keep;
    asm volatile("s_nop 4\n\ts_mov_b32 %0, m0\n\ts_mov_b32 m0, %6\n\ts_nop 0\n\t"
                 "buffer_load_dwordx4 %1, %3, %4 offen nt lds\n\ts_add_u32 m0, m0, 0x400\n\ts_nop 0\n\t"
                 "buffer_load_dwordx4 %2, %3, %4 offen nt lds\n\ts_add_u32 m0, m0, 0x400\n\ts_nop 0\n\t"
                 "buffer_load_dwordx4 %1, %3, %5 offen nt lds\n\ts_add_u32 m0, m0, 0x400\n\ts_nop 0\n\t"
                 "buffer_load_dwordx4 %2, %3, %5 offen nt lds\n\ts_mov_b32 m0, %0"
                 : "=&s"(keep) : "v"(v0), "v"(v1), "s"(srd), "s"(sA), "s"(sB), "s"(lds) : "memory", "scc");
}
__device__ __forceinline__ void dma_1k(v4u srd, u32 voff, u32 soff, u32 lds) {
    u32 keep;
    asm volatile("s_nop 4\n\ts_mov_b32 %0, m0\n\ts_mov_b32 m0, %4\n\ts_nop 0\n\tbuffer_load_dwordx4 %1, %2, %3 offen lds\n\ts_mov_b32 m0, %0"
                 : "=&s"(keep) : "v"(voff), "s"(srd), "s"(soff), "s"(lds) : "memory");
}
struct WStream { v4u srd; u32 base, pitch, v0, v1; };
__device__ __forceinline__ WStream mk_stream(v4u srd, u32 base, u32 pitch, int l) {
    WStream s; s.srd = srd; s.base = base; s.pitch = pitch;
    const u32 sw = 2u * ((l >> 4) & 1);
    s.v0 = (l >> 3) * pitch + (((l & 7) ^ sw) << 4);
    s.v1 = (8 + (l >> 3)) * pitch + (((l & 7) ^ (sw | 4u)) << 4);
    return s; }
__device__ __forceinline__ void issue(const WStream& s, int n, u32 lds) { const u32 a = s.base + (u32)n * 32u * s.pitch; dma_unit(s.srd, s.v0, s.v1, a, a + 16u * s.pitch, lds); }
__device__ __forceinline__ void read_unit(LAS unsigned char* L, const u32 (&ta)[4], int slot, v4u (&f)[4]) {
#pragma unroll
    for (int T = 0; T < 4; ++T) {
        const v2u lo = __builtin_bit_cast(v2u, __builtin_amdgcn_ds_read_tr16_b64_v4i16((LAS v4s*)(L + ta[T] + slot * 4096)));
        const v2u hi = __builtin_bit_cast(v2u, __builtin_amdgcn_ds_read_tr16_b64_v4i16((LAS v4s*)(L + ta[T] + slot * 4096 + 512)));
        f[T] = v4u{lo[0], lo[1], hi[0], hi[1]};
    } }
__device__ __forceinline__ f4 sel_tile(const f4 (&a)[4], int t) { return t == 0 ? a[0] : t == 1 ? a[1] : t == 2 ? a[2] : a[3]; }

__device__ __forceinline__ void dma_kv(v4u srd, u32 vo, u32 so, u32 lds) {
    u32 keep;
    asm volatile("s_nop 4\n\ts_mov_b32 %0, m0\n\ts_mov_b32 m0, %4\n\ts_nop 0\n\t"
                 "buffer_load_dwordx4 %1, %2, %3 offen nt lds\n\ts_add_u32 m0, m0, 0x380\n\ts_nop 0\n\t"
                 "buffer_load_dwordx4 %1, %2, %3 offen offset:128 nt lds\n\ts_add_u32 m0, m0, 0x380\n\ts_nop 0\n\t"
                 "buffer_load_dwordx4 %1, %2, %3 offen offset:256 nt lds\n\ts_mov_b32 m0, %0"
                 : "=&s"(keep) : "v"(vo), "s"(srd), "s"(so), "s"(lds) : "memory", "scc");
}
__device__ __forceinline__ float qk_unit(v4u k0, v4u k1, v4u k2, const v4u (&q)[3]) {
    float s = 0.f, t = 0.f;
#pragma unroll
    for (int r = 0; r < 4; r += 2) { s = dot2(k0[r], q[0][r], s); t = dot2(k0[r + 1], q[0][r + 1], t); }
#pragma unroll
    for (int r = 0; r < 4; r += 2) { s = dot2(k1[r], q[1][r], s); t = dot2(k1[r + 1], q[1][r + 1], t); }
#pragma unroll
    for (int r = 0; r < 4; r += 2) { s = dot2(k2[r], q[2][r], s); t = dot2(k2[r + 1], q[2][r + 1], t); }
    return s + t;
}
__device__ __forceinline__ void pv_unit(v4u v0, v4u v1, v4u v2, u32 plo, float (&acc)[24]) {
    const u32 phi = plo << 16;
#pragma unroll
    for (int r = 0; r < 4; ++r) {
        acc[2 * r] = dot2(v0[r], plo, acc[2 * r]); acc[2 * r + 1] = dot2(v0[r], phi, acc[2 * r + 1]);
        acc[8 + 2 * r] = dot2(v1[r], plo, acc[8 + 2 * r]); acc[8 + 2 * r + 1] = dot2(v1[r], phi, acc[8 + 2 * r + 1]);
        acc[16 + 2 * r] = dot2(v2[r], plo, acc[16 + 2 * r]); acc[16 + 2 * r + 1] = dot2(v2[r], phi, acc[16 + 2 * r + 1]);
    }
}

__device__ __forceinline__ void kv_path(const Params& p, LAS unsigned char* L, int j, int w, int l) {
    const rsrc_t rws = __builtin_amdgcn_make_buffer_rsrc((void*)p.ws, 0, WS_END, 0x00020000);
    const rsrc_t rk = __builtin_amdgcn_make_buffer_rsrc((void*)p.kc, 0, 4 * NH * SKV * DH * 2, 0x00020000);
    const rsrc_t rv = __builtin_amdgcn_make_buffer_rsrc((void*)p.vc, 0, 4 * NH * SKV * DH * 2, 0x00020000);
    volatile LAS u32* misc = (volatile LAS u32*)(L + L_MISC);
    const int rk3 = (j >> 3) / 3, t3 = (j & 7) * 8 + rk3, pos = (j >> 3) - 3 * rk3;
    const int slot = pos == 1 ? (w & 1) : 0;
    const int bh = 2 * t3 + (pos == 2 ? 1 : (pos == 1 ? slot : 0));
    const int part = pos == 0 ? 0 : (pos == 2 ? 1 : (slot == 0 ? 1 : 0));
    const int wi = pos == 1 ? (w >> 1) : w;
    const int ub = pos == 0 ? 0 : (pos == 2 ? 43 : (slot == 0 ? 85 : 0));
    const int u1 = ub + 11 * wi - (wi > 5 ? wi - 5 : 0);
    const int nl = (pos == 1 ? wi == 3 : wi >= 5) ? 2 : 3;
    const bool lead = w == 0 || (w == 1 && pos == 1);
    const u32 opb = WS_OP + (bh * 2 + part) * 768, mlb = WS_ML + ((bh & 31) * 4 + (bh >> 5)) * 16 + part * 8;
    if (threadIdx.x < 12) misc[threadIdx.x] = 0;
    BAR();
    STAMP(0);
    if (w == 4) {
        __builtin_amdgcn_s_setprio(3);
        const u64 T = __hip_atomic_load((GAS u64*)(p.ws + WS_E), RLX_AGENT) + 1ull;
        st_sc1(rws, l < 48 ? opb + l * 16 : OOB, v4u{SENT, SENT, SENT, SENT});
        st_sc1_2(rws, l == 0 ? mlb : OOB, v2u{SENT, SENT});
        const int bh1 = 2 * t3 + 1;
        st_sc1(rws, (pos == 1 && l < 48) ? WS_OP + bh1 * 2 * 768 + l * 16 : OOB, v4u{SENT, SENT, SENT, SENT});
        st_sc1_2(rws, (pos == 1 && l == 0) ? WS_ML + ((bh1 & 31) * 4 + (bh1 >> 5)) * 16 : OOB, v2u{SENT, SENT});
        VMW(0);
        if (l == 0) {
            __hip_atomic_store((GAS u64*)(p.ws + WS_FLAG) + blockIdx.x, T, RLX_AGENT);
            misc[4] = (u32)T; misc[5] = (u32)(T >> 32); LGKM0(); misc[2] = 1;
        }
        STAMP(9);
        __builtin_amdgcn_s_setprio(0);
    }
    const u32 vo = (l >> 3) * 384 + (l & 7) * 16;
    const u32 base = ((u32)bh * SKV + (SKV - WIN)) * 384u + (u32)u1 * 3072u;
    const u32 wl = w * K_W;
    const int c = l & 7;
    float m, lsum, fsc;
    const u32 nslot = pos == 1 ? 4u : 8u;
    auto slot_stats = [&](float& M, float& mi, float& li) {
        const v2u t = *(LAS v2u*)(L + K_ML + c * 8);
        const bool mine = pos != 1 || (l & 1) == slot;
        mi = mine ? asf(t[0]) : -INFINITY; li = mine ? asf(t[1]) : 0.f;
        M = fmaxf(mi, dppf<0xB1>(mi)); M = fmaxf(M, dppf<0x4E>(M)); M = fmaxf(M, dppf<0x141>(M));
    };
    auto post = [&]() {
        u32 old = 0;
        if (l == 0) { *(LAS v2u*)(L + K_ML + w * 8) = v2u{asu(m), asu(lsum)}; LGKM0(); old = __hip_atomic_fetch_add((LAS u32*)(L + L_MISC + 32 + slot * 4), 1u, __ATOMIC_RELAXED, __HIP_MEMORY_SCOPE_WORKGROUP); }
        if (__builtin_amdgcn_readfirstlane(old) == nslot - 1u) {
            MEMBAR();
            float M, mi, li; slot_stats(M, mi, li);
            float e = li * __builtin_amdgcn_exp2f((mi - M) * LOG2E);
            e += dppf<0xB1>(e); e += dppf<0x4E>(e); e += dppf<0x141>(e);
            st_sc1_2(rws, l == 0 ? mlb : OOB, v2u{canon(asu(M)), canon(asu(e))});
            STAMP(5);
        }
    };
    auto scale = [&]() {
        for (int sp = 0; misc[8 + slot] < nslot && sp < SPIN_L; ++sp) __builtin_amdgcn_s_sleep(1);
        MEMBAR();
        float M, mi, li; slot_stats(M, mi, li);
        fsc = __builtin_amdgcn_exp2f((m - M) * LOG2E);
    };
    {
        const v4u sk = mk_srd(p.kc, 4 * NH * SKV * DH * 2), sv = mk_srd(p.vc, 4 * NH * SKV * DH * 2);
        const u32 lb = (u32)(size_t)L + wl;
        dma_kv(sk, vo, base + 8 * 3072, lb); dma_kv(sk, vo, base + 9 * 3072, lb + 3072);
        if (nl == 3) dma_kv(sk, vo, base + 10 * 3072, lb + 6144);
        dma_kv(sv, vo, base + 8 * 3072, lb + K_V); dma_kv(sv, vo, base + 9 * 3072, lb + K_V + 3072);
        if (nl == 3) dma_kv(sv, vo, base + 10 * 3072, lb + K_V + 6144);
    }
    v4u K[24], V[24];
#pragma unroll
    for (int jj = 0; jj < 8; ++jj)
#pragma unroll
        for (int i = 0; i < 3; ++i) K[jj * 3 + i] = __builtin_amdgcn_raw_buffer_load_b128(rk, vo + i * 128, base + jj * 3072, 2);
#pragma unroll
    for (int jj = 0; jj < 8; ++jj)
#pragma unroll
        for (int i = 0; i < 3; ++i) V[jj * 3 + i] = __builtin_amdgcn_raw_buffer_load_b128(rv, vo + i * 128, base + jj * 3072, 2);
    __builtin_amdgcn_sched_barrier(0);
    if (w == 0) STAMP(1);
    if (w == 7) STAMP(2);
    if (w < 4) {
        for (int sp = 0; misc[2] == 0 && sp < SPIN_L; ++sp) __builtin_amdgcn_s_sleep(1);
        const u32 tlo = misc[4], thi = misc[5];
        for (int sp = 0; sp < SPIN_G && misc[6] == 0; ++sp) {
            MEMBAR();
            const v2u f = ld_sc1_2(rws, WS_FLAG + l * 8);
            if (__builtin_amdgcn_ballot_w64(f[0] != tlo || f[1] != thi) == 0ull) misc[6] = 1;
        }
        const u32 qo = WS_QP + (u32)((bh >> 5) * NQ + (bh & 31) * DH + 4 * l) * 4u;
        v4u a[5] = {};
        bool need = l < 48;
        for (int sp = 0; sp < SPIN_G; ++sp) {
            MEMBAR();
            if (__builtin_amdgcn_readfirstlane(misc[slot]) != 0u) break;
            if (need) {
                u32 mx = 0;
#pragma unroll
                for (int ks = 0; ks < 5; ++ks) a[ks] = ld_sc1(rws, qo + ks * (4 * NQ * 4));
#pragma unroll
                for (int ks = 0; ks < 5; ++ks) mx = vmax4(mx, a[ks]);
                need = mx == SENT;
            }
            if (__builtin_amdgcn_ballot_w64(need) == 0ull || sp == SPIN_G - 1) {
                const f4 qs = (((asf4(a[0]) + asf4(a[1])) + asf4(a[2])) + asf4(a[3])) + asf4(a[4]);
                if (l < 48) *(LAS v2u*)(L + K_QS + slot * 384 + l * 8) = v2u{pack2(qs[0], qs[1]), pack2(qs[2], qs[3])};
                LGKM0();
                misc[slot] = 1;
                break;
            }
        }
        if (w == 0) STAMP(3);
    } else {
        for (int sp = 0; misc[slot] == 0 && sp < SPIN_L; ++sp) __builtin_amdgcn_s_sleep(1);
    }
    VMW(48);
    v4u p0, p1, p2;
    {
        v4u q[3];
#pragma unroll
        for (int i = 0; i < 3; ++i) q[i] = *(LAS v4u*)(L + K_QS + slot * 384 + (c + 8 * i) * 16);
        float y[4], x[8];
        LAS v4u* KL = (LAS v4u*)(L + wl + l * 16);
#pragma unroll
        for (int jj = 0; jj < 3; ++jj) { y[jj] = qk_unit(KL[jj * 192], KL[jj * 192 + 64], KL[jj * 192 + 128], q); __builtin_amdgcn_sched_barrier(0); }
#pragma unroll
        for (int jj = 0; jj < 8; ++jj) x[jj] = qk_unit(K[3 * jj], K[3 * jj + 1], K[3 * jj + 2], q);
#pragma unroll
        for (int h = 0; h < 4; ++h) { const float a = x[h] + dppf<0x141>(x[h]), b = x[h + 4] + dppf<0x141>(x[h + 4]); x[h] = (c & 4) ? b : a; }
        { const float a = y[0] + dppf<0x141>(y[0]), b = y[2] + dppf<0x141>(y[2]), a1 = y[1] + dppf<0x141>(y[1]); y[0] = (c & 4) ? b : a; y[1] = a1; }
#pragma unroll
        for (int h = 0; h < 2; ++h) { const float a = x[h] + dppf<0x4E>(x[h]), b = x[h + 2] + dppf<0x4E>(x[h + 2]); x[h] = (c & 2) ? b : a; }
        { const float a = y[0] + dppf<0x4E>(y[0]), b = y[1] + dppf<0x4E>(y[1]); y[0] = (c & 2) ? b : a; }
        { const float a = x[0] + dppf<0xB1>(x[0]), b = x[1] + dppf<0xB1>(x[1]); x[0] = (c & 1) ? b : a; }
        y[0] += dppf<0xB1>(y[0]);
        const float s1 = rbf(rbf(x[0]) * 0.07216878364870322f);
        const float s2 = (c >> 1) < nl ? rbf(rbf(y[0]) * 0.07216878364870322f) : -INFINITY;
        m = wave_max(fmaxf(s1, s2));
        const float e1 = __builtin_amdgcn_exp2f((s1 - m) * LOG2E), e2 = __builtin_amdgcn_exp2f((s2 - m) * LOG2E);
        *(LAS u32*)(L + wl + K_PX + l * 4) = pack2(e1, 0.f);
        *(LAS u32*)(L + wl + K_PX2 + (l >> 1) * 4) = pack2(e2, 0.f);
        lsum = wave_sum(e1 + ((c & 1) ? 0.f : e2));
        post();
        LGKM0();
        p0 = *(LAS v4u*)(L + wl + K_PX + (l >> 3) * 32); p1 = *(LAS v4u*)(L + wl + K_PX + (l >> 3) * 32 + 16); p2 = *(LAS v4u*)(L + wl + K_PX2 + (l >> 3) * 16);
    }
    if (w == 0) STAMP(4);
    float acc[24];
#pragma unroll
    for (int i = 0; i < 24; ++i) acc[i] = 0.f;
    {
        LAS v4u* VL = (LAS v4u*)(L + wl + K_V + l * 16);
        pv_unit(VL[0], VL[64], VL[128], p2[0], acc);
        pv_unit(VL[192], VL[256], VL[320], p2[1], acc);
        if (nl == 3) pv_unit(VL[384], VL[448], VL[512], p2[2], acc);
    }
#pragma unroll
    for (int jj = 0; jj < 6; ++jj) pv_unit(V[3 * jj], V[3 * jj + 1], V[3 * jj + 2], jj < 4 ? p0[jj & 3] : p1[jj & 3], acc);
    scale();
#pragma unroll
    for (int jj = 6; jj < 8; ++jj) pv_unit(V[3 * jj], V[3 * jj + 1], V[3 * jj + 2], p1[jj & 3], acc);
    if (w == 7) STAMP(6);
    float y[12], z[6];
#pragma unroll
    for (int n = 0; n < 12; ++n) { auto s = __builtin_amdgcn_permlane32_swap(asu(acc[n]), asu(acc[n + 12]), false, false); y[n] = asf(s[0]) + asf(s[1]); }
#pragma unroll
    for (int n = 0; n < 6; ++n) { auto s = __builtin_amdgcn_permlane16_swap(asu(y[n]), asu(y[n + 6]), false, false); z[n] = asf(s[0]) + asf(s[1]); }
#pragma unroll
    for (int n = 0; n < 3; ++n) {
        const float a = z[n] + dppf<0x128>(z[n]), b = z[n + 3] + dppf<0x128>(z[n + 3]);
        const int ai = 3 * (l >> 3) + n;
        *(LAS float*)(L + wl + (64 * (ai >> 3) + 8 * c + (ai & 7)) * 4) = ((l & 8) ? b : a) * fsc;
    }
    BAR();
    if (w == 0) STAMP(7);
    if (lead) {
        const u32 pb = slot * K_W + (l < 48 ? l : 47) * 16;
        f4 o = (*(LAS f4*)(L + pb) + *(LAS f4*)(L + pb + 2 * K_W)) + (*(LAS f4*)(L + pb + 4 * K_W) + *(LAS f4*)(L + pb + 6 * K_W));
        if (pos != 1) o += (*(LAS f4*)(L + pb + K_W) + *(LAS f4*)(L + pb + 3 * K_W)) + (*(LAS f4*)(L + pb + 5 * K_W) + *(LAS f4*)(L + pb + 7 * K_W));
        st_sc1(rws, l < 48 ? opb + l * 16 : OOB, canon4(o));
        if (w == 0) STAMP(8);
    }
}

enum { R_SA = 0, R_SB = 1, R_SC = 2 };
#define UNDEF4(v) asm volatile("" : "=v"(v))
template <int ROLE>
__device__ __forceinline__ void w_path(const Params& p, LAS unsigned char* L, int u, int w, int l) {
    const rsrc_t rws = __builtin_amdgcn_make_buffer_rsrc((void*)p.ws, 0, WS_END, 0x00020000);
    const int tid = threadIdx.x, g = l >> 4, tq = (l & 15) >> 2, b4 = l & 3;
    const u32 lbase = (u32)(size_t)L, stg = lbase + w * 16384;
    const bool hasF1 = u < 50, hasF2 = u < 60;
    const int kf = u / 10, lp = u - 10 * kf, kf2 = u / 12, cs = u - 12 * kf2;
    const int ri = ROLE == R_SA ? u : (ROLE == R_SB ? u - 30 : u - 54);
    const WStream sF1 = mk_stream(mk_srd(p.wm, hasF1 ? DM * 3840 * 2 : 0), (u32)((128 * kf + 32 * (w >> 1)) * 7680 + (128 * lp + 64 * (w & 1)) * 2), 7680, l);
    const WStream sF2 = mk_stream(mk_srd(p.wq, hasF2 ? DM * NQ * 2 : 0), (u32)(128 * kf2 * 12288 + (512 * cs + 64 * w) * 2), 12288, l);
    const int mid = ROLE == R_SC ? (w < 4 ? (ri >= 6 ? ri : -1) : 30 + ri) : (w < 4 ? (ri < 6 ? ri : ri + 4) : (ri == 20 ? 28 : (ri == 21 ? 29 : -1)));
    const bool hasM = ROLE != R_SA && mid >= 0;
    const WStream sM = mk_stream(mk_srd(p.wm, hasM ? DM * 3840 * 2 : 0), (u32)(160 * (w & 3) * 7680 + (1280 + 64 * mid) * 2), 7680, l);
    const int ra = ri / 10, ca = ri - 10 * ra;
    const WStream sT = ROLE == R_SA ? mk_stream(mk_srd(p.wo, NQ * DM * 2), (u32)((2048 * ra + 256 * w) * 1280 + ca * 128), 1280, l)
                     : ROLE == R_SB ? mk_stream(mk_srd((w & 1) ? p.wu : p.wg, DM * FF * 2), (u32)(160 * (w >> 1) * 3072 + ri * 128), 3072, l)
                                    : mk_stream(mk_srd(p.wd, FF * DM * 2), (u32)(192 * w * 1280 + ri * 128), 1280, l);
    constexpr int NM = ROLE == R_SA ? 0 : 5, NT = ROLE == R_SA ? 8 : (ROLE == R_SB ? 5 : 6), NA = NM + NT;
    u32 ta[4];
    { const u32 f = 2u * (tq >> 1) | 4u * (g & 1);
#pragma unroll
      for (int T = 0; T < 4; ++T) ta[T] = w * 16384 + (8 * g + tq) * 128 + ((((u32)(2 * T)) ^ f) + (b4 >> 1)) * 16 + (b4 & 1) * 8; }
    const u32 dcol = 16 * tq + 4 * g;
    const u32 f1o = hasF1 ? WS_F1P + (u32)((kf * 4 + b4) * 1280 + 128 * lp + 64 * w + dcol) * 4u : OOB;
    const u32 qpo = hasF2 ? WS_QP + (u32)((kf2 * 4 + b4) * NQ + 512 * cs + 64 * w + dcol) * 4u : OOB;
    const u32 mfo = hasM ? WS_MODF + (u32)(b4 * 2560 + 64 * mid + dcol) * 2u : OOB;
    const u32 outo = ROLE == R_SA ? WS_AP + (u32)((ra * 4 + b4) * DM + 64 * ca + dcol) * 4u : WS_HID + (u32)(b4 * FF + 64 * ri + dcol) * 2u;
    volatile LAS u32* misc = (volatile LAS u32*)(L + L_MISC);
    STAMP(0);
    if (w < 5) { dma_1k(mk_srd(p.c, 5120), l * 16, w * 1024, lbase + L_CS + w * 1024); dma_1k(mk_srd(p.x, 5120), l * 16, w * 1024, lbase + L_XS + w * 1024); }
    issue(sF1, 0, stg);
    { u32 keep;
      asm volatile("s_nop 4\n\ts_mov_b32 %0, m0\n\ts_mov_b32 m0, %5\n\ts_nop 0\n\tbuffer_load_dword %1, %2, %3 offen nt lds\n\tbuffer_load_dword %1, %4, 0 offen nt lds\n\ts_mov_b32 m0, %0"
                   : "=&s"(keep) : "v"(l == 0 ? 0u : OOB), "s"(sT.srd), "s"(sT.base), "s"(mk_srd(p.out, 5120)), "s"(lbase + L_GB) : "memory"); }
    if (w == 0) {
        u32 keep;
        asm volatile("s_nop 4\n\ts_mov_b32 %0, m0\n\ts_mov_b32 m0, %3\n\ts_nop 0\n\tbuffer_load_dword %1, %2, 0 offen sc1 lds\n\ts_mov_b32 m0, %0"
                     : "=&s"(keep) : "v"(l < 2 ? WS_E + l * 4 : OOB), "s"(mk_srd(p.ws, WS_END)), "s"(lbase + L_GA) : "memory");
    }
    { const v4u s4 = {SENT, SENT, SENT, SENT};
      st_sc1(rws, qpo, s4);
      if (w < 2) st_sc1(rws, f1o, s4);
      if (ROLE != R_SA && (w & 3) == 0) st_sc1_2(rws, mfo, v2u{SENT, SENT});
      if (w == 0) {
          if (ROLE == R_SA) st_sc1(rws, outo, s4);
          if (ROLE == R_SB) st_sc1_2(rws, outo, v2u{SENT, SENT});
      } }
    if (tid == 0) { misc[0] = 0; misc[1] = 0; }
    BAR();
    issue(sF2, 0, stg + 4096);
    auto issueB = [&](int n) { if (n < NM) issue(sM, n, stg + ((n + 1) & 3) * 4096); else issue(sT, n - NM, stg + ((n + 1) & 3) * 4096); };
    {
        v4u ft[NT][4];
        {
            VMW(4);
            BAR();
            STAMP(1);
            const u64 T = *(LAS u64*)(L + L_GA) + 1ull;
            const u32 tlo = (u32)T, thi = (u32)(T >> 32);
            if (tid == 0) __hip_atomic_store((GAS u64*)(p.ws + WS_FLAG) + blockIdx.x, T, RLX_AGENT);
            {
                v4u f[4]; read_unit(L, ta, 0, f);
                const v4u a = silu8(*(LAS v4u*)(L + L_CS + b4 * 1280 + (128 * kf + 32 * (w >> 1) + 8 * g) * 2));
                f4 d[4];
#pragma unroll
                for (int T2 = 0; T2 < 4; ++T2) d[T2] = mfma(f[T2], a, f4{0.f, 0.f, 0.f, 0.f});
                *(LAS f4*)(L + L_RED + w * 1024 + l * 16) = sel_tile(d, tq);
                if (w >= 4) {
                    float xv[10], s = 0.f;
#pragma unroll
                    for (int i = 0; i < 5; ++i) { const u32 t = *(LAS u32*)(L + L_XS + (w - 4) * 1280 + (l + 64 * i) * 4); xv[2 * i] = bflo(t); xv[2 * i + 1] = bfhi(t); s += xv[2 * i] + xv[2 * i + 1]; }
                    const float mean = wave_sum(s) * (1.f / DM);
                    float v = 0.f;
#pragma unroll
                    for (int i = 0; i < 10; ++i) v = fmaf(xv[i] - mean, xv[i] - mean, v);
                    const float var = wave_sum(v) * (1.f / DM);
                    if (l == 0) *(LAS v2u*)(L + L_ST + (w - 4) * 8) = v2u{asu(rbf(mean)), asu(rbf(__builtin_amdgcn_sqrtf(rbf(rbf(var) + 1e-5f))))};
                }
                BAR();
                if (w < 2) {
                    LAS f4* R = (LAS f4*)(L + L_RED + w * 1024 + l * 16);
                    st_sc1(rws, f1o, canon4((R[0] + R[128]) + (R[256] + R[384])));
                }
                if (w == 0) STAMP(2);
                issue(sF2, 1, stg + 8192); issue(sF2, 2, stg + 12288); issue(sF2, 3, stg);
            }
            if (w < 2) {
                for (int sp = 0; sp < SPIN_G; ++sp) {
                    MEMBAR();
                    const v2u f = ld_sc1_2(rws, WS_FLAG + l * 8);
                    if (__builtin_amdgcn_ballot_w64(f[0] != tlo || f[1] != thi) == 0ull) break;
                }
                if (w == 0) STAMP(19);
                const int hb = tid >> 5, hi = tid & 31, col = 128 * kf2 + 4 * hi;
                const u32 o = WS_F1P + (u32)(hb * 1280 + col) * 4u;
                v4u a[5] = {}, c2[5] = {};
                bool need = hasF2;
                for (int sp = 0; sp < SPIN_G; ++sp) {
                    MEMBAR();
                    if (need) {
                        u32 mx = 0;
#pragma unroll
                        for (int ks = 0; ks < 5; ++ks) { a[ks] = ld_sc1(rws, o + ks * 20480); c2[ks] = ld_sc1(rws, o + ks * 20480 + 2560); }
#pragma unroll
                        for (int ks = 0; ks < 5; ++ks) mx = vmax4(vmax4(mx, a[ks]), c2[ks]);
                        need = mx == SENT;
                    }
                    if (__builtin_amdgcn_ballot_w64(need) == 0ull) break;
                }
                const f4 sa = (((asf4(a[0]) + asf4(a[1])) + asf4(a[2])) + asf4(a[3])) + asf4(a[4]);
                const f4 sc = (((asf4(c2[0]) + asf4(c2[1])) + asf4(c2[2])) + asf4(c2[3])) + asf4(c2[4]);
                const v2u st = *(LAS v2u*)(L + L_ST + hb * 8), xr = *(LAS v2u*)(L + L_XS + hb * 1280 + (hasF2 ? col : 0) * 2);
                const float den = asf(st[1]), rc = __builtin_amdgcn_rcpf(den);
                const f4 e = rb4(unpk(xr) - asf(st[0])), q1 = e * rc;
                const f4 h = rb4(rb4(fma4(fma4(-q1, bc4(den), e), bc4(rc), q1)) * rb4(rb4(sc) + 1.f)) + rb4(sa);
                *(LAS v2u*)(L + L_HB + hb * 256 + hi * 8) = v2u{pack2(h[0], h[1]), pack2(h[2], h[3])};
                LGKM0();
                if (l == 0) __hip_atomic_fetch_add((LAS u32*)(L + L_MISC), 1u, __ATOMIC_RELAXED, __HIP_MEMORY_SCOPE_WORKGROUP);
                if (w == 0) STAMP(4);
            }
            {
                v4u fq[4][4];
                VMW(12); read_unit(L, ta, 1, fq[0]); LGKM0();
                VMW(8); read_unit(L, ta, 2, fq[1]);
                VMW(4); read_unit(L, ta, 3, fq[2]);
                issueB(0);
                VMW(4); read_unit(L, ta, 0, fq[3]);
                if (w == 7) STAMP(3);
                for (int sp = 0; misc[0] < 2u && sp < SPIN_L; ++sp) __builtin_amdgcn_s_sleep(1);
                MEMBAR();
                f4 d[4] = {};
#pragma unroll
                for (int n = 0; n < 4; ++n) {
                    const v4u a = *(LAS v4u*)(L + L_HB + b4 * 256 + (32 * n + 8 * g) * 2);
#pragma unroll
                    for (int T2 = 0; T2 < 4; ++T2) d[T2] = mfma(fq[n][T2], a, d[T2]);
                }
                st_sc1(rws, qpo, canon4(sel_tile(d, tq)));
                BAR();
                if (w == 7) STAMP(5);
                issueB(1); issueB(2); issueB(3);
            }
            if (NM) {
                f4 d[4] = {};
#pragma unroll
                for (int n = 0; n < NM; ++n) {
                    VMW(12);
                    v4u f[4]; read_unit(L, ta, (n + 1) & 3, f);
                    const v4u a = silu8(*(LAS v4u*)(L + L_CS + b4 * 1280 + (160 * (w & 3) + 32 * n + 8 * g) * 2));
                    LGKM0();
                    issueB(n + 4);
#pragma unroll
                    for (int T2 = 0; T2 < 4; ++T2) d[T2] = mfma(f[T2], a, d[T2]);
                }
                *(LAS f4*)(L + L_RED + w * 1024 + l * 16) = sel_tile(d, tq);
                BAR();
                if ((w & 3) == 0) {
                    LAS f4* R = (LAS f4*)(L + L_RED + w * 1024 + l * 16);
                    const f4 s = (R[0] + R[64]) + (R[128] + R[192]);
                    st_sc1_2(rws, mfo, v2u{canon(pack2(s[0], s[1])), canon(pack2(s[2], s[3]))});
                }
                BAR();
            }
#pragma unroll
            for (int n = 0; n < NT; ++n) {
                const int a = NM + n, younger = (NA - 1 - a) < 3 ? (NA - 1 - a) : 3;
                if (younger == 3) VMW(12); else if (younger == 2) VMW(8); else if (younger == 1) VMW(4); else VMW(0);
                read_unit(L, ta, (a + 1) & 3, ft[n]);
                if (a + 4 < NA) { LGKM0(); issueB(a + 4); }
                if (ROLE == R_SA && n == 1 && w == 0) {
                    for (int sp = 0; sp < SPIN_G; ++sp) {
                        MEMBAR();
                        const v4u f0 = ld_sc1(rws, WS_FLAG + l * 16), f1 = ld_sc1(rws, WS_FLAG + 1024 + l * 16);
                        const bool bad = f0[0] != tlo || f0[1] != thi || f0[2] != tlo || f0[3] != thi || f1[0] != tlo || f1[1] != thi || f1[2] != tlo || f1[3] != thi;
                        if (__builtin_amdgcn_ballot_w64(bad) == 0ull) break;
                    }
                    if (u == 0 && l == 0) __hip_atomic_store((GAS u64*)(p.ws + WS_E), T, RLX_AGENT);
                }
            }
            LGKM0();
            STAMP(20 + w);
            BAR();
            STAMP(6);
        }
        if (ROLE == R_SA) {
            f4 res[2];
            UNDEF4(res[0]); UNDEF4(res[1]);
            u32 oo[2], mo[2];
#pragma unroll
            for (int r = 0; r < 2; ++r) {
                const int row = 2048 * ra + 256 * w + 32 * (4 * r + tq) + 8 * g, hd = row / DH, dd = row - hd * DH;
                oo[r] = WS_OP + (u32)((b4 * 32 + hd) * 2) * 768u + dd * 4; mo[r] = WS_ML + (u32)(hd * 4 + b4) * 16u;
            }
            bool done[2] = {false, false}, wdone[2] = {false, false}, need[2] = {true, true}, needm[2] = {true, true};
            v4u a[2][4], ml[2];
            float w0[2], w1[2];
#pragma unroll
            for (int r = 0; r < 2; ++r) {
                UNDEF4(ml[r]); UNDEF4(w0[r]); UNDEF4(w1[r]);
#pragma unroll
                for (int i = 0; i < 4; ++i) UNDEF4(a[r][i]);
            }
            for (int sp = 0; sp < SPIN_G && !(done[0] && done[1]); ++sp) {
                MEMBAR();
#pragma unroll
                for (int r = 0; r < 2; ++r) {
                    if (needm[r]) ml[r] = ld_sc1(rws, mo[r]);
                    else if (need[r]) { a[r][0] = ld_sc1(rws, oo[r]); a[r][1] = ld_sc1(rws, oo[r] + 16); a[r][2] = ld_sc1(rws, oo[r] + 768); a[r][3] = ld_sc1(rws, oo[r] + 784); }
                }
                const bool last = sp == SPIN_G - 1;
                if (!(wdone[0] && wdone[1])) __builtin_amdgcn_s_sleep(POLL_SLEEP);
#pragma unroll
                for (int r = 0; r < 2; ++r) {
                    const bool hadm = !needm[r];
                    if (!wdone[r]) {
                        needm[r] = vmax4(0u, ml[r]) == SENT;
                        if (__builtin_amdgcn_ballot_w64(needm[r]) == 0ull || last) {
                            const float m0 = asf(ml[r][0]), l0 = asf(ml[r][1]), m1 = asf(ml[r][2]), l1 = asf(ml[r][3]), M = fmaxf(m0, m1);
                            const float e0 = __builtin_amdgcn_exp2f((m0 - M) * LOG2E), e1 = __builtin_amdgcn_exp2f((m1 - M) * LOG2E);
                            const float inv = fdiv(1.f, fmaf(e0, l0, e1 * l1));
                            w0[r] = e0 * inv; w1[r] = e1 * inv; wdone[r] = true;
                        }
                    }
                    if (!done[r]) {
                        need[r] = !hadm || vmax4(vmax4(vmax4(vmax4(0u, a[r][0]), a[r][1]), a[r][2]), a[r][3]) == SENT;
                        if ((__builtin_amdgcn_ballot_w64(need[r]) == 0ull && wdone[r]) || last) {
                            const f4 x0 = fma4(asf4(a[r][0]), bc4(w0[r]), asf4(a[r][2]) * w1[r]), x1 = fma4(asf4(a[r][1]), bc4(w0[r]), asf4(a[r][3]) * w1[r]);
                            const v4u bq = {pack2(x0[0], x0[1]), pack2(x0[2], x0[3]), pack2(x1[0], x1[1]), pack2(x1[2], x1[3])};
                            const v4u bk[4] = {bq, ror4k<1>(bq), ror4k<2>(bq), ror4k<3>(bq)};
                            f4 d[4] = {};
#pragma unroll
                            for (int s = 0; s < 4; ++s)
#pragma unroll
                                for (int T2 = 0; T2 < 4; ++T2) d[(T2 + s) & 3] = mfma(ft[4 * r + s][(T2 + s) & 3], bk[T2], d[(T2 + s) & 3]);
                            res[r] = sel_tile(d, tq);
                            done[r] = true;
                        }
                    }
                }
            }
            if (w == 0) STAMP(7);
            *(LAS f4*)(L + L_RED + w * 1024 + l * 16) = res[0] + res[1];
            BAR();
            if (w == 0) {
                STAMP(8);
                LAS f4* R = (LAS f4*)(L + L_RED + l * 16);
                st_sc1(rws, outo, canon4(((R[0] + R[64]) + (R[128] + R[192])) + ((R[256] + R[320]) + (R[384] + R[448]))));
                STAMP(9);
            }
        } else if (ROLE == R_SB) {
            const int i16 = l & 15, col = 128 * w + 8 * i16;
            f4 xa, xb, c1a, c1b, sba, sbb;
            if (w < 5) {
                v4u gav, sbv, cbv, a[6];
                const u32 mo = WS_MODF + (u32)(g * 2560 + col) * 2u;
                for (int sp = 0; sp < SPIN_G; ++sp) {
                    MEMBAR();
                    gav = ld_sc1(rws, mo); sbv = ld_sc1(rws, mo + 1280); cbv = ld_sc1(rws, mo + 2560);
                    if (all_valid(vmax4(vmax4(vmax4(0u, gav), sbv), cbv))) break;
                }
                const v4u xr = *(LAS v4u*)(L + L_XS + g * 1280 + col * 2);
                xa = unpk(v2u{xr[0], xr[1]}); xb = unpk(v2u{xr[2], xr[3]});
                f4 gaa = unpk(v2u{gav[0], gav[1]}), gab = unpk(v2u{gav[2], gav[3]});
                c1a = rb4(unpk(v2u{cbv[0], cbv[1]}) + 1.f); c1b = rb4(unpk(v2u{cbv[2], cbv[3]}) + 1.f);
                sba = unpk(v2u{sbv[0], sbv[1]}); sbb = unpk(v2u{sbv[2], sbv[3]});
                asm volatile("" : "+v"(xa), "+v"(xb), "+v"(gaa), "+v"(gab), "+v"(c1a), "+v"(c1b), "+v"(sba), "+v"(sbb));
                if (w == 0) STAMP(10);
                const u32 ao = WS_AP + (u32)(g * DM + col) * 4u;
#pragma unroll
                for (int r = 0; r < 6; ++r) UNDEF4(a[r]);
                bool need = true;
                for (int sp = 0; sp < SPIN_G; ++sp) {
                    MEMBAR();
                    if (need) {
                        u32 mx = 0;
#pragma unroll
                        for (int r = 0; r < 3; ++r) { a[2 * r] = ld_sc1(rws, ao + r * 10240); a[2 * r + 1] = ld_sc1(rws, ao + r * 10240 + 16); }
#pragma unroll
                        for (int r = 0; r < 6; ++r) mx = vmax4(mx, a[r]);
                        need = mx == SENT;
                    }
                    if (__builtin_amdgcn_ballot_w64(need) == 0ull) break;
                }
                if (w == 0) STAMP(11);
                xa = rb4(xa + rb4(gaa * rb4((asf4(a[0]) + asf4(a[2])) + asf4(a[4]))));
                xb = rb4(xb + rb4(gab * rb4((asf4(a[1]) + asf4(a[3])) + asf4(a[5]))));
                const f4 t = xa + xb;
                const float mw = row_sum((t[0] + t[1]) + (t[2] + t[3])) * (1.f / 128.f);
                const f4 da = xa - mw, db = xb - mw, q2 = fma4(da, da, db * db);
                const float v = row_sum((q2[0] + q2[1]) + (q2[2] + q2[3]));
                *(LAS v2u*)(L + L_ST + 64 + g * 48 + w * 8) = v2u{asu(mw), asu(v)};
            }
            BAR();
            if (w < 5) {
                const f4 s0 = *(LAS f4*)(L + L_ST + 64 + g * 48), s1 = *(LAS f4*)(L + L_ST + 64 + g * 48 + 16);
                const f2 s2 = *(LAS f2*)(L + L_ST + 64 + g * 48 + 32);
                const float mean = (((s0[0] + s0[2]) + (s1[0] + s1[2])) + s2[0]) * 0.2f;
                const f4 dm = f4{s0[0], s0[2], s1[0], s1[2]} - mean, dq = dm * dm; const float d4 = s2[0] - mean;
                const float M2 = fmaf(128.f, fmaf(d4, d4, (dq[0] + dq[1]) + (dq[2] + dq[3])), ((s0[1] + s0[3]) + (s1[1] + s1[3])) + s2[1]);
                const float mb = rbf(mean), den = rbf(__builtin_amdgcn_sqrtf(rbf(rbf(M2 * (1.f / DM)) + 1e-5f))), rc = __builtin_amdgcn_rcpf(den);
                const f4 ea = rb4(xa - mb), eb = rb4(xb - mb), qa = ea * rc, qb = eb * rc;
                const f4 ha = rb4(rb4(fma4(fma4(-qa, bc4(den), ea), bc4(rc), qa)) * c1a) + sba, hb = rb4(rb4(fma4(fma4(-qb, bc4(den), eb), bc4(rc), qb)) * c1b) + sbb;
                *(LAS v4u*)(L + L_HB + g * 1280 + col * 2) = v4u{pack2(ha[0], ha[1]), pack2(ha[2], ha[3]), pack2(hb[0], hb[1]), pack2(hb[2], hb[3])};
            }
            BAR();
            if (w == 0) STAMP(12);
            f4 d[4] = {};
#pragma unroll
            for (int n = 0; n < 5; ++n) {
                const v4u a = *(LAS v4u*)(L + L_HB + b4 * 1280 + (160 * (w >> 1) + 32 * n + 8 * g) * 2);
#pragma unroll
                for (int T2 = 0; T2 < 4; ++T2) d[T2] = mfma(ft[n][T2], a, d[T2]);
            }
            *(LAS f4*)(L + L_RED + w * 1024 + l * 16) = sel_tile(d, tq);
            BAR();
            if (w == 0) {
                STAMP(13);
                LAS f4* R = (LAS f4*)(L + L_RED + l * 16);
                const f4 gt = rb4((R[0] + R[128]) + (R[256] + R[384])), up = rb4((R[64] + R[192]) + (R[320] + R[448]));
                const f4 mk = {gt[0] > 0.f ? 1.f : 0.f, gt[1] > 0.f ? 1.f : 0.f, gt[2] > 0.f ? 1.f : 0.f, gt[3] > 0.f ? 1.f : 0.f};
                const f4 hh = (gt * mk) * up;
                st_sc1_2(rws, outo, v2u{canon(pack2(hh[0], hh[1])), canon(pack2(hh[2], hh[3]))});
                STAMP(14);
            }
        } else {
            f4 x1, gbf;
            UNDEF4(x1); UNDEF4(gbf);
            if (w == 0) {
                v2u ga, gb;
                const u32 mo = WS_MODF + (u32)(b4 * 2560 + 64 * ri + dcol) * 2u;
                for (int sp = 0; sp < SPIN_G; ++sp) {
                    MEMBAR();
                    ga = ld_sc1_2(rws, mo); gb = ld_sc1_2(rws, mo + 3840);
                    if (all_valid(umax3(umax3(ga[0], ga[1], gb[0]), gb[1], 0u))) break;
                }
                const u32 ao = WS_AP + (u32)(b4 * DM + 64 * ri + dcol) * 4u;
                f4 s;
                for (int sp = 0; sp < SPIN_G; ++sp) {
                    MEMBAR();
                    const v4u p0 = ld_sc1(rws, ao), p1 = ld_sc1(rws, ao + 10240), p2 = ld_sc1(rws, ao + 20480);
                    s = (asf4(p0) + asf4(p1)) + asf4(p2);
                    if (all_valid(vmax4(vmax4(vmax4(0u, p0), p1), p2))) break;
                }
                x1 = rb4(unpk(*(LAS v2u*)(L + L_XS + b4 * 1280 + (64 * ri + dcol) * 2)) + rb4(unpk(ga) * rb4(s)));
                gbf = unpk(gb);
                asm volatile("" : "+v"(x1), "+v"(gbf));
                STAMP(15);
            }
            v4u a0, a1;
            UNDEF4(a0); UNDEF4(a1);
            const u32 h0 = WS_HID + (u32)(b4 * FF + 192 * w + 32 * tq + 8 * g) * 2u, h1 = tq < 2 ? h0 + 256 : OOB;
            bool need = true;
            for (int sp = 0; sp < SPIN_G; ++sp) {
                MEMBAR();
                if (need) { a0 = ld_sc1(rws, h0); a1 = ld_sc1(rws, h1); need = vmax4(vmax4(0u, a0), a1) == SENT; }
                if (__builtin_amdgcn_ballot_w64(need) == 0ull) break;
            }
            if (w == 0) STAMP(16);
            const v4u bk[4] = {a0, ror4k<1>(a0), ror4k<2>(a0), ror4k<3>(a0)}, bl[4] = {a1, ror4k<1>(a1), ror4k<2>(a1), ror4k<3>(a1)};
            f4 d[4] = {};
#pragma unroll
            for (int k = 0; k < 4; ++k)
#pragma unroll
                for (int s = 0; s < 6; ++s) d[(s + k) & 3] = mfma(ft[s][(s + k) & 3], s < 4 ? bk[k] : bl[k], d[(s + k) & 3]);
            *(LAS f4*)(L + L_RED + w * 1024 + l * 16) = sel_tile(d, tq);
            BAR();
            if (w == 0) {
                STAMP(17);
                LAS f4* R = (LAS f4*)(L + L_RED + l * 16);
                const f4 o = x1 + rb4(gbf * rb4(((R[0] + R[64]) + (R[128] + R[192])) + ((R[256] + R[320]) + (R[384] + R[448]))));
                __builtin_amdgcn_raw_buffer_store_b64(v2u{pack2(o[0], o[1]), pack2(o[2], o[3])}, __builtin_amdgcn_make_buffer_rsrc((void*)p.out, 0, 4 * DM * 2, 0x00020000), (u32)(b4 * DM + 64 * ri + dcol) * 2u, 0, OUTAUX);
                STAMP(18);
            }
        }
    }
}

typedef const __attribute__((address_space(4))) Params* kargp;
#define GET_PARAMS() kargp pp_ = kp; asm volatile("" : "+s"(pp_)); \
    const Params q_ = {pp_->x, pp_->kc, pp_->vc, pp_->wq, pp_->wo, pp_->wg, pp_->wu, pp_->wd, pp_->c, pp_->wm, pp_->out, pp_->ws};
__global__ void __launch_bounds__(512, 2) fused_block(Params p) {
    extern __shared__ __attribute__((aligned(16))) unsigned char smem[];
    LAS unsigned char* L = (LAS unsigned char*)smem;
    const kargp kp = (kargp)__builtin_amdgcn_kernarg_segment_ptr();
    asm volatile("s_load_dword s98, %0, 0x0\n\ts_load_dword s99, %0, 0x40" :: "s"(kp) : "s98", "s99");
    const int l = threadIdx.x & 63, w = __builtin_amdgcn_readfirstlane(threadIdx.x >> 6), c = blockIdx.x;
    if (__builtin_expect(c >= NW, 1)) { GET_PARAMS(); kv_path(q_, L, c - NW, w, l); }
    else if (c < 30) { GET_PARAMS(); w_path<R_SA>(q_, L, c, w, l); }
    else if (c < 54) { GET_PARAMS(); w_path<R_SB>(q_, L, c, w, l); }
    else { GET_PARAMS(); w_path<R_SC>(q_, L, c, w, l); }
}

extern "C" void kernel_launch(void* const* d_in, const int* in_sizes, int n_in, void* d_out, int out_size, void* d_ws, size_t ws_size, hipStream_t stream) {
    Params p;
    p.x = (const u16*)d_in[0]; p.kc = (const u16*)d_in[1]; p.vc = (const u16*)d_in[2]; p.wq = (const u16*)d_in[3]; p.wo = (const u16*)d_in[4];
    p.wg = (const u16*)d_in[6]; p.wu = (const u16*)d_in[7]; p.wd = (const u16*)d_in[8]; p.c = (const u16*)d_in[9]; p.wm = (const u16*)d_in[10];
    p.out = (u16*)d_out; p.ws = (unsigned char*)d_ws;
    (void)hipFuncSetAttribute((const void*)fused_block, hipFuncAttributeMaxDynamicSharedMemorySize, (int)L_END);
    hipLaunchKernelGGL(fused_block, dim3(256), dim3(512), L_END, stream, p);
}
```

```cpp
#include <hip/hip_runtime.h>
#include <stdint.h>
#pragma clang fp contract(off)

typedef unsigned short u16;
typedef unsigned u32;
typedef unsigned long long u64;
typedef u32 v4u __attribute__((ext_vector_type(4)));
typedef u32 v2u __attribute__((ext_vector_type(2)));
typedef float f4 __attribute__((ext_vector_type(4)));
typedef short v4s __attribute__((ext_vector_type(4)));
typedef __bf16 bf8 __attribute__((ext_vector_type(8)));
typedef __bf16 bf2 __attribute__((ext_vector_type(2)));
#define LAS __attribute__((address_space(3)))
#define GAS __attribute__((address_space(1)))
#define RLX_AGENT __ATOMIC_RELAXED, __HIP_MEMORY_SCOPE_AGENT

#define PRETOUCH 1
#define POLL_SLEEP 2
#define SLA 0
#define SLB 0
#define FRQ 30
#define FRS 6
#define GATE 1
#define GATE0 1
#define ILV 1
#define JIT 0
#define JIT_B 64
#define JIT_C 160
#define INIT_MODE 1

constexpr int DM = 640, NH = 32, SKV = 4096, DH = 192, WIN = 1024, FF = 1536, NQ = NH * DH;
constexpr u32 SENT = 0xFFFFFFFFu;
constexpr int NW = 64;
constexpr u32 WS_E = 0, WS_FLAG = 4096, WS_F1P = 8192, WS_MODF = WS_F1P + 5 * 4 * 1280 * 4, WS_QP = WS_MODF + 4 * 2560 * 2,
              WS_OP = WS_QP + 5 * 4 * NQ * 4, WS_ML = WS_OP + 128 * 2 * DH * 2, WS_AP = WS_ML + 32 * 4 * 16, WS_HID = WS_AP + 3 * 4 * DM * 4,
              WS_END = WS_HID + 4 * FF * 2;
constexpr u32 OOB = 0x7FFFFF00u;
constexpr u32 L_XS = 131072, L_CS = L_XS + 5120, L_HB = L_CS + 5120, L_RED = L_HB + 5120, L_ST = L_RED + 8192, L_GA = L_ST + 256, L_GB = L_GA + 1024,
              L_X1 = L_GB + 1024, L_MISC = L_X1 + 1024, L_STAMP = L_MISC + 64, L_END = L_STAMP + 256;
constexpr u32 K_W = 18432, K_PX = 1024, K_PX2 = 1536, K_V = 9216, K_QS = 8 * K_W, K_ML = K_QS + 768, K_END = K_ML + 64;
static_assert(K_END <= L_MISC, "LDS");
constexpr float LOG2E = 1.4426950408889634f;
static_assert(L_END <= 163840, "LDS");
constexpr int SPIN_G = 40000, SPIN_L = 2000000;

#define STAMP(i) do {} while (0)

struct Params { const u16 *x, *kc, *vc, *wq, *wo, *wg, *wu, *wd, *c, *wm; u16* out; unsigned char* ws; };

__device__ __forceinline__ v4u mk_srd(const void* p, u32 bytes) { const u64 a = (u64)p; return v4u{(u32)a, (u32)(a >> 32) & 0xffffu, bytes, 0x00020000u}; }
__device__ __forceinline__ float asf(u32 u) { return __builtin_bit_cast(float, u); }
__device__ __forceinline__ u32 asu(float f) { return __builtin_bit_cast(u32, f); }
__device__ __forceinline__ float bflo(u32 u) { return asf(u << 16); }
__device__ __forceinline__ float bfhi(u32 u) { return asf(u & 0xffff0000u); }
__device__ __forceinline__ u32 pack2(float lo, float hi) { bf2 v; v[0] = (__bf16)lo; v[1] = (__bf16)hi; return __builtin_bit_cast(u32, v); }
__device__ __forceinline__ float rbf(float f) { return (float)(__bf16)f; }
typedef float f2 __attribute__((ext_vector_type(2)));
__device__ __forceinline__ f2 rb2(f2 v) { return f2{rbf(v[0]), rbf(v[1])}; }
__device__ __forceinline__ f4 rb4(f4 v) {
    float a = v[0], b = v[1], c = v[2], d = v[3];
    asm("v_cvt_pk_bf16_f32 %0, 0, %0\n\tv_cvt_pk_bf16_f32 %1, 0, %1\n\tv_cvt_pk_bf16_f32 %2, 0, %2\n\tv_cvt_pk_bf16_f32 %3, 0, %3" : "+v"(a), "+v"(b), "+v"(c), "+v"(d));
    return f4{a, b, c, d}; }
__device__ __forceinline__ f4 unpk(v2u v) { return f4{bflo(v[0]), bfhi(v[0]), bflo(v[1]), bfhi(v[1])}; }
__device__ __forceinline__ f4 fma4(f4 a, f4 b, f4 c) { return __builtin_elementwise_fma(a, b, c); }
__device__ __forceinline__ f4 bc4(float x) { return f4{x, x, x, x}; }
__device__ __forceinline__ u32 canon(u32 u) { return u < 0xFFFFFFFEu ? u : 0xFFFFFFFEu; }
__device__ __forceinline__ v4u canon4(f4 v) { return v4u{canon(asu(v[0])), canon(asu(v[1])), canon(asu(v[2])), canon(asu(v[3]))}; }
__device__ __forceinline__ u32 umax3(u32 a, u32 b, u32 c) { const u32 m = a > b ? a : b; return m > c ? m : c; }
__device__ __forceinline__ u32 vmax4(u32 m, v4u v) { return umax3(umax3(m, v[0], v[1]), v[2], v[3]); }
__device__ __forceinline__ bool all_valid(u32 m) { return __builtin_amdgcn_ballot_w64(m == SENT) == 0ull; }
__device__ __forceinline__ float dot2(u32 a, u32 b, float c) { return __builtin_amdgcn_fdot2_f32_bf16(__builtin_bit_cast(bf2, a), __builtin_bit_cast(bf2, b), c, false); }
__device__ __forceinline__ f4 mfma(v4u a, v4u b, f4 c) { return __builtin_amdgcn_mfma_f32_16x16x32_bf16(__builtin_bit_cast(bf8, a), __builtin_bit_cast(bf8, b), c, 0, 0, 0); }
template <int CTRL> __device__ __forceinline__ u32 dppu(u32 x) { return (u32)__builtin_amdgcn_mov_dpp((int)x, CTRL, 0xf, 0xf, true); }
template <int CTRL> __device__ __forceinline__ float dppf(float x) { return asf(dppu<CTRL>(asu(x))); }
template <int K> __device__ __forceinline__ v4u ror4k(v4u v) {
    if constexpr (K == 0) return v; else return v4u{dppu<0x120 + 4 * K>(v[0]), dppu<0x120 + 4 * K>(v[1]), dppu<0x120 + 4 * K>(v[2]), dppu<0x120 + 4 * K>(v[3])}; }
__device__ __forceinline__ float row_sum(float x) { x += dppf<0x128>(x); x += dppf<0x124>(x); x += dppf<0x122>(x); x += dppf<0x121>(x); return x; }
__device__ __forceinline__ float row_max(float x) { x = fmaxf(x, dppf<0x128>(x)); x = fmaxf(x, dppf<0x124>(x)); x = fmaxf(x, dppf<0x122>(x)); x = fmaxf(x, dppf<0x121>(x)); return x; }
__device__ __forceinline__ float xrow_sum(float x) {
    auto s = __builtin_amdgcn_permlane16_swap(asu(x), asu(x), false, false); x = asf(s[0]) + asf(s[1]);
    auto t = __builtin_amdgcn_permlane32_swap(asu(x), asu(x), false, false); return asf(t[0]) + asf(t[1]); }
__device__ __forceinline__ float xrow_max(float x) {
    auto s = __builtin_amdgcn_permlane16_swap(asu(x), asu(x), false, false); x = fmaxf(asf(s[0]), asf(s[1]));
    auto t = __builtin_amdgcn_permlane32_swap(asu(x), asu(x), false, false); return fmaxf(asf(t[0]), asf(t[1])); }
__device__ __forceinline__ float wave_sum(float x) { return xrow_sum(row_sum(x)); }
__device__ __forceinline__ float wave_max(float x) { return xrow_max(row_max(x)); }
__device__ __forceinline__ float fdiv(float a, float b) { const float r = __builtin_amdgcn_rcpf(b); const float q = a * r; return fmaf(fmaf(-q, b, a), r, q); }
__device__ __forceinline__ float silu_bf(float v) { return fdiv(v, 1.f + __builtin_amdgcn_exp2f(fminf(-v * 1.4426950408889634f, 126.f))); }
__device__ __forceinline__ v4u silu8(v4u c) {
    v4u r;
#pragma unroll
    for (int i = 0; i < 4; ++i) r[i] = pack2(silu_bf(bflo(c[i])), silu_bf(bfhi(c[i])));
    return r; }
#define BAR() asm volatile("s_waitcnt lgkmcnt(0)\n\ts_barrier" ::: "memory")
#define LGKM0() asm volatile("s_waitcnt lgkmcnt(0)" ::: "memory")
#define VMW(n) asm volatile("s_waitcnt vmcnt(%0)" :: "i"(n) : "memory")
#define MEMBAR() asm volatile("" ::: "memory")
typedef __amdgpu_buffer_rsrc_t rsrc_t;
__device__ __forceinline__ v4u ld_sc1(rsrc_t r, u32 off) { return __builtin_amdgcn_raw_buffer_load_b128(r, off, 0, 16); }
__device__ __forceinline__ v2u ld_sc1_2(rsrc_t r, u32 off) { return __builtin_amdgcn_raw_buffer_load_b64(r, off, 0, 16); }
__device__ __forceinline__ void st_sc1(rsrc_t r, u32 off, v4u v) { __builtin_amdgcn_raw_buffer_store_b128(v, r, off, 0, 16); }
__device__ __forceinline__ void st_sc1_2(rsrc_t r, u32 off, v2u v) { __builtin_amdgcn_raw_buffer_store_b64(v, r, off, 0, 16); }
__device__ __forceinline__ f4 asf4(v4u v) { return __builtin_bit_cast(f4, v); }

__device__ __forceinline__ void dma_unit(v4u srd, u32 v0, u32 v1, u32 sA, u32 sB, u32 lds) {
    u32 keep;
    asm volatile("s_nop 4\n\ts_mov_b32 %0, m0\n\ts_mov_b32 m0, %6\n\ts_nop 0\n\t"
                 "buffer_load_dwordx4 %1, %3, %4 offen nt lds\n\ts_add_u32 m0, m0, 0x400\n\ts_nop 0\n\t"
                 "buffer_load_dwordx4 %2, %3, %4 offen nt lds\n\ts_add_u32 m0, m0, 0x400\n\ts_nop 0\n\t"
                 "buffer_load_dwordx4 %1, %3, %5 offen nt lds\n\ts_add_u32 m0, m0, 0x400\n\ts_nop 0\n\t"
                 "buffer_load_dwordx4 %2, %3, %5 offen nt lds\n\ts_mov_b32 m0, %0"
                 : "=&s"(keep) : "v"(v0), "v"(v1), "s"(srd), "s"(sA), "s"(sB), "s"(lds) : "memory", "scc");
}
__device__ __forceinline__ void dma_1k(v4u srd, u32 voff, u32 soff, u32 lds) {
    u32 keep;
    asm volatile("s_nop 4\n\ts_mov_b32 %0, m0\n\ts_mov_b32 m0, %4\n\ts_nop 0\n\tbuffer_load_dwordx4 %1, %2, %3 offen lds\n\ts_mov_b32 m0, %0"
                 : "=&s"(keep) : "v"(voff), "s"(srd), "s"(soff), "s"(lds) : "memory");
}
struct WStream { v4u srd; u32 base, pitch, v0, v1; };
template <bool HALF = false>
__device__ __forceinline__ WStream mk_stream(v4u srd, u32 base, u32 pitch, int l) {
    WStream s; s.srd = srd; s.base = base; s.pitch = pitch;
    const u32 sw = HALF ? 2u * ((l >> 4) & 3) : 2u * ((l >> 4) & 1);
    s.v0 = (l >> 3) * pitch + (((l & 7) ^ sw) << 4);
    s.v1 = (8 + (l >> 3)) * pitch + (((l & 7) ^ (HALF ? sw : (sw | 4u))) << 4);
    return s; }
__device__ __forceinline__ void issue(const WStream& s, int n, u32 lds) { const u32 a = s.base + (u32)n * 32u * s.pitch; dma_unit(s.srd, s.v0, s.v1, a, a + 16u * s.pitch, lds); }
template <int HOFF = 512>
__device__ __forceinline__ void read_unit(LAS unsigned char* L, const u32 (&ta)[4], int slot, v4u (&f)[4]) {
#pragma unroll
    for (int T = 0; T < 4; ++T) {
        const v2u lo = __builtin_bit_cast(v2u, __builtin_amdgcn_ds_read_tr16_b64_v4i16((LAS v4s*)(L + ta[T] + slot * 4096)));
        const v2u hi = __builtin_bit_cast(v2u, __builtin_amdgcn_ds_read_tr16_b64_v4i16((LAS v4s*)(L + ta[T] + slot * 4096 + HOFF)));
        f[T] = v4u{lo[0], lo[1], hi[0], hi[1]};
    } }
__device__ __forceinline__ f4 sel_tile(const f4 (&a)[4], int t) {
    const bool b0 = t & 1, b1 = t & 2;
    f4 r;
#pragma unroll
    for (int i = 0; i < 4; ++i) { const float lo = b0 ? a[1][i] : a[0][i], hi = b0 ? a[3][i] : a[2][i]; r[i] = b1 ? hi : lo; }
    return r; }

__device__ __forceinline__ void dma_kv(v4u srd, u32 vo, u32 so, u32 lds) {
    u32 keep;
    asm volatile("s_nop 4\n\ts_mov_b32 %0, m0\n\ts_mov_b32 m0, %4\n\ts_nop 0\n\t"
                 "buffer_load_dwordx4 %1, %2, %3 offen nt lds\n\ts_add_u32 m0, m0, 0x380\n\ts_nop 0\n\t"
                 "buffer_load_dwordx4 %1, %2, %3 offen offset:128 nt lds\n\ts_add_u32 m0, m0, 0x380\n\ts_nop 0\n\t"
                 "buffer_load_dwordx4 %1, %2, %3 offen offset:256 nt lds\n\ts_mov_b32 m0, %0"
                 : "=&s"(keep) : "v"(vo), "s"(srd), "s"(so), "s"(lds) : "memory", "scc");
}
__device__ __forceinline__ float qk_unit(v4u k0, v4u k1, v4u k2, const v4u (&q)[3]) {
    float s = 0.f, t = 0.f;
#pragma unroll
    for (int r = 0; r < 4; r += 2) { s = dot2(k0[r], q[0][r], s); t = dot2(k0[r + 1], q[0][r + 1], t); }
#pragma unroll
    for (int r = 0; r < 4; r += 2) { s = dot2(k1[r], q[1][r], s); t = dot2(k1[r + 1], q[1][r + 1], t); }
#pragma unroll
    for (int r = 0; r < 4; r += 2) { s = dot2(k2[r], q[2][r], s); t = dot2(k2[r + 1], q[2][r + 1], t); }
    return s + t;
}
__device__ __forceinline__ void pv_unit(v4u v0, v4u v1, v4u v2, u32 plo, float (&acc)[24]) {
    const u32 phi = plo << 16;
#pragma unroll
    for (int r = 0; r < 4; ++r) {
        acc[2 * r] = dot2(v0[r], plo, acc[2 * r]); acc[2 * r + 1] = dot2(v0[r], phi, acc[2 * r + 1]);
        acc[8 + 2 * r] = dot2(v1[r], plo, acc[8 + 2 * r]); acc[8 + 2 * r + 1] = dot2(v1[r], phi, acc[8 + 2 * r + 1]);
        acc[16 + 2 * r] = dot2(v2[r], plo, acc[16 + 2 * r]); acc[16 + 2 * r + 1] = dot2(v2[r], phi, acc[16 + 2 * r + 1]);
    }
}

__device__ __forceinline__ void kv_path(const Params& p, LAS unsigned char* L, int j, int w, int l) {
    const rsrc_t rws = __builtin_amdgcn_make_buffer_rsrc((void*)p.ws, 0, WS_END, 0x00020000);
    const rsrc_t rk = __builtin_amdgcn_make_buffer_rsrc((void*)p.kc, 0, 4 * NH * SKV * DH * 2, 0x00020000);
    const rsrc_t rv = __builtin_amdgcn_make_buffer_rsrc((void*)p.vc, 0, 4 * NH * SKV * DH * 2, 0x00020000);
    volatile LAS u32* misc = (volatile LAS u32*)(L + L_MISC);
    const int rk3 = (j >> 3) / 3, t3 = (j & 7) * 8 + rk3, pos = (j >> 3) - 3 * rk3;
    const int slot = pos == 1 ? (w & 1) : 0;
    const int bh = 2 * t3 + (pos == 2 ? 1 : (pos == 1 ? slot : 0));
    const int part = pos == 0 ? 0 : (pos == 2 ? 1 : (slot == 0 ? 1 : 0));
    const int wi = pos == 1 ? (w >> 1) : w;
    const int ub = pos == 0 ? 0 : (pos == 2 ? 43 : (slot == 0 ? 85 : 0));
    const int ng = pos == 1 ? 2 : 4, u1 = ub + (wi >= ng ? 11 * ng + wi - ng : wi);
    const u32 ust = ng * 3072;
    const int nl = (pos == 1 ? wi == 3 : wi >= 5) ? 2 : 3;
    const bool lead = w == 0 || (w == 1 && pos == 1);
    const u32 opb = WS_OP + (bh * 2 + part) * 384, mlb = WS_ML + ((bh & 31) * 4 + (bh >> 5)) * 16 + part * 8;
    if (threadIdx.x < 12) misc[threadIdx.x] = 0;
    BAR();
    STAMP(0);
    if (w == 4) {
        __builtin_amdgcn_s_setprio(3);
        const u64 T = __hip_atomic_load((GAS u64*)(p.ws + WS_E), RLX_AGENT) + 1ull;
        st_sc1(rws, l < 24 ? opb + l * 16 : OOB, v4u{SENT, SENT, SENT, SENT});
        st_sc1_2(rws, l == 0 ? mlb : OOB, v2u{SENT, SENT});
        const int bh1 = 2 * t3 + 1;
        st_sc1(rws, (pos == 1 && l < 24) ? WS_OP + bh1 * 2 * 384 + l * 16 : OOB, v4u{SENT, SENT, SENT, SENT});
        st_sc1_2(rws, (pos == 1 && l == 0) ? WS_ML + ((bh1 & 31) * 4 + (bh1 >> 5)) * 16 : OOB, v2u{SENT, SENT});
        VMW(0);
        if (l == 0) {
            __hip_atomic_store((GAS u64*)(p.ws + WS_FLAG) + blockIdx.x, T, RLX_AGENT);
            misc[4] = (u32)T; misc[5] = (u32)(T >> 32); LGKM0(); misc[2] = 1;
        }
        STAMP(9);
        __builtin_amdgcn_s_setprio(0);
    }
    const u32 vo = (l >> 3) * 384 + (l & 7) * 16;
    const u32 base = ((u32)bh * SKV + (SKV - WIN)) * 384u + (u32)u1 * 3072u;
    const u32 wl = w * K_W;
    const int c = l & 7;
    float m, lsum, fsc;
    const u32 nslot = pos == 1 ? 4u : 8u;
    auto slot_stats = [&](float& M, float& mi, float& li) {
        const v2u t = *(LAS v2u*)(L + K_ML + c * 8);
        const bool mine = pos != 1 || (l & 1) == slot;
        mi = mine ? asf(t[0]) : -INFINITY; li = mine ? asf(t[1]) : 0.f;
        M = fmaxf(mi, dppf<0xB1>(mi)); M = fmaxf(M, dppf<0x4E>(M)); M = fmaxf(M, dppf<0x141>(M));
    };
    auto post = [&]() {
        u32 old = 0;
        if (l == 0) { *(LAS v2u*)(L + K_ML + w * 8) = v2u{asu(m), asu(lsum)}; LGKM0(); old = __hip_atomic_fetch_add((LAS u32*)(L + L_MISC + 32 + slot * 4), 1u, __ATOMIC_RELAXED, __HIP_MEMORY_SCOPE_WORKGROUP); }
        if (__builtin_amdgcn_readfirstlane(old) == nslot - 1u) {
            MEMBAR();
            float M, mi, li; slot_stats(M, mi, li);
            float e = li * __builtin_amdgcn_exp2f((mi - M) * LOG2E);
            e += dppf<0xB1>(e); e += dppf<0x4E>(e); e += dppf<0x141>(e);
            st_sc1_2(rws, l == 0 ? mlb : OOB, v2u{canon(asu(M)), canon(asu(e))});
            STAMP(5);
        }
    };
    auto scale = [&]() {
        for (int sp = 0; misc[8 + slot] < nslot && sp < SPIN_L; ++sp) __builtin_amdgcn_s_sleep(1);
        MEMBAR();
        float M, mi, li; slot_stats(M, mi, li);
        float e = li * __builtin_amdgcn_exp2f((mi - M) * LOG2E);
        e += dppf<0xB1>(e); e += dppf<0x4E>(e); e += dppf<0x141>(e);
        fsc = fdiv(__builtin_amdgcn_exp2f((m - M) * LOG2E), e);
    };
    {
        const v4u sk = mk_srd(p.kc, 4 * NH * SKV * DH * 2), sv = mk_srd(p.vc, 4 * NH * SKV * DH * 2);
        const u32 lb = (u32)(size_t)L + wl;
        dma_kv(sk, vo, base + 8 * ust, lb); dma_kv(sk, vo, base + 9 * ust, lb + 3072);
        if (nl == 3) dma_kv(sk, vo, base + 10 * ust, lb + 6144);
        dma_kv(sv, vo, base + 8 * ust, lb + K_V); dma_kv(sv, vo, base + 9 * ust, lb + K_V + 3072);
        if (nl == 3) dma_kv(sv, vo, base + 10 * ust, lb + K_V + 6144);
    }
    v4u K[24], V[24];
#pragma unroll
    for (int jj = 0; jj < 8; ++jj)
#pragma unroll
        for (int i = 0; i < 3; ++i) K[jj * 3 + i] = __builtin_amdgcn_raw_buffer_load_b128(rk, vo + i * 128, base + jj * ust, 2);
#pragma unroll
    for (int jj = 0; jj < 8; ++jj)
#pragma unroll
        for (int i = 0; i < 3; ++i) V[jj * 3 + i] = __builtin_amdgcn_raw_buffer_load_b128(rv, vo + i * 128, base + jj * ust, 2);
    __builtin_amdgcn_sched_barrier(0);
    if (w == 0) STAMP(1);
    if (w == 7) STAMP(2);
    if (w < 4) {
        for (int sp = 0; misc[2] == 0 && sp < SPIN_L; ++sp) __builtin_amdgcn_s_sleep(1);
        const u32 tlo = misc[4], thi = misc[5];
        for (int sp = 0; sp < SPIN_G && misc[6] == 0; ++sp) {
            MEMBAR();
            const v2u f = ld_sc1_2(rws, WS_FLAG + l * 8);
            if (__builtin_amdgcn_ballot_w64(f[0] != tlo || f[1] != thi) == 0ull) misc[6] = 1;
        }
        const u32 qo = WS_QP + (u32)((bh >> 5) * NQ + (bh & 31) * DH + 4 * l) * 4u;
        v4u a[5] = {};
        bool need = l < 48;
        for (int sp = 0; sp < SPIN_G; ++sp) {
            MEMBAR();
            if (__builtin_amdgcn_readfirstlane(misc[slot]) != 0u) break;
            if (need) {
                u32 mx = 0;
#pragma unroll
                for (int ks = 0; ks < 5; ++ks) a[ks] = ld_sc1(rws, qo + ks * (4 * NQ * 4));
#pragma unroll
                for (int ks = 0; ks < 5; ++ks) mx = vmax4(mx, a[ks]);
                need = mx == SENT;
            }
            if (__builtin_amdgcn_ballot_w64(need) == 0ull || sp == SPIN_G - 1) {
                const f4 qs = (((asf4(a[0]) + asf4(a[1])) + asf4(a[2])) + asf4(a[3])) + asf4(a[4]);
                if (l < 48) *(LAS v2u*)(L + K_QS + slot * 384 + l * 8) = v2u{pack2(qs[0], qs[1]), pack2(qs[2], qs[3])};
                LGKM0();
                misc[slot] = 1;
                break;
            }
        }
        if (w == 0) STAMP(3);
    } else {
        for (int sp = 0; misc[slot] == 0 && sp < SPIN_L; ++sp) __builtin_amdgcn_s_sleep(1);
    }
    VMW(48);
    v4u p0, p1, p2;
    {
        v4u q[3];
#pragma unroll
        for (int i = 0; i < 3; ++i) q[i] = *(LAS v4u*)(L + K_QS + slot * 384 + (c + 8 * i) * 16);
        float y[4], x[8];
        LAS v4u* KL = (LAS v4u*)(L + wl + l * 16);
#pragma unroll
        for (int jj = 0; jj < 3; ++jj) { y[jj] = qk_unit(KL[jj * 192], KL[jj * 192 + 64], KL[jj * 192 + 128], q); __builtin_amdgcn_sched_barrier(0); }
#pragma unroll
        for (int jj = 0; jj < 8; ++jj) x[jj] = qk_unit(K[3 * jj], K[3 * jj + 1], K[3 * jj + 2], q);
#pragma unroll
        for (int h = 0; h < 4; ++h) { const float a = x[h] + dppf<0x141>(x[h]), b = x[h + 4] + dppf<0x141>(x[h + 4]); x[h] = (c & 4) ? b : a; }
        { const float a = y[0] + dppf<0x141>(y[0]), b = y[2] + dppf<0x141>(y[2]), a1 = y[1] + dppf<0x141>(y[1]); y[0] = (c & 4) ? b : a; y[1] = a1; }
#pragma unroll
        for (int h = 0; h < 2; ++h) { const float a = x[h] + dppf<0x4E>(x[h]), b = x[h + 2] + dppf<0x4E>(x[h + 2]); x[h] = (c & 2) ? b : a; }
        { const float a = y[0] + dppf<0x4E>(y[0]), b = y[1] + dppf<0x4E>(y[1]); y[0] = (c & 2) ? b : a; }
        { const float a = x[0] + dppf<0xB1>(x[0]), b = x[1] + dppf<0xB1>(x[1]); x[0] = (c & 1) ? b : a; }
        y[0] += dppf<0xB1>(y[0]);
        const float s1 = rbf(rbf(x[0]) * 0.07216878364870322f);
        const float s2 = (c >> 1) < nl ? rbf(rbf(y[0]) * 0.07216878364870322f) : -INFINITY;
        m = wave_max(fmaxf(s1, s2));
        const float e1 = __builtin_amdgcn_exp2f((s1 - m) * LOG2E), e2 = __builtin_amdgcn_exp2f((s2 - m) * LOG2E);
        *(LAS u32*)(L + wl + K_PX + l * 4) = pack2(e1, 0.f);
        *(LAS u32*)(L + wl + K_PX2 + (l >> 1) * 4) = pack2(e2, 0.f);
        lsum = wave_sum(e1 + ((c & 1) ? 0.f : e2));
        post();
        LGKM0();
        p0 = *(LAS v4u*)(L + wl + K_PX + (l >> 3) * 32); p1 = *(LAS v4u*)(L + wl + K_PX + (l >> 3) * 32 + 16); p2 = *(LAS v4u*)(L + wl + K_PX2 + (l >> 3) * 16);
    }
    if (w == 0) STAMP(4);
    float acc[24];
#pragma unroll
    for (int i = 0; i < 24; ++i) acc[i] = 0.f;
    {
        LAS v4u* VL = (LAS v4u*)(L + wl + K_V + l * 16);
        pv_unit(VL[0], VL[64], VL[128], p2[0], acc);
        pv_unit(VL[192], VL[256], VL[320], p2[1], acc);
        if (nl == 3) pv_unit(VL[384], VL[448], VL[512], p2[2], acc);
    }
#pragma unroll
    for (int jj = 0; jj < 6; ++jj) pv_unit(V[3 * jj], V[3 * jj + 1], V[3 * jj + 2], jj < 4 ? p0[jj & 3] : p1[jj & 3], acc);
    scale();
#pragma unroll
    for (int jj = 6; jj < 8; ++jj) pv_unit(V[3 * jj], V[3 * jj + 1], V[3 * jj + 2], p1[jj & 3], acc);
    if (w == 7) STAMP(6);
    float y[12], z[6];
#pragma unroll
    for (int n = 0; n < 12; ++n) { auto s = __builtin_amdgcn_permlane32_swap(asu(acc[n]), asu(acc[n + 12]), false, false); y[n] = asf(s[0]) + asf(s[1]); }
#pragma unroll
    for (int n = 0; n < 6; ++n) { auto s = __builtin_amdgcn_permlane16_swap(asu(y[n]), asu(y[n + 6]), false, false); z[n] = asf(s[0]) + asf(s[1]); }
#pragma unroll
    for (int n = 0; n < 3; ++n) {
        const float a = z[n] + dppf<0x128>(z[n]), b = z[n + 3] + dppf<0x128>(z[n + 3]);
        const int ai = 3 * (l >> 3) + n;
        *(LAS float*)(L + wl + (64 * (ai >> 3) + 8 * c + (ai & 7)) * 4) = ((l & 8) ? b : a) * fsc;
    }
    BAR();
    if (w == 0) STAMP(7);
    if (lead) {
        const u32 pb = slot * K_W + (l < 48 ? l : 47) * 16;
        f4 o = (*(LAS f4*)(L + pb) + *(LAS f4*)(L + pb + 2 * K_W)) + (*(LAS f4*)(L + pb + 4 * K_W) + *(LAS f4*)(L + pb + 6 * K_W));
        if (pos != 1) o += (*(LAS f4*)(L + pb + K_W) + *(LAS f4*)(L + pb + 3 * K_W)) + (*(LAS f4*)(L + pb + 5 * K_W) + *(LAS f4*)(L + pb + 7 * K_W));
        st_sc1_2(rws, l < 48 ? opb + l * 8 : OOB, v2u{canon(pack2(o[0], o[1])), canon(pack2(o[2], o[3]))});
        if (w == 0) STAMP(8); else STAMP(10);
    }
}

enum { R_SA = 0, R_SB = 1, R_SC = 2 };
#define UNDEF4(v) asm volatile("" : "=v"(v))
template <int ROLE>
__device__ __forceinline__ void w_path(const Params& p, LAS unsigned char* L, int u, int w, int l) {
    const rsrc_t rws = __builtin_amdgcn_make_buffer_rsrc((void*)p.ws, 0, WS_END, 0x00020000);
    const int tid = threadIdx.x, g = l >> 4, tq = (l & 15) >> 2, b4 = l & 3;
    const u32 lbase = (u32)(size_t)L, stg = lbase + w * 16384;
    const bool hasF1 = u < 50, hasF2 = u < 60;
    const int kf = u / 10, lp = u - 10 * kf, kf2 = u / 12, cs = u - 12 * kf2;
    const int ri = ROLE == R_SA ? u : (ROLE == R_SB ? u - 30 : u - 54);
    const WStream sF1 = mk_stream(mk_srd(p.wm, hasF1 ? DM * 3840 * 2 : 0), (u32)((128 * kf + 32 * (w >> 1)) * 7680 + (128 * lp + 64 * (w & 1)) * 2), 7680, l);
    const WStream sF2 = mk_stream(mk_srd(p.wq, hasF2 ? DM * NQ * 2 : 0), (u32)(128 * kf2 * 12288 + (512 * cs + 64 * w) * 2), 12288, l);
    const int mid = ROLE == R_SC ? (w < 4 ? (ri >= 6 ? ri : -1) : 30 + ri) : (w < 4 ? (ri < 6 ? ri : ri + 4) : (ri == 20 ? 28 : (ri == 21 ? 29 : -1)));
    const bool hasM = ROLE != R_SA && mid >= 0;
    const WStream sM = mk_stream(mk_srd(p.wm, hasM ? DM * 3840 * 2 : 0), (u32)(160 * (w & 3) * 7680 + (1280 + 64 * mid) * 2), 7680, l);
    const int ra = ri / 10, ca = ri - 10 * ra;
    const WStream sT = ROLE == R_SA ? mk_stream(mk_srd(p.wo, NQ * DM * 2), (u32)((2048 * ra + 256 * w) * 1280 + ca * 128), 1280, l)
                     : ROLE == R_SB ? mk_stream(mk_srd((w & 1) ? p.wu : p.wg, DM * FF * 2), (u32)(160 * (w >> 1) * 3072 + ri * 128), 3072, l)
                                    : mk_stream(mk_srd(p.wd, FF * DM * 2), (u32)(192 * w * 1280 + ri * 128), 1280, l);
    constexpr int NM = ROLE == R_SA ? 0 : 5, NT = ROLE == R_SA ? 8 : (ROLE == R_SB ? 5 : 6), NA = NM + NT;
    u32 ta[4];
    { const u32 f = 2u * (tq >> 1) | 4u * (g & 1);
#pragma unroll
      for (int T = 0; T < 4; ++T) ta[T] = w * 16384 + (8 * g + tq) * 128 + ((((u32)(2 * T)) ^ f) + (b4 >> 1)) * 16 + (b4 & 1) * 8; }
    const u32 dcol = 16 * tq + 4 * g;
    const u32 f1o = hasF1 ? WS_F1P + (u32)((kf * 4 + b4) * 1280 + 128 * lp + 64 * w + dcol) * 4u : OOB;
    const u32 qpo = hasF2 ? WS_QP + (u32)((kf2 * 4 + b4) * NQ + 512 * cs + 64 * w + dcol) * 4u : OOB;
    const u32 mfo = hasM ? WS_MODF + (u32)(b4 * 2560 + 64 * mid + dcol) * 2u : OOB;
    const u32 outo = ROLE == R_SA ? WS_AP + (u32)((ra * 4 + b4) * DM + 64 * ca + dcol) * 4u : WS_HID + (u32)(b4 * FF + 64 * ri + dcol) * 2u;
    volatile LAS u32* misc = (volatile LAS u32*)(L + L_MISC);
    STAMP(0);
    if (w < 5) { dma_1k(mk_srd(p.c, 5120), l * 16, w * 1024, lbase + L_CS + w * 1024); dma_1k(mk_srd(p.x, 5120), l * 16, w * 1024, lbase + L_XS + w * 1024); }
    issue(sF1, 0, stg);
    { u32 keep;
      asm volatile("s_nop 4\n\ts_mov_b32 %0, m0\n\ts_mov_b32 m0, %5\n\ts_nop 0\n\tbuffer_load_dword %1, %2, %3 offen nt lds\n\tbuffer_load_dword %1, %4, 0 offen nt lds\n\ts_mov_b32 m0, %0"
                   : "=&s"(keep) : "v"(l == 0 ? 0u : OOB), "s"(sT.srd), "s"(sT.base), "s"(mk_srd(p.out, 5120)), "s"(lbase + L_GB) : "memory"); }
    if (w == 0) {
        u32 keep;
        asm volatile("s_nop 4\n\ts_mov_b32 %0, m0\n\ts_mov_b32 m0, %3\n\ts_nop 0\n\tbuffer_load_dword %1, %2, 0 offen sc1 lds\n\ts_mov_b32 m0, %0"
                     : "=&s"(keep) : "v"(l < 2 ? WS_E + l * 4 : OOB), "s"(mk_srd(p.ws, WS_END)), "s"(lbase + L_GA) : "memory");
    }
    { const v4u s4 = {SENT, SENT, SENT, SENT};
      st_sc1(rws, qpo, s4);
      if (w < 2) st_sc1(rws, f1o, s4);
      if (ROLE != R_SA && (w & 3) == 0) st_sc1_2(rws, mfo, v2u{SENT, SENT});
      if (w == 0) {
          if (ROLE == R_SA) st_sc1(rws, outo, s4);
          if (ROLE == R_SB) st_sc1_2(rws, outo, v2u{SENT, SENT});
      } }
    if (tid == 0) { misc[0] = 0; misc[1] = 0; }
    BAR();
    issue(sF2, 0, stg + 4096);
    auto issueB = [&](int n) { if (n < NM) issue(sM, n, stg + ((n + 1) & 3) * 4096); else issue(sT, n - NM, stg + ((n + 1) & 3) * 4096); };
    {
        v4u ft[NT][4];
        {
            VMW(4);
            BAR();
            STAMP(1);
            const u64 T = *(LAS u64*)(L + L_GA) + 1ull;
            const u32 tlo = (u32)T, thi = (u32)(T >> 32);
            if (tid == 0) __hip_atomic_store((GAS u64*)(p.ws + WS_FLAG) + blockIdx.x, T, RLX_AGENT);
            {
                v4u f[4]; read_unit(L, ta, 0, f);
                const v4u a = silu8(*(LAS v4u*)(L + L_CS + b4 * 1280 + (128 * kf + 32 * (w >> 1) + 8 * g) * 2));
                f4 d[4];
#pragma unroll
                for (int T2 = 0; T2 < 4; ++T2) d[T2] = mfma(f[T2], a, f4{0.f, 0.f, 0.f, 0.f});
                *(LAS f4*)(L + L_RED + w * 1024 + l * 16) = sel_tile(d, tq);
                if (w >= 4) {
                    float xv[10], s = 0.f;
#pragma unroll
                    for (int i = 0; i < 5; ++i) { const u32 t = *(LAS u32*)(L + L_XS + (w - 4) * 1280 + (l + 64 * i) * 4); xv[2 * i] = bflo(t); xv[2 * i + 1] = bfhi(t); s += xv[2 * i] + xv[2 * i + 1]; }
                    const float mean = wave_sum(s) * (1.f / DM);
                    float v = 0.f;
#pragma unroll
                    for (int i = 0; i < 10; ++i) v = fmaf(xv[i] - mean, xv[i] - mean, v);
                    const float var = wave_sum(v) * (1.f / DM);
                    if (l == 0) *(LAS v2u*)(L + L_ST + (w - 4) * 8) = v2u{asu(rbf(mean)), asu(rbf(__builtin_amdgcn_sqrtf(rbf(rbf(var) + 1e-5f))))};
                }
                BAR();
                if (w < 2) {
                    LAS f4* R = (LAS f4*)(L + L_RED + w * 1024 + l * 16);
                    st_sc1(rws, f1o, canon4((R[0] + R[128]) + (R[256] + R[384])));
                }
                if (w == 0) STAMP(2);
                issue(sF2, 1, stg + 8192); issue(sF2, 2, stg + 12288); issue(sF2, 3, stg);
            }
            if (w < 2) {
                for (int sp = 0; sp < SPIN_G; ++sp) {
                    MEMBAR();
                    const v2u f = ld_sc1_2(rws, WS_FLAG + l * 8);
                    if (__builtin_amdgcn_ballot_w64(f[0] != tlo || f[1] != thi) == 0ull) break;
                }
                if (w == 0) STAMP(19);
                const int hb = tid >> 5, hi = tid & 31, col = 128 * kf2 + 4 * hi;
                const u32 o = WS_F1P + (u32)(hb * 1280 + col) * 4u;
                v4u a[5] = {}, c2[5] = {};
                bool need = hasF2;
                for (int sp = 0; sp < SPIN_G; ++sp) {
                    MEMBAR();
                    if (need) {
                        u32 mx = 0;
#pragma unroll
                        for (int ks = 0; ks < 5; ++ks) { a[ks] = ld_sc1(rws, o + ks * 20480); c2[ks] = ld_sc1(rws, o + ks * 20480 + 2560); }
#pragma unroll
                        for (int ks = 0; ks < 5; ++ks) mx = vmax4(vmax4(mx, a[ks]), c2[ks]);
                        need = mx == SENT;
                    }
                    if (__builtin_amdgcn_ballot_w64(need) == 0ull) break;
                }
                const f4 sa = (((asf4(a[0]) + asf4(a[1])) + asf4(a[2])) + asf4(a[3])) + asf4(a[4]);
                const f4 sc = (((asf4(c2[0]) + asf4(c2[1])) + asf4(c2[2])) + asf4(c2[3])) + asf4(c2[4]);
                const v2u st = *(LAS v2u*)(L + L_ST + hb * 8), xr = *(LAS v2u*)(L + L_XS + hb * 1280 + (hasF2 ? col : 0) * 2);
                const float den = asf(st[1]), rc = __builtin_amdgcn_rcpf(den);
                const f4 e = rb4(unpk(xr) - asf(st[0])), q1 = e * rc;
                const f4 h = rb4(rb4(fma4(fma4(-q1, bc4(den), e), bc4(rc), q1)) * rb4(rb4(sc) + 1.f)) + rb4(sa);
                *(LAS v2u*)(L + L_HB + hb * 256 + hi * 8) = v2u{pack2(h[0], h[1]), pack2(h[2], h[3])};
                LGKM0();
                if (l == 0) __hip_atomic_fetch_add((LAS u32*)(L + L_MISC), 1u, __ATOMIC_RELAXED, __HIP_MEMORY_SCOPE_WORKGROUP);
                if (w == 0) STAMP(4);
            }
            {
                v4u fq[4][4];
                VMW(12); read_unit(L, ta, 1, fq[0]); LGKM0();
                VMW(8); read_unit(L, ta, 2, fq[1]);
                VMW(4); read_unit(L, ta, 3, fq[2]);
                issueB(0);
                VMW(4); read_unit(L, ta, 0, fq[3]);
                if (w == 7) STAMP(3);
                for (int sp = 0; misc[0] < 2u && sp < SPIN_L; ++sp) __builtin_amdgcn_s_sleep(1);
                MEMBAR();
                f4 d[4] = {};
#pragma unroll
                for (int n = 0; n < 4; ++n) {
                    const v4u a = *(LAS v4u*)(L + L_HB + b4 * 256 + (32 * n + 8 * g) * 2);
#pragma unroll
                    for (int T2 = 0; T2 < 4; ++T2) d[T2] = mfma(fq[n][T2], a, d[T2]);
                }
                st_sc1(rws, qpo, canon4(sel_tile(d, tq)));
                BAR();
                if (w == 7) STAMP(5);
                issueB(1); issueB(2); issueB(3);
            }
            constexpr bool LATE = JIT && NM;
            if (NM) {
                f4 d[4] = {};
#pragma unroll
                for (int n = 0; n < NM; ++n) {
                    if (!LATE || n < 2) VMW(12); else if (n == 2) VMW(8); else if (n == 3) VMW(4); else VMW(0);
                    v4u f[4]; read_unit(L, ta, (n + 1) & 3, f);
                    const v4u a = silu8(*(LAS v4u*)(L + L_CS + b4 * 1280 + (160 * (w & 3) + 32 * n + 8 * g) * 2));
                    LGKM0();
                    if (!LATE || n == 0) issueB(n + 4);
#pragma unroll
                    for (int T2 = 0; T2 < 4; ++T2) d[T2] = mfma(f[T2], a, d[T2]);
                }
                *(LAS f4*)(L + L_RED + w * 1024 + l * 16) = sel_tile(d, tq);
                BAR();
                if ((w & 3) == 0) {
                    LAS f4* R = (LAS f4*)(L + L_RED + w * 1024 + l * 16);
                    const f4 s = (R[0] + R[64]) + (R[128] + R[192]);
                    st_sc1_2(rws, mfo, v2u{canon(pack2(s[0], s[1])), canon(pack2(s[2], s[3]))});
                }
                if (LATE && w == 1) {
                    for (int sp = 0; sp < SPIN_G; ++sp) {
                        MEMBAR();
                        const v4u e0 = ld_sc1(rws, WS_ML + l * 16), e1 = ld_sc1(rws, WS_ML + 1024 + l * 16);
                        const int cnt = __builtin_popcountll(__builtin_amdgcn_ballot_w64(e0[0] != SENT)) + __builtin_popcountll(__builtin_amdgcn_ballot_w64(e0[2] != SENT))
                                      + __builtin_popcountll(__builtin_amdgcn_ballot_w64(e1[0] != SENT)) + __builtin_popcountll(__builtin_amdgcn_ballot_w64(e1[2] != SENT));
                        if (cnt >= (ROLE == R_SB ? JIT_B : JIT_C)) break;
                        __builtin_amdgcn_s_sleep(8);
                    }
                }
                BAR();
                if (LATE) { if (w == 0) STAMP(29); issueB(5); issueB(6); issueB(7); issueB(8); }
            }
#pragma unroll
            for (int n = 0; n < NT; ++n) {
                const int a = NM + n, younger = (NA - 1 - a) < 3 ? (NA - 1 - a) : 3;
                if (younger == 3) VMW(12); else if (younger == 2) VMW(8); else if (younger == 1) VMW(4); else VMW(0);
                read_unit(L, ta, (a + 1) & 3, ft[n]);
                if (a + 4 < NA) { LGKM0(); issueB(a + 4); }
                if (ROLE == R_SA && n == 1 && w == 0) {
                    for (int sp = 0; sp < SPIN_G; ++sp) {
                        MEMBAR();
                        const v4u f0 = ld_sc1(rws, WS_FLAG + l * 16), f1 = ld_sc1(rws, WS_FLAG + 1024 + l * 16);
                        const bool bad = f0[0] != tlo || f0[1] != thi || f0[2] != tlo || f0[3] != thi || f1[0] != tlo || f1[1] != thi || f1[2] != tlo || f1[3] != thi;
                        if (__builtin_amdgcn_ballot_w64(bad) == 0ull) break;
                    }
                    if (u == 0 && l == 0) __hip_atomic_store((GAS u64*)(p.ws + WS_E), T, RLX_AGENT);
                }
            }
            LGKM0();
            BAR();
            STAMP(6);
        }
        if (ROLE == R_SA) {
            f4 res[2];
            UNDEF4(res[0]); UNDEF4(res[1]);
            u32 oo[2], mo[2];
#pragma unroll
            for (int r = 0; r < 2; ++r) {
                const int row = 2048 * ra + 256 * w + 32 * (4 * r + tq) + 8 * g, hd = row / DH, dd = row - hd * DH;
                oo[r] = WS_OP + (u32)((b4 * 32 + hd) * 2) * 384u + dd * 2; mo[r] = WS_ML + (u32)(hd * 4 + b4) * 16u;
            }
            bool done[2] = {false, false}, wdone[2] = {false, false}, obad[2] = {true, true}, mbad[2] = {true, true};
            v4u a[2][2], ml[2];
            float w0[2], w1[2];
#pragma unroll
            for (int r = 0; r < 2; ++r) { UNDEF4(ml[r]); UNDEF4(w0[r]); UNDEF4(w1[r]); UNDEF4(a[r][0]); UNDEF4(a[r][1]); }
            for (int sp = 0; sp < SPIN_G && !(done[0] && done[1]); ++sp) {
                MEMBAR();
                bool ldo[2];
#pragma unroll
                for (int r = 0; r < 2; ++r) {
                    ldo[r] = obad[r] && (!mbad[r] || (GATE0 && sp == 0) || !GATE);
                    if (mbad[r]) ml[r] = ld_sc1(rws, mo[r]);
                    if (ldo[r]) { a[r][0] = ld_sc1(rws, oo[r]); a[r][1] = ld_sc1(rws, oo[r] + 384); }
                }
                const bool last = sp == SPIN_G - 1;
#pragma unroll
                for (int r = 0; r < 2; ++r) {
                    if (!wdone[r]) {
                        mbad[r] = vmax4(0u, ml[r]) == SENT;
                        if (__builtin_amdgcn_ballot_w64(mbad[r]) == 0ull || last) {
                            const float m0 = asf(ml[r][0]), m1 = asf(ml[r][2]), M = fmaxf(m0, m1);
                            const float e0 = asf(ml[r][1]) * __builtin_amdgcn_exp2f((m0 - M) * LOG2E), e1 = asf(ml[r][3]) * __builtin_amdgcn_exp2f((m1 - M) * LOG2E);
                            const float inv = fdiv(1.f, e0 + e1);
                            w0[r] = e0 * inv; w1[r] = e1 * inv; wdone[r] = true;
                        }
                    }
                    if (!done[r]) {
                        obad[r] = obad[r] && (!ldo[r] || vmax4(vmax4(0u, a[r][0]), a[r][1]) == SENT);
                        if ((__builtin_amdgcn_ballot_w64(obad[r]) == 0ull && wdone[r]) || last) {
                            STAMP(20 + w);
                            const f4 x0 = fma4(unpk(v2u{a[r][0][0], a[r][0][1]}), bc4(w0[r]), unpk(v2u{a[r][1][0], a[r][1][1]}) * w1[r]);
                            const f4 x1 = fma4(unpk(v2u{a[r][0][2], a[r][0][3]}), bc4(w0[r]), unpk(v2u{a[r][1][2], a[r][1][3]}) * w1[r]);
                            const v4u bq = {pack2(x0[0], x0[1]), pack2(x0[2], x0[3]), pack2(x1[0], x1[1]), pack2(x1[2], x1[3])};
                            const v4u bk[4] = {bq, ror4k<1>(bq), ror4k<2>(bq), ror4k<3>(bq)};
                            f4 d[4] = {};
#pragma unroll
                            for (int s = 0; s < 4; ++s)
#pragma unroll
                                for (int T2 = 0; T2 < 4; ++T2) d[(T2 + s) & 3] = mfma(ft[4 * r + s][(T2 + s) & 3], bk[T2], d[(T2 + s) & 3]);
                            res[r] = sel_tile(d, tq);
                            done[r] = true;
                        }
                    }
                }
            }
            if (w == 0) STAMP(7);
            *(LAS f4*)(L + L_RED + w * 1024 + l * 16) = res[0] + res[1];
            BAR();
            if (w == 0) {
                STAMP(8);
                LAS f4* R = (LAS f4*)(L + L_RED + l * 16);
                st_sc1(rws, outo, canon4(((R[0] + R[64]) + (R[128] + R[192])) + ((R[256] + R[320]) + (R[384] + R[448]))));
                STAMP(9);
            }
        } else if (ROLE == R_SB) {
            const int i16 = l & 15, col = 128 * w + 4 * i16;
            f4 xa, xb, c1a, c1b, sba, sbb;
            if (w < 5) {
                v2u gv[2], sv[2], cv[2]; v4u a[6];
                const u32 mo = WS_MODF + (u32)(g * 2560 + col) * 2u;
                for (int sp = 0; sp < SPIN_G; ++sp) {
                    MEMBAR();
                    u32 mx = 0;
#pragma unroll
                    for (int h = 0; h < 2; ++h) { gv[h] = ld_sc1_2(rws, mo + h * 128); sv[h] = ld_sc1_2(rws, mo + 1280 + h * 128); cv[h] = ld_sc1_2(rws, mo + 2560 + h * 128); }
#pragma unroll
                    for (int h = 0; h < 2; ++h) mx = umax3(umax3(umax3(mx, gv[h][0], gv[h][1]), sv[h][0], sv[h][1]), cv[h][0], cv[h][1]);
                    if (all_valid(mx)) break;
                }
                xa = unpk(*(LAS v2u*)(L + L_XS + g * 1280 + col * 2)); xb = unpk(*(LAS v2u*)(L + L_XS + g * 1280 + col * 2 + 128));
                f4 gaa = unpk(gv[0]), gab = unpk(gv[1]);
                c1a = rb4(unpk(cv[0]) + 1.f); c1b = rb4(unpk(cv[1]) + 1.f);
                sba = unpk(sv[0]); sbb = unpk(sv[1]);
                asm volatile("" : "+v"(xa), "+v"(xb), "+v"(gaa), "+v"(gab), "+v"(c1a), "+v"(c1b), "+v"(sba), "+v"(sbb));
                if (w == 0) STAMP(10);
                const u32 ao = WS_AP + (u32)(g * DM + col) * 4u;
#pragma unroll
                for (int r = 0; r < 6; ++r) UNDEF4(a[r]);
                bool need = true;
                for (int sp = 0; sp < SPIN_G; ++sp) {
                    MEMBAR();
                    if (need) {
                        u32 mx = 0;
#pragma unroll
                        for (int r = 0; r < 3; ++r) { a[2 * r] = ld_sc1(rws, ao + r * 10240); a[2 * r + 1] = ld_sc1(rws, ao + r * 10240 + 256); }
#pragma unroll
                        for (int r = 0; r < 6; ++r) mx = vmax4(mx, a[r]);
                        need = mx == SENT;
                    }
                    if (__builtin_amdgcn_ballot_w64(need) == 0ull) break;
                    if (SLB) __builtin_amdgcn_s_sleep(SLB);
                }
                if (w == 0) STAMP(11);
                xa = rb4(xa + rb4(gaa * rb4((asf4(a[0]) + asf4(a[2])) + asf4(a[4]))));
                xb = rb4(xb + rb4(gab * rb4((asf4(a[1]) + asf4(a[3])) + asf4(a[5]))));
                const f4 t = xa + xb;
                const float mw = row_sum((t[0] + t[1]) + (t[2] + t[3])) * (1.f / 128.f);
                const f4 da = xa - mw, db = xb - mw, q2 = fma4(da, da, db * db);
                const float v = row_sum((q2[0] + q2[1]) + (q2[2] + q2[3]));
                *(LAS v2u*)(L + L_ST + 64 + g * 48 + w * 8) = v2u{asu(mw), asu(v)};
            }
            BAR();
            if (w < 5) {
                const f4 s0 = *(LAS f4*)(L + L_ST + 64 + g * 48), s1 = *(LAS f4*)(L + L_ST + 64 + g * 48 + 16);
                const f2 s2 = *(LAS f2*)(L + L_ST + 64 + g * 48 + 32);
                const float mean = (((s0[0] + s0[2]) + (s1[0] + s1[2])) + s2[0]) * 0.2f;
                const f4 dm = f4{s0[0], s0[2], s1[0], s1[2]} - mean, dq = dm * dm; const float d4 = s2[0] - mean;
                const float M2 = fmaf(128.f, fmaf(d4, d4, (dq[0] + dq[1]) + (dq[2] + dq[3])), ((s0[1] + s0[3]) + (s1[1] + s1[3])) + s2[1]);
                const float mb = rbf(mean), den = rbf(__builtin_amdgcn_sqrtf(rbf(rbf(M2 * (1.f / DM)) + 1e-5f))), rc = __builtin_amdgcn_rcpf(den);
                const f4 ea = rb4(xa - mb), eb = rb4(xb - mb), qa = ea * rc, qb = eb * rc;
                const f4 ha = rb4(rb4(fma4(fma4(-qa, bc4(den), ea), bc4(rc), qa)) * c1a) + sba, hb = rb4(rb4(fma4(fma4(-qb, bc4(den), eb), bc4(rc), qb)) * c1b) + sbb;
                *(LAS v2u*)(L + L_HB + g * 1280 + col * 2) = v2u{pack2(ha[0], ha[1]), pack2(ha[2], ha[3])};
                *(LAS v2u*)(L + L_HB + g * 1280 + col * 2 + 128) = v2u{pack2(hb[0], hb[1]), pack2(hb[2], hb[3])};
            }
            BAR();
            if (w == 0) STAMP(12);
            f4 d[4] = {};
#pragma unroll
            for (int n = 0; n < 5; ++n) {
                const v4u a = *(LAS v4u*)(L + L_HB + b4 * 1280 + (160 * (w >> 1) + 32 * n + 8 * g) * 2);
#pragma unroll
                for (int T2 = 0; T2 < 4; ++T2) d[T2] = mfma(ft[n][T2], a, d[T2]);
            }
            *(LAS f4*)(L + L_RED + w * 1024 + l * 16) = sel_tile(d, tq);
            BAR();
            if (w == 0) {
                STAMP(13);
                LAS f4* R = (LAS f4*)(L + L_RED + l * 16);
                const f4 gt = rb4((R[0] + R[128]) + (R[256] + R[384])), up = rb4((R[64] + R[192]) + (R[320] + R[448]));
                const f4 mk = {gt[0] > 0.f ? 1.f : 0.f, gt[1] > 0.f ? 1.f : 0.f, gt[2] > 0.f ? 1.f : 0.f, gt[3] > 0.f ? 1.f : 0.f};
                const f4 hh = (gt * mk) * up;
                st_sc1_2(rws, outo, v2u{canon(pack2(hh[0], hh[1])), canon(pack2(hh[2], hh[3]))});
                STAMP(14);
            }
        } else {
            f4 x1, gbf;
            UNDEF4(x1); UNDEF4(gbf);
            if (w == 0) {
                v2u ga, gb;
                const u32 mo = WS_MODF + (u32)(b4 * 2560 + 64 * ri + dcol) * 2u;
                for (int sp = 0; sp < SPIN_G; ++sp) {
                    MEMBAR();
                    ga = ld_sc1_2(rws, mo); gb = ld_sc1_2(rws, mo + 3840);
                    if (all_valid(umax3(umax3(ga[0], ga[1], gb[0]), gb[1], 0u))) break;
                }
                const u32 ao = WS_AP + (u32)(b4 * DM + 64 * ri + dcol) * 4u;
                f4 s;
                for (int sp = 0; sp < SPIN_G; ++sp) {
                    MEMBAR();
                    const v4u p0 = ld_sc1(rws, ao), p1 = ld_sc1(rws, ao + 10240), p2 = ld_sc1(rws, ao + 20480);
                    s = (asf4(p0) + asf4(p1)) + asf4(p2);
                    if (all_valid(vmax4(vmax4(vmax4(0u, p0), p1), p2))) break;
                }
                x1 = rb4(unpk(*(LAS v2u*)(L + L_XS + b4 * 1280 + (64 * ri + dcol) * 2)) + rb4(unpk(ga) * rb4(s)));
                gbf = unpk(gb);
                asm volatile("" : "+v"(x1), "+v"(gbf));
                STAMP(15);
            }
            v4u a0, a1;
            UNDEF4(a0); UNDEF4(a1);
            const u32 h0 = WS_HID + (u32)(b4 * FF + 192 * w + 32 * tq + 8 * g) * 2u, h1 = tq < 2 ? h0 + 256 : OOB;
            bool need = true;
            for (int sp = 0; sp < SPIN_G; ++sp) {
                MEMBAR();
                if (need) { a0 = ld_sc1(rws, h0); a1 = ld_sc1(rws, h1); need = vmax4(vmax4(0u, a0), a1) == SENT; }
                if (__builtin_amdgcn_ballot_w64(need) == 0ull) break;
                if (SLB) __builtin_amdgcn_s_sleep(SLB);
            }
            if (w == 0) STAMP(16);
            const v4u bk[4] = {a0, ror4k<1>(a0), ror4k<2>(a0), ror4k<3>(a0)}, bl[4] = {a1, ror4k<1>(a1), ror4k<2>(a1), ror4k<3>(a1)};
            f4 d[4] = {};
#pragma unroll
            for (int k = 0; k < 4; ++k)
#pragma unroll
                for (int s = 0; s < 6; ++s) d[(s + k) & 3] = mfma(ft[s][(s + k) & 3], s < 4 ? bk[k] : bl[k], d[(s + k) & 3]);
            *(LAS f4*)(L + L_RED + w * 1024 + l * 16) = sel_tile(d, tq);
            BAR();
            if (w == 0) {
                STAMP(17);
                LAS f4* R = (LAS f4*)(L + L_RED + l * 16);
                const f4 o = x1 + rb4(gbf * rb4(((R[0] + R[64]) + (R[128] + R[192])) + ((R[256] + R[320]) + (R[384] + R[448]))));
                __builtin_amdgcn_raw_buffer_store_b64(v2u{pack2(o[0], o[1]), pack2(o[2], o[3])}, __builtin_amdgcn_make_buffer_rsrc((void*)p.out, 0, 4 * DM * 2, 0x00020000),
                                                      (u32)(b4 * DM + 64 * ri + dcol) * 2u, 0, 16);
                STAMP(18);
            }
        }
    }
}

typedef const __attribute__((address_space(4))) Params* kargp;
#define GET_PARAMS() kargp pp_ = kp; asm volatile("" : "+s"(pp_)); \
    const Params q_ = {pp_->x, pp_->kc, pp_->vc, pp_->wq, pp_->wo, pp_->wg, pp_->wu, pp_->wd, pp_->c, pp_->wm, pp_->out, pp_->ws};
__global__ void __launch_bounds__(512, 2) fused_block(Params p) {
    extern __shared__ __attribute__((aligned(16))) unsigned char smem[];
    LAS unsigned char* L = (LAS unsigned char*)smem;
    const kargp kp = (kargp)__builtin_amdgcn_kernarg_segment_ptr();
    asm volatile("s_load_dword s98, %0, 0x0\n\ts_load_dword s99, %0, 0x40" :: "s"(kp) : "s98", "s99");
    const int l = threadIdx.x & 63, w = __builtin_amdgcn_readfirstlane(threadIdx.x >> 6), c = blockIdx.x;
    const int x = c & 7, y = c >> 3;
    const int u = (x < 6 && y < 5) ? 10 * (x >> 1) + 5 * (x & 1) + y
                : (x >= 6 && y < 5) ? 54 + 5 * (x - 6) + y
                : 30 + 8 * (y - 5) + x;
    if (__builtin_expect(c >= NW, 1)) { GET_PARAMS(); kv_path(q_, L, c - NW, w, l); }
    else if (u < 30) { GET_PARAMS(); w_path<R_SA>(q_, L, u, w, l); }
    else if (u < 54) { GET_PARAMS(); w_path<R_SB>(q_, L, u, w, l); }
    else { GET_PARAMS(); w_path<R_SC>(q_, L, u, w, l); }
}

extern "C" void kernel_launch(void* const* d_in, const int* in_sizes, int n_in, void* d_out, int out_size, void* d_ws, size_t ws_size, hipStream_t stream) {
    Params p;
    p.x = (const u16*)d_in[0]; p.kc = (const u16*)d_in[1]; p.vc = (const u16*)d_in[2]; p.wq = (const u16*)d_in[3]; p.wo = (const u16*)d_in[4];
    p.wg = (const u16*)d_in[6]; p.wu = (const u16*)d_in[7]; p.wd = (const u16*)d_in[8]; p.c = (const u16*)d_in[9]; p.wm = (const u16*)d_in[10];
    p.out = (u16*)d_out; p.ws = (unsigned char*)d_ws;
    (void)hipFuncSetAttribute((const void*)fused_block, hipFuncAttributeMaxDynamicSharedMemorySize, (int)L_END);
    hipLaunchKernelGGL(fused_block, dim3(256), dim3(512), L_END, stream, p);
}
```

```cpp
#include <hip/hip_runtime.h>
#include <stdint.h>
#pragma clang fp contract(off)

typedef unsigned short u16;
typedef unsigned u32;
typedef unsigned long long u64;
typedef u32 v4u __attribute__((ext_vector_type(4)));
typedef u32 v2u __attribute__((ext_vector_type(2)));
typedef float f4 __attribute__((ext_vector_type(4)));
typedef short v4s __attribute__((ext_vector_type(4)));
typedef __bf16 bf8 __attribute__((ext_vector_type(8)));
typedef __bf16 bf2 __attribute__((ext_vector_type(2)));
#define LAS __attribute__((address_space(3)))
#define GAS __attribute__((address_space(1)))
#define RLX_AGENT __ATOMIC_RELAXED, __HIP_MEMORY_SCOPE_AGENT

#define PRETOUCH 1
#define POLL_SLEEP 2
#define SLA 0
#define SLB 0
#define FRQ 30
#define FRS 6
#define GATE 1
#define GATE0 1
#define ILV 1
#define JIT 0
#define JIT_B 64
#define JIT_C 160
#define INIT_MODE 1

constexpr int DM = 640, NH = 32, SKV = 4096, DH = 192, WIN = 1024, FF = 1536, NQ = NH * DH;
constexpr u32 SENT = 0xFFFFFFFFu;
constexpr int NW = 64;
constexpr u32 WS_E = 0, WS_FLAG = 4096, WS_F1P = 8192, WS_MODF = WS_F1P + 5 * 4 * 1280 * 4, WS_QP = WS_MODF + 4 * 2560 * 2,
              WS_OP = WS_QP + 5 * 4 * NQ * 4, WS_ML = WS_OP + 128 * 2 * DH * 2, WS_AP = WS_ML + 32 * 4 * 16, WS_HID = WS_AP + 3 * 4 * DM * 4,
              WS_END = WS_HID + 4 * FF * 2;
constexpr u32 OOB = 0x7FFFFF00u;
constexpr u32 L_XS = 131072, L_CS = L_XS + 5120, L_HB = L_CS + 5120, L_RED = L_HB + 5120, L_ST = L_RED + 8192, L_GA = L_ST + 256, L_GB = L_GA + 1024,
              L_X1 = L_GB + 1024, L_MISC = L_X1 + 1024, L_STAMP = L_MISC + 64, L_END = L_STAMP + 256;
constexpr u32 K_W = 18432, K_PX = 1024, K_PX2 = 1536, K_V = 9216, K_QS = 8 * K_W, K_ML = K_QS + 768, K_END = K_ML + 64;
static_assert(K_END <= L_MISC, "LDS");
constexpr float LOG2E = 1.4426950408889634f;
static_assert(L_END <= 163840, "LDS");
constexpr int SPIN_G = 40000, SPIN_L = 2000000;

#define STAMP(i) do {} while (0)

struct Params { const u16 *x, *kc, *vc, *wq, *wo, *wg, *wu, *wd, *c, *wm; u16* out; unsigned char* ws; };

__device__ __forceinline__ v4u mk_srd(const void* p, u32 bytes) { const u64 a = (u64)p; return v4u{(u32)a, (u32)(a >> 32) & 0xffffu, bytes, 0x00020000u}; }
__device__ __forceinline__ float asf(u32 u) { return __builtin_bit_cast(float, u); }
__device__ __forceinline__ u32 asu(float f) { return __builtin_bit_cast(u32, f); }
__device__ __forceinline__ float bflo(u32 u) { return asf(u << 16); }
__device__ __forceinline__ float bfhi(u32 u) { return asf(u & 0xffff0000u); }
__device__ __forceinline__ u32 pack2(float lo, float hi) { bf2 v; v[0] = (__bf16)lo; v[1] = (__bf16)hi; return __builtin_bit_cast(u32, v); }
__device__ __forceinline__ float rbf(float f) { return (float)(__bf16)f; }
typedef float f2 __attribute__((ext_vector_type(2)));
__device__ __forceinline__ f2 rb2(f2 v) { return f2{rbf(v[0]), rbf(v[1])}; }
__device__ __forceinline__ f4 rb4(f4 v) {
    float a = v[0], b = v[1], c = v[2], d = v[3];
    asm("v_cvt_pk_bf16_f32 %0, 0, %0\n\tv_cvt_pk_bf16_f32 %1, 0, %1\n\tv_cvt_pk_bf16_f32 %2, 0, %2\n\tv_cvt_pk_bf16_f32 %3, 0, %3" : "+v"(a), "+v"(b), "+v"(c), "+v"(d));
    return f4{a, b, c, d}; }
__device__ __forceinline__ f4 unpk(v2u v) { return f4{bflo(v[0]), bfhi(v[0]), bflo(v[1]), bfhi(v[1])}; }
__device__ __forceinline__ f4 fma4(f4 a, f4 b, f4 c) { return __builtin_elementwise_fma(a, b, c); }
__device__ __forceinline__ f4 bc4(float x) { return f4{x, x, x, x}; }
__device__ __forceinline__ u32 canon(u32 u) { return u < 0xFFFFFFFEu ? u : 0xFFFFFFFEu; }
__device__ __forceinline__ v4u canon4(f4 v) { return v4u{canon(asu(v[0])), canon(asu(v[1])), canon(asu(v[2])), canon(asu(v[3]))}; }
__device__ __forceinline__ u32 umax3(u32 a, u32 b, u32 c) { const u32 m = a > b ? a : b; return m > c ? m : c; }
__device__ __forceinline__ u32 vmax4(u32 m, v4u v) { return umax3(umax3(m, v[0], v[1]), v[2], v[3]); }
__device__ __forceinline__ bool all_valid(u32 m) { return __builtin_amdgcn_ballot_w64(m == SENT) == 0ull; }
__device__ __forceinline__ float dot2(u32 a, u32 b, float c) { return __builtin_amdgcn_fdot2_f32_bf16(__builtin_bit_cast(bf2, a), __builtin_bit_cast(bf2, b), c, false); }
__device__ __forceinline__ f4 mfma(v4u a, v4u b, f4 c) { return __builtin_amdgcn_mfma_f32_16x16x32_bf16(__builtin_bit_cast(bf8, a), __builtin_bit_cast(bf8, b), c, 0, 0, 0); }
template <int CTRL> __device__ __forceinline__ u32 dppu(u32 x) { return (u32)__builtin_amdgcn_mov_dpp((int)x, CTRL, 0xf, 0xf, true); }
template <int CTRL> __device__ __forceinline__ float dppf(float x) { return asf(dppu<CTRL>(asu(x))); }
template <int K> __device__ __forceinline__ v4u ror4k(v4u v) {
    if constexpr (K == 0) return v; else return v4u{dppu<0x120 + 4 * K>(v[0]), dppu<0x120 + 4 * K>(v[1]), dppu<0x120 + 4 * K>(v[2]), dppu<0x120 + 4 * K>(v[3])}; }
__device__ __forceinline__ float row_sum(float x) { x += dppf<0x128>(x); x += dppf<0x124>(x); x += dppf<0x122>(x); x += dppf<0x121>(x); return x; }
__device__ __forceinline__ float row_max(float x) { x = fmaxf(x, dppf<0x128>(x)); x = fmaxf(x, dppf<0x124>(x)); x = fmaxf(x, dppf<0x122>(x)); x = fmaxf(x, dppf<0x121>(x)); return x; }
__device__ __forceinline__ float xrow_sum(float x) {
    auto s = __builtin_amdgcn_permlane16_swap(asu(x), asu(x), false, false); x = asf(s[0]) + asf(s[1]);
    auto t = __builtin_amdgcn_permlane32_swap(asu(x), asu(x), false, false); return asf(t[0]) + asf(t[1]); }
__device__ __forceinline__ float xrow_max(float x) {
    auto s = __builtin_amdgcn_permlane16_swap(asu(x), asu(x), false, false); x = fmaxf(asf(s[0]), asf(s[1]));
    auto t = __builtin_amdgcn_permlane32_swap(asu(x), asu(x), false, false); return fmaxf(asf(t[0]), asf(t[1])); }
__device__ __forceinline__ float wave_sum(float x) { return xrow_sum(row_sum(x)); }
__device__ __forceinline__ float wave_max(float x) { return xrow_max(row_max(x)); }
__device__ __forceinline__ float fdiv(float a, float b) { const float r = __builtin_amdgcn_rcpf(b); const float q = a * r; return fmaf(fmaf(-q, b, a), r, q); }
__device__ __forceinline__ float silu_bf(float v) { return fdiv(v, 1.f + __builtin_amdgcn_exp2f(fminf(-v * 1.4426950408889634f, 126.f))); }
__device__ __forceinline__ v4u silu8(v4u c) {
    v4u r;
#pragma unroll
    for (int i = 0; i < 4; ++i) r[i] = pack2(silu_bf(bflo(c[i])), silu_bf(bfhi(c[i])));
    return r; }
#define BAR() asm volatile("s_waitcnt lgkmcnt(0)\n\ts_barrier" ::: "memory")
#define LGKM0() asm volatile("s_waitcnt lgkmcnt(0)" ::: "memory")
#define VMW(n) asm volatile("s_waitcnt vmcnt(%0)" :: "i"(n) : "memory")
#define MEMBAR() asm volatile("" ::: "memory")
typedef __amdgpu_buffer_rsrc_t rsrc_t;
__device__ __forceinline__ v4u ld_sc1(rsrc_t r, u32 off) { return __builtin_amdgcn_raw_buffer_load_b128(r, off, 0, 16); }
__device__ __forceinline__ v2u ld_sc1_2(rsrc_t r, u32 off) { return __builtin_amdgcn_raw_buffer_load_b64(r, off, 0, 16); }
__device__ __forceinline__ void st_sc1(rsrc_t r, u32 off, v4u v) { __builtin_amdgcn_raw_buffer_store_b128(v, r, off, 0, 16); }
__device__ __forceinline__ void st_sc1_2(rsrc_t r, u32 off, v2u v) { __builtin_amdgcn_raw_buffer_store_b64(v, r, off, 0, 16); }
__device__ __forceinline__ f4 asf4(v4u v) { return __builtin_bit_cast(f4, v); }

__device__ __forceinline__ void dma_unit(v4u srd, u32 v0, u32 v1, u32 sA, u32 sB, u32 lds) {
    u32 keep;
    asm volatile("s_nop 4\n\ts_mov_b32 %0, m0\n\ts_mov_b32 m0, %6\n\ts_nop 0\n\t"
                 "buffer_load_dwordx4 %1, %3, %4 offen sc0 nt lds\n\ts_add_u32 m0, m0, 0x400\n\ts_nop 0\n\t"
                 "buffer_load_dwordx4 %2, %3, %4 offen sc0 nt lds\n\ts_add_u32 m0, m0, 0x400\n\ts_nop 0\n\t"
                 "buffer_load_dwordx4 %1, %3, %5 offen sc0 nt lds\n\ts_add_u32 m0, m0, 0x400\n\ts_nop 0\n\t"
                 "buffer_load_dwordx4 %2, %3, %5 offen sc0 nt lds\n\ts_mov_b32 m0, %0"
                 : "=&s"(keep) : "v"(v0), "v"(v1), "s"(srd), "s"(sA), "s"(sB), "s"(lds) : "memory", "scc");
}
__device__ __forceinline__ void dma_1k(v4u srd, u32 voff, u32 soff, u32 lds) {
    u32 keep;
    asm volatile("s_nop 4\n\ts_mov_b32 %0, m0\n\ts_mov_b32 m0, %4\n\ts_nop 0\n\tbuffer_load_dwordx4 %1, %2, %3 offen lds\n\ts_mov_b32 m0, %0"
                 : "=&s"(keep) : "v"(voff), "s"(srd), "s"(soff), "s"(lds) : "memory");
}
struct WStream { v4u srd; u32 base, pitch, v0, v1; };
template <bool HALF = false>
__device__ __forceinline__ WStream mk_stream(v4u srd, u32 base, u32 pitch, int l) {
    WStream s; s.srd = srd; s.base = base; s.pitch = pitch;
    const u32 sw = HALF ? 2u * ((l >> 4) & 3) : 2u * ((l >> 4) & 1);
    s.v0 = (l >> 3) * pitch + (((l & 7) ^ sw) << 4);
    s.v1 = (8 + (l >> 3)) * pitch + (((l & 7) ^ (HALF ? sw : (sw | 4u))) << 4);
    return s; }
__device__ __forceinline__ void issue(const WStream& s, int n, u32 lds) { const u32 a = s.base + (u32)n * 32u * s.pitch; dma_unit(s.srd, s.v0, s.v1, a, a + 16u * s.pitch, lds); }
template <int HOFF = 512>
__device__ __forceinline__ void read_unit(LAS unsigned char* L, const u32 (&ta)[4], int slot, v4u (&f)[4]) {
#pragma unroll
    for (int T = 0; T < 4; ++T) {
        const v2u lo = __builtin_bit_cast(v2u, __builtin_amdgcn_ds_read_tr16_b64_v4i16((LAS v4s*)(L + ta[T] + slot * 4096)));
        const v2u hi = __builtin_bit_cast(v2u, __builtin_amdgcn_ds_read_tr16_b64_v4i16((LAS v4s*)(L + ta[T] + slot * 4096 + HOFF)));
        f[T] = v4u{lo[0], lo[1], hi[0], hi[1]};
    } }
__device__ __forceinline__ f4 sel_tile(const f4 (&a)[4], int t) {
    const bool b0 = t & 1, b1 = t & 2;
    f4 r;
#pragma unroll
    for (int i = 0; i < 4; ++i) { const float lo = b0 ? a[1][i] : a[0][i], hi = b0 ? a[3][i] : a[2][i]; r[i] = b1 ? hi : lo; }
    return r; }

__device__ __forceinline__ void dma_kv(v4u srd, u32 vo, u32 so, u32 lds) {
    u32 keep;
    asm volatile("s_nop 4\n\ts_mov_b32 %0, m0\n\ts_mov_b32 m0, %4\n\ts_nop 0\n\t"
                 "buffer_load_dwordx4 %1, %2, %3 offen sc0 nt lds\n\ts_add_u32 m0, m0, 0x380\n\ts_nop 0\n\t"
                 "buffer_load_dwordx4 %1, %2, %3 offen offset:128 sc0 nt lds\n\ts_add_u32 m0, m0, 0x380\n\ts_nop 0\n\t"
                 "buffer_load_dwordx4 %1, %2, %3 offen offset:256 sc0 nt lds\n\ts_mov_b32 m0, %0"
                 : "=&s"(keep) : "v"(vo), "s"(srd), "s"(so), "s"(lds) : "memory", "scc");
}
__device__ __forceinline__ float qk_unit(v4u k0, v4u k1, v4u k2, const v4u (&q)[3]) {
    float s = 0.f, t = 0.f;
#pragma unroll
    for (int r = 0; r < 4; r += 2) { s = dot2(k0[r], q[0][r], s); t = dot2(k0[r + 1], q[0][r + 1], t); }
#pragma unroll
    for (int r = 0; r < 4; r += 2) { s = dot2(k1[r], q[1][r], s); t = dot2(k1[r + 1], q[1][r + 1], t); }
#pragma unroll
    for (int r = 0; r < 4; r += 2) { s = dot2(k2[r], q[2][r], s); t = dot2(k2[r + 1], q[2][r + 1], t); }
    return s + t;
}
__device__ __forceinline__ void pv_unit(v4u v0, v4u v1, v4u v2, u32 plo, float (&acc)[24]) {
    const u32 phi = plo << 16;
#pragma unroll
    for (int r = 0; r < 4; ++r) {
        acc[2 * r] = dot2(v0[r], plo, acc[2 * r]); acc[2 * r + 1] = dot2(v0[r], phi, acc[2 * r + 1]);
        acc[8 + 2 * r] = dot2(v1[r], plo, acc[8 + 2 * r]); acc[8 + 2 * r + 1] = dot2(v1[r], phi, acc[8 + 2 * r + 1]);
        acc[16 + 2 * r] = dot2(v2[r], plo, acc[16 + 2 * r]); acc[16 + 2 * r + 1] = dot2(v2[r], phi, acc[16 + 2 * r + 1]);
    }
}

__device__ __forceinline__ void kv_path(const Params& p, LAS unsigned char* L, int j, int w, int l) {
    const rsrc_t rws = __builtin_amdgcn_make_buffer_rsrc((void*)p.ws, 0, WS_END, 0x00020000);
    const rsrc_t rk = __builtin_amdgcn_make_buffer_rsrc((void*)p.kc, 0, 4 * NH * SKV * DH * 2, 0x00020000);
    const rsrc_t rv = __builtin_amdgcn_make_buffer_rsrc((void*)p.vc, 0, 4 * NH * SKV * DH * 2, 0x00020000);
    volatile LAS u32* misc = (volatile LAS u32*)(L + L_MISC);
    const int rk3 = (j >> 3) / 3, t3 = (j & 7) * 8 + rk3, pos = (j >> 3) - 3 * rk3;
    const int slot = pos == 1 ? (w & 1) : 0;
    const int bh = 2 * t3 + (pos == 2 ? 1 : (pos == 1 ? slot : 0));
    const int part = pos == 0 ? 0 : (pos == 2 ? 1 : (slot == 0 ? 1 : 0));
    const int wi = pos == 1 ? (w >> 1) : w;
    const int ub = pos == 0 ? 0 : (pos == 2 ? 43 : (slot == 0 ? 85 : 0));
    const int ng = pos == 1 ? 2 : 4, u1 = ub + (wi >= ng ? 11 * ng + wi - ng : wi);
    const u32 ust = ng * 3072;
    const int nl = (pos == 1 ? wi == 3 : wi >= 5) ? 2 : 3;
    const bool lead = w == 0 || (w == 1 && pos == 1);
    const u32 opb = WS_OP + (bh * 2 + part) * 384, mlb = WS_ML + ((bh & 31) * 4 + (bh >> 5)) * 16 + part * 8;
    if (threadIdx.x < 12) misc[threadIdx.x] = 0;
    BAR();
    STAMP(0);
    if (w == 4) {
        __builtin_amdgcn_s_setprio(3);
        const u64 T = __hip_atomic_load((GAS u64*)(p.ws + WS_E), RLX_AGENT) + 1ull;
        st_sc1(rws, l < 24 ? opb + l * 16 : OOB, v4u{SENT, SENT, SENT, SENT});
        st_sc1_2(rws, l == 0 ? mlb : OOB, v2u{SENT, SENT});
        const int bh1 = 2 * t3 + 1;
        st_sc1(rws, (pos == 1 && l < 24) ? WS_OP + bh1 * 2 * 384 + l * 16 : OOB, v4u{SENT, SENT, SENT, SENT});
        st_sc1_2(rws, (pos == 1 && l == 0) ? WS_ML + ((bh1 & 31) * 4 + (bh1 >> 5)) * 16 : OOB, v2u{SENT, SENT});
        VMW(0);
        if (l == 0) {
            __hip_atomic_store((GAS u64*)(p.ws + WS_FLAG) + blockIdx.x, T, RLX_AGENT);
            misc[4] = (u32)T; misc[5] = (u32)(T >> 32); LGKM0(); misc[2] = 1;
        }
        STAMP(9);
        __builtin_amdgcn_s_setprio(0);
    }
    const u32 vo = (l >> 3) * 384 + (l & 7) * 16;
    const u32 base = ((u32)bh * SKV + (SKV - WIN)) * 384u + (u32)u1 * 3072u;
    const u32 wl = w * K_W;
    const int c = l & 7;
    float m, lsum, fsc;
    const u32 nslot = pos == 1 ? 4u : 8u;
    auto slot_stats = [&](float& M, float& mi, float& li) {
        const v2u t = *(LAS v2u*)(L + K_ML + c * 8);
        const bool mine = pos != 1 || (l & 1) == slot;
        mi = mine ? asf(t[0]) : -INFINITY; li = mine ? asf(t[1]) : 0.f;
        M = fmaxf(mi, dppf<0xB1>(mi)); M = fmaxf(M, dppf<0x4E>(M)); M = fmaxf(M, dppf<0x141>(M));
    };
    auto post = [&]() {
        u32 old = 0;
        if (l == 0) { *(LAS v2u*)(L + K_ML + w * 8) = v2u{asu(m), asu(lsum)}; LGKM0(); old = __hip_atomic_fetch_add((LAS u32*)(L + L_MISC + 32 + slot * 4), 1u, __ATOMIC_RELAXED, __HIP_MEMORY_SCOPE_WORKGROUP); }
        if (__builtin_amdgcn_readfirstlane(old) == nslot - 1u) {
            MEMBAR();
            float M, mi, li; slot_stats(M, mi, li);
            float e = li * __builtin_amdgcn_exp2f((mi - M) * LOG2E);
            e += dppf<0xB1>(e); e += dppf<0x4E>(e); e += dppf<0x141>(e);
            st_sc1_2(rws, l == 0 ? mlb : OOB, v2u{canon(asu(M)), canon(asu(e))});
            STAMP(5);
        }
    };
    auto scale = [&]() {
        for (int sp = 0; misc[8 + slot] < nslot && sp < SPIN_L; ++sp) __builtin_amdgcn_s_sleep(1);
        MEMBAR();
        float M, mi, li; slot_stats(M, mi, li);
        float e = li * __builtin_amdgcn_exp2f((mi - M) * LOG2E);
        e += dppf<0xB1>(e); e += dppf<0x4E>(e); e += dppf<0x141>(e);
        fsc = fdiv(__builtin_amdgcn_exp2f((m - M) * LOG2E), e);
    };
    {
        const v4u sk = mk_srd(p.kc, 4 * NH * SKV * DH * 2), sv = mk_srd(p.vc, 4 * NH * SKV * DH * 2);
        const u32 lb = (u32)(size_t)L + wl;
        dma_kv(sk, vo, base + 8 * ust, lb); dma_kv(sk, vo, base + 9 * ust, lb + 3072);
        if (nl == 3) dma_kv(sk, vo, base + 10 * ust, lb + 6144);
        dma_kv(sv, vo, base + 8 * ust, lb + K_V); dma_kv(sv, vo, base + 9 * ust, lb + K_V + 3072);
        if (nl == 3) dma_kv(sv, vo, base + 10 * ust, lb + K_V + 6144);
    }
    v4u K[24], V[24];
#pragma unroll
    for (int jj = 0; jj < 8; ++jj)
#pragma unroll
        for (int i = 0; i < 3; ++i) K[jj * 3 + i] = __builtin_amdgcn_raw_buffer_load_b128(rk, vo + i * 128, base + jj * ust, 3);
#pragma unroll
    for (int jj = 0; jj < 8; ++jj)
#pragma unroll
        for (int i = 0; i < 3; ++i) V[jj * 3 + i] = __builtin_amdgcn_raw_buffer_load_b128(rv, vo + i * 128, base + jj * ust, 3);
    __builtin_amdgcn_sched_barrier(0);
    if (w == 0) STAMP(1);
    if (w == 7) STAMP(2);
    if (w < 4) {
        for (int sp = 0; misc[2] == 0 && sp < SPIN_L; ++sp) __builtin_amdgcn_s_sleep(1);
        const u32 tlo = misc[4], thi = misc[5];
        for (int sp = 0; sp < SPIN_G && misc[6] == 0; ++sp) {
            MEMBAR();
            const v2u f = ld_sc1_2(rws, WS_FLAG + l * 8);
            if (__builtin_amdgcn_ballot_w64(f[0] != tlo || f[1] != thi) == 0ull) misc[6] = 1;
        }
        const u32 qo = WS_QP + (u32)((bh >> 5) * NQ + (bh & 31) * DH + 4 * l) * 4u;
        v4u a[5] = {};
        bool need = l < 48;
        for (int sp = 0; sp < SPIN_G; ++sp) {
            MEMBAR();
            if (__builtin_amdgcn_readfirstlane(misc[slot]) != 0u) break;
            if (need) {
                u32 mx = 0;
#pragma unroll
                for (int ks = 0; ks < 5; ++ks) a[ks] = ld_sc1(rws, qo + ks * (4 * NQ * 4));
#pragma unroll
                for (int ks = 0; ks < 5; ++ks) mx = vmax4(mx, a[ks]);
                need = mx == SENT;
            }
            if (__builtin_amdgcn_ballot_w64(need) == 0ull || sp == SPIN_G - 1) {
                const f4 qs = (((asf4(a[0]) + asf4(a[1])) + asf4(a[2])) + asf4(a[3])) + asf4(a[4]);
                if (l < 48) *(LAS v2u*)(L + K_QS + slot * 384 + l * 8) = v2u{pack2(qs[0], qs[1]), pack2(qs[2], qs[3])};
                LGKM0();
                misc[slot] = 1;
                break;
            }
        }
        if (w == 0) STAMP(3);
    } else {
        for (int sp = 0; misc[slot] == 0 && sp < SPIN_L; ++sp) __builtin_amdgcn_s_sleep(1);
    }
    VMW(48);
    v4u p0, p1, p2;
    {
        v4u q[3];
#pragma unroll
        for (int i = 0; i < 3; ++i) q[i] = *(LAS v4u*)(L + K_QS + slot * 384 + (c + 8 * i) * 16);
        float y[4], x[8];
        LAS v4u* KL = (LAS v4u*)(L + wl + l * 16);
#pragma unroll
        for (int jj = 0; jj < 3; ++jj) { y[jj] = qk_unit(KL[jj * 192], KL[jj * 192 + 64], KL[jj * 192 + 128], q); __builtin_amdgcn_sched_barrier(0); }
#pragma unroll
        for (int jj = 0; jj < 8; ++jj) x[jj] = qk_unit(K[3 * jj], K[3 * jj + 1], K[3 * jj + 2], q);
#pragma unroll
        for (int h = 0; h < 4; ++h) { const float a = x[h] + dppf<0x141>(x[h]), b = x[h + 4] + dppf<0x141>(x[h + 4]); x[h] = (c & 4) ? b : a; }
        { const float a = y[0] + dppf<0x141>(y[0]), b = y[2] + dppf<0x141>(y[2]), a1 = y[1] + dppf<0x141>(y[1]); y[0] = (c & 4) ? b : a; y[1] = a1; }
#pragma unroll
        for (int h = 0; h < 2; ++h) { const float a = x[h] + dppf<0x4E>(x[h]), b = x[h + 2] + dppf<0x4E>(x[h + 2]); x[h] = (c & 2) ? b : a; }
        { const float a = y[0] + dppf<0x4E>(y[0]), b = y[1] + dppf<0x4E>(y[1]); y[0] = (c & 2) ? b : a; }
        { const float a = x[0] + dppf<0xB1>(x[0]), b = x[1] + dppf<0xB1>(x[1]); x[0] = (c & 1) ? b : a; }
        y[0] += dppf<0xB1>(y[0]);
        const float s1 = rbf(rbf(x[0]) * 0.07216878364870322f);
        const float s2 = (c >> 1) < nl ? rbf(rbf(y[0]) * 0.07216878364870322f) : -INFINITY;
        m = wave_max(fmaxf(s1, s2));
        const float e1 = __builtin_amdgcn_exp2f((s1 - m) * LOG2E), e2 = __builtin_amdgcn_exp2f((s2 - m) * LOG2E);
        *(LAS u32*)(L + wl + K_PX + l * 4) = pack2(e1, 0.f);
        *(LAS u32*)(L + wl + K_PX2 + (l >> 1) * 4) = pack2(e2, 0.f);
        lsum = wave_sum(e1 + ((c & 1) ? 0.f : e2));
        post();
        LGKM0();
        p0 = *(LAS v4u*)(L + wl + K_PX + (l >> 3) * 32); p1 = *(LAS v4u*)(L + wl + K_PX + (l >> 3) * 32 + 16); p2 = *(LAS v4u*)(L + wl + K_PX2 + (l >> 3) * 16);
    }
    if (w == 0) STAMP(4);
    float acc[24];
#pragma unroll
    for (int i = 0; i < 24; ++i) acc[i] = 0.f;
    {
        LAS v4u* VL = (LAS v4u*)(L + wl + K_V + l * 16);
        pv_unit(VL[0], VL[64], VL[128], p2[0], acc);
        pv_unit(VL[192], VL[256], VL[320], p2[1], acc);
        if (nl == 3) pv_unit(VL[384], VL[448], VL[512], p2[2], acc);
    }
#pragma unroll
    for (int jj = 0; jj < 6; ++jj) pv_unit(V[3 * jj], V[3 * jj + 1], V[3 * jj + 2], jj < 4 ? p0[jj & 3] : p1[jj & 3], acc);
    scale();
#pragma unroll
    for (int jj = 6; jj < 8; ++jj) pv_unit(V[3 * jj], V[3 * jj + 1], V[3 * jj + 2], p1[jj & 3], acc);
    if (w == 7) STAMP(6);
    float y[12], z[6];
#pragma unroll
    for (int n = 0; n < 12; ++n) { auto s = __builtin_amdgcn_permlane32_swap(asu(acc[n]), asu(acc[n + 12]), false, false); y[n] = asf(s[0]) + asf(s[1]); }
#pragma unroll
    for (int n = 0; n < 6; ++n) { auto s = __builtin_amdgcn_permlane16_swap(asu(y[n]), asu(y[n + 6]), false, false); z[n] = asf(s[0]) + asf(s[1]); }
#pragma unroll
    for (int n = 0; n < 3; ++n) {
        const float a = z[n] + dppf<0x128>(z[n]), b = z[n + 3] + dppf<0x128>(z[n + 3]);
        const int ai = 3 * (l >> 3) + n;
        *(LAS float*)(L + wl + (64 * (ai >> 3) + 8 * c + (ai & 7)) * 4) = ((l & 8) ? b : a) * fsc;
    }
    BAR();
    if (w == 0) STAMP(7);
    if (lead) {
        const u32 pb = slot * K_W + (l < 48 ? l : 47) * 16;
        f4 o = (*(LAS f4*)(L + pb) + *(LAS f4*)(L + pb + 2 * K_W)) + (*(LAS f4*)(L + pb + 4 * K_W) + *(LAS f4*)(L + pb + 6 * K_W));
        if (pos != 1) o += (*(LAS f4*)(L + pb + K_W) + *(LAS f4*)(L + pb + 3 * K_W)) + (*(LAS f4*)(L + pb + 5 * K_W) + *(LAS f4*)(L + pb + 7 * K_W));
        st_sc1_2(rws, l < 48 ? opb + l * 8 : OOB, v2u{canon(pack2(o[0], o[1])), canon(pack2(o[2], o[3]))});
        if (w == 0) STAMP(8); else STAMP(10);
    }
}

enum { R_SA = 0, R_SB = 1, R_SC = 2 };
#define UNDEF4(v) asm volatile("" : "=v"(v))
template <int ROLE>
__device__ __forceinline__ void w_path(const Params& p, LAS unsigned char* L, int u, int w, int l) {
    const rsrc_t rws = __builtin_amdgcn_make_buffer_rsrc((void*)p.ws, 0, WS_END, 0x00020000);
    const int tid = threadIdx.x, g = l >> 4, tq = (l & 15) >> 2, b4 = l & 3;
    const u32 lbase = (u32)(size_t)L, stg = lbase + w * 16384;
    const bool hasF1 = u < 50, hasF2 = u < 60;
    const int kf = u / 10, lp = u - 10 * kf, kf2 = u / 12, cs = u - 12 * kf2;
    const int ri = ROLE == R_SA ? u : (ROLE == R_SB ? u - 30 : u - 54);
    const WStream sF1 = mk_stream(mk_srd(p.wm, hasF1 ? DM * 3840 * 2 : 0), (u32)((128 * kf + 32 * (w >> 1)) * 7680 + (128 * lp + 64 * (w & 1)) * 2), 7680, l);
    const WStream sF2 = mk_stream(mk_srd(p.wq, hasF2 ? DM * NQ * 2 : 0), (u32)(128 * kf2 * 12288 + (512 * cs + 64 * w) * 2), 12288, l);
    const int mid = ROLE == R_SC ? (w < 4 ? (ri >= 6 ? ri : -1) : 30 + ri) : (w < 4 ? (ri < 6 ? ri : ri + 4) : (ri == 20 ? 28 : (ri == 21 ? 29 : -1)));
    const bool hasM = ROLE != R_SA && mid >= 0;
    const WStream sM = mk_stream(mk_srd(p.wm, hasM ? DM * 3840 * 2 : 0), (u32)(160 * (w & 3) * 7680 + (1280 + 64 * mid) * 2), 7680, l);
    const int ra = ri / 10, ca = ri - 10 * ra;
    const WStream sT = ROLE == R_SA ? mk_stream(mk_srd(p.wo, NQ * DM * 2), (u32)((2048 * ra + 256 * w) * 1280 + ca * 128), 1280, l)
                     : ROLE == R_SB ? mk_stream(mk_srd((w & 1) ? p.wu : p.wg, DM * FF * 2), (u32)(160 * (w >> 1) * 3072 + ri * 128), 3072, l)
                                    : mk_stream(mk_srd(p.wd, FF * DM * 2), (u32)(192 * w * 1280 + ri * 128), 1280, l);
    constexpr int NM = ROLE == R_SA ? 0 : 5, NT = ROLE == R_SA ? 8 : (ROLE == R_SB ? 5 : 6), NA = NM + NT;
    u32 ta[4];
    { const u32 f = 2u * (tq >> 1) | 4u * (g & 1);
#pragma unroll
      for (int T = 0; T < 4; ++T) ta[T] = w * 16384 + (8 * g + tq) * 128 + ((((u32)(2 * T)) ^ f) + (b4 >> 1)) * 16 + (b4 & 1) * 8; }
    const u32 dcol = 16 * tq + 4 * g;
    const u32 f1o = hasF1 ? WS_F1P + (u32)((kf * 4 + b4) * 1280 + 128 * lp + 64 * w + dcol) * 4u : OOB;
    const u32 qpo = hasF2 ? WS_QP + (u32)((kf2 * 4 + b4) * NQ + 512 * cs + 64 * w + dcol) * 4u : OOB;
    const u32 mfo = hasM ? WS_MODF + (u32)(b4 * 2560 + 64 * mid + dcol) * 2u : OOB;
    const u32 outo = ROLE == R_SA ? WS_AP + (u32)((ra * 4 + b4) * DM + 64 * ca + dcol) * 4u : WS_HID + (u32)(b4 * FF + 64 * ri + dcol) * 2u;
    volatile LAS u32* misc = (volatile LAS u32*)(L + L_MISC);
    STAMP(0);
    if (w < 5) { dma_1k(mk_srd(p.c, 5120), l * 16, w * 1024, lbase + L_CS + w * 1024); dma_1k(mk_srd(p.x, 5120), l * 16, w * 1024, lbase + L_XS + w * 1024); }
    issue(sF1, 0, stg);
    { u32 keep;
      asm volatile("s_nop 4\n\ts_mov_b32 %0, m0\n\ts_mov_b32 m0, %5\n\ts_nop 0\n\tbuffer_load_dword %1, %2, %3 offen sc0 nt lds\n\tbuffer_load_dword %1, %4, 0 offen sc0 nt lds\n\ts_mov_b32 m0, %0"
                   : "=&s"(keep) : "v"(l == 0 ? 0u : OOB), "s"(sT.srd), "s"(sT.base), "s"(mk_srd(p.out, 5120)), "s"(lbase + L_GB) : "memory"); }
    if (w == 0) {
        u32 keep;
        asm volatile("s_nop 4\n\ts_mov_b32 %0, m0\n\ts_mov_b32 m0, %3\n\ts_nop 0\n\tbuffer_load_dword %1, %2, 0 offen sc1 lds\n\ts_mov_b32 m0, %0"
                     : "=&s"(keep) : "v"(l < 2 ? WS_E + l * 4 : OOB), "s"(mk_srd(p.ws, WS_END)), "s"(lbase + L_GA) : "memory");
    }
    { const v4u s4 = {SENT, SENT, SENT, SENT};
      st_sc1(rws, qpo, s4);
      if (w < 2) st_sc1(rws, f1o, s4);
      if (ROLE != R_SA && (w & 3) == 0) st_sc1_2(rws, mfo, v2u{SENT, SENT});
      if (w == 0) {
          if (ROLE == R_SA) st_sc1(rws, outo, s4);
          if (ROLE == R_SB) st_sc1_2(rws, outo, v2u{SENT, SENT});
      } }
    if (tid == 0) { misc[0] = 0; misc[1] = 0; }
    BAR();
    issue(sF2, 0, stg + 4096);
    auto issueB = [&](int n) { if (n < NM) issue(sM, n, stg + ((n + 1) & 3) * 4096); else issue(sT, n - NM, stg + ((n + 1) & 3) * 4096); };
    {
        v4u ft[NT][4];
        {
            VMW(4);
            BAR();
            STAMP(1);
            const u64 T = *(LAS u64*)(L + L_GA) + 1ull;
            const u32 tlo = (u32)T, thi = (u32)(T >> 32);
            if (tid == 0) __hip_atomic_store((GAS u64*)(p.ws + WS_FLAG) + blockIdx.x, T, RLX_AGENT);
            {
                v4u f[4]; read_unit(L, ta, 0, f);
                const v4u a = silu8(*(LAS v4u*)(L + L_CS + b4 * 1280 + (128 * kf + 32 * (w >> 1) + 8 * g) * 2));
                f4 d[4];
#pragma unroll
                for (int T2 = 0; T2 < 4; ++T2) d[T2] = mfma(f[T2], a, f4{0.f, 0.f, 0.f, 0.f});
                *(LAS f4*)(L + L_RED + w * 1024 + l * 16) = sel_tile(d, tq);
                if (w >= 4) {
                    float xv[10], s = 0.f;
#pragma unroll
                    for (int i = 0; i < 5; ++i) { const u32 t = *(LAS u32*)(L + L_XS + (w - 4) * 1280 + (l + 64 * i) * 4); xv[2 * i] = bflo(t); xv[2 * i + 1] = bfhi(t); s += xv[2 * i] + xv[2 * i + 1]; }
                    const float mean = wave_sum(s) * (1.f / DM);
                    float v = 0.f;
#pragma unroll
                    for (int i = 0; i < 10; ++i) v = fmaf(xv[i] - mean, xv[i] - mean, v);
                    const float var = wave_sum(v) * (1.f / DM);
                    if (l == 0) *(LAS v2u*)(L + L_ST + (w - 4) * 8) = v2u{asu(rbf(mean)), asu(rbf(__builtin_amdgcn_sqrtf(rbf(rbf(var) + 1e-5f))))};
                }
                BAR();
                if (w < 2) {
                    LAS f4* R = (LAS f4*)(L + L_RED + w * 1024 + l * 16);
                    st_sc1(rws, f1o, canon4((R[0] + R[128]) + (R[256] + R[384])));
                }
                if (w == 0) STAMP(2);
                issue(sF2, 1, stg + 8192); issue(sF2, 2, stg + 12288); issue(sF2, 3, stg);
            }
            if (w < 2) {
                for (int sp = 0; sp < SPIN_G; ++sp) {
                    MEMBAR();
                    const v2u f = ld_sc1_2(rws, WS_FLAG + l * 8);
                    if (__builtin_amdgcn_ballot_w64(f[0] != tlo || f[1] != thi) == 0ull) break;
                }
                if (w == 0) STAMP(19);
                const int hb = tid >> 5, hi = tid & 31, col = 128 * kf2 + 4 * hi;
                const u32 o = WS_F1P + (u32)(hb * 1280 + col) * 4u;
                v4u a[5] = {}, c2[5] = {};
                bool need = hasF2;
                for (int sp = 0; sp < SPIN_G; ++sp) {
                    MEMBAR();
                    if (need) {
                        u32 mx = 0;
#pragma unroll
                        for (int ks = 0; ks < 5; ++ks) { a[ks] = ld_sc1(rws, o + ks * 20480); c2[ks] = ld_sc1(rws, o + ks * 20480 + 2560); }
#pragma unroll
                        for (int ks = 0; ks < 5; ++ks) mx = vmax4(vmax4(mx, a[ks]), c2[ks]);
                        need = mx == SENT;
                    }
                    if (__builtin_amdgcn_ballot_w64(need) == 0ull) break;
                }
                const f4 sa = (((asf4(a[0]) + asf4(a[1])) + asf4(a[2])) + asf4(a[3])) + asf4(a[4]);
                const f4 sc = (((asf4(c2[0]) + asf4(c2[1])) + asf4(c2[2])) + asf4(c2[3])) + asf4(c2[4]);
                const v2u st = *(LAS v2u*)(L + L_ST + hb * 8), xr = *(LAS v2u*)(L + L_XS + hb * 1280 + (hasF2 ? col : 0) * 2);
                const float den = asf(st[1]), rc = __builtin_amdgcn_rcpf(den);
                const f4 e = rb4(unpk(xr) - asf(st[0])), q1 = e * rc;
                const f4 h = rb4(rb4(fma4(fma4(-q1, bc4(den), e), bc4(rc), q1)) * rb4(rb4(sc) + 1.f)) + rb4(sa);
                *(LAS v2u*)(L + L_HB + hb * 256 + hi * 8) = v2u{pack2(h[0], h[1]), pack2(h[2], h[3])};
                LGKM0();
                if (l == 0) __hip_atomic_fetch_add((LAS u32*)(L + L_MISC), 1u, __ATOMIC_RELAXED, __HIP_MEMORY_SCOPE_WORKGROUP);
                if (w == 0) STAMP(4);
            }
            {
                v4u fq[4][4];
                VMW(12); read_unit(L, ta, 1, fq[0]); LGKM0();
                VMW(8); read_unit(L, ta, 2, fq[1]);
                VMW(4); read_unit(L, ta, 3, fq[2]);
                issueB(0);
                VMW(4); read_unit(L, ta, 0, fq[3]);
                if (w == 7) STAMP(3);
                for (int sp = 0; misc[0] < 2u && sp < SPIN_L; ++sp) __builtin_amdgcn_s_sleep(1);
                MEMBAR();
                f4 d[4] = {};
#pragma unroll
                for (int n = 0; n < 4; ++n) {
                    const v4u a = *(LAS v4u*)(L + L_HB + b4 * 256 + (32 * n + 8 * g) * 2);
#pragma unroll
                    for (int T2 = 0; T2 < 4; ++T2) d[T2] = mfma(fq[n][T2], a, d[T2]);
                }
                st_sc1(rws, qpo, canon4(sel_tile(d, tq)));
                BAR();
                if (w == 7) STAMP(5);
                issueB(1); issueB(2); issueB(3);
            }
            constexpr bool LATE = JIT && NM;
            if (NM) {
                f4 d[4] = {};
#pragma unroll
                for (int n = 0; n < NM; ++n) {
                    if (!LATE || n < 2) VMW(12); else if (n == 2) VMW(8); else if (n == 3) VMW(4); else VMW(0);
                    v4u f[4]; read_unit(L, ta, (n + 1) & 3, f);
                    const v4u a = silu8(*(LAS v4u*)(L + L_CS + b4 * 1280 + (160 * (w & 3) + 32 * n + 8 * g) * 2));
                    LGKM0();
                    if (!LATE || n == 0) issueB(n + 4);
#pragma unroll
                    for (int T2 = 0; T2 < 4; ++T2) d[T2] = mfma(f[T2], a, d[T2]);
                }
                *(LAS f4*)(L + L_RED + w * 1024 + l * 16) = sel_tile(d, tq);
                BAR();
                if ((w & 3) == 0) {
                    LAS f4* R = (LAS f4*)(L + L_RED + w * 1024 + l * 16);
                    const f4 s = (R[0] + R[64]) + (R[128] + R[192]);
                    st_sc1_2(rws, mfo, v2u{canon(pack2(s[0], s[1])), canon(pack2(s[2], s[3]))});
                }
                if (LATE && w == 1) {
                    for (int sp = 0; sp < SPIN_G; ++sp) {
                        MEMBAR();
                        const v4u e0 = ld_sc1(rws, WS_ML + l * 16), e1 = ld_sc1(rws, WS_ML + 1024 + l * 16);
                        const int cnt = __builtin_popcountll(__builtin_amdgcn_ballot_w64(e0[0] != SENT)) + __builtin_popcountll(__builtin_amdgcn_ballot_w64(e0[2] != SENT))
                                      + __builtin_popcountll(__builtin_amdgcn_ballot_w64(e1[0] != SENT)) + __builtin_popcountll(__builtin_amdgcn_ballot_w64(e1[2] != SENT));
                        if (cnt >= (ROLE == R_SB ? JIT_B : JIT_C)) break;
                        __builtin_amdgcn_s_sleep(8);
                    }
                }
                BAR();
                if (LATE) { if (w == 0) STAMP(29); issueB(5); issueB(6); issueB(7); issueB(8); }
            }
#pragma unroll
            for (int n = 0; n < NT; ++n) {
                const int a = NM + n, younger = (NA - 1 - a) < 3 ? (NA - 1 - a) : 3;
                if (younger == 3) VMW(12); else if (younger == 2) VMW(8); else if (younger == 1) VMW(4); else VMW(0);
                read_unit(L, ta, (a + 1) & 3, ft[n]);
                if (a + 4 < NA) { LGKM0(); issueB(a + 4); }
                if (ROLE == R_SA && n == 1 && w == 0) {
                    for (int sp = 0; sp < SPIN_G; ++sp) {
                        MEMBAR();
                        const v4u f0 = ld_sc1(rws, WS_FLAG + l * 16), f1 = ld_sc1(rws, WS_FLAG + 1024 + l * 16);
                        const bool bad = f0[0] != tlo || f0[1] != thi || f0[2] != tlo || f0[3] != thi || f1[0] != tlo || f1[1] != thi || f1[2] != tlo || f1[3] != thi;
                        if (__builtin_amdgcn_ballot_w64(bad) == 0ull) break;
                    }
                    if (u == 0 && l == 0) __hip_atomic_store((GAS u64*)(p.ws + WS_E), T, RLX_AGENT);
                }
            }
            LGKM0();
            BAR();
            STAMP(6);
        }
        if (ROLE == R_SA) {
            f4 res[2];
            UNDEF4(res[0]); UNDEF4(res[1]);
            u32 oo[2], mo[2];
#pragma unroll
            for (int r = 0; r < 2; ++r) {
                const int row = 2048 * ra + 256 * w + 32 * (4 * r + tq) + 8 * g, hd = row / DH, dd = row - hd * DH;
                oo[r] = WS_OP + (u32)((b4 * 32 + hd) * 2) * 384u + dd * 2; mo[r] = WS_ML + (u32)(hd * 4 + b4) * 16u;
            }
            bool done[2] = {false, false}, wdone[2] = {false, false}, obad[2] = {true, true}, mbad[2] = {true, true};
            v4u a[2][2], ml[2];
            float w0[2], w1[2];
#pragma unroll
            for (int r = 0; r < 2; ++r) { UNDEF4(ml[r]); UNDEF4(w0[r]); UNDEF4(w1[r]); UNDEF4(a[r][0]); UNDEF4(a[r][1]); }
            for (int sp = 0; sp < SPIN_G && !(done[0] && done[1]); ++sp) {
                MEMBAR();
                bool ldo[2];
#pragma unroll
                for (int r = 0; r < 2; ++r) {
                    ldo[r] = obad[r] && (!mbad[r] || (GATE0 && sp == 0) || !GATE);
                    if (mbad[r]) ml[r] = ld_sc1(rws, mo[r]);
                    if (ldo[r]) { a[r][0] = ld_sc1(rws, oo[r]); a[r][1] = ld_sc1(rws, oo[r] + 384); }
                }
                const bool last = sp == SPIN_G - 1;
#pragma unroll
                for (int r = 0; r < 2; ++r) {
                    if (!wdone[r]) {
                        mbad[r] = vmax4(0u, ml[r]) == SENT;
                        if (__builtin_amdgcn_ballot_w64(mbad[r]) == 0ull || last) {
                            const float m0 = asf(ml[r][0]), m1 = asf(ml[r][2]), M = fmaxf(m0, m1);
                            const float e0 = asf(ml[r][1]) * __builtin_amdgcn_exp2f((m0 - M) * LOG2E), e1 = asf(ml[r][3]) * __builtin_amdgcn_exp2f((m1 - M) * LOG2E);
                            const float inv = fdiv(1.f, e0 + e1);
                            w0[r] = e0 * inv; w1[r] = e1 * inv; wdone[r] = true;
                        }
                    }
                    if (!done[r]) {
                        obad[r] = obad[r] && (!ldo[r] || vmax4(vmax4(0u, a[r][0]), a[r][1]) == SENT);
                        if ((__builtin_amdgcn_ballot_w64(obad[r]) == 0ull && wdone[r]) || last) {
                            STAMP(20 + w);
                            const f4 x0 = fma4(unpk(v2u{a[r][0][0], a[r][0][1]}), bc4(w0[r]), unpk(v2u{a[r][1][0], a[r][1][1]}) * w1[r]);
                            const f4 x1 = fma4(unpk(v2u{a[r][0][2], a[r][0][3]}), bc4(w0[r]), unpk(v2u{a[r][1][2], a[r][1][3]}) * w1[r]);
                            const v4u bq = {pack2(x0[0], x0[1]), pack2(x0[2], x0[3]), pack2(x1[0], x1[1]), pack2(x1[2], x1[3])};
                            const v4u bk[4] = {bq, ror4k<1>(bq), ror4k<2>(bq), ror4k<3>(bq)};
                            f4 d[4] = {};
#pragma unroll
                            for (int s = 0; s < 4; ++s)
#pragma unroll
                                for (int T2 = 0; T2 < 4; ++T2) d[(T2 + s) & 3] = mfma(ft[4 * r + s][(T2 + s) & 3], bk[T2], d[(T2 + s) & 3]);
                            res[r] = sel_tile(d, tq);
                            done[r] = true;
                        }
                    }
                }
            }
            if (w == 0) STAMP(7);
            *(LAS f4*)(L + L_RED + w * 1024 + l * 16) = res[0] + res[1];
            BAR();
            if (w == 0) {
                STAMP(8);
                LAS f4* R = (LAS f4*)(L + L_RED + l * 16);
                st_sc1(rws, outo, canon4(((R[0] + R[64]) + (R[128] + R[192])) + ((R[256] + R[320]) + (R[384] + R[448]))));
                STAMP(9);
            }
        } else if (ROLE == R_SB) {
            const int i16 = l & 15, col = 128 * w + 4 * i16;
            f4 xa, xb, c1a, c1b, sba, sbb;
            if (w < 5) {
                v2u gv[2], sv[2], cv[2]; v4u a[6];
                const u32 mo = WS_MODF + (u32)(g * 2560 + col) * 2u;
                for (int sp = 0; sp < SPIN_G; ++sp) {
                    MEMBAR();
                    u32 mx = 0;
#pragma unroll
                    for (int h = 0; h < 2; ++h) { gv[h] = ld_sc1_2(rws, mo + h * 128); sv[h] = ld_sc1_2(rws, mo + 1280 + h * 128); cv[h] = ld_sc1_2(rws, mo + 2560 + h * 128); }
#pragma unroll
                    for (int h = 0; h < 2; ++h) mx = umax3(umax3(umax3(mx, gv[h][0], gv[h][1]), sv[h][0], sv[h][1]), cv[h][0], cv[h][1]);
                    if (all_valid(mx)) break;
                }
                xa = unpk(*(LAS v2u*)(L + L_XS + g * 1280 + col * 2)); xb = unpk(*(LAS v2u*)(L + L_XS + g * 1280 + col * 2 + 128));
                f4 gaa = unpk(gv[0]), gab = unpk(gv[1]);
                c1a = rb4(unpk(cv[0]) + 1.f); c1b = rb4(unpk(cv[1]) + 1.f);
                sba = unpk(sv[0]); sbb = unpk(sv[1]);
                asm volatile("" : "+v"(xa), "+v"(xb), "+v"(gaa), "+v"(gab), "+v"(c1a), "+v"(c1b), "+v"(sba), "+v"(sbb));
                if (w == 0) STAMP(10);
                const u32 ao = WS_AP + (u32)(g * DM + col) * 4u;
#pragma unroll
                for (int r = 0; r < 6; ++r) UNDEF4(a[r]);
                bool need = true;
                for (int sp = 0; sp < SPIN_G; ++sp) {
                    MEMBAR();
                    if (need) {
                        u32 mx = 0;
#pragma unroll
                        for (int r = 0; r < 3; ++r) { a[2 * r] = ld_sc1(rws, ao + r * 10240); a[2 * r + 1] = ld_sc1(rws, ao + r * 10240 + 256); }
#pragma unroll
                        for (int r = 0; r < 6; ++r) mx = vmax4(mx, a[r]);
                        need = mx == SENT;
                    }
                    if (__builtin_amdgcn_ballot_w64(need) == 0ull) break;
                    if (SLB) __builtin_amdgcn_s_sleep(SLB);
                }
                if (w == 0) STAMP(11);
                xa = rb4(xa + rb4(gaa * rb4((asf4(a[0]) + asf4(a[2])) + asf4(a[4]))));
                xb = rb4(xb + rb4(gab * rb4((asf4(a[1]) + asf4(a[3])) + asf4(a[5]))));
                const f4 t = xa + xb;
                const float mw = row_sum((t[0] + t[1]) + (t[2] + t[3])) * (1.f / 128.f);
                const f4 da = xa - mw, db = xb - mw, q2 = fma4(da, da, db * db);
                const float v = row_sum((q2[0] + q2[1]) + (q2[2] + q2[3]));
                *(LAS v2u*)(L + L_ST + 64 + g * 48 + w * 8) = v2u{asu(mw), asu(v)};
            }
            BAR();
            if (w < 5) {
                const f4 s0 = *(LAS f4*)(L + L_ST + 64 + g * 48), s1 = *(LAS f4*)(L + L_ST + 64 + g * 48 + 16);
                const f2 s2 = *(LAS f2*)(L + L_ST + 64 + g * 48 + 32);
                const float mean = (((s0[0] + s0[2]) + (s1[0] + s1[2])) + s2[0]) * 0.2f;
                const f4 dm = f4{s0[0], s0[2], s1[0], s1[2]} - mean, dq = dm * dm; const float d4 = s2[0] - mean;
                const float M2 = fmaf(128.f, fmaf(d4, d4, (dq[0] + dq[1]) + (dq[2] + dq[3])), ((s0[1] + s0[3]) + (s1[1] + s1[3])) + s2[1]);
                const float mb = rbf(mean), den = rbf(__builtin_amdgcn_sqrtf(rbf(rbf(M2 * (1.f / DM)) + 1e-5f))), rc = __builtin_amdgcn_rcpf(den);
                const f4 ea = rb4(xa - mb), eb = rb4(xb - mb), qa = ea * rc, qb = eb * rc;
                const f4 ha = rb4(rb4(fma4(fma4(-qa, bc4(den), ea), bc4(rc), qa)) * c1a) + sba, hb = rb4(rb4(fma4(fma4(-qb, bc4(den), eb), bc4(rc), qb)) * c1b) + sbb;
                *(LAS v2u*)(L + L_HB + g * 1280 + col * 2) = v2u{pack2(ha[0], ha[1]), pack2(ha[2], ha[3])};
                *(LAS v2u*)(L + L_HB + g * 1280 + col * 2 + 128) = v2u{pack2(hb[0], hb[1]), pack2(hb[2], hb[3])};
            }
            BAR();
            if (w == 0) STAMP(12);
            f4 d[4] = {};
#pragma unroll
            for (int n = 0; n < 5; ++n) {
                const v4u a = *(LAS v4u*)(L + L_HB + b4 * 1280 + (160 * (w >> 1) + 32 * n + 8 * g) * 2);
#pragma unroll
                for (int T2 = 0; T2 < 4; ++T2) d[T2] = mfma(ft[n][T2], a, d[T2]);
            }
            *(LAS f4*)(L + L_RED + w * 1024 + l * 16) = sel_tile(d, tq);
            BAR();
            if (w == 0) {
                STAMP(13);
                LAS f4* R = (LAS f4*)(L + L_RED + l * 16);
                const f4 gt = rb4((R[0] + R[128]) + (R[256] + R[384])), up = rb4((R[64] + R[192]) + (R[320] + R[448]));
                const f4 mk = {gt[0] > 0.f ? 1.f : 0.f, gt[1] > 0.f ? 1.f : 0.f, gt[2] > 0.f ? 1.f : 0.f, gt[3] > 0.f ? 1.f : 0.f};
                const f4 hh = (gt * mk) * up;
                st_sc1_2(rws, outo, v2u{canon(pack2(hh[0], hh[1])), canon(pack2(hh[2], hh[3]))});
                STAMP(14);
            }
        } else {
            f4 x1, gbf;
            UNDEF4(x1); UNDEF4(gbf);
            if (w == 0) {
                v2u ga, gb;
                const u32 mo = WS_MODF + (u32)(b4 * 2560 + 64 * ri + dcol) * 2u;
                for (int sp = 0; sp < SPIN_G; ++sp) {
                    MEMBAR();
                    ga = ld_sc1_2(rws, mo); gb = ld_sc1_2(rws, mo + 3840);
                    if (all_valid(umax3(umax3(ga[0], ga[1], gb[0]), gb[1], 0u))) break;
                }
                const u32 ao = WS_AP + (u32)(b4 * DM + 64 * ri + dcol) * 4u;
                f4 s;
                for (int sp = 0; sp < SPIN_G; ++sp) {
                    MEMBAR();
                    const v4u p0 = ld_sc1(rws, ao), p1 = ld_sc1(rws, ao + 10240), p2 = ld_sc1(rws, ao + 20480);
                    s = (asf4(p0) + asf4(p1)) + asf4(p2);
                    if (all_valid(vmax4(vmax4(vmax4(0u, p0), p1), p2))) break;
                }
                x1 = rb4(unpk(*(LAS v2u*)(L + L_XS + b4 * 1280 + (64 * ri + dcol) * 2)) + rb4(unpk(ga) * rb4(s)));
                gbf = unpk(gb);
                asm volatile("" : "+v"(x1), "+v"(gbf));
                STAMP(15);
            }
            v4u a0, a1;
            UNDEF4(a0); UNDEF4(a1);
            const u32 h0 = WS_HID + (u32)(b4 * FF + 192 * w + 32 * tq + 8 * g) * 2u, h1 = tq < 2 ? h0 + 256 : OOB;
            bool need = true;
            for (int sp = 0; sp < SPIN_G; ++sp) {
                MEMBAR();
                if (need) { a0 = ld_sc1(rws, h0); a1 = ld_sc1(rws, h1); need = vmax4(vmax4(0u, a0), a1) == SENT; }
                if (__builtin_amdgcn_ballot_w64(need) == 0ull) break;
                if (SLB) __builtin_amdgcn_s_sleep(SLB);
            }
            if (w == 0) STAMP(16);
            const v4u bk[4] = {a0, ror4k<1>(a0), ror4k<2>(a0), ror4k<3>(a0)}, bl[4] = {a1, ror4k<1>(a1), ror4k<2>(a1), ror4k<3>(a1)};
            f4 d[4] = {};
#pragma unroll
            for (int k = 0; k < 4; ++k)
#pragma unroll
                for (int s = 0; s < 6; ++s) d[(s + k) & 3] = mfma(ft[s][(s + k) & 3], s < 4 ? bk[k] : bl[k], d[(s + k) & 3]);
            *(LAS f4*)(L + L_RED + w * 1024 + l * 16) = sel_tile(d, tq);
            BAR();
            if (w == 0) {
                STAMP(17);
                LAS f4* R = (LAS f4*)(L + L_RED + l * 16);
                const f4 o = x1 + rb4(gbf * rb4(((R[0] + R[64]) + (R[128] + R[192])) + ((R[256] + R[320]) + (R[384] + R[448]))));
                __builtin_amdgcn_raw_buffer_store_b64(v2u{pack2(o[0], o[1]), pack2(o[2], o[3])}, __builtin_amdgcn_make_buffer_rsrc((void*)p.out, 0, 4 * DM * 2, 0x00020000),
                                                      (u32)(b4 * DM + 64 * ri + dcol) * 2u, 0, 16);
                STAMP(18);
            }
        }
    }
}

typedef const __attribute__((address_space(4))) Params* kargp;
#define GET_PARAMS() kargp pp_ = kp; asm volatile("" : "+s"(pp_)); \
    const Params q_ = {pp_->x, pp_->kc, pp_->vc, pp_->wq, pp_->wo, pp_->wg, pp_->wu, pp_->wd, pp_->c, pp_->wm, pp_->out, pp_->ws};
__global__ void __launch_bounds__(512, 2) fused_block(Params p) {
    extern __shared__ __attribute__((aligned(16))) unsigned char smem[];
    LAS unsigned char* L = (LAS unsigned char*)smem;
    const kargp kp = (kargp)__builtin_amdgcn_kernarg_segment_ptr();
    asm volatile("s_load_dword s98, %0, 0x0\n\ts_load_dword s99, %0, 0x40" :: "s"(kp) : "s98", "s99");
    const int l = threadIdx.x & 63, w = __builtin_amdgcn_readfirstlane(threadIdx.x >> 6), c = blockIdx.x;
    const int x = c & 7, y = c >> 3;
    const int u = (x < 6 && y < 5) ? 10 * (x >> 1) + 5 * (x & 1) + y
                : (x >= 6 && y < 5) ? 54 + 5 * (x - 6) + y
                : 30 + 8 * (y - 5) + x;
    if (__builtin_expect(c >= NW, 1)) { GET_PARAMS(); kv_path(q_, L, c - NW, w, l); }
    else if (u < 30) { GET_PARAMS(); w_path<R_SA>(q_, L, u, w, l); }
    else if (u < 54) { GET_PARAMS(); w_path<R_SB>(q_, L, u, w, l); }
    else { GET_PARAMS(); w_path<R_SC>(q_, L, u, w, l); }
}

extern "C" void kernel_launch(void* const* d_in, const int* in_sizes, int n_in, void* d_out, int out_size, void* d_ws, size_t ws_size, hipStream_t stream) {
    Params p;
    p.x = (const u16*)d_in[0]; p.kc = (const u16*)d_in[1]; p.vc = (const u16*)d_in[2]; p.wq = (const u16*)d_in[3]; p.wo = (const u16*)d_in[4];
    p.wg = (const u16*)d_in[6]; p.wu = (const u16*)d_in[7]; p.wd = (const u16*)d_in[8]; p.c = (const u16*)d_in[9]; p.wm = (const u16*)d_in[10];
    p.out = (u16*)d_out; p.ws = (unsigned char*)d_ws;
    (void)hipFuncSetAttribute((const void*)fused_block, hipFuncAttributeMaxDynamicSharedMemorySize, (int)L_END);
    hipLaunchKernelGGL(fused_block, dim3(256), dim3(512), L_END, stream, p);
}
```

```cpp
#define SC0 0
#define JIT 0
#include <hip/hip_runtime.h>
#include <stdint.h>
#pragma clang fp contract(off)

typedef unsigned short u16;
typedef unsigned u32;
typedef unsigned long long u64;
typedef u32 v4u __attribute__((ext_vector_type(4)));
typedef u32 v2u __attribute__((ext_vector_type(2)));
typedef float f4 __attribute__((ext_vector_type(4)));
typedef short v4s __attribute__((ext_vector_type(4)));
typedef __bf16 bf8 __attribute__((ext_vector_type(8)));
typedef __bf16 bf2 __attribute__((ext_vector_type(2)));
#define LAS __attribute__((address_space(3)))
#define GAS __attribute__((address_space(1)))
#define RLX_AGENT __ATOMIC_RELAXED, __HIP_MEMORY_SCOPE_AGENT

#define PRETOUCH 1
#define POLL_SLEEP 2
#define SLA 0
#define SLB 0
#define FRQ 30
#define FRS 6
#define GATE 1
#define GATE0 1
#define ILV 1
#define JIT_B 16
#define JIT_C 176
#define KVAUX 2
#define WPOL "nt"
#define INIT_MODE 1

constexpr int DM = 640, NH = 32, SKV = 4096, DH = 192, WIN = 1024, FF = 1536, NQ = NH * DH;
constexpr u32 SENT = 0xFFFFFFFFu;
constexpr int NW = 64;
constexpr u32 WS_E = 0, WS_FLAG = 4096, WS_F1P = 8192, WS_MODF = WS_F1P + 5 * 4 * 1280 * 4, WS_QP = WS_MODF + 4 * 2560 * 2,
              WS_OP = WS_QP + 5 * 4 * NQ * 4, WS_ML = WS_OP + 128 * 2 * DH * 2, WS_AP = WS_ML + 32 * 4 * 16, WS_HID = WS_AP + 3 * 4 * DM * 4,
              WS_END = WS_HID + 4 * FF * 2;
constexpr u32 OOB = 0x7FFFFF00u;
constexpr u32 L_XS = 131072, L_CS = L_XS + 5120, L_HB = L_CS + 5120, L_RED = L_HB + 5120, L_ST = L_RED + 8192, L_GA = L_ST + 256, L_GB = L_GA + 1024,
              L_X1 = L_GB + 1024, L_MISC = L_X1 + 1024, L_STAMP = L_MISC + 64, L_END = L_STAMP + 256;
constexpr u32 K_W = 18432, K_PX = 1024, K_PX2 = 1536, K_V = 9216, K_QS = 8 * K_W, K_ML = K_QS + 768, K_END = K_ML + 64;
static_assert(K_END <= L_MISC, "LDS");
constexpr float LOG2E = 1.4426950408889634f;
static_assert(L_END <= 163840, "LDS");
constexpr int SPIN_G = 40000, SPIN_L = 2000000;

#define STAMP(i) do {} while (0)

struct Params { const u16 *x, *kc, *vc, *wq, *wo, *wg, *wu, *wd, *c, *wm; u16* out; unsigned char* ws; };

__device__ __forceinline__ v4u mk_srd(const void* p, u32 bytes) { const u64 a = (u64)p; return v4u{(u32)a, (u32)(a >> 32) & 0xffffu, bytes, 0x00020000u}; }
__device__ __forceinline__ float asf(u32 u) { return __builtin_bit_cast(float, u); }
__device__ __forceinline__ u32 asu(float f) { return __builtin_bit_cast(u32, f); }
__device__ __forceinline__ float bflo(u32 u) { return asf(u << 16); }
__device__ __forceinline__ float bfhi(u32 u) { return asf(u & 0xffff0000u); }
__device__ __forceinline__ u32 pack2(float lo, float hi) { bf2 v; v[0] = (__bf16)lo; v[1] = (__bf16)hi; return __builtin_bit_cast(u32, v); }
__device__ __forceinline__ float rbf(float f) { return (float)(__bf16)f; }
typedef float f2 __attribute__((ext_vector_type(2)));
__device__ __forceinline__ f2 rb2(f2 v) { return f2{rbf(v[0]), rbf(v[1])}; }
__device__ __forceinline__ f4 rb4(f4 v) {
    float a = v[0], b = v[1], c = v[2], d = v[3];
    asm("v_cvt_pk_bf16_f32 %0, 0, %0\n\tv_cvt_pk_bf16_f32 %1, 0, %1\n\tv_cvt_pk_bf16_f32 %2, 0, %2\n\tv_cvt_pk_bf16_f32 %3, 0, %3" : "+v"(a), "+v"(b), "+v"(c), "+v"(d));
    return f4{a, b, c, d}; }
__device__ __forceinline__ void rb8(f4& u, f4& v) {
    float a = u[0], b = u[1], c = u[2], d = u[3], e = v[0], f = v[1], g = v[2], h = v[3];
    asm("v_cvt_pk_bf16_f32 %0, 0, %0\n\tv_cvt_pk_bf16_f32 %1, 0, %1\n\tv_cvt_pk_bf16_f32 %2, 0, %2\n\tv_cvt_pk_bf16_f32 %3, 0, %3\n\t"
        "v_cvt_pk_bf16_f32 %4, 0, %4\n\tv_cvt_pk_bf16_f32 %5, 0, %5\n\tv_cvt_pk_bf16_f32 %6, 0, %6\n\tv_cvt_pk_bf16_f32 %7, 0, %7"
        : "+v"(a), "+v"(b), "+v"(c), "+v"(d), "+v"(e), "+v"(f), "+v"(g), "+v"(h));
    u = f4{a, b, c, d}; v = f4{e, f, g, h}; }
__device__ __forceinline__ f4 unpk(v2u v) { return f4{bflo(v[0]), bfhi(v[0]), bflo(v[1]), bfhi(v[1])}; }
__device__ __forceinline__ f4 fma4(f4 a, f4 b, f4 c) { return __builtin_elementwise_fma(a, b, c); }
__device__ __forceinline__ f4 bc4(float x) { return f4{x, x, x, x}; }
__device__ __forceinline__ u32 canon(u32 u) { return u < 0xFFFFFFFEu ? u : 0xFFFFFFFEu; }
__device__ __forceinline__ v4u canon4(f4 v) { return v4u{canon(asu(v[0])), canon(asu(v[1])), canon(asu(v[2])), canon(asu(v[3]))}; }
__device__ __forceinline__ u32 umax3(u32 a, u32 b, u32 c) { const u32 m = a > b ? a : b; return m > c ? m : c; }
__device__ __forceinline__ u32 vmax4(u32 m, v4u v) { return umax3(umax3(m, v[0], v[1]), v[2], v[3]); }
__device__ __forceinline__ bool all_valid(u32 m) { return __builtin_amdgcn_ballot_w64(m == SENT) == 0ull; }
__device__ __forceinline__ float dot2(u32 a, u32 b, float c) { return __builtin_amdgcn_fdot2_f32_bf16(__builtin_bit_cast(bf2, a), __builtin_bit_cast(bf2, b), c, false); }
__device__ __forceinline__ f4 mfma(v4u a, v4u b, f4 c) { return __builtin_amdgcn_mfma_f32_16x16x32_bf16(__builtin_bit_cast(bf8, a), __builtin_bit_cast(bf8, b), c, 0, 0, 0); }
template <int CTRL> __device__ __forceinline__ u32 dppu(u32 x) { return (u32)__builtin_amdgcn_mov_dpp((int)x, CTRL, 0xf, 0xf, true); }
template <int CTRL> __device__ __forceinline__ float dppf(float x) { return asf(dppu<CTRL>(asu(x))); }
template <int K> __device__ __forceinline__ v4u ror4k(v4u v) {
    if constexpr (K == 0) return v; else return v4u{dppu<0x120 + 4 * K>(v[0]), dppu<0x120 + 4 * K>(v[1]), dppu<0x120 + 4 * K>(v[2]), dppu<0x120 + 4 * K>(v[3])}; }
__device__ __forceinline__ float row_sum(float x) { x += dppf<0x128>(x); x += dppf<0x124>(x); x += dppf<0x122>(x); x += dppf<0x121>(x); return x; }
__device__ __forceinline__ float row_max(float x) { x = fmaxf(x, dppf<0x128>(x)); x = fmaxf(x, dppf<0x124>(x)); x = fmaxf(x, dppf<0x122>(x)); x = fmaxf(x, dppf<0x121>(x)); return x; }
__device__ __forceinline__ float xrow_sum(float x) {
    auto s = __builtin_amdgcn_permlane16_swap(asu(x), asu(x), false, false); x = asf(s[0]) + asf(s[1]);
    auto t = __builtin_amdgcn_permlane32_swap(asu(x), asu(x), false, false); return asf(t[0]) + asf(t[1]); }
__device__ __forceinline__ float xrow_max(float x) {
    auto s = __builtin_amdgcn_permlane16_swap(asu(x), asu(x), false, false); x = fmaxf(asf(s[0]), asf(s[1]));
    auto t = __builtin_amdgcn_permlane32_swap(asu(x), asu(x), false, false); return fmaxf(asf(t[0]), asf(t[1])); }
__device__ __forceinline__ float wave_sum(float x) { return xrow_sum(row_sum(x)); }
__device__ __forceinline__ float wave_max(float x) { return xrow_max(row_max(x)); }
__device__ __forceinline__ float fdiv(float a, float b) { const float r = __builtin_amdgcn_rcpf(b); const float q = a * r; return fmaf(fmaf(-q, b, a), r, q); }
__device__ __forceinline__ float silu_bf(float v) { return fdiv(v, 1.f + __builtin_amdgcn_exp2f(fminf(-v * 1.4426950408889634f, 126.f))); }
__device__ __forceinline__ v4u silu8(v4u c) {
    v4u r;
#pragma unroll
    for (int i = 0; i < 4; ++i) r[i] = pack2(silu_bf(bflo(c[i])), silu_bf(bfhi(c[i])));
    return r; }
#define BAR() asm volatile("s_waitcnt lgkmcnt(0)\n\ts_barrier" ::: "memory")
#define LGKM0() asm volatile("s_waitcnt lgkmcnt(0)" ::: "memory")
#define VMW(n) asm volatile("s_waitcnt vmcnt(%0)" :: "i"(n) : "memory")
#define MEMBAR() asm volatile("" ::: "memory")
typedef __amdgpu_buffer_rsrc_t rsrc_t;
__device__ __forceinline__ v4u ld_sc1(rsrc_t r, u32 off) { return __builtin_amdgcn_raw_buffer_load_b128(r, off, 0, 16); }
__device__ __forceinline__ v2u ld_sc1_2(rsrc_t r, u32 off) { return __builtin_amdgcn_raw_buffer_load_b64(r, off, 0, 16); }
__device__ __forceinline__ void st_sc1(rsrc_t r, u32 off, v4u v) { __builtin_amdgcn_raw_buffer_store_b128(v, r, off, 0, 16); }
__device__ __forceinline__ void st_sc1_2(rsrc_t r, u32 off, v2u v) { __builtin_amdgcn_raw_buffer_store_b64(v, r, off, 0, 16); }
__device__ __forceinline__ f4 asf4(v4u v) { return __builtin_bit_cast(f4, v); }

__device__ __forceinline__ void dma_unit(v4u srd, u32 v0, u32 v1, u32 sA, u32 sB, u32 lds) {
    u32 keep;
    asm volatile("s_nop 4\n\ts_mov_b32 %0, m0\n\ts_mov_b32 m0, %6\n\ts_nop 0\n\t"
                 "buffer_load_dwordx4 %1, %3, %4 offen " WPOL " lds\n\ts_add_u32 m0, m0, 0x400\n\ts_nop 0\n\t"
                 "buffer_load_dwordx4 %2, %3, %4 offen " WPOL " lds\n\ts_add_u32 m0, m0, 0x400\n\ts_nop 0\n\t"
                 "buffer_load_dwordx4 %1, %3, %5 offen " WPOL " lds\n\ts_add_u32 m0, m0, 0x400\n\ts_nop 0\n\t"
                 "buffer_load_dwordx4 %2, %3, %5 offen " WPOL " lds\n\ts_mov_b32 m0, %0"
                 : "=&s"(keep) : "v"(v0), "v"(v1), "s"(srd), "s"(sA), "s"(sB), "s"(lds) : "memory", "scc");
}
__device__ __forceinline__ void dma_1k(v4u srd, u32 voff, u32 soff, u32 lds) {
    u32 keep;
    asm volatile("s_nop 4\n\ts_mov_b32 %0, m0\n\ts_mov_b32 m0, %4\n\ts_nop 0\n\tbuffer_load_dwordx4 %1, %2, %3 offen lds\n\ts_mov_b32 m0, %0"
                 : "=&s"(keep) : "v"(voff), "s"(srd), "s"(soff), "s"(lds) : "memory");
}
struct WStream { v4u srd; u32 base, pitch, v0, v1; };
template <bool HALF = false>
__device__ __forceinline__ WStream mk_stream(v4u srd, u32 base, u32 pitch, int l) {
    WStream s; s.srd = srd; s.base = base; s.pitch = pitch;
    const u32 sw = HALF ? 2u * ((l >> 4) & 3) : 2u * ((l >> 4) & 1);
    s.v0 = (l >> 3) * pitch + (((l & 7) ^ sw) << 4);
    s.v1 = (8 + (l >> 3)) * pitch + (((l & 7) ^ (HALF ? sw : (sw | 4u))) << 4);
    return s; }
__device__ __forceinline__ void issue(const WStream& s, int n, u32 lds) { const u32 a = s.base + (u32)n * 32u * s.pitch; dma_unit(s.srd, s.v0, s.v1, a, a + 16u * s.pitch, lds); }
template <int HOFF = 512>
__device__ __forceinline__ void read_unit(LAS unsigned char* L, const u32 (&ta)[4], int slot, v4u (&f)[4]) {
#pragma unroll
    for (int T = 0; T < 4; ++T) {
        const v2u lo = __builtin_bit_cast(v2u, __builtin_amdgcn_ds_read_tr16_b64_v4i16((LAS v4s*)(L + ta[T] + slot * 4096)));
        const v2u hi = __builtin_bit_cast(v2u, __builtin_amdgcn_ds_read_tr16_b64_v4i16((LAS v4s*)(L + ta[T] + slot * 4096 + HOFF)));
        f[T] = v4u{lo[0], lo[1], hi[0], hi[1]};
    } }
__device__ __forceinline__ f4 sel_tile(const f4 (&a)[4], int t) {
    const bool b0 = t & 1, b1 = t & 2;
    f4 r;
#pragma unroll
    for (int i = 0; i < 4; ++i) { const float lo = b0 ? a[1][i] : a[0][i], hi = b0 ? a[3][i] : a[2][i]; r[i] = b1 ? hi : lo; }
    return r; }

__device__ __forceinline__ void dma_kv(v4u srd, u32 vo, u32 so, u32 lds) {
    u32 keep;
    asm volatile("s_nop 4\n\ts_mov_b32 %0, m0\n\ts_mov_b32 m0, %4\n\ts_nop 0\n\t"
                 "buffer_load_dwordx4 %1, %2, %3 offen " WPOL " lds\n\ts_add_u32 m0, m0, 0x380\n\ts_nop 0\n\t"
                 "buffer_load_dwordx4 %1, %2, %3 offen offset:128 " WPOL " lds\n\ts_add_u32 m0, m0, 0x380\n\ts_nop 0\n\t"
                 "buffer_load_dwordx4 %1, %2, %3 offen offset:256 " WPOL " lds\n\ts_mov_b32 m0, %0"
                 : "=&s"(keep) : "v"(vo), "s"(srd), "s"(so), "s"(lds) : "memory", "scc");
}
__device__ __forceinline__ float qk_unit(v4u k0, v4u k1, v4u k2, const v4u (&q)[3]) {
    float s = 0.f, t = 0.f;
#pragma unroll
    for (int r = 0; r < 4; r += 2) { s = dot2(k0[r], q[0][r], s); t = dot2(k0[r + 1], q[0][r + 1], t); }
#pragma unroll
    for (int r = 0; r < 4; r += 2) { s = dot2(k1[r], q[1][r], s); t = dot2(k1[r + 1], q[1][r + 1], t); }
#pragma unroll
    for (int r = 0; r < 4; r += 2) { s = dot2(k2[r], q[2][r], s); t = dot2(k2[r + 1], q[2][r + 1], t); }
    return s + t;
}
__device__ __forceinline__ void pv_unit(v4u v0, v4u v1, v4u v2, u32 plo, float (&acc)[24]) {
    const u32 phi = plo << 16;
#pragma unroll
    for (int r = 0; r < 4; ++r) {
        acc[2 * r] = dot2(v0[r], plo, acc[2 * r]); acc[2 * r + 1] = dot2(v0[r], phi, acc[2 * r + 1]);
        acc[8 + 2 * r] = dot2(v1[r], plo, acc[8 + 2 * r]); acc[8 + 2 * r + 1] = dot2(v1[r], phi, acc[8 + 2 * r + 1]);
        acc[16 + 2 * r] = dot2(v2[r], plo, acc[16 + 2 * r]); acc[16 + 2 * r + 1] = dot2(v2[r], phi, acc[16 + 2 * r + 1]);
    }
}

__device__ __forceinline__ void kv_path(const Params& p, LAS unsigned char* L, int j, int w, int l) {
    const rsrc_t rws = __builtin_amdgcn_make_buffer_rsrc((void*)p.ws, 0, WS_END, 0x00020000);
    const rsrc_t rk = __builtin_amdgcn_make_buffer_rsrc((void*)p.kc, 0, 4 * NH * SKV * DH * 2, 0x00020000);
    const rsrc_t rv = __builtin_amdgcn_make_buffer_rsrc((void*)p.vc, 0, 4 * NH * SKV * DH * 2, 0x00020000);
    volatile LAS u32* misc = (volatile LAS u32*)(L + L_MISC);
    const int rk3 = (j >> 3) / 3, t3 = (j & 7) * 8 + rk3, pos = (j >> 3) - 3 * rk3;
    const int slot = pos == 1 ? (w & 1) : 0;
    const int bh = 2 * t3 + (pos == 2 ? 1 : (pos == 1 ? slot : 0));
    const int part = pos == 0 ? 0 : (pos == 2 ? 1 : (slot == 0 ? 1 : 0));
    const int wi = pos == 1 ? (w >> 1) : w;
    const int ub = pos == 0 ? 0 : (pos == 2 ? 43 : (slot == 0 ? 85 : 0));
    const int ng = pos == 1 ? 2 : 4, u1 = ub + (wi >= ng ? 11 * ng + wi - ng : wi);
    const u32 ust = ng * 3072;
    const int nl = (pos == 1 ? wi == 3 : wi >= 5) ? 2 : 3;
    const bool lead = w == 0 || (w == 1 && pos == 1);
    const u32 opb = WS_OP + (bh * 2 + part) * 384, mlb = WS_ML + ((bh & 31) * 4 + (bh >> 5)) * 16 + part * 8;
    if (threadIdx.x < 12) misc[threadIdx.x] = 0;
    BAR();
    STAMP(0);
    if (w == 4) {
        __builtin_amdgcn_s_setprio(3);
        const u64 T = __hip_atomic_load((GAS u64*)(p.ws + WS_E), RLX_AGENT) + 1ull;
        st_sc1(rws, l < 24 ? opb + l * 16 : OOB, v4u{SENT, SENT, SENT, SENT});
        st_sc1_2(rws, l == 0 ? mlb : OOB, v2u{SENT, SENT});
        const int bh1 = 2 * t3 + 1;
        st_sc1(rws, (pos == 1 && l < 24) ? WS_OP + bh1 * 2 * 384 + l * 16 : OOB, v4u{SENT, SENT, SENT, SENT});
        st_sc1_2(rws, (pos == 1 && l == 0) ? WS_ML + ((bh1 & 31) * 4 + (bh1 >> 5)) * 16 : OOB, v2u{SENT, SENT});
        VMW(0);
        if (l == 0) {
            __hip_atomic_store((GAS u64*)(p.ws + WS_FLAG) + blockIdx.x, T, RLX_AGENT);
            misc[4] = (u32)T; misc[5] = (u32)(T >> 32); LGKM0(); misc[2] = 1;
        }
        STAMP(9);
        __builtin_amdgcn_s_setprio(0);
    }
    const u32 vo = (l >> 3) * 384 + (l & 7) * 16;
    const u32 base = ((u32)bh * SKV + (SKV - WIN)) * 384u + (u32)u1 * 3072u;
    const u32 wl = w * K_W;
    const int c = l & 7;
    float m, lsum, fsc;
    const u32 nslot = pos == 1 ? 4u : 8u;
    auto slot_stats = [&](float& M, float& mi, float& li) {
        const v2u t = *(LAS v2u*)(L + K_ML + c * 8);
        const bool mine = pos != 1 || (l & 1) == slot;
        mi = mine ? asf(t[0]) : -INFINITY; li = mine ? asf(t[1]) : 0.f;
        M = fmaxf(mi, dppf<0xB1>(mi)); M = fmaxf(M, dppf<0x4E>(M)); M = fmaxf(M, dppf<0x141>(M));
    };
    auto post = [&]() {
        u32 old = 0;
        if (l == 0) { *(LAS v2u*)(L + K_ML + w * 8) = v2u{asu(m), asu(lsum)}; LGKM0(); old = __hip_atomic_fetch_add((LAS u32*)(L + L_MISC + 32 + slot * 4), 1u, __ATOMIC_RELAXED, __HIP_MEMORY_SCOPE_WORKGROUP); }
        if (__builtin_amdgcn_readfirstlane(old) == nslot - 1u) {
            MEMBAR();
            float M, mi, li; slot_stats(M, mi, li);
            float e = li * __builtin_amdgcn_exp2f((mi - M) * LOG2E);
            e += dppf<0xB1>(e); e += dppf<0x4E>(e); e += dppf<0x141>(e);
            st_sc1_2(rws, l == 0 ? mlb : OOB, v2u{canon(asu(M)), canon(asu(e))});
            STAMP(5);
        }
    };
    auto scale = [&]() {
        for (int sp = 0; misc[8 + slot] < nslot && sp < SPIN_L; ++sp) __builtin_amdgcn_s_sleep(1);
        MEMBAR();
        float M, mi, li; slot_stats(M, mi, li);
        float e = li * __builtin_amdgcn_exp2f((mi - M) * LOG2E);
        e += dppf<0xB1>(e); e += dppf<0x4E>(e); e += dppf<0x141>(e);
        fsc = fdiv(__builtin_amdgcn_exp2f((m - M) * LOG2E), e);
    };
    {
        const v4u sk = mk_srd(p.kc, 4 * NH * SKV * DH * 2), sv = mk_srd(p.vc, 4 * NH * SKV * DH * 2);
        const u32 lb = (u32)(size_t)L + wl;
        dma_kv(sk, vo, base + 8 * ust, lb); dma_kv(sk, vo, base + 9 * ust, lb + 3072);
        if (nl == 3) dma_kv(sk, vo, base + 10 * ust, lb + 6144);
        dma_kv(sv, vo, base + 8 * ust, lb + K_V); dma_kv(sv, vo, base + 9 * ust, lb + K_V + 3072);
        if (nl == 3) dma_kv(sv, vo, base + 10 * ust, lb + K_V + 6144);
    }
    v4u K[24], V[24];
#pragma unroll
    for (int jj = 0; jj < 8; ++jj)
#pragma unroll
        for (int i = 0; i < 3; ++i) K[jj * 3 + i] = __builtin_amdgcn_raw_buffer_load_b128(rk, vo + i * 128, base + jj * ust, KVAUX);
#pragma unroll
    for (int jj = 0; jj < 8; ++jj)
#pragma unroll
        for (int i = 0; i < 3; ++i) V[jj * 3 + i] = __builtin_amdgcn_raw_buffer_load_b128(rv, vo + i * 128, base + jj * ust, KVAUX);
    __builtin_amdgcn_sched_barrier(0);
    if (w == 0) STAMP(1);
    if (w == 7) STAMP(2);
    if (w < 4) {
        for (int sp = 0; misc[2] == 0 && sp < SPIN_L; ++sp) __builtin_amdgcn_s_sleep(1);
        const u32 tlo = misc[4], thi = misc[5];
        for (int sp = 0; sp < SPIN_G && misc[6] == 0; ++sp) {
            MEMBAR();
            const v2u f = ld_sc1_2(rws, WS_FLAG + l * 8);
            if (__builtin_amdgcn_ballot_w64(f[0] != tlo || f[1] != thi) == 0ull) misc[6] = 1;
        }
        const u32 qo = WS_QP + (u32)((bh >> 5) * NQ + (bh & 31) * DH + 4 * l) * 4u;
        v4u a[5] = {};
        bool need = l < 48;
        for (int sp = 0; sp < SPIN_G; ++sp) {
            MEMBAR();
            if (__builtin_amdgcn_readfirstlane(misc[slot]) != 0u) break;
            if (need) {
                u32 mx = 0;
#pragma unroll
                for (int ks = 0; ks < 5; ++ks) a[ks] = ld_sc1(rws, qo + ks * (4 * NQ * 4));
#pragma unroll
                for (int ks = 0; ks < 5; ++ks) mx = vmax4(mx, a[ks]);
                need = mx == SENT;
            }
            if (__builtin_amdgcn_ballot_w64(need) == 0ull || sp == SPIN_G - 1) {
                const f4 qs = (((asf4(a[0]) + asf4(a[1])) + asf4(a[2])) + asf4(a[3])) + asf4(a[4]);
                if (l < 48) *(LAS v2u*)(L + K_QS + slot * 384 + l * 8) = v2u{pack2(qs[0], qs[1]), pack2(qs[2], qs[3])};
                LGKM0();
                misc[slot] = 1;
                break;
            }
        }
        if (w == 0) STAMP(3);
    } else {
        for (int sp = 0; misc[slot] == 0 && sp < SPIN_L; ++sp) __builtin_amdgcn_s_sleep(1);
    }
    VMW(48);
    v4u p0, p1, p2;
    {
        v4u q[3];
#pragma unroll
        for (int i = 0; i < 3; ++i) q[i] = *(LAS v4u*)(L + K_QS + slot * 384 + (c + 8 * i) * 16);
        float y[4], x[8];
        LAS v4u* KL = (LAS v4u*)(L + wl + l * 16);
#pragma unroll
        for (int jj = 0; jj < 3; ++jj) { y[jj] = qk_unit(KL[jj * 192], KL[jj * 192 + 64], KL[jj * 192 + 128], q); __builtin_amdgcn_sched_barrier(0); }
#pragma unroll
        for (int jj = 0; jj < 8; ++jj) x[jj] = qk_unit(K[3 * jj], K[3 * jj + 1], K[3 * jj + 2], q);
#pragma unroll
        for (int h = 0; h < 4; ++h) { const float a = x[h] + dppf<0x141>(x[h]), b = x[h + 4] + dppf<0x141>(x[h + 4]); x[h] = (c & 4) ? b : a; }
        { const float a = y[0] + dppf<0x141>(y[0]), b = y[2] + dppf<0x141>(y[2]), a1 = y[1] + dppf<0x141>(y[1]); y[0] = (c & 4) ? b : a; y[1] = a1; }
#pragma unroll
        for (int h = 0; h < 2; ++h) { const float a = x[h] + dppf<0x4E>(x[h]), b = x[h + 2] + dppf<0x4E>(x[h + 2]); x[h] = (c & 2) ? b : a; }
        { const float a = y[0] + dppf<0x4E>(y[0]), b = y[1] + dppf<0x4E>(y[1]); y[0] = (c & 2) ? b : a; }
        { const float a = x[0] + dppf<0xB1>(x[0]), b = x[1] + dppf<0xB1>(x[1]); x[0] = (c & 1) ? b : a; }
        y[0] += dppf<0xB1>(y[0]);
        const float s1 = rbf(rbf(x[0]) * 0.07216878364870322f);
        const float s2 = (c >> 1) < nl ? rbf(rbf(y[0]) * 0.07216878364870322f) : -INFINITY;
        m = wave_max(fmaxf(s1, s2));
        const float e1 = __builtin_amdgcn_exp2f((s1 - m) * LOG2E), e2 = __builtin_amdgcn_exp2f((s2 - m) * LOG2E);
        *(LAS u32*)(L + wl + K_PX + l * 4) = pack2(e1, 0.f);
        *(LAS u32*)(L + wl + K_PX2 + (l >> 1) * 4) = pack2(e2, 0.f);
        lsum = wave_sum(e1 + ((c & 1) ? 0.f : e2));
        post();
        LGKM0();
        p0 = *(LAS v4u*)(L + wl + K_PX + (l >> 3) * 32); p1 = *(LAS v4u*)(L + wl + K_PX + (l >> 3) * 32 + 16); p2 = *(LAS v4u*)(L + wl + K_PX2 + (l >> 3) * 16);
    }
    if (w == 0) STAMP(4);
    float acc[24];
#pragma unroll
    for (int i = 0; i < 24; ++i) acc[i] = 0.f;
    {
        LAS v4u* VL = (LAS v4u*)(L + wl + K_V + l * 16);
        pv_unit(VL[0], VL[64], VL[128], p2[0], acc);
        pv_unit(VL[192], VL[256], VL[320], p2[1], acc);
        if (nl == 3) pv_unit(VL[384], VL[448], VL[512], p2[2], acc);
    }
#pragma unroll
    for (int jj = 0; jj < 6; ++jj) pv_unit(V[3 * jj], V[3 * jj + 1], V[3 * jj + 2], jj < 4 ? p0[jj & 3] : p1[jj & 3], acc);
    scale();
#pragma unroll
    for (int jj = 6; jj < 8; ++jj) pv_unit(V[3 * jj], V[3 * jj + 1], V[3 * jj + 2], p1[jj & 3], acc);
    if (w == 7) STAMP(6);
    float y[12], z[6];
#pragma unroll
    for (int n = 0; n < 12; ++n) { auto s = __builtin_amdgcn_permlane32_swap(asu(acc[n]), asu(acc[n + 12]), false, false); y[n] = asf(s[0]) + asf(s[1]); }
#pragma unroll
    for (int n = 0; n < 6; ++n) { auto s = __builtin_amdgcn_permlane16_swap(asu(y[n]), asu(y[n + 6]), false, false); z[n] = asf(s[0]) + asf(s[1]); }
#pragma unroll
    for (int n = 0; n < 3; ++n) {
        const float a = z[n] + dppf<0x128>(z[n]), b = z[n + 3] + dppf<0x128>(z[n + 3]);
        const int ai = 3 * (l >> 3) + n;
        *(LAS float*)(L + wl + (64 * (ai >> 3) + 8 * c + (ai & 7)) * 4) = ((l & 8) ? b : a) * fsc;
    }
    BAR();
    if (w == 0) STAMP(7);
    if (lead) {
        const u32 pb = slot * K_W + (l < 48 ? l : 47) * 16;
        f4 o = (*(LAS f4*)(L + pb) + *(LAS f4*)(L + pb + 2 * K_W)) + (*(LAS f4*)(L + pb + 4 * K_W) + *(LAS f4*)(L + pb + 6 * K_W));
        if (pos != 1) o += (*(LAS f4*)(L + pb + K_W) + *(LAS f4*)(L + pb + 3 * K_W)) + (*(LAS f4*)(L + pb + 5 * K_W) + *(LAS f4*)(L + pb + 7 * K_W));
        st_sc1_2(rws, l < 48 ? opb + l * 8 : OOB, v2u{canon(pack2(o[0], o[1])), canon(pack2(o[2], o[3]))});
        if (w == 0) STAMP(8); else STAMP(10);
    }
}

enum { R_SA = 0, R_SB = 1, R_SC = 2 };
#define UNDEF4(v) asm volatile("" : "=v"(v))
template <int ROLE>
__device__ __forceinline__ void w_path(const Params& p, LAS unsigned char* L, int u, int w, int l) {
    const rsrc_t rws = __builtin_amdgcn_make_buffer_rsrc((void*)p.ws, 0, WS_END, 0x00020000);
    const int tid = threadIdx.x, g = l >> 4, tq = (l & 15) >> 2, b4 = l & 3;
    const u32 lbase = (u32)(size_t)L, stg = lbase + w * 16384;
    const bool hasF1 = u < 50, hasF2 = u < 60;
    const int kf = u / 10, lp = u - 10 * kf, kf2 = u / 12, cs = u - 12 * kf2;
    const int ri = ROLE == R_SA ? u : (ROLE == R_SB ? u - 30 : u - 54);
    const WStream sF1 = mk_stream(mk_srd(p.wm, hasF1 ? DM * 3840 * 2 : 0), (u32)((128 * kf + 32 * (w >> 1)) * 7680 + (128 * lp + 64 * (w & 1)) * 2), 7680, l);
    const WStream sF2 = mk_stream(mk_srd(p.wq, hasF2 ? DM * NQ * 2 : 0), (u32)(128 * kf2 * 12288 + (512 * cs + 64 * w) * 2), 12288, l);
    const int mid = ROLE == R_SC ? (w < 4 ? (ri >= 6 ? ri : -1) : 30 + ri) : (w < 4 ? (ri < 6 ? ri : ri + 4) : (ri == 20 ? 28 : (ri == 21 ? 29 : -1)));
    const bool hasM = ROLE != R_SA && mid >= 0;
    const WStream sM = mk_stream(mk_srd(p.wm, hasM ? DM * 3840 * 2 : 0), (u32)(160 * (w & 3) * 7680 + (1280 + 64 * mid) * 2), 7680, l);
    const int ra = ri / 10, ca = ri - 10 * ra;
    const WStream sT = ROLE == R_SA ? mk_stream(mk_srd(p.wo, NQ * DM * 2), (u32)((2048 * ra + 256 * w) * 1280 + ca * 128), 1280, l)
                     : ROLE == R_SB ? mk_stream(mk_srd((w & 1) ? p.wu : p.wg, DM * FF * 2), (u32)(160 * (w >> 1) * 3072 + ri * 128), 3072, l)
                                    : mk_stream(mk_srd(p.wd, FF * DM * 2), (u32)(192 * w * 1280 + ri * 128), 1280, l);
    constexpr int NM = ROLE == R_SA ? 0 : 5, NT = ROLE == R_SA ? 8 : (ROLE == R_SB ? 5 : 6), NA = NM + NT;
    u32 ta[4];
    { const u32 f = 2u * (tq >> 1) | 4u * (g & 1);
#pragma unroll
      for (int T = 0; T < 4; ++T) ta[T] = w * 16384 + (8 * g + tq) * 128 + ((((u32)(2 * T)) ^ f) + (b4 >> 1)) * 16 + (b4 & 1) * 8; }
    const u32 dcol = 16 * tq + 4 * g;
    const u32 f1o = hasF1 ? WS_F1P + (u32)((kf * 4 + b4) * 1280 + 128 * lp + 64 * w + dcol) * 4u : OOB;
    const u32 qpo = hasF2 ? WS_QP + (u32)((kf2 * 4 + b4) * NQ + 512 * cs + 64 * w + dcol) * 4u : OOB;
    const u32 mfo = hasM ? WS_MODF + (u32)(b4 * 2560 + 64 * mid + dcol) * 2u : OOB;
    const u32 outo = ROLE == R_SA ? WS_AP + (u32)((ra * 4 + b4) * DM + 64 * ca + dcol) * 4u : WS_HID + (u32)(b4 * FF + 64 * ri + dcol) * 2u;
    volatile LAS u32* misc = (volatile LAS u32*)(L + L_MISC);
    STAMP(0);
    if (w < 5) { dma_1k(mk_srd(p.c, 5120), l * 16, w * 1024, lbase + L_CS + w * 1024); dma_1k(mk_srd(p.x, 5120), l * 16, w * 1024, lbase + L_XS + w * 1024); }
    issue(sF1, 0, stg);
    { u32 keep;
      asm volatile("s_nop 4\n\ts_mov_b32 %0, m0\n\ts_mov_b32 m0, %5\n\ts_nop 0\n\tbuffer_load_dword %1, %2, %3 offen " WPOL " lds\n\tbuffer_load_dword %1, %4, 0 offen " WPOL " lds\n\ts_mov_b32 m0, %0"
                   : "=&s"(keep) : "v"(l == 0 ? 0u : OOB), "s"(sT.srd), "s"(sT.base), "s"(mk_srd(p.out, 5120)), "s"(lbase + L_GB) : "memory"); }
    if (w == 0) {
        u32 keep;
        asm volatile("s_nop 4\n\ts_mov_b32 %0, m0\n\ts_mov_b32 m0, %3\n\ts_nop 0\n\tbuffer_load_dword %1, %2, 0 offen sc1 lds\n\ts_mov_b32 m0, %0"
                     : "=&s"(keep) : "v"(l < 2 ? WS_E + l * 4 : OOB), "s"(mk_srd(p.ws, WS_END)), "s"(lbase + L_GA) : "memory");
    }
    { const v4u s4 = {SENT, SENT, SENT, SENT};
      st_sc1(rws, qpo, s4);
      if (w < 2) st_sc1(rws, f1o, s4);
      if (ROLE != R_SA && (w & 3) == 0) st_sc1_2(rws, mfo, v2u{SENT, SENT});
      if (w == 0) {
          if (ROLE == R_SA) st_sc1(rws, outo, s4);
          if (ROLE == R_SB) st_sc1_2(rws, outo, v2u{SENT, SENT});
      } }
    if (tid == 0) { misc[0] = 0; misc[1] = 0; }
    BAR();
    issue(sF2, 0, stg + 4096);
    auto issueB = [&](int n) { if (n < NM) issue(sM, n, stg + ((n + 1) & 3) * 4096); else issue(sT, n - NM, stg + ((n + 1) & 3) * 4096); };
    {
        v4u ft[NT][4];
        {
            VMW(4);
            BAR();
            STAMP(1);
            const u64 T = *(LAS u64*)(L + L_GA) + 1ull;
            const u32 tlo = (u32)T, thi = (u32)(T >> 32);
            if (tid == 0) __hip_atomic_store((GAS u64*)(p.ws + WS_FLAG) + blockIdx.x, T, RLX_AGENT);
            {
                v4u f[4]; read_unit(L, ta, 0, f);
                const v4u a = silu8(*(LAS v4u*)(L + L_CS + b4 * 1280 + (128 * kf + 32 * (w >> 1) + 8 * g) * 2));
                f4 d[4];
#pragma unroll
                for (int T2 = 0; T2 < 4; ++T2) d[T2] = mfma(f[T2], a, f4{0.f, 0.f, 0.f, 0.f});
                *(LAS f4*)(L + L_RED + w * 1024 + l * 16) = sel_tile(d, tq);
                if (w >= 4) {
                    float xv[10], s = 0.f;
#pragma unroll
                    for (int i = 0; i < 5; ++i) { const u32 t = *(LAS u32*)(L + L_XS + (w - 4) * 1280 + (l + 64 * i) * 4); xv[2 * i] = bflo(t); xv[2 * i + 1] = bfhi(t); s += xv[2 * i] + xv[2 * i + 1]; }
                    const float mean = wave_sum(s) * (1.f / DM);
                    float v = 0.f;
#pragma unroll
                    for (int i = 0; i < 10; ++i) v = fmaf(xv[i] - mean, xv[i] - mean, v);
                    const float var = wave_sum(v) * (1.f / DM);
                    if (l == 0) *(LAS v2u*)(L + L_ST + (w - 4) * 8) = v2u{asu(rbf(mean)), asu(rbf(__builtin_amdgcn_sqrtf(rbf(rbf(var) + 1e-5f))))};
                }
                BAR();
                if (w < 2) {
                    LAS f4* R = (LAS f4*)(L + L_RED + w * 1024 + l * 16);
                    st_sc1(rws, f1o, canon4((R[0] + R[128]) + (R[256] + R[384])));
                }
                if (w == 0) STAMP(2);
                issue(sF2, 1, stg + 8192); issue(sF2, 2, stg + 12288); issue(sF2, 3, stg);
            }
            if (w < 2) {
                for (int sp = 0; sp < SPIN_G; ++sp) {
                    MEMBAR();
                    const v2u f = ld_sc1_2(rws, WS_FLAG + l * 8);
                    if (__builtin_amdgcn_ballot_w64(f[0] != tlo || f[1] != thi) == 0ull) break;
                }
                if (w == 0) STAMP(19);
                const int hb = tid >> 5, hi = tid & 31, col = 128 * kf2 + 4 * hi;
                const u32 o = WS_F1P + (u32)(hb * 1280 + col) * 4u;
                v4u a[5] = {}, c2[5] = {};
                bool need = hasF2;
                for (int sp = 0; sp < SPIN_G; ++sp) {
                    MEMBAR();
                    if (need) {
                        u32 mx = 0;
#pragma unroll
                        for (int ks = 0; ks < 5; ++ks) { a[ks] = ld_sc1(rws, o + ks * 20480); c2[ks] = ld_sc1(rws, o + ks * 20480 + 2560); }
#pragma unroll
                        for (int ks = 0; ks < 5; ++ks) mx = vmax4(vmax4(mx, a[ks]), c2[ks]);
                        need = mx == SENT;
                    }
                    if (__builtin_amdgcn_ballot_w64(need) == 0ull) break;
                }
                const f4 sa = (((asf4(a[0]) + asf4(a[1])) + asf4(a[2])) + asf4(a[3])) + asf4(a[4]);
                const f4 sc = (((asf4(c2[0]) + asf4(c2[1])) + asf4(c2[2])) + asf4(c2[3])) + asf4(c2[4]);
                const v2u st = *(LAS v2u*)(L + L_ST + hb * 8), xr = *(LAS v2u*)(L + L_XS + hb * 1280 + (hasF2 ? col : 0) * 2);
                const float den = asf(st[1]), rc = __builtin_amdgcn_rcpf(den);
                const f4 e = rb4(unpk(xr) - asf(st[0])), q1 = e * rc;
                const f4 h = rb4(rb4(fma4(fma4(-q1, bc4(den), e), bc4(rc), q1)) * rb4(rb4(sc) + 1.f)) + rb4(sa);
                *(LAS v2u*)(L + L_HB + hb * 256 + hi * 8) = v2u{pack2(h[0], h[1]), pack2(h[2], h[3])};
                LGKM0();
                if (l == 0) __hip_atomic_fetch_add((LAS u32*)(L + L_MISC), 1u, __ATOMIC_RELAXED, __HIP_MEMORY_SCOPE_WORKGROUP);
                if (w == 0) STAMP(4);
            }
            {
                v4u fq[4][4];
                VMW(12); read_unit(L, ta, 1, fq[0]); LGKM0();
                VMW(8); read_unit(L, ta, 2, fq[1]);
                VMW(4); read_unit(L, ta, 3, fq[2]);
                issueB(0);
                VMW(4); read_unit(L, ta, 0, fq[3]);
                if (w == 7) STAMP(3);
                for (int sp = 0; misc[0] < 2u && sp < SPIN_L; ++sp) __builtin_amdgcn_s_sleep(1);
                MEMBAR();
                f4 d[4] = {};
#pragma unroll
                for (int n = 0; n < 4; ++n) {
                    const v4u a = *(LAS v4u*)(L + L_HB + b4 * 256 + (32 * n + 8 * g) * 2);
#pragma unroll
                    for (int T2 = 0; T2 < 4; ++T2) d[T2] = mfma(fq[n][T2], a, d[T2]);
                }
                st_sc1(rws, qpo, canon4(sel_tile(d, tq)));
                BAR();
                if (w == 7) STAMP(5);
                issueB(1); issueB(2); issueB(3);
            }
            constexpr bool LATE = JIT && NM;
            if (NM) {
                f4 d[4] = {};
#pragma unroll
                for (int n = 0; n < NM; ++n) {
                    if (!LATE || n < 2) VMW(12); else if (n == 2) VMW(8); else if (n == 3) VMW(4); else VMW(0);
                    v4u f[4]; read_unit(L, ta, (n + 1) & 3, f);
                    const v4u a = silu8(*(LAS v4u*)(L + L_CS + b4 * 1280 + (160 * (w & 3) + 32 * n + 8 * g) * 2));
                    LGKM0();
                    if (!LATE || n == 0) issueB(n + 4);
#pragma unroll
                    for (int T2 = 0; T2 < 4; ++T2) d[T2] = mfma(f[T2], a, d[T2]);
                }
                *(LAS f4*)(L + L_RED + w * 1024 + l * 16) = sel_tile(d, tq);
                BAR();
                if ((w & 3) == 0) {
                    LAS f4* R = (LAS f4*)(L + L_RED + w * 1024 + l * 16);
                    const f4 s = (R[0] + R[64]) + (R[128] + R[192]);
                    st_sc1_2(rws, mfo, v2u{canon(pack2(s[0], s[1])), canon(pack2(s[2], s[3]))});
                }
                if (LATE && w == 1) {
                    for (int sp = 0; sp < SPIN_G; ++sp) {
                        MEMBAR();
                        const v4u e0 = ld_sc1(rws, WS_ML + l * 16), e1 = ld_sc1(rws, WS_ML + 1024 + l * 16);
                        const int cnt = __builtin_popcountll(__builtin_amdgcn_ballot_w64(e0[0] != SENT)) + __builtin_popcountll(__builtin_amdgcn_ballot_w64(e0[2] != SENT))
                                      + __builtin_popcountll(__builtin_amdgcn_ballot_w64(e1[0] != SENT)) + __builtin_popcountll(__builtin_amdgcn_ballot_w64(e1[2] != SENT));
                        if (cnt >= (ROLE == R_SB ? JIT_B : JIT_C)) break;
                        __builtin_amdgcn_s_sleep(8);
                    }
                }
                BAR();
                if (LATE) { if (w == 0) STAMP(29); issueB(5); issueB(6); issueB(7); issueB(8); }
            }
#pragma unroll
            for (int n = 0; n < NT; ++n) {
                const int a = NM + n, younger = (NA - 1 - a) < 3 ? (NA - 1 - a) : 3;
                if (younger == 3) VMW(12); else if (younger == 2) VMW(8); else if (younger == 1) VMW(4); else VMW(0);
                read_unit(L, ta, (a + 1) & 3, ft[n]);
                if (a + 4 < NA) { LGKM0(); issueB(a + 4); }
                if (ROLE == R_SA && n == 1 && w == 0) {
                    for (int sp = 0; sp < SPIN_G; ++sp) {
                        MEMBAR();
                        const v4u f0 = ld_sc1(rws, WS_FLAG + l * 16), f1 = ld_sc1(rws, WS_FLAG + 1024 + l * 16);
                        const bool bad = f0[0] != tlo || f0[1] != thi || f0[2] != tlo || f0[3] != thi || f1[0] != tlo || f1[1] != thi || f1[2] != tlo || f1[3] != thi;
                        if (__builtin_amdgcn_ballot_w64(bad) == 0ull) break;
                    }
                    if (u == 0 && l == 0) __hip_atomic_store((GAS u64*)(p.ws + WS_E), T, RLX_AGENT);
                }
            }
            LGKM0();
            BAR();
            STAMP(6);
        }
        if (ROLE == R_SA) {
            f4 res[2];
            UNDEF4(res[0]); UNDEF4(res[1]);
            u32 oo[2], mo[2];
#pragma unroll
            for (int r = 0; r < 2; ++r) {
                const int row = 2048 * ra + 256 * w + 32 * (4 * r + tq) + 8 * g, hd = row / DH, dd = row - hd * DH;
                oo[r] = WS_OP + (u32)((b4 * 32 + hd) * 2) * 384u + dd * 2; mo[r] = WS_ML + (u32)(hd * 4 + b4) * 16u;
            }
            bool done[2] = {false, false}, wdone[2] = {false, false}, obad[2] = {true, true}, mbad[2] = {true, true};
            v4u a[2][2], ml[2];
            float w0[2], w1[2];
#pragma unroll
            for (int r = 0; r < 2; ++r) { UNDEF4(ml[r]); UNDEF4(w0[r]); UNDEF4(w1[r]); UNDEF4(a[r][0]); UNDEF4(a[r][1]); }
            for (int sp = 0; sp < SPIN_G && !(done[0] && done[1]); ++sp) {
                MEMBAR();
                bool ldo[2];
#pragma unroll
                for (int r = 0; r < 2; ++r) {
                    ldo[r] = obad[r] && (!mbad[r] || (GATE0 && sp == 0) || !GATE);
                    if (mbad[r]) ml[r] = ld_sc1(rws, mo[r]);
                    if (ldo[r]) { a[r][0] = ld_sc1(rws, oo[r]); a[r][1] = ld_sc1(rws, oo[r] + 384); }
                }
                const bool last = sp == SPIN_G - 1;
#pragma unroll
                for (int r = 0; r < 2; ++r) {
                    if (!wdone[r]) {
                        mbad[r] = vmax4(0u, ml[r]) == SENT;
                        if (__builtin_amdgcn_ballot_w64(mbad[r]) == 0ull || last) {
                            const float m0 = asf(ml[r][0]), m1 = asf(ml[r][2]), M = fmaxf(m0, m1);
                            const float e0 = asf(ml[r][1]) * __builtin_amdgcn_exp2f((m0 - M) * LOG2E), e1 = asf(ml[r][3]) * __builtin_amdgcn_exp2f((m1 - M) * LOG2E);
                            const float inv = fdiv(1.f, e0 + e1);
                            w0[r] = e0 * inv; w1[r] = e1 * inv; wdone[r] = true;
                        }
                    }
                    if (!done[r]) {
                        obad[r] = obad[r] && (!ldo[r] || vmax4(vmax4(0u, a[r][0]), a[r][1]) == SENT);
                        if ((__builtin_amdgcn_ballot_w64(obad[r]) == 0ull && wdone[r]) || last) {
                            STAMP(20 + w);
                            const f4 x0 = fma4(unpk(v2u{a[r][0][0], a[r][0][1]}), bc4(w0[r]), unpk(v2u{a[r][1][0], a[r][1][1]}) * w1[r]);
                            const f4 x1 = fma4(unpk(v2u{a[r][0][2], a[r][0][3]}), bc4(w0[r]), unpk(v2u{a[r][1][2], a[r][1][3]}) * w1[r]);
                            const v4u bq = {pack2(x0[0], x0[1]), pack2(x0[2], x0[3]), pack2(x1[0], x1[1]), pack2(x1[2], x1[3])};
                            const v4u bk[4] = {bq, ror4k<1>(bq), ror4k<2>(bq), ror4k<3>(bq)};
                            f4 d[4] = {};
#pragma unroll
                            for (int s = 0; s < 4; ++s)
#pragma unroll
                                for (int T2 = 0; T2 < 4; ++T2) d[(T2 + s) & 3] = mfma(ft[4 * r + s][(T2 + s) & 3], bk[T2], d[(T2 + s) & 3]);
                            res[r] = sel_tile(d, tq);
                            done[r] = true;
                        }
                    }
                }
            }
            if (w == 0) STAMP(7);
            *(LAS f4*)(L + L_RED + w * 1024 + l * 16) = res[0] + res[1];
            BAR();
            if (w == 0) {
                STAMP(8);
                LAS f4* R = (LAS f4*)(L + L_RED + l * 16);
                st_sc1(rws, outo, canon4(((R[0] + R[64]) + (R[128] + R[192])) + ((R[256] + R[320]) + (R[384] + R[448]))));
                STAMP(9);
            }
        } else if (ROLE == R_SB) {
            const int i16 = l & 15, col = 128 * w + 4 * i16;
            f4 xa, xb, c1a, c1b, sba, sbb;
            if (w < 5) {
                v2u gv[2], sv[2], cv[2]; v4u a[6];
                const u32 mo = WS_MODF + (u32)(g * 2560 + col) * 2u;
                for (int sp = 0; sp < SPIN_G; ++sp) {
                    MEMBAR();
                    u32 mx = 0;
#pragma unroll
                    for (int h = 0; h < 2; ++h) { gv[h] = ld_sc1_2(rws, mo + h * 128); sv[h] = ld_sc1_2(rws, mo + 1280 + h * 128); cv[h] = ld_sc1_2(rws, mo + 2560 + h * 128); }
#pragma unroll
                    for (int h = 0; h < 2; ++h) mx = umax3(umax3(umax3(mx, gv[h][0], gv[h][1]), sv[h][0], sv[h][1]), cv[h][0], cv[h][1]);
                    if (all_valid(mx)) break;
                }
                xa = unpk(*(LAS v2u*)(L + L_XS + g * 1280 + col * 2)); xb = unpk(*(LAS v2u*)(L + L_XS + g * 1280 + col * 2 + 128));
                f4 gaa = unpk(gv[0]), gab = unpk(gv[1]);
                c1a = rb4(unpk(cv[0]) + 1.f); c1b = rb4(unpk(cv[1]) + 1.f);
                sba = unpk(sv[0]); sbb = unpk(sv[1]);
                asm volatile("" : "+v"(xa), "+v"(xb), "+v"(gaa), "+v"(gab), "+v"(c1a), "+v"(c1b), "+v"(sba), "+v"(sbb));
                if (w == 0) STAMP(10);
                const u32 ao = WS_AP + (u32)(g * DM + col) * 4u;
#pragma unroll
                for (int r = 0; r < 6; ++r) UNDEF4(a[r]);
                bool need = true;
                for (int sp = 0; sp < SPIN_G; ++sp) {
                    MEMBAR();
                    if (need) {
                        u32 mx = 0;
#pragma unroll
                        for (int r = 0; r < 3; ++r) { a[2 * r] = ld_sc1(rws, ao + r * 10240); a[2 * r + 1] = ld_sc1(rws, ao + r * 10240 + 256); }
#pragma unroll
                        for (int r = 0; r < 6; ++r) mx = vmax4(mx, a[r]);
                        need = mx == SENT;
                    }
                    if (__builtin_amdgcn_ballot_w64(need) == 0ull) break;
                    if (SLB) __builtin_amdgcn_s_sleep(SLB);
                }
                if (w == 0) STAMP(11);
                f4 ta2 = (asf4(a[0]) + asf4(a[2])) + asf4(a[4]), tb2 = (asf4(a[1]) + asf4(a[3])) + asf4(a[5]);
                rb8(ta2, tb2); ta2 *= gaa; tb2 *= gab; rb8(ta2, tb2); xa += ta2; xb += tb2; rb8(xa, xb);
                const float sh = dppf<0x150>(xa[0] + 0.f);
                const f4 da = xa - sh, db = xb - sh, t = da + db, q2 = fma4(da, da, db * db);
                float s1 = (t[0] + t[1]) + (t[2] + t[3]), s2 = (q2[0] + q2[1]) + (q2[2] + q2[3]);
                s1 += dppf<0x128>(s1); s2 += dppf<0x128>(s2); s1 += dppf<0x124>(s1); s2 += dppf<0x124>(s2);
                s1 += dppf<0x122>(s1); s2 += dppf<0x122>(s2); s1 += dppf<0x121>(s1); s2 += dppf<0x121>(s2);
                const float dm1 = s1 * (1.f / 128.f), mw = sh + dm1, v = fmaf(-dm1, s1, s2);
                *(LAS v2u*)(L + L_ST + 64 + g * 48 + w * 8) = v2u{asu(mw), asu(v)};
            }
            BAR();
            if (w < 5) {
                const f4 s0 = *(LAS f4*)(L + L_ST + 64 + g * 48), s1 = *(LAS f4*)(L + L_ST + 64 + g * 48 + 16);
                const f2 s2 = *(LAS f2*)(L + L_ST + 64 + g * 48 + 32);
                const float mean = (((s0[0] + s0[2]) + (s1[0] + s1[2])) + s2[0]) * 0.2f;
                const f4 dm = f4{s0[0], s0[2], s1[0], s1[2]} - mean, dq = dm * dm; const float d4 = s2[0] - mean;
                const float M2 = fmaf(128.f, fmaf(d4, d4, (dq[0] + dq[1]) + (dq[2] + dq[3])), ((s0[1] + s0[3]) + (s1[1] + s1[3])) + s2[1]);
                const float mb = rbf(mean), den = rbf(__builtin_amdgcn_sqrtf(rbf(rbf(M2 * (1.f / DM)) + 1e-5f))), rc = __builtin_amdgcn_rcpf(den);
                f4 ea = xa - mb, eb = xb - mb;
                rb8(ea, eb);
                const f4 qa = ea * rc, qb = eb * rc;
                f4 ha = fma4(fma4(-qa, bc4(den), ea), bc4(rc), qa), hb = fma4(fma4(-qb, bc4(den), eb), bc4(rc), qb);
                rb8(ha, hb); ha *= c1a; hb *= c1b; rb8(ha, hb); ha += sba; hb += sbb;
                *(LAS v2u*)(L + L_HB + g * 1280 + col * 2) = v2u{pack2(ha[0], ha[1]), pack2(ha[2], ha[3])};
                *(LAS v2u*)(L + L_HB + g * 1280 + col * 2 + 128) = v2u{pack2(hb[0], hb[1]), pack2(hb[2], hb[3])};
            }
            BAR();
            if (w == 0) STAMP(12);
            f4 d[4] = {};
#pragma unroll
            for (int n = 0; n < 5; ++n) {
                const v4u a = *(LAS v4u*)(L + L_HB + b4 * 1280 + (160 * (w >> 1) + 32 * n + 8 * g) * 2);
#pragma unroll
                for (int T2 = 0; T2 < 4; ++T2) d[T2] = mfma(ft[n][T2], a, d[T2]);
            }
            *(LAS f4*)(L + L_RED + w * 1024 + l * 16) = sel_tile(d, tq);
            BAR();
            if (w == 0) {
                STAMP(13);
                LAS f4* R = (LAS f4*)(L + L_RED + l * 16);
                const f4 gt = rb4((R[0] + R[128]) + (R[256] + R[384])), up = rb4((R[64] + R[192]) + (R[320] + R[448]));
                const f4 mk = {gt[0] > 0.f ? 1.f : 0.f, gt[1] > 0.f ? 1.f : 0.f, gt[2] > 0.f ? 1.f : 0.f, gt[3] > 0.f ? 1.f : 0.f};
                const f4 hh = (gt * mk) * up;
                st_sc1_2(rws, outo, v2u{canon(pack2(hh[0], hh[1])), canon(pack2(hh[2], hh[3]))});
                STAMP(14);
            }
        } else {
            f4 x1, gbf;
            UNDEF4(x1); UNDEF4(gbf);
            if (w == 0) {
                v2u ga, gb;
                const u32 mo = WS_MODF + (u32)(b4 * 2560 + 64 * ri + dcol) * 2u;
                for (int sp = 0; sp < SPIN_G; ++sp) {
                    MEMBAR();
                    ga = ld_sc1_2(rws, mo); gb = ld_sc1_2(rws, mo + 3840);
                    if (all_valid(umax3(umax3(ga[0], ga[1], gb[0]), gb[1], 0u))) break;
                }
                const u32 ao = WS_AP + (u32)(b4 * DM + 64 * ri + dcol) * 4u;
                f4 s;
                for (int sp = 0; sp < SPIN_G; ++sp) {
                    MEMBAR();
                    const v4u p0 = ld_sc1(rws, ao), p1 = ld_sc1(rws, ao + 10240), p2 = ld_sc1(rws, ao + 20480);
                    s = (asf4(p0) + asf4(p1)) + asf4(p2);
                    if (all_valid(vmax4(vmax4(vmax4(0u, p0), p1), p2))) break;
                }
                x1 = rb4(unpk(*(LAS v2u*)(L + L_XS + b4 * 1280 + (64 * ri + dcol) * 2)) + rb4(unpk(ga) * rb4(s)));
                gbf = unpk(gb);
                asm volatile("" : "+v"(x1), "+v"(gbf));
                STAMP(15);
            }
            v4u a0, a1;
            UNDEF4(a0); UNDEF4(a1);
            const u32 h0 = WS_HID + (u32)(b4 * FF + 192 * w + 32 * tq + 8 * g) * 2u, h1 = tq < 2 ? h0 + 256 : OOB;
            bool need = true;
            for (int sp = 0; sp < SPIN_G; ++sp) {
                MEMBAR();
                if (need) { a0 = ld_sc1(rws, h0); a1 = ld_sc1(rws, h1); need = vmax4(vmax4(0u, a0), a1) == SENT; }
                if (__builtin_amdgcn_ballot_w64(need) == 0ull) break;
                if (SLB) __builtin_amdgcn_s_sleep(SLB);
            }
            if (w == 0) STAMP(16);
            const v4u bk[4] = {a0, ror4k<1>(a0), ror4k<2>(a0), ror4k<3>(a0)}, bl[4] = {a1, ror4k<1>(a1), ror4k<2>(a1), ror4k<3>(a1)};
            f4 d[4] = {};
#pragma unroll
            for (int k = 0; k < 4; ++k)
#pragma unroll
                for (int s = 0; s < 6; ++s) d[(s + k) & 3] = mfma(ft[s][(s + k) & 3], s < 4 ? bk[k] : bl[k], d[(s + k) & 3]);
            *(LAS f4*)(L + L_RED + w * 1024 + l * 16) = sel_tile(d, tq);
            BAR();
            if (w == 0) {
                STAMP(17);
                LAS f4* R = (LAS f4*)(L + L_RED + l * 16);
                const f4 o = x1 + rb4(gbf * rb4(((R[0] + R[64]) + (R[128] + R[192])) + ((R[256] + R[320]) + (R[384] + R[448]))));
                __builtin_amdgcn_raw_buffer_store_b64(v2u{pack2(o[0], o[1]), pack2(o[2], o[3])}, __builtin_amdgcn_make_buffer_rsrc((void*)p.out, 0, 4 * DM * 2, 0x00020000),
                                                      (u32)(b4 * DM + 64 * ri + dcol) * 2u, 0, 16);
                STAMP(18);
            }
        }
    }
}

typedef const __attribute__((address_space(4))) Params* kargp;
#define GET_PARAMS() kargp pp_ = kp; asm volatile("" : "+s"(pp_)); \
    const Params q_ = {pp_->x, pp_->kc, pp_->vc, pp_->wq, pp_->wo, pp_->wg, pp_->wu, pp_->wd, pp_->c, pp_->wm, pp_->out, pp_->ws};
__global__ void __launch_bounds__(512, 2) fused_block(Params p) {
    extern __shared__ __attribute__((aligned(16))) unsigned char smem[];
    LAS unsigned char* L = (LAS unsigned char*)smem;
    const kargp kp = (kargp)__builtin_amdgcn_kernarg_segment_ptr();
    asm volatile("s_load_dword s98, %0, 0x0\n\ts_load_dword s99, %0, 0x40" :: "s"(kp) : "s98", "s99");
    const int l = threadIdx.x & 63, w = __builtin_amdgcn_readfirstlane(threadIdx.x >> 6), c = blockIdx.x;
    const int x = c & 7, y = c >> 3;
    const int u = (x < 6 && y < 5) ? 10 * (x >> 1) + 5 * (x & 1) + y
                : (x >= 6 && y < 5) ? 54 + 5 * (x - 6) + y
                : 30 + 8 * (y - 5) + x;
    if (__builtin_expect(c >= NW, 1)) { GET_PARAMS(); kv_path(q_, L, c - NW, w, l); }
    else if (u < 30) { GET_PARAMS(); w_path<R_SA>(q_, L, u, w, l); }
    else if (u < 54) { GET_PARAMS(); w_path<R_SB>(q_, L, u, w, l); }
    else { GET_PARAMS(); w_path<R_SC>(q_, L, u, w, l); }
}

extern "C" void kernel_launch(void* const* d_in, const int* in_sizes, int n_in, void* d_out, int out_size, void* d_ws, size_t ws_size, hipStream_t stream) {
    Params p;
    p.x = (const u16*)d_in[0]; p.kc = (const u16*)d_in[1]; p.vc = (const u16*)d_in[2]; p.wq = (const u16*)d_in[3]; p.wo = (const u16*)d_in[4];
    p.wg = (const u16*)d_in[6]; p.wu = (const u16*)d_in[7]; p.wd = (const u16*)d_in[8]; p.c = (const u16*)d_in[9]; p.wm = (const u16*)d_in[10];
    p.out = (u16*)d_out; p.ws = (unsigned char*)d_ws;
    (void)hipFuncSetAttribute((const void*)fused_block, hipFuncAttributeMaxDynamicSharedMemorySize, (int)L_END);
    hipLaunchKernelGGL(fused_block, dim3(256), dim3(512), L_END, stream, p);
}
```

```cpp
#include <hip/hip_runtime.h>
#include <stdint.h>
#pragma clang fp contract(off)

typedef unsigned short u16;
typedef unsigned u32;
typedef unsigned long long u64;
typedef u32 v4u __attribute__((ext_vector_type(4)));
typedef u32 v2u __attribute__((ext_vector_type(2)));
typedef float f4 __attribute__((ext_vector_type(4)));
typedef short v4s __attribute__((ext_vector_type(4)));
typedef __bf16 bf8 __attribute__((ext_vector_type(8)));
typedef __bf16 bf2 __attribute__((ext_vector_type(2)));
#define LAS __attribute__((address_space(3)))
#define GAS __attribute__((address_space(1)))
#define RLX_AGENT __ATOMIC_RELAXED, __HIP_MEMORY_SCOPE_AGENT

constexpr int DM = 640, NH = 32, SKV = 4096, DH = 192, WIN = 1024, FF = 1536, NQ = NH * DH;
constexpr u32 SENT = 0xFFFFFFFFu;
constexpr int NW = 64;
constexpr u32 WS_E = 0, WS_FLAG = 4096, WS_F1P = 8192, WS_MODF = WS_F1P + 5 * 4 * 1280 * 4, WS_QP = WS_MODF + 4 * 2560 * 2,
              WS_OP = WS_QP + 5 * 4 * NQ * 4, WS_ML = WS_OP + 128 * 2 * DH * 2, WS_AP = WS_ML + 32 * 4 * 16, WS_HID = WS_AP + 3 * 4 * DM * 4,
              WS_END = WS_HID + 4 * FF * 2;
constexpr u32 OOB = 0x7FFFFF00u;
constexpr u32 L_XS = 131072, L_CS = L_XS + 5120, L_HB = L_CS + 5120, L_RED = L_HB + 5120, L_ST = L_RED + 8192, L_GA = L_ST + 256, L_GB = L_GA + 1024,
              L_X1 = L_GB + 1024, L_MISC = L_X1 + 1024, L_STAMP = L_MISC + 64, L_END = L_STAMP + 256;
constexpr u32 K_W = 18432, K_PX = 1024, K_PX2 = 1536, K_V = 9216, K_QS = 8 * K_W, K_ML = K_QS + 768, K_END = K_ML + 64;
static_assert(K_END <= L_MISC, "LDS");
constexpr float LOG2E = 1.4426950408889634f;
static_assert(L_END <= 163840, "LDS");
constexpr int SPIN_G = 40000, SPIN_L = 2000000;
constexpr int JIT_B = 2, JIT_C = 176;

#define STAMP(i) do {} while (0)

struct Params { const u16 *x, *kc, *vc, *wq, *wo, *wg, *wu, *wd, *c, *wm; u16* out; unsigned char* ws; };

__device__ __forceinline__ v4u mk_srd(const void* p, u32 bytes) { const u64 a = (u64)p; return v4u{(u32)a, (u32)(a >> 32) & 0xffffu, bytes, 0x00020000u}; }
__device__ __forceinline__ float asf(u32 u) { return __builtin_bit_cast(float, u); }
__device__ __forceinline__ u32 asu(float f) { return __builtin_bit_cast(u32, f); }
__device__ __forceinline__ float bflo(u32 u) { return asf(u << 16); }
__device__ __forceinline__ float bfhi(u32 u) { return asf(u & 0xffff0000u); }
__device__ __forceinline__ u32 pack2(float lo, float hi) { bf2 v; v[0] = (__bf16)lo; v[1] = (__bf16)hi; return __builtin_bit_cast(u32, v); }
__device__ __forceinline__ float rbf(float f) { return (float)(__bf16)f; }
typedef float f2 __attribute__((ext_vector_type(2)));
__device__ __forceinline__ f4 rb4(f4 v) {
    float a = v[0], b = v[1], c = v[2], d = v[3];
    asm("v_cvt_pk_bf16_f32 %0, 0, %0\n\tv_cvt_pk_bf16_f32 %1, 0, %1\n\tv_cvt_pk_bf16_f32 %2, 0, %2\n\tv_cvt_pk_bf16_f32 %3, 0, %3" : "+v"(a), "+v"(b), "+v"(c), "+v"(d));
    return f4{a, b, c, d}; }
__device__ __forceinline__ void rb8(f4& u, f4& v) {
    float a = u[0], b = u[1], c = u[2], d = u[3], e = v[0], f = v[1], g = v[2], h = v[3];
    asm("v_cvt_pk_bf16_f32 %0, 0, %0\n\tv_cvt_pk_bf16_f32 %1, 0, %1\n\tv_cvt_pk_bf16_f32 %2, 0, %2\n\tv_cvt_pk_bf16_f32 %3, 0, %3\n\t"
        "v_cvt_pk_bf16_f32 %4, 0, %4\n\tv_cvt_pk_bf16_f32 %5, 0, %5\n\tv_cvt_pk_bf16_f32 %6, 0, %6\n\tv_cvt_pk_bf16_f32 %7, 0, %7"
        : "+v"(a), "+v"(b), "+v"(c), "+v"(d), "+v"(e), "+v"(f), "+v"(g), "+v"(h));
    u = f4{a, b, c, d}; v = f4{e, f, g, h}; }
__device__ __forceinline__ f4 unpk(v2u v) { return f4{bflo(v[0]), bfhi(v[0]), bflo(v[1]), bfhi(v[1])}; }
__device__ __forceinline__ f4 fma4(f4 a, f4 b, f4 c) { return __builtin_elementwise_fma(a, b, c); }
__device__ __forceinline__ f4 bc4(float x) { return f4{x, x, x, x}; }
__device__ __forceinline__ u32 canon(u32 u) { return u < 0xFFFFFFFEu ? u : 0xFFFFFFFEu; }
__device__ __forceinline__ v4u canon4(f4 v) { return v4u{canon(asu(v[0])), canon(asu(v[1])), canon(asu(v[2])), canon(asu(v[3]))}; }
__device__ __forceinline__ u32 umax3(u32 a, u32 b, u32 c) { const u32 m = a > b ? a : b; return m > c ? m : c; }
__device__ __forceinline__ u32 vmax4(u32 m, v4u v) { return umax3(umax3(m, v[0], v[1]), v[2], v[3]); }
__device__ __forceinline__ bool all_valid(u32 m) { return __builtin_amdgcn_ballot_w64(m == SENT) == 0ull; }
__device__ __forceinline__ float dot2(u32 a, u32 b, float c) { return __builtin_amdgcn_fdot2_f32_bf16(__builtin_bit_cast(bf2, a), __builtin_bit_cast(bf2, b), c, false); }
__device__ __forceinline__ f4 mfma(v4u a, v4u b, f4 c) { return __builtin_amdgcn_mfma_f32_16x16x32_bf16(__builtin_bit_cast(bf8, a), __builtin_bit_cast(bf8, b), c, 0, 0, 0); }
template <int CTRL> __device__ __forceinline__ u32 dppu(u32 x) { return (u32)__builtin_amdgcn_mov_dpp((int)x, CTRL, 0xf, 0xf, true); }
template <int CTRL> __device__ __forceinline__ float dppf(float x) { return asf(dppu<CTRL>(asu(x))); }
template <int K> __device__ __forceinline__ v4u ror4k(v4u v) {
    if constexpr (K == 0) return v; else return v4u{dppu<0x120 + 4 * K>(v[0]), dppu<0x120 + 4 * K>(v[1]), dppu<0x120 + 4 * K>(v[2]), dppu<0x120 + 4 * K>(v[3])}; }
__device__ __forceinline__ float row_sum(float x) { x += dppf<0x128>(x); x += dppf<0x124>(x); x += dppf<0x122>(x); x += dppf<0x121>(x); return x; }
__device__ __forceinline__ float row_max(float x) { x = fmaxf(x, dppf<0x128>(x)); x = fmaxf(x, dppf<0x124>(x)); x = fmaxf(x, dppf<0x122>(x)); x = fmaxf(x, dppf<0x121>(x)); return x; }
__device__ __forceinline__ float xrow_sum(float x) {
    auto s = __builtin_amdgcn_permlane16_swap(asu(x), asu(x), false, false); x = asf(s[0]) + asf(s[1]);
    auto t = __builtin_amdgcn_permlane32_swap(asu(x), asu(x), false, false); return asf(t[0]) + asf(t[1]); }
__device__ __forceinline__ float xrow_max(float x) {
    auto s = __builtin_amdgcn_permlane16_swap(asu(x), asu(x), false, false); x = fmaxf(asf(s[0]), asf(s[1]));
    auto t = __builtin_amdgcn_permlane32_swap(asu(x), asu(x), false, false); return fmaxf(asf(t[0]), asf(t[1])); }
__device__ __forceinline__ float wave_sum(float x) { return xrow_sum(row_sum(x)); }
__device__ __forceinline__ float wave_max(float x) { return xrow_max(row_max(x)); }
__device__ __forceinline__ float fdiv(float a, float b) { const float r = __builtin_amdgcn_rcpf(b); const float q = a * r; return fmaf(fmaf(-q, b, a), r, q); }
__device__ __forceinline__ float silu_bf(float v) { return fdiv(v, 1.f + __builtin_amdgcn_exp2f(fminf(-v * 1.4426950408889634f, 126.f))); }
__device__ __forceinline__ v4u silu8(v4u c) {
    v4u r;
#pragma unroll
    for (int i = 0; i < 4; ++i) r[i] = pack2(silu_bf(bflo(c[i])), silu_bf(bfhi(c[i])));
    return r; }
#define BAR() asm volatile("s_waitcnt lgkmcnt(0)\n\ts_barrier" ::: "memory")
#define LGKM0() asm volatile("s_waitcnt lgkmcnt(0)" ::: "memory")
#define VMW(n) asm volatile("s_waitcnt vmcnt(%0)" :: "i"(n) : "memory")
#define MEMBAR() asm volatile("" ::: "memory")
typedef __amdgpu_buffer_rsrc_t rsrc_t;
__device__ __forceinline__ v4u ld_sc1(rsrc_t r, u32 off) { return __builtin_amdgcn_raw_buffer_load_b128(r, off, 0, 16); }
__device__ __forceinline__ v2u ld_sc1_2(rsrc_t r, u32 off) { return __builtin_amdgcn_raw_buffer_load_b64(r, off, 0, 16); }
__device__ __forceinline__ void st_sc1(rsrc_t r, u32 off, v4u v) { __builtin_amdgcn_raw_buffer_store_b128(v, r, off, 0, 16); }
__device__ __forceinline__ void st_sc1_2(rsrc_t r, u32 off, v2u v) { __builtin_amdgcn_raw_buffer_store_b64(v, r, off, 0, 16); }
__device__ __forceinline__ f4 asf4(v4u v) { return __builtin_bit_cast(f4, v); }

__device__ __forceinline__ void dma_unit(v4u srd, u32 v0, u32 v1, u32 sA, u32 sB, u32 lds) {
    u32 keep;
    asm volatile("s_nop 4\n\ts_mov_b32 %0, m0\n\ts_mov_b32 m0, %6\n\ts_nop 0\n\t"
                 "buffer_load_dwordx4 %1, %3, %4 offen nt lds\n\ts_add_u32 m0, m0, 0x400\n\ts_nop 0\n\t"
                 "buffer_load_dwordx4 %2, %3, %4 offen nt lds\n\ts_add_u32 m0, m0, 0x400\n\ts_nop 0\n\t"
                 "buffer_load_dwordx4 %1, %3, %5 offen nt lds\n\ts_add_u32 m0, m0, 0x400\n\ts_nop 0\n\t"
                 "buffer_load_dwordx4 %2, %3, %5 offen nt lds\n\ts_mov_b32 m0, %0"
                 : "=&s"(keep) : "v"(v0), "v"(v1), "s"(srd), "s"(sA), "s"(sB), "s"(lds) : "memory", "scc");
}
__device__ __forceinline__ void dma_1k(v4u srd, u32 voff, u32 soff, u32 lds) {
    u32 keep;
    asm volatile("s_nop 4\n\ts_mov_b32 %0, m0\n\ts_mov_b32 m0, %4\n\ts_nop 0\n\tbuffer_load_dwordx4 %1, %2, %3 offen lds\n\ts_mov_b32 m0, %0"
                 : "=&s"(keep) : "v"(voff), "s"(srd), "s"(soff), "s"(lds) : "memory");
}
struct WStream { v4u srd; u32 base, pitch, v0, v1; };
__device__ __forceinline__ WStream mk_stream(v4u srd, u32 base, u32 pitch, int l) {
    WStream s; s.srd = srd; s.base = base; s.pitch = pitch;
    const u32 sw = 2u * ((l >> 4) & 1);
    s.v0 = (l >> 3) * pitch + (((l & 7) ^ sw) << 4);
    s.v1 = (8 + (l >> 3)) * pitch + (((l & 7) ^ (sw | 4u)) << 4);
    return s; }
__device__ __forceinline__ void issue(const WStream& s, int n, u32 lds) { const u32 a = s.base + (u32)n * 32u * s.pitch; dma_unit(s.srd, s.v0, s.v1, a, a + 16u * s.pitch, lds); }
__device__ __forceinline__ void read_unit(LAS unsigned char* L, const u32 (&ta)[4], int slot, v4u (&f)[4]) {
#pragma unroll
    for (int T = 0; T < 4; ++T) {
        const v2u lo = __builtin_bit_cast(v2u, __builtin_amdgcn_ds_read_tr16_b64_v4i16((LAS v4s*)(L + ta[T] + slot * 4096)));
        const v2u hi = __builtin_bit_cast(v2u, __builtin_amdgcn_ds_read_tr16_b64_v4i16((LAS v4s*)(L + ta[T] + slot * 4096 + 512)));
        f[T] = v4u{lo[0], lo[1], hi[0], hi[1]};
    } }
__device__ __forceinline__ f4 sel_tile(const f4 (&a)[4], int t) {
    const bool b0 = t & 1, b1 = t & 2;
    f4 r;
#pragma unroll
    for (int i = 0; i < 4; ++i) { const float lo = b0 ? a[1][i] : a[0][i], hi = b0 ? a[3][i] : a[2][i]; r[i] = b1 ? hi : lo; }
    return r; }

__device__ __forceinline__ void dma_kv(v4u srd, u32 vo, u32 so, u32 lds) {
    u32 keep;
    asm volatile("s_nop 4\n\ts_mov_b32 %0, m0\n\ts_mov_b32 m0, %4\n\ts_nop 0\n\t"
                 "buffer_load_dwordx4 %1, %2, %3 offen nt lds\n\ts_add_u32 m0, m0, 0x380\n\ts_nop 0\n\t"
                 "buffer_load_dwordx4 %1, %2, %3 offen offset:128 nt lds\n\ts_add_u32 m0, m0, 0x380\n\ts_nop 0\n\t"
                 "buffer_load_dwordx4 %1, %2, %3 offen offset:256 nt lds\n\ts_mov_b32 m0, %0"
                 : "=&s"(keep) : "v"(vo), "s"(srd), "s"(so), "s"(lds) : "memory", "scc");
}
__device__ __forceinline__ float qk_unit(v4u k0, v4u k1, v4u k2, const v4u (&q)[3]) {
    float s = 0.f, t = 0.f;
#pragma unroll
    for (int r = 0; r < 4; r += 2) { s = dot2(k0[r], q[0][r], s); t = dot2(k0[r + 1], q[0][r + 1], t); }
#pragma unroll
    for (int r = 0; r < 4; r += 2) { s = dot2(k1[r], q[1][r], s); t = dot2(k1[r + 1], q[1][r + 1], t); }
#pragma unroll
    for (int r = 0; r < 4; r += 2) { s = dot2(k2[r], q[2][r], s); t = dot2(k2[r + 1], q[2][r + 1], t); }
    return s + t;
}
__device__ __forceinline__ void pv_unit(v4u v0, v4u v1, v4u v2, u32 plo, float (&acc)[24]) {
    const u32 phi = plo << 16;
#pragma unroll
    for (int r = 0; r < 4; ++r) {
        acc[2 * r] = dot2(v0[r], plo, acc[2 * r]); acc[2 * r + 1] = dot2(v0[r], phi, acc[2 * r + 1]);
        acc[8 + 2 * r] = dot2(v1[r], plo, acc[8 + 2 * r]); acc[8 + 2 * r + 1] = dot2(v1[r], phi, acc[8 + 2 * r + 1]);
        acc[16 + 2 * r] = dot2(v2[r], plo, acc[16 + 2 * r]); acc[16 + 2 * r + 1] = dot2(v2[r], phi, acc[16 + 2 * r + 1]);
    }
}

__device__ __forceinline__ void kv_path(const Params& p, LAS unsigned char* L, int j, int w, int l) {
    const rsrc_t rws = __builtin_amdgcn_make_buffer_rsrc((void*)p.ws, 0, WS_END, 0x00020000);
    const rsrc_t rk = __builtin_amdgcn_make_buffer_rsrc((void*)p.kc, 0, 4 * NH * SKV * DH * 2, 0x00020000);
    const rsrc_t rv = __builtin_amdgcn_make_buffer_rsrc((void*)p.vc, 0, 4 * NH * SKV * DH * 2, 0x00020000);
    volatile LAS u32* misc = (volatile LAS u32*)(L + L_MISC);
    const int rk3 = (j >> 3) / 3, t3 = (j & 7) * 8 + rk3, pos = (j >> 3) - 3 * rk3;
    const int slot = pos == 1 ? (w & 1) : 0;
    const int bh = 2 * t3 + (pos == 2 ? 1 : (pos == 1 ? slot : 0));
    const int part = pos == 0 ? 0 : (pos == 2 ? 1 : (slot == 0 ? 1 : 0));
    const int wi = pos == 1 ? (w >> 1) : w;
    const int ub = pos == 0 ? 0 : (pos == 2 ? 43 : (slot == 0 ? 85 : 0));
    const int ng = pos == 1 ? 2 : 4, u1 = ub + (wi >= ng ? 11 * ng + wi - ng : wi);
    const u32 ust = ng * 3072;
    const int nl = (pos == 1 ? wi == 3 : wi >= 5) ? 2 : 3;
    const bool lead = w == 0 || (w == 1 && pos == 1);
    const u32 opb = WS_OP + (bh * 2 + part) * 384, mlb = WS_ML + ((bh & 31) * 4 + (bh >> 5)) * 16 + part * 8;
    const u32 vo = (l >> 3) * 384 + (l & 7) * 16;
    const u32 base = ((u32)bh * SKV + (SKV - WIN)) * 384u + (u32)u1 * 3072u;
    const u32 wl = w * K_W, lb = (u32)(size_t)L + wl;
    const v4u sk = mk_srd(p.kc, 4 * NH * SKV * DH * 2), sv = mk_srd(p.vc, 4 * NH * SKV * DH * 2);
    STAMP(0);
    if (w != 4) { dma_kv(sk, vo, base + 8 * ust, lb); dma_kv(sv, vo, base + 8 * ust, lb + K_V); }
    if (threadIdx.x < 12) misc[threadIdx.x] = 0;
    BAR();
    if (w == 4) {
        __builtin_amdgcn_s_setprio(3);
        const u64 T = __hip_atomic_load((GAS u64*)(p.ws + WS_E), RLX_AGENT) + 1ull;
        st_sc1(rws, l < 24 ? opb + l * 16 : OOB, v4u{SENT, SENT, SENT, SENT});
        st_sc1_2(rws, l == 0 ? mlb : OOB, v2u{SENT, SENT});
        const int bh1 = 2 * t3 + 1;
        st_sc1(rws, (pos == 1 && l < 24) ? WS_OP + bh1 * 2 * 384 + l * 16 : OOB, v4u{SENT, SENT, SENT, SENT});
        st_sc1_2(rws, (pos == 1 && l == 0) ? WS_ML + ((bh1 & 31) * 4 + (bh1 >> 5)) * 16 : OOB, v2u{SENT, SENT});
        VMW(0);
        if (l == 0) {
            __hip_atomic_store((GAS u64*)(p.ws + WS_FLAG) + blockIdx.x, T, RLX_AGENT);
            misc[4] = (u32)T; misc[5] = (u32)(T >> 32); LGKM0(); misc[2] = 1;
        }
        STAMP(9);
        __builtin_amdgcn_s_setprio(0);
        dma_kv(sk, vo, base + 8 * ust, lb); dma_kv(sv, vo, base + 8 * ust, lb + K_V);
    }
    const int c = l & 7;
    float m, lsum, fsc;
    const u32 nslot = pos == 1 ? 4u : 8u;
    auto slot_stats = [&](float& M, float& mi, float& li) {
        const v2u t = *(LAS v2u*)(L + K_ML + c * 8);
        const bool mine = pos != 1 || (l & 1) == slot;
        mi = mine ? asf(t[0]) : -INFINITY; li = mine ? asf(t[1]) : 0.f;
        M = fmaxf(mi, dppf<0xB1>(mi)); M = fmaxf(M, dppf<0x4E>(M)); M = fmaxf(M, dppf<0x141>(M));
    };
    auto post = [&]() {
        u32 old = 0;
        if (l == 0) { *(LAS v2u*)(L + K_ML + w * 8) = v2u{asu(m), asu(lsum)}; LGKM0(); old = __hip_atomic_fetch_add((LAS u32*)(L + L_MISC + 32 + slot * 4), 1u, __ATOMIC_RELAXED, __HIP_MEMORY_SCOPE_WORKGROUP); }
        if (__builtin_amdgcn_readfirstlane(old) == nslot - 1u) {
            MEMBAR();
            float M, mi, li; slot_stats(M, mi, li);
            float e = li * __builtin_amdgcn_exp2f((mi - M) * LOG2E);
            e += dppf<0xB1>(e); e += dppf<0x4E>(e); e += dppf<0x141>(e);
            st_sc1_2(rws, l == 0 ? mlb : OOB, v2u{canon(asu(M)), canon(asu(e))});
            STAMP(5);
        }
    };
    auto scale = [&]() {
        for (int sp = 0; misc[8 + slot] < nslot && sp < SPIN_L; ++sp) __builtin_amdgcn_s_sleep(1);
        MEMBAR();
        float M, mi, li; slot_stats(M, mi, li);
        float e = li * __builtin_amdgcn_exp2f((mi - M) * LOG2E);
        e += dppf<0xB1>(e); e += dppf<0x4E>(e); e += dppf<0x141>(e);
        fsc = fdiv(__builtin_amdgcn_exp2f((m - M) * LOG2E), e);
    };
    dma_kv(sk, vo, base + 9 * ust, lb + 3072); dma_kv(sv, vo, base + 9 * ust, lb + K_V + 3072);
    if (nl == 3) { dma_kv(sk, vo, base + 10 * ust, lb + 6144); dma_kv(sv, vo, base + 10 * ust, lb + K_V + 6144); }
    v4u K[24], V[24];
#pragma unroll
    for (int jj = 0; jj < 8; ++jj)
#pragma unroll
        for (int i = 0; i < 3; ++i) K[jj * 3 + i] = __builtin_amdgcn_raw_buffer_load_b128(rk, vo + i * 128, base + jj * ust, 2);
#pragma unroll
    for (int jj = 0; jj < 8; ++jj)
#pragma unroll
        for (int i = 0; i < 3; ++i) V[jj * 3 + i] = __builtin_amdgcn_raw_buffer_load_b128(rv, vo + i * 128, base + jj * ust, 2);
    __builtin_amdgcn_sched_barrier(0);
    if (w == 0) STAMP(1);
    if (w == 7) STAMP(2);
    if (w < 4) {
        for (int sp = 0; misc[2] == 0 && sp < SPIN_L; ++sp) __builtin_amdgcn_s_sleep(1);
        const u32 tlo = misc[4], thi = misc[5];
        for (int sp = 0; sp < SPIN_G && misc[6] == 0; ++sp) {
            MEMBAR();
            const v2u f = ld_sc1_2(rws, WS_FLAG + l * 8);
            if (__builtin_amdgcn_ballot_w64(f[0] != tlo || f[1] != thi) == 0ull) misc[6] = 1;
        }
        const u32 qo = WS_QP + (u32)((bh >> 5) * NQ + (bh & 31) * DH + 4 * l) * 4u;
        v4u a[5] = {};
        bool need = l < 48;
        for (int sp = 0; sp < SPIN_G; ++sp) {
            MEMBAR();
            if (__builtin_amdgcn_readfirstlane(misc[slot]) != 0u) break;
            if (need) {
                u32 mx = 0;
#pragma unroll
                for (int ks = 0; ks < 5; ++ks) a[ks] = ld_sc1(rws, qo + ks * (4 * NQ * 4));
#pragma unroll
                for (int ks = 0; ks < 5; ++ks) mx = vmax4(mx, a[ks]);
                need = mx == SENT;
            }
            if (__builtin_amdgcn_ballot_w64(need) == 0ull || sp == SPIN_G - 1) {
                const f4 qs = (((asf4(a[0]) + asf4(a[1])) + asf4(a[2])) + asf4(a[3])) + asf4(a[4]);
                if (l < 48) *(LAS v2u*)(L + K_QS + slot * 384 + l * 8) = v2u{pack2(qs[0], qs[1]), pack2(qs[2], qs[3])};
                LGKM0();
                misc[slot] = 1;
                break;
            }
        }
        if (w == 0) STAMP(3);
    } else {
        for (int sp = 0; misc[slot] == 0 && sp < SPIN_L; ++sp) __builtin_amdgcn_s_sleep(1);
    }
    VMW(48);
    v4u p0, p1, p2;
    {
        v4u q[3];
#pragma unroll
        for (int i = 0; i < 3; ++i) q[i] = *(LAS v4u*)(L + K_QS + slot * 384 + (c + 8 * i) * 16);
        float y[4], x[8];
        LAS v4u* KL = (LAS v4u*)(L + wl + l * 16);
#pragma unroll
        for (int jj = 0; jj < 3; ++jj) { y[jj] = qk_unit(KL[jj * 192], KL[jj * 192 + 64], KL[jj * 192 + 128], q); __builtin_amdgcn_sched_barrier(0); }
#pragma unroll
        for (int jj = 0; jj < 8; ++jj) x[jj] = qk_unit(K[3 * jj], K[3 * jj + 1], K[3 * jj + 2], q);
#pragma unroll
        for (int h = 0; h < 4; ++h) { const float a = x[h] + dppf<0x141>(x[h]), b = x[h + 4] + dppf<0x141>(x[h + 4]); x[h] = (c & 4) ? b : a; }
        { const float a = y[0] + dppf<0x141>(y[0]), b = y[2] + dppf<0x141>(y[2]), a1 = y[1] + dppf<0x141>(y[1]); y[0] = (c & 4) ? b : a; y[1] = a1; }
#pragma unroll
        for (int h = 0; h < 2; ++h) { const float a = x[h] + dppf<0x4E>(x[h]), b = x[h + 2] + dppf<0x4E>(x[h + 2]); x[h] = (c & 2) ? b : a; }
        { const float a = y[0] + dppf<0x4E>(y[0]), b = y[1] + dppf<0x4E>(y[1]); y[0] = (c & 2) ? b : a; }
        { const float a = x[0] + dppf<0xB1>(x[0]), b = x[1] + dppf<0xB1>(x[1]); x[0] = (c & 1) ? b : a; }
        y[0] += dppf<0xB1>(y[0]);
        const float s1 = rbf(rbf(x[0]) * 0.07216878364870322f);
        const float s2 = (c >> 1) < nl ? rbf(rbf(y[0]) * 0.07216878364870322f) : -INFINITY;
        m = wave_max(fmaxf(s1, s2));
        const float e1 = __builtin_amdgcn_exp2f((s1 - m) * LOG2E), e2 = __builtin_amdgcn_exp2f((s2 - m) * LOG2E);
        *(LAS u32*)(L + wl + K_PX + l * 4) = pack2(e1, 0.f);
        *(LAS u32*)(L + wl + K_PX2 + (l >> 1) * 4) = pack2(e2, 0.f);
        lsum = wave_sum(e1 + ((c & 1) ? 0.f : e2));
        post();
        LGKM0();
        p0 = *(LAS v4u*)(L + wl + K_PX + (l >> 3) * 32); p1 = *(LAS v4u*)(L + wl + K_PX + (l >> 3) * 32 + 16); p2 = *(LAS v4u*)(L + wl + K_PX2 + (l >> 3) * 16);
    }
    if (w == 0) STAMP(4);
    float acc[24];
#pragma unroll
    for (int i = 0; i < 24; ++i) acc[i] = 0.f;
    {
        LAS v4u* VL = (LAS v4u*)(L + wl + K_V + l * 16);
        pv_unit(VL[0], VL[64], VL[128], p2[0], acc);
        pv_unit(VL[192], VL[256], VL[320], p2[1], acc);
        if (nl == 3) pv_unit(VL[384], VL[448], VL[512], p2[2], acc);
    }
#pragma unroll
    for (int jj = 0; jj < 6; ++jj) pv_unit(V[3 * jj], V[3 * jj + 1], V[3 * jj + 2], jj < 4 ? p0[jj & 3] : p1[jj & 3], acc);
    scale();
#pragma unroll
    for (int jj = 6; jj < 8; ++jj) pv_unit(V[3 * jj], V[3 * jj + 1], V[3 * jj + 2], p1[jj & 3], acc);
    if (w == 7) STAMP(6);
    float y[12], z[6];
#pragma unroll
    for (int n = 0; n < 12; ++n) { auto s = __builtin_amdgcn_permlane32_swap(asu(acc[n]), asu(acc[n + 12]), false, false); y[n] = asf(s[0]) + asf(s[1]); }
#pragma unroll
    for (int n = 0; n < 6; ++n) { auto s = __builtin_amdgcn_permlane16_swap(asu(y[n]), asu(y[n + 6]), false, false); z[n] = asf(s[0]) + asf(s[1]); }
#pragma unroll
    for (int n = 0; n < 3; ++n) {
        const float a = z[n] + dppf<0x128>(z[n]), b = z[n + 3] + dppf<0x128>(z[n + 3]);
        const int ai = 3 * (l >> 3) + n;
        *(LAS float*)(L + wl + (64 * (ai >> 3) + 8 * c + (ai & 7)) * 4) = ((l & 8) ? b : a) * fsc;
    }
    BAR();
    if (w == 0) STAMP(7);
    if (lead) {
        const u32 pb = slot * K_W + (l < 48 ? l : 47) * 16;
        f4 o = (*(LAS f4*)(L + pb) + *(LAS f4*)(L + pb + 2 * K_W)) + (*(LAS f4*)(L + pb + 4 * K_W) + *(LAS f4*)(L + pb + 6 * K_W));
        if (pos != 1) o += (*(LAS f4*)(L + pb + K_W) + *(LAS f4*)(L + pb + 3 * K_W)) + (*(LAS f4*)(L + pb + 5 * K_W) + *(LAS f4*)(L + pb + 7 * K_W));
        st_sc1_2(rws, l < 48 ? opb + l * 8 : OOB, v2u{canon(pack2(o[0], o[1])), canon(pack2(o[2], o[3]))});
        if (w == 0) STAMP(8); else STAMP(10);
    }
}

enum { R_SA = 0, R_SB = 1, R_SC = 2 };
#define UNDEF4(v) asm volatile("" : "=v"(v))
template <int ROLE>
__device__ __forceinline__ void w_path(const Params& p, LAS unsigned char* L, int u, int w, int l) {
    const rsrc_t rws = __builtin_amdgcn_make_buffer_rsrc((void*)p.ws, 0, WS_END, 0x00020000);
    const int tid = threadIdx.x, g = l >> 4, tq = (l & 15) >> 2, b4 = l & 3;
    const u32 lbase = (u32)(size_t)L, stg = lbase + w * 16384;
    const bool hasF1 = u < 50, hasF2 = u < 60;
    const int kf = u / 10, lp = u - 10 * kf, kf2 = u / 12, cs = u - 12 * kf2;
    const int ri = ROLE == R_SA ? u : (ROLE == R_SB ? u - 30 : u - 54);
    const WStream sF1 = mk_stream(mk_srd(p.wm, hasF1 ? DM * 3840 * 2 : 0), (u32)((128 * kf + 32 * (w >> 1)) * 7680 + (128 * lp + 64 * (w & 1)) * 2), 7680, l);
    const WStream sF2 = mk_stream(mk_srd(p.wq, hasF2 ? DM * NQ * 2 : 0), (u32)(128 * kf2 * 12288 + (512 * cs + 64 * w) * 2), 12288, l);
    const int mid = ROLE == R_SC ? (w < 4 ? (ri >= 6 ? ri : -1) : 30 + ri) : (w < 4 ? (ri < 6 ? ri : ri + 4) : (ri == 20 ? 28 : (ri == 21 ? 29 : -1)));
    const bool hasM = ROLE != R_SA && mid >= 0;
    const WStream sM = mk_stream(mk_srd(p.wm, hasM ? DM * 3840 * 2 : 0), (u32)(160 * (w & 3) * 7680 + (1280 + 64 * mid) * 2), 7680, l);
    const int ra = ri / 10, ca = ri - 10 * ra;
    const WStream sT = ROLE == R_SA ? mk_stream(mk_srd(p.wo, NQ * DM * 2), (u32)((2048 * ra + 256 * w) * 1280 + ca * 128), 1280, l)
                     : ROLE == R_SB ? mk_stream(mk_srd((w & 1) ? p.wu : p.wg, DM * FF * 2), (u32)(160 * (w >> 1) * 3072 + ri * 128), 3072, l)
                                    : mk_stream(mk_srd(p.wd, FF * DM * 2), (u32)(192 * w * 1280 + ri * 128), 1280, l);
    constexpr int NM = ROLE == R_SA ? 0 : 5, NT = ROLE == R_SA ? 8 : (ROLE == R_SB ? 5 : 6), NA = NM + NT;
    u32 ta[4];
    { const u32 f = 2u * (tq >> 1) | 4u * (g & 1);
#pragma unroll
      for (int T = 0; T < 4; ++T) ta[T] = w * 16384 + (8 * g + tq) * 128 + ((((u32)(2 * T)) ^ f) + (b4 >> 1)) * 16 + (b4 & 1) * 8; }
    const u32 dcol = 16 * tq + 4 * g;
    const u32 f1o = hasF1 ? WS_F1P + (u32)((kf * 4 + b4) * 1280 + 128 * lp + 64 * w + dcol) * 4u : OOB;
    const u32 qpo = hasF2 ? WS_QP + (u32)((kf2 * 4 + b4) * NQ + 512 * cs + 64 * w + dcol) * 4u : OOB;
    const u32 mfo = hasM ? WS_MODF + (u32)(b4 * 2560 + 64 * mid + dcol) * 2u : OOB;
    const u32 outo = ROLE == R_SA ? WS_AP + (u32)((ra * 4 + b4) * DM + 64 * ca + dcol) * 4u : WS_HID + (u32)(b4 * FF + 64 * ri + dcol) * 2u;
    volatile LAS u32* misc = (volatile LAS u32*)(L + L_MISC);
    STAMP(0);
    if (w < 5) { dma_1k(mk_srd(p.c, 5120), l * 16, w * 1024, lbase + L_CS + w * 1024); dma_1k(mk_srd(p.x, 5120), l * 16, w * 1024, lbase + L_XS + w * 1024); }
    issue(sF1, 0, stg);
    { u32 keep;
      asm volatile("s_nop 4\n\ts_mov_b32 %0, m0\n\ts_mov_b32 m0, %5\n\ts_nop 0\n\tbuffer_load_dword %1, %2, %3 offen nt lds\n\tbuffer_load_dword %1, %4, 0 offen nt lds\n\ts_mov_b32 m0, %0"
                   : "=&s"(keep) : "v"(l == 0 ? 0u : OOB), "s"(sT.srd), "s"(sT.base), "s"(mk_srd(p.out, 5120)), "s"(lbase + L_GB) : "memory"); }
    if (w == 0) {
        u32 keep;
        asm volatile("s_nop 4\n\ts_mov_b32 %0, m0\n\ts_mov_b32 m0, %3\n\ts_nop 0\n\tbuffer_load_dword %1, %2, 0 offen sc1 lds\n\ts_mov_b32 m0, %0"
                     : "=&s"(keep) : "v"(l < 2 ? WS_E + l * 4 : OOB), "s"(mk_srd(p.ws, WS_END)), "s"(lbase + L_GA) : "memory");
    }
    { const v4u s4 = {SENT, SENT, SENT, SENT};
      st_sc1(rws, qpo, s4);
      if (w < 2) st_sc1(rws, f1o, s4);
      if (ROLE != R_SA && (w & 3) == 0) st_sc1_2(rws, mfo, v2u{SENT, SENT});
      if (w == 0) {
          if (ROLE == R_SA) st_sc1(rws, outo, s4);
          if (ROLE == R_SB) st_sc1_2(rws, outo, v2u{SENT, SENT});
      } }
    if (tid == 0) { misc[0] = 0; misc[1] = 0; }
    BAR();
    issue(sF2, 0, stg + 4096);
    auto issueB = [&](int n) { if (n < NM) issue(sM, n, stg + ((n + 1) & 3) * 4096); else issue(sT, n - NM, stg + ((n + 1) & 3) * 4096); };
    {
        v4u ft[NT][4];
        f4 xa, xb, gaa, gab, c1a, c1b, sba, sbb;
        const int col = 128 * w + 4 * (l & 15);
        {
            VMW(4);
            BAR();
            STAMP(1);
            const u64 T = *(LAS u64*)(L + L_GA) + 1ull;
            const u32 tlo = (u32)T, thi = (u32)(T >> 32);
            if (tid == 0) __hip_atomic_store((GAS u64*)(p.ws + WS_FLAG) + blockIdx.x, T, RLX_AGENT);
            auto wait_all_flags = [&]() {
                for (int sp = 0; sp < SPIN_G; ++sp) {
                    MEMBAR();
                    const v4u f0 = ld_sc1(rws, WS_FLAG + l * 16), f1 = ld_sc1(rws, WS_FLAG + 1024 + l * 16);
                    const bool bad = f0[0] != tlo || f0[1] != thi || f0[2] != tlo || f0[3] != thi || f1[0] != tlo || f1[1] != thi || f1[2] != tlo || f1[3] != thi;
                    if (__builtin_amdgcn_ballot_w64(bad) == 0ull) break;
                }
            };
            {
                v4u f[4]; read_unit(L, ta, 0, f);
                const v4u a = silu8(*(LAS v4u*)(L + L_CS + b4 * 1280 + (128 * kf + 32 * (w >> 1) + 8 * g) * 2));
                f4 d[4];
#pragma unroll
                for (int T2 = 0; T2 < 4; ++T2) d[T2] = mfma(f[T2], a, f4{0.f, 0.f, 0.f, 0.f});
                *(LAS f4*)(L + L_RED + w * 1024 + l * 16) = sel_tile(d, tq);
                if (w >= 4) {
                    float xv[10], s = 0.f;
#pragma unroll
                    for (int i = 0; i < 5; ++i) { const u32 t = *(LAS u32*)(L + L_XS + (w - 4) * 1280 + (l + 64 * i) * 4); xv[2 * i] = bflo(t); xv[2 * i + 1] = bfhi(t); s += xv[2 * i] + xv[2 * i + 1]; }
                    const float mean = wave_sum(s) * (1.f / DM);
                    float v = 0.f;
#pragma unroll
                    for (int i = 0; i < 10; ++i) v = fmaf(xv[i] - mean, xv[i] - mean, v);
                    const float var = wave_sum(v) * (1.f / DM);
                    if (l == 0) *(LAS v2u*)(L + L_ST + (w - 4) * 8) = v2u{asu(rbf(mean)), asu(rbf(__builtin_amdgcn_sqrtf(rbf(rbf(var) + 1e-5f))))};
                }
                BAR();
                if (w < 2) {
                    LAS f4* R = (LAS f4*)(L + L_RED + w * 1024 + l * 16);
                    st_sc1(rws, f1o, canon4((R[0] + R[128]) + (R[256] + R[384])));
                }
                if (w == 0) STAMP(2);
                issue(sF2, 1, stg + 8192); issue(sF2, 2, stg + 12288); issue(sF2, 3, stg);
            }
            if (w < 2) {
                for (int sp = 0; sp < SPIN_G; ++sp) {
                    MEMBAR();
                    const v2u f = ld_sc1_2(rws, WS_FLAG + l * 8);
                    if (__builtin_amdgcn_ballot_w64(f[0] != tlo || f[1] != thi) == 0ull) break;
                }
                if (w == 0) STAMP(19);
                const int hb = tid >> 5, hi = tid & 31, col = 128 * kf2 + 4 * hi;
                const u32 o = WS_F1P + (u32)(hb * 1280 + col) * 4u;
                v4u a[5] = {}, c2[5] = {};
                bool need = hasF2;
                for (int sp = 0; sp < SPIN_G; ++sp) {
                    MEMBAR();
                    if (need) {
                        u32 mx = 0;
#pragma unroll
                        for (int ks = 0; ks < 5; ++ks) { a[ks] = ld_sc1(rws, o + ks * 20480); c2[ks] = ld_sc1(rws, o + ks * 20480 + 2560); }
#pragma unroll
                        for (int ks = 0; ks < 5; ++ks) mx = vmax4(vmax4(mx, a[ks]), c2[ks]);
                        need = mx == SENT;
                    }
                    if (__builtin_amdgcn_ballot_w64(need) == 0ull) break;
                }
                const f4 sa = (((asf4(a[0]) + asf4(a[1])) + asf4(a[2])) + asf4(a[3])) + asf4(a[4]);
                const f4 sc = (((asf4(c2[0]) + asf4(c2[1])) + asf4(c2[2])) + asf4(c2[3])) + asf4(c2[4]);
                const v2u st = *(LAS v2u*)(L + L_ST + hb * 8), xr = *(LAS v2u*)(L + L_XS + hb * 1280 + (hasF2 ? col : 0) * 2);
                const float den = asf(st[1]), rc = __builtin_amdgcn_rcpf(den);
                const f4 e = rb4(unpk(xr) - asf(st[0])), q1 = e * rc;
                const f4 h = rb4(rb4(fma4(fma4(-q1, bc4(den), e), bc4(rc), q1)) * rb4(rb4(sc) + 1.f)) + rb4(sa);
                *(LAS v2u*)(L + L_HB + hb * 256 + hi * 8) = v2u{pack2(h[0], h[1]), pack2(h[2], h[3])};
                LGKM0();
                if (l == 0) __hip_atomic_fetch_add((LAS u32*)(L + L_MISC), 1u, __ATOMIC_RELAXED, __HIP_MEMORY_SCOPE_WORKGROUP);
                if (w == 0) STAMP(4);
            }
            {
                v4u fq[4][4];
                VMW(12); read_unit(L, ta, 1, fq[0]); LGKM0();
                VMW(8); read_unit(L, ta, 2, fq[1]);
                VMW(4); read_unit(L, ta, 3, fq[2]);
                issueB(0);
                VMW(4); read_unit(L, ta, 0, fq[3]);
                if (w == 7) STAMP(3);
                for (int sp = 0; misc[0] < 2u && sp < SPIN_L; ++sp) __builtin_amdgcn_s_sleep(1);
                MEMBAR();
                f4 d[4] = {};
#pragma unroll
                for (int n = 0; n < 4; ++n) {
                    const v4u a = *(LAS v4u*)(L + L_HB + b4 * 256 + (32 * n + 8 * g) * 2);
#pragma unroll
                    for (int T2 = 0; T2 < 4; ++T2) d[T2] = mfma(fq[n][T2], a, d[T2]);
                }
                st_sc1(rws, qpo, canon4(sel_tile(d, tq)));
                BAR();
                if (w == 7) STAMP(5);
                issueB(1); issueB(2); issueB(3);
            }
            if (NM) {
                f4 d[4] = {};
#pragma unroll
                for (int n = 0; n < NM; ++n) {
                    if (n < 2) VMW(12); else if (n == 2) VMW(8); else if (n == 3) VMW(4); else VMW(0);
                    v4u f[4]; read_unit(L, ta, (n + 1) & 3, f);
                    const v4u a = silu8(*(LAS v4u*)(L + L_CS + b4 * 1280 + (160 * (w & 3) + 32 * n + 8 * g) * 2));
                    LGKM0();
                    if (n == 0) issueB(4);
#pragma unroll
                    for (int T2 = 0; T2 < 4; ++T2) d[T2] = mfma(f[T2], a, d[T2]);
                }
                *(LAS f4*)(L + L_RED + w * 1024 + l * 16) = sel_tile(d, tq);
                BAR();
                if ((w & 3) == 0) {
                    LAS f4* R = (LAS f4*)(L + L_RED + w * 1024 + l * 16);
                    const f4 s = (R[0] + R[64]) + (R[128] + R[192]);
                    st_sc1_2(rws, mfo, v2u{canon(pack2(s[0], s[1])), canon(pack2(s[2], s[3]))});
                }
                UNDEF4(xa); UNDEF4(xb); UNDEF4(gaa); UNDEF4(gab); UNDEF4(c1a); UNDEF4(c1b); UNDEF4(sba); UNDEF4(sbb);
                if (ROLE == R_SB && w < 5) {
                    v2u gv[2], sv[2], cv[2];
                    const u32 mo = WS_MODF + (u32)(g * 2560 + col) * 2u;
                    for (int sp = 0; sp < SPIN_G; ++sp) {
                        MEMBAR();
                        u32 mx = 0;
#pragma unroll
                        for (int h = 0; h < 2; ++h) { gv[h] = ld_sc1_2(rws, mo + h * 128); sv[h] = ld_sc1_2(rws, mo + 1280 + h * 128); cv[h] = ld_sc1_2(rws, mo + 2560 + h * 128); }
#pragma unroll
                        for (int h = 0; h < 2; ++h) mx = umax3(umax3(umax3(mx, gv[h][0], gv[h][1]), sv[h][0], sv[h][1]), cv[h][0], cv[h][1]);
                        if (all_valid(mx)) break;
                    }
                    xa = unpk(*(LAS v2u*)(L + L_XS + g * 1280 + col * 2)); xb = unpk(*(LAS v2u*)(L + L_XS + g * 1280 + col * 2 + 128));
                    gaa = unpk(gv[0]); gab = unpk(gv[1]);
                    c1a = rb4(unpk(cv[0]) + 1.f); c1b = rb4(unpk(cv[1]) + 1.f);
                    sba = unpk(sv[0]); sbb = unpk(sv[1]);
                    asm volatile("" : "+v"(xa), "+v"(xb), "+v"(gaa), "+v"(gab), "+v"(c1a), "+v"(c1b), "+v"(sba), "+v"(sbb));
                }
                if (ROLE == R_SC && w == 0) {
                    v2u ga, gb;
                    const u32 mo = WS_MODF + (u32)(b4 * 2560 + 64 * ri + dcol) * 2u;
                    for (int sp = 0; sp < SPIN_G; ++sp) {
                        MEMBAR();
                        ga = ld_sc1_2(rws, mo); gb = ld_sc1_2(rws, mo + 3840);
                        if (all_valid(umax3(umax3(ga[0], ga[1], gb[0]), gb[1], 0u))) break;
                    }
                    xa = unpk(*(LAS v2u*)(L + L_XS + b4 * 1280 + (64 * ri + dcol) * 2)); gaa = unpk(ga); gab = unpk(gb);
                    asm volatile("" : "+v"(xa), "+v"(gaa), "+v"(gab));
                }
                if (w == 5) {
                    wait_all_flags();
                    for (int sp = 0; sp < SPIN_G; ++sp) {
                        MEMBAR();
                        const v4u e0 = ld_sc1(rws, WS_ML + l * 16), e1 = ld_sc1(rws, WS_ML + 1024 + l * 16);
                        const int cnt = __builtin_popcountll(__builtin_amdgcn_ballot_w64(e0[0] != SENT)) + __builtin_popcountll(__builtin_amdgcn_ballot_w64(e0[2] != SENT))
                                      + __builtin_popcountll(__builtin_amdgcn_ballot_w64(e1[0] != SENT)) + __builtin_popcountll(__builtin_amdgcn_ballot_w64(e1[2] != SENT));
                        if (cnt >= (ROLE == R_SB ? JIT_B : JIT_C)) break;
                        __builtin_amdgcn_s_sleep(8);
                    }
                }
                BAR();
                if (w == 0) STAMP(29);
                issueB(5); issueB(6); issueB(7); issueB(8);
            }
#pragma unroll
            for (int n = 0; n < NT; ++n) {
                const int a = NM + n, younger = (NA - 1 - a) < 3 ? (NA - 1 - a) : 3;
                if (younger == 3) VMW(12); else if (younger == 2) VMW(8); else if (younger == 1) VMW(4); else VMW(0);
                read_unit(L, ta, (a + 1) & 3, ft[n]);
                if (a + 4 < NA) { LGKM0(); issueB(a + 4); }
                if (ROLE == R_SA && n == 1 && w == 0) {
                    wait_all_flags();
                    if (u == 0 && l == 0) __hip_atomic_store((GAS u64*)(p.ws + WS_E), T, RLX_AGENT);
                }
            }
            LGKM0();
            BAR();
            STAMP(6);
        }
        if (ROLE == R_SA) {
            f4 res[2];
            UNDEF4(res[0]); UNDEF4(res[1]);
            u32 oo[2], mo[2];
#pragma unroll
            for (int r = 0; r < 2; ++r) {
                const int row = 2048 * ra + 256 * w + 32 * (4 * r + tq) + 8 * g, hd = row / DH, dd = row - hd * DH;
                oo[r] = WS_OP + (u32)((b4 * 32 + hd) * 2) * 384u + dd * 2; mo[r] = WS_ML + (u32)(hd * 4 + b4) * 16u;
            }
            bool done[2] = {false, false}, wdone[2] = {false, false}, obad[2] = {true, true}, mbad[2] = {true, true};
            v4u a[2][2], ml[2];
            float w0[2], w1[2];
#pragma unroll
            for (int r = 0; r < 2; ++r) { UNDEF4(ml[r]); UNDEF4(w0[r]); UNDEF4(w1[r]); UNDEF4(a[r][0]); UNDEF4(a[r][1]); }
            for (int sp = 0; sp < SPIN_G && !(done[0] && done[1]); ++sp) {
                MEMBAR();
                bool ldo[2];
#pragma unroll
                for (int r = 0; r < 2; ++r) {
                    ldo[r] = obad[r] && (!mbad[r] || sp == 0);
                    if (mbad[r]) ml[r] = ld_sc1(rws, mo[r]);
                    if (ldo[r]) { a[r][0] = ld_sc1(rws, oo[r]); a[r][1] = ld_sc1(rws, oo[r] + 384); }
                }
                const bool last = sp == SPIN_G - 1;
#pragma unroll
                for (int r = 0; r < 2; ++r) {
                    if (!wdone[r]) {
                        mbad[r] = vmax4(0u, ml[r]) == SENT;
                        if (__builtin_amdgcn_ballot_w64(mbad[r]) == 0ull || last) {
                            const float m0 = asf(ml[r][0]), m1 = asf(ml[r][2]), M = fmaxf(m0, m1);
                            const float e0 = asf(ml[r][1]) * __builtin_amdgcn_exp2f((m0 - M) * LOG2E), e1 = asf(ml[r][3]) * __builtin_amdgcn_exp2f((m1 - M) * LOG2E);
                            const float inv = fdiv(1.f, e0 + e1);
                            w0[r] = e0 * inv; w1[r] = e1 * inv; wdone[r] = true;
                        }
                    }
                    if (!done[r]) {
                        obad[r] = obad[r] && (!ldo[r] || vmax4(vmax4(0u, a[r][0]), a[r][1]) == SENT);
                        if ((__builtin_amdgcn_ballot_w64(obad[r]) == 0ull && wdone[r]) || last) {
                            STAMP(20 + w);
                            const f4 x0 = fma4(unpk(v2u{a[r][0][0], a[r][0][1]}), bc4(w0[r]), unpk(v2u{a[r][1][0], a[r][1][1]}) * w1[r]);
                            const f4 x1 = fma4(unpk(v2u{a[r][0][2], a[r][0][3]}), bc4(w0[r]), unpk(v2u{a[r][1][2], a[r][1][3]}) * w1[r]);
                            const v4u bq = {pack2(x0[0], x0[1]), pack2(x0[2], x0[3]), pack2(x1[0], x1[1]), pack2(x1[2], x1[3])};
                            const v4u bk[4] = {bq, ror4k<1>(bq), ror4k<2>(bq), ror4k<3>(bq)};
                            f4 d[4] = {};
#pragma unroll
                            for (int s = 0; s < 4; ++s)
#pragma unroll
                                for (int T2 = 0; T2 < 4; ++T2) d[(T2 + s) & 3] = mfma(ft[4 * r + s][(T2 + s) & 3], bk[T2], d[(T2 + s) & 3]);
                            res[r] = sel_tile(d, tq);
                            done[r] = true;
                        }
                    }
                }
            }
            if (w == 0) STAMP(7);
            *(LAS f4*)(L + L_RED + w * 1024 + l * 16) = res[0] + res[1];
            BAR();
            if (w == 0) {
                STAMP(8);
                LAS f4* R = (LAS f4*)(L + L_RED + l * 16);
                st_sc1(rws, outo, canon4(((R[0] + R[64]) + (R[128] + R[192])) + ((R[256] + R[320]) + (R[384] + R[448]))));
                STAMP(9);
            }
        } else if (ROLE == R_SB) {
            if (w < 5) {
                v4u a[6];
                if (w == 0) STAMP(10);
                const u32 ao = WS_AP + (u32)(g * DM + col) * 4u;
#pragma unroll
                for (int r = 0; r < 6; ++r) UNDEF4(a[r]);
                bool need = true;
                for (int sp = 0; sp < SPIN_G; ++sp) {
                    MEMBAR();
                    if (need) {
                        u32 mx = 0;
#pragma unroll
                        for (int r = 0; r < 3; ++r) { a[2 * r] = ld_sc1(rws, ao + r * 10240); a[2 * r + 1] = ld_sc1(rws, ao + r * 10240 + 256); }
#pragma unroll
                        for (int r = 0; r < 6; ++r) mx = vmax4(mx, a[r]);
                        need = mx == SENT;
                    }
                    if (__builtin_amdgcn_ballot_w64(need) == 0ull) break;
                }
                if (w == 0) STAMP(11);
                f4 ta2 = (asf4(a[0]) + asf4(a[2])) + asf4(a[4]), tb2 = (asf4(a[1]) + asf4(a[3])) + asf4(a[5]);
                rb8(ta2, tb2); ta2 *= gaa; tb2 *= gab; rb8(ta2, tb2); xa += ta2; xb += tb2; rb8(xa, xb);
                const float sh = dppf<0x150>(xa[0] + 0.f);
                const f4 da = xa - sh, db = xb - sh, t = da + db, q2 = fma4(da, da, db * db);
                float s1 = (t[0] + t[1]) + (t[2] + t[3]), s2 = (q2[0] + q2[1]) + (q2[2] + q2[3]);
                s1 += dppf<0x128>(s1); s2 += dppf<0x128>(s2); s1 += dppf<0x124>(s1); s2 += dppf<0x124>(s2);
                s1 += dppf<0x122>(s1); s2 += dppf<0x122>(s2); s1 += dppf<0x121>(s1); s2 += dppf<0x121>(s2);
                const float dm1 = s1 * (1.f / 128.f), mw = sh + dm1, v = fmaf(-dm1, s1, s2);
                *(LAS v2u*)(L + L_ST + 64 + g * 48 + w * 8) = v2u{asu(mw), asu(v)};
            }
            BAR();
            if (w < 5) {
                const f4 s0 = *(LAS f4*)(L + L_ST + 64 + g * 48), s1 = *(LAS f4*)(L + L_ST + 64 + g * 48 + 16);
                const f2 s2 = *(LAS f2*)(L + L_ST + 64 + g * 48 + 32);
                const float mean = (((s0[0] + s0[2]) + (s1[0] + s1[2])) + s2[0]) * 0.2f;
                const f4 dm = f4{s0[0], s0[2], s1[0], s1[2]} - mean, dq = dm * dm; const float d4 = s2[0] - mean;
                const float M2 = fmaf(128.f, fmaf(d4, d4, (dq[0] + dq[1]) + (dq[2] + dq[3])), ((s0[1] + s0[3]) + (s1[1] + s1[3])) + s2[1]);
                const float mb = rbf(mean), den = rbf(__builtin_amdgcn_sqrtf(rbf(rbf(M2 * (1.f / DM)) + 1e-5f))), rc = __builtin_amdgcn_rcpf(den);
                f4 ea = xa - mb, eb = xb - mb;
                rb8(ea, eb);
                const f4 qa = ea * rc, qb = eb * rc;
                f4 ha = fma4(fma4(-qa, bc4(den), ea), bc4(rc), qa), hb = fma4(fma4(-qb, bc4(den), eb), bc4(rc), qb);
                rb8(ha, hb); ha *= c1a; hb *= c1b; rb8(ha, hb); ha += sba; hb += sbb;
                *(LAS v2u*)(L + L_HB + g * 1280 + col * 2) = v2u{pack2(ha[0], ha[1]), pack2(ha[2], ha[3])};
                *(LAS v2u*)(L + L_HB + g * 1280 + col * 2 + 128) = v2u{pack2(hb[0], hb[1]), pack2(hb[2], hb[3])};
            }
            BAR();
            if (w == 0) STAMP(12);
            f4 d[4] = {};
#pragma unroll
            for (int n = 0; n < 5; ++n) {
                const v4u a = *(LAS v4u*)(L + L_HB + b4 * 1280 + (160 * (w >> 1) + 32 * n + 8 * g) * 2);
#pragma unroll
                for (int T2 = 0; T2 < 4; ++T2) d[T2] = mfma(ft[n][T2], a, d[T2]);
            }
            *(LAS f4*)(L + L_RED + w * 1024 + l * 16) = sel_tile(d, tq);
            BAR();
            if (w == 0) {
                STAMP(13);
                LAS f4* R = (LAS f4*)(L + L_RED + l * 16);
                const f4 gt = rb4((R[0] + R[128]) + (R[256] + R[384])), up = rb4((R[64] + R[192]) + (R[320] + R[448]));
                const f4 mk = {gt[0] > 0.f ? 1.f : 0.f, gt[1] > 0.f ? 1.f : 0.f, gt[2] > 0.f ? 1.f : 0.f, gt[3] > 0.f ? 1.f : 0.f};
                const f4 hh = (gt * mk) * up;
                st_sc1_2(rws, outo, v2u{canon(pack2(hh[0], hh[1])), canon(pack2(hh[2], hh[3]))});
                STAMP(14);
            }
        } else {
            f4 x1;
            UNDEF4(x1);
            if (w == 0) {
                const u32 ao = WS_AP + (u32)(b4 * DM + 64 * ri + dcol) * 4u;
                f4 s;
                for (int sp = 0; sp < SPIN_G; ++sp) {
                    MEMBAR();
                    const v4u p0 = ld_sc1(rws, ao), p1 = ld_sc1(rws, ao + 10240), p2 = ld_sc1(rws, ao + 20480);
                    s = (asf4(p0) + asf4(p1)) + asf4(p2);
                    if (all_valid(vmax4(vmax4(vmax4(0u, p0), p1), p2))) break;
                }
                x1 = rb4(xa + rb4(gaa * rb4(s)));
                asm volatile("" : "+v"(x1));
                STAMP(15);
            }
            v4u a0, a1;
            UNDEF4(a0); UNDEF4(a1);
            const u32 h0 = WS_HID + (u32)(b4 * FF + 192 * w + 32 * tq + 8 * g) * 2u, h1 = tq < 2 ? h0 + 256 : OOB;
            bool need = true;
            for (int sp = 0; sp < SPIN_G; ++sp) {
                MEMBAR();
                if (need) { a0 = ld_sc1(rws, h0); a1 = ld_sc1(rws, h1); need = vmax4(vmax4(0u, a0), a1) == SENT; }
                if (__builtin_amdgcn_ballot_w64(need) == 0ull) break;
            }
            if (w == 0) STAMP(16);
            const v4u bk[4] = {a0, ror4k<1>(a0), ror4k<2>(a0), ror4k<3>(a0)}, bl[4] = {a1, ror4k<1>(a1), ror4k<2>(a1), ror4k<3>(a1)};
            f4 d[4] = {};
#pragma unroll
            for (int k = 0; k < 4; ++k)
#pragma unroll
                for (int s = 0; s < 6; ++s) d[(s + k) & 3] = mfma(ft[s][(s + k) & 3], s < 4 ? bk[k] : bl[k], d[(s + k) & 3]);
            *(LAS f4*)(L + L_RED + w * 1024 + l * 16) = sel_tile(d, tq);
            BAR();
            if (w == 0) {
                STAMP(17);
                LAS f4* R = (LAS f4*)(L + L_RED + l * 16);
                const f4 o = x1 + rb4(gab * rb4(((R[0] + R[64]) + (R[128] + R[192])) + ((R[256] + R[320]) + (R[384] + R[448]))));
                __builtin_amdgcn_raw_buffer_store_b64(v2u{pack2(o[0], o[1]), pack2(o[2], o[3])}, __builtin_amdgcn_make_buffer_rsrc((void*)p.out, 0, 4 * DM * 2, 0x00020000),
                                                      (u32)(b4 * DM + 64 * ri + dcol) * 2u, 0, 16);
                STAMP(18);
            }
        }
    }
}

typedef const __attribute__((address_space(4))) Params* kargp;
#define GET_PARAMS() kargp pp_ = kp; asm volatile("" : "+s"(pp_)); \
    const Params q_ = {pp_->x, pp_->kc, pp_->vc, pp_->wq, pp_->wo, pp_->wg, pp_->wu, pp_->wd, pp_->c, pp_->wm, pp_->out, pp_->ws};
__global__ void __launch_bounds__(512, 2) fused_block(Params p) {
    extern __shared__ __attribute__((aligned(16))) unsigned char smem[];
    LAS unsigned char* L = (LAS unsigned char*)smem;
    const kargp kp = (kargp)__builtin_amdgcn_kernarg_segment_ptr();
    asm volatile("s_load_dword s98, %0, 0x0\n\ts_load_dword s99, %0, 0x40" :: "s"(kp) : "s98", "s99");
    const int l = threadIdx.x & 63, w = __builtin_amdgcn_readfirstlane(threadIdx.x >> 6), c = blockIdx.x;
    const int x = c & 7, y = c >> 3;
    if (__builtin_expect(c >= NW, 1)) { GET_PARAMS(); kv_path(q_, L, c - NW, w, l); }
    else if (y >= 5) { GET_PARAMS(); w_path<R_SB>(q_, L, 30 + 8 * (y - 5) + x, w, l); }
    else if (x < 6) { GET_PARAMS(); w_path<R_SA>(q_, L, 10 * (x >> 1) + 5 * (x & 1) + y, w, l); }
    else { GET_PARAMS(); w_path<R_SC>(q_, L, 54 + 5 * (x - 6) + y, w, l); }
}

extern "C" void kernel_launch(void* const* d_in, const int* in_sizes, int n_in, void* d_out, int out_size, void* d_ws, size_t ws_size, hipStream_t stream) {
    Params p;
    p.x = (const u16*)d_in[0]; p.kc = (const u16*)d_in[1]; p.vc = (const u16*)d_in[2]; p.wq = (const u16*)d_in[3]; p.wo = (const u16*)d_in[4];
    p.wg = (const u16*)d_in[6]; p.wu = (const u16*)d_in[7]; p.wd = (const u16*)d_in[8]; p.c = (const u16*)d_in[9]; p.wm = (const u16*)d_in[10];
    p.out = (u16*)d_out; p.ws = (unsigned char*)d_ws;
    (void)hipFuncSetAttribute((const void*)fused_block, hipFuncAttributeMaxDynamicSharedMemorySize, (int)L_END);
    hipLaunchKernelGGL(fused_block, dim3(256), dim3(512), L_END, stream, p);
}
```

```cpp
#include <hip/hip_runtime.h>
#include <stdint.h>
#pragma clang fp contract(off)

typedef unsigned short u16;
typedef unsigned u32;
typedef unsigned long long u64;
typedef u32 v4u __attribute__((ext_vector_type(4)));
typedef u32 v2u __attribute__((ext_vector_type(2)));
typedef float f4 __attribute__((ext_vector_type(4)));
typedef short v4s __attribute__((ext_vector_type(4)));
typedef __bf16 bf8 __attribute__((ext_vector_type(8)));
typedef __bf16 bf2 __attribute__((ext_vector_type(2)));
#define LAS __attribute__((address_space(3)))
#define GAS __attribute__((address_space(1)))
#define RLX_AGENT __ATOMIC_RELAXED, __HIP_MEMORY_SCOPE_AGENT

#define VSPL 4
constexpr int DM = 640, NH = 32, SKV = 4096, DH = 192, WIN = 1024, FF = 1536, NQ = NH * DH;
constexpr u32 SENT = 0xFFFFFFFFu;
constexpr int NW = 64;
constexpr u32 WS_E = 0, WS_FLAG = 4096, WS_F1P = 8192, WS_MODF = WS_F1P + 5 * 4 * 1280 * 4, WS_QP = WS_MODF + 4 * 2560 * 2,
              WS_OP = WS_QP + 5 * 4 * NQ * 4, WS_ML = WS_OP + 128 * 2 * DH * 2, WS_AP = WS_ML + 32 * 4 * 16, WS_HID = WS_AP + 3 * 4 * DM * 4,
              WS_END = WS_HID + 4 * FF * 2;
constexpr u32 OOB = 0x7FFFFF00u;
constexpr u32 L_XS = 131072, L_CS = L_XS + 5120, L_HB = L_CS + 5120, L_RED = L_HB + 5120, L_ST = L_RED + 8192, L_GA = L_ST + 256, L_GB = L_GA + 1024,
              L_X1 = L_GB + 1024, L_MISC = L_X1 + 1024, L_STAMP = L_MISC + 64, L_END = L_STAMP + 256;
constexpr u32 K_W = 18432, K_PX = 1024, K_PX2 = 1536, K_V = 9216, K_QS = 8 * K_W, K_ML = K_QS + 768, K_END = K_ML + 64;
static_assert(K_END <= L_MISC, "LDS");
constexpr float LOG2E = 1.4426950408889634f;
static_assert(L_END <= 163840, "LDS");
constexpr int SPIN_G = 40000, SPIN_L = 2000000;
constexpr int JIT_B = 2, JIT_C = 176;

#define STAMP(i) do {} while (0)

struct Params { const u16 *x, *kc, *vc, *wq, *wo, *wg, *wu, *wd, *c, *wm; u16* out; unsigned char* ws; };

__device__ __forceinline__ v4u mk_srd(const void* p, u32 bytes) { const u64 a = (u64)p; return v4u{(u32)a, (u32)(a >> 32) & 0xffffu, bytes, 0x00020000u}; }
__device__ __forceinline__ float asf(u32 u) { return __builtin_bit_cast(float, u); }
__device__ __forceinline__ u32 asu(float f) { return __builtin_bit_cast(u32, f); }
__device__ __forceinline__ float bflo(u32 u) { return asf(u << 16); }
__device__ __forceinline__ float bfhi(u32 u) { return asf(u & 0xffff0000u); }
__device__ __forceinline__ u32 pack2(float lo, float hi) { bf2 v; v[0] = (__bf16)lo; v[1] = (__bf16)hi; return __builtin_bit_cast(u32, v); }
__device__ __forceinline__ float rbf(float f) { return (float)(__bf16)f; }
typedef float f2 __attribute__((ext_vector_type(2)));
__device__ __forceinline__ f4 rb4(f4 v) {
    float a = v[0], b = v[1], c = v[2], d = v[3];
    asm("v_cvt_pk_bf16_f32 %0, 0, %0\n\tv_cvt_pk_bf16_f32 %1, 0, %1\n\tv_cvt_pk_bf16_f32 %2, 0, %2\n\tv_cvt_pk_bf16_f32 %3, 0, %3" : "+v"(a), "+v"(b), "+v"(c), "+v"(d));
    return f4{a, b, c, d}; }
__device__ __forceinline__ void rb8(f4& u, f4& v) {
    float a = u[0], b = u[1], c = u[2], d = u[3], e = v[0], f = v[1], g = v[2], h = v[3];
    asm("v_cvt_pk_bf16_f32 %0, 0, %0\n\tv_cvt_pk_bf16_f32 %1, 0, %1\n\tv_cvt_pk_bf16_f32 %2, 0, %2\n\tv_cvt_pk_bf16_f32 %3, 0, %3\n\t"
        "v_cvt_pk_bf16_f32 %4, 0, %4\n\tv_cvt_pk_bf16_f32 %5, 0, %5\n\tv_cvt_pk_bf16_f32 %6, 0, %6\n\tv_cvt_pk_bf16_f32 %7, 0, %7"
        : "+v"(a), "+v"(b), "+v"(c), "+v"(d), "+v"(e), "+v"(f), "+v"(g), "+v"(h));
    u = f4{a, b, c, d}; v = f4{e, f, g, h}; }
__device__ __forceinline__ f4 unpk(v2u v) { return f4{bflo(v[0]), bfhi(v[0]), bflo(v[1]), bfhi(v[1])}; }
__device__ __forceinline__ f4 fma4(f4 a, f4 b, f4 c) { return __builtin_elementwise_fma(a, b, c); }
__device__ __forceinline__ f4 bc4(float x) { return f4{x, x, x, x}; }
__device__ __forceinline__ u32 canon(u32 u) { return u < 0xFFFFFFFEu ? u : 0xFFFFFFFEu; }
__device__ __forceinline__ v4u canon4(f4 v) { return v4u{canon(asu(v[0])), canon(asu(v[1])), canon(asu(v[2])), canon(asu(v[3]))}; }
__device__ __forceinline__ u32 umax3(u32 a, u32 b, u32 c) { const u32 m = a > b ? a : b; return m > c ? m : c; }
__device__ __forceinline__ u32 vmax4(u32 m, v4u v) { return umax3(umax3(m, v[0], v[1]), v[2], v[3]); }
__device__ __forceinline__ bool all_valid(u32 m) { return __builtin_amdgcn_ballot_w64(m == SENT) == 0ull; }
__device__ __forceinline__ float dot2(u32 a, u32 b, float c) { return __builtin_amdgcn_fdot2_f32_bf16(__builtin_bit_cast(bf2, a), __builtin_bit_cast(bf2, b), c, false); }
__device__ __forceinline__ f4 mfma(v4u a, v4u b, f4 c) { return __builtin_amdgcn_mfma_f32_16x16x32_bf16(__builtin_bit_cast(bf8, a), __builtin_bit_cast(bf8, b), c, 0, 0, 0); }
template <int CTRL> __device__ __forceinline__ u32 dppu(u32 x) { return (u32)__builtin_amdgcn_mov_dpp((int)x, CTRL, 0xf, 0xf, true); }
template <int CTRL> __device__ __forceinline__ float dppf(float x) { return asf(dppu<CTRL>(asu(x))); }
template <int K> __device__ __forceinline__ v4u ror4k(v4u v) {
    if constexpr (K == 0) return v; else return v4u{dppu<0x120 + 4 * K>(v[0]), dppu<0x120 + 4 * K>(v[1]), dppu<0x120 + 4 * K>(v[2]), dppu<0x120 + 4 * K>(v[3])}; }
__device__ __forceinline__ float row_sum(float x) { x += dppf<0x128>(x); x += dppf<0x124>(x); x += dppf<0x122>(x); x += dppf<0x121>(x); return x; }
__device__ __forceinline__ float row_max(float x) { x = fmaxf(x, dppf<0x128>(x)); x = fmaxf(x, dppf<0x124>(x)); x = fmaxf(x, dppf<0x122>(x)); x = fmaxf(x, dppf<0x121>(x)); return x; }
__device__ __forceinline__ float xrow_sum(float x) {
    auto s = __builtin_amdgcn_permlane16_swap(asu(x), asu(x), false, false); x = asf(s[0]) + asf(s[1]);
    auto t = __builtin_amdgcn_permlane32_swap(asu(x), asu(x), false, false); return asf(t[0]) + asf(t[1]); }
__device__ __forceinline__ float xrow_max(float x) {
    auto s = __builtin_amdgcn_permlane16_swap(asu(x), asu(x), false, false); x = fmaxf(asf(s[0]), asf(s[1]));
    auto t = __builtin_amdgcn_permlane32_swap(asu(x), asu(x), false, false); return fmaxf(asf(t[0]), asf(t[1])); }
__device__ __forceinline__ float wave_sum(float x) { return xrow_sum(row_sum(x)); }
__device__ __forceinline__ float wave_max(float x) { return xrow_max(row_max(x)); }
__device__ __forceinline__ float fdiv(float a, float b) { const float r = __builtin_amdgcn_rcpf(b); const float q = a * r; return fmaf(fmaf(-q, b, a), r, q); }
__device__ __forceinline__ float silu_bf(float v) { return fdiv(v, 1.f + __builtin_amdgcn_exp2f(fminf(-v * 1.4426950408889634f, 126.f))); }
__device__ __forceinline__ v4u silu8(v4u c) {
    v4u r;
#pragma unroll
    for (int i = 0; i < 4; ++i) r[i] = pack2(silu_bf(bflo(c[i])), silu_bf(bfhi(c[i])));
    return r; }
#define BAR() asm volatile("s_waitcnt lgkmcnt(0)\n\ts_barrier" ::: "memory")
#define LGKM0() asm volatile("s_waitcnt lgkmcnt(0)" ::: "memory")
#define VMW(n) asm volatile("s_waitcnt vmcnt(%0)" :: "i"(n) : "memory")
#define MEMBAR() asm volatile("" ::: "memory")
typedef __amdgpu_buffer_rsrc_t rsrc_t;
__device__ __forceinline__ v4u ld_sc1(rsrc_t r, u32 off) { return __builtin_amdgcn_raw_buffer_load_b128(r, off, 0, 16); }
__device__ __forceinline__ v2u ld_sc1_2(rsrc_t r, u32 off) { return __builtin_amdgcn_raw_buffer_load_b64(r, off, 0, 16); }
__device__ __forceinline__ void st_sc1(rsrc_t r, u32 off, v4u v) { __builtin_amdgcn_raw_buffer_store_b128(v, r, off, 0, 16); }
__device__ __forceinline__ void st_sc1_2(rsrc_t r, u32 off, v2u v) { __builtin_amdgcn_raw_buffer_store_b64(v, r, off, 0, 16); }
__device__ __forceinline__ f4 asf4(v4u v) { return __builtin_bit_cast(f4, v); }

__device__ __forceinline__ void dma_unit(v4u srd, u32 v0, u32 v1, u32 sA, u32 sB, u32 lds) {
    u32 keep;
    asm volatile("s_nop 4\n\ts_mov_b32 %0, m0\n\ts_mov_b32 m0, %6\n\ts_nop 0\n\t"
                 "buffer_load_dwordx4 %1, %3, %4 offen nt lds\n\ts_add_u32 m0, m0, 0x400\n\ts_nop 0\n\t"
                 "buffer_load_dwordx4 %2, %3, %4 offen nt lds\n\ts_add_u32 m0, m0, 0x400\n\ts_nop 0\n\t"
                 "buffer_load_dwordx4 %1, %3, %5 offen nt lds\n\ts_add_u32 m0, m0, 0x400\n\ts_nop 0\n\t"
                 "buffer_load_dwordx4 %2, %3, %5 offen nt lds\n\ts_mov_b32 m0, %0"
                 : "=&s"(keep) : "v"(v0), "v"(v1), "s"(srd), "s"(sA), "s"(sB), "s"(lds) : "memory", "scc");
}
__device__ __forceinline__ void dma_1k(v4u srd, u32 voff, u32 soff, u32 lds) {
    u32 keep;
    asm volatile("s_nop 4\n\ts_mov_b32 %0, m0\n\ts_mov_b32 m0, %4\n\ts_nop 0\n\tbuffer_load_dwordx4 %1, %2, %3 offen lds\n\ts_mov_b32 m0, %0"
                 : "=&s"(keep) : "v"(voff), "s"(srd), "s"(soff), "s"(lds) : "memory");
}
struct WStream { v4u srd; u32 base, pitch, v0, v1; };
__device__ __forceinline__ WStream mk_stream(v4u srd, u32 base, u32 pitch, int l) {
    WStream s; s.srd = srd; s.base = base; s.pitch = pitch;
    const u32 sw = 2u * ((l >> 4) & 1);
    s.v0 = (l >> 3) * pitch + (((l & 7) ^ sw) << 4);
    s.v1 = (8 + (l >> 3)) * pitch + (((l & 7) ^ (sw | 4u)) << 4);
    return s; }
__device__ __forceinline__ void issue(const WStream& s, int n, u32 lds) { const u32 a = s.base + (u32)n * 32u * s.pitch; dma_unit(s.srd, s.v0, s.v1, a, a + 16u * s.pitch, lds); }
__device__ __forceinline__ void read_unit(LAS unsigned char* L, const u32 (&ta)[4], int slot, v4u (&f)[4]) {
#pragma unroll
    for (int T = 0; T < 4; ++T) {
        const v2u lo = __builtin_bit_cast(v2u, __builtin_amdgcn_ds_read_tr16_b64_v4i16((LAS v4s*)(L + ta[T] + slot * 4096)));
        const v2u hi = __builtin_bit_cast(v2u, __builtin_amdgcn_ds_read_tr16_b64_v4i16((LAS v4s*)(L + ta[T] + slot * 4096 + 512)));
        f[T] = v4u{lo[0], lo[1], hi[0], hi[1]};
    } }
__device__ __forceinline__ f4 sel_tile(const f4 (&a)[4], int t) {
    const bool b0 = t & 1, b1 = t & 2;
    f4 r;
#pragma unroll
    for (int i = 0; i < 4; ++i) { const float lo = b0 ? a[1][i] : a[0][i], hi = b0 ? a[3][i] : a[2][i]; r[i] = b1 ? hi : lo; }
    return r; }

__device__ __forceinline__ void dma_kv(v4u srd, u32 vo, u32 so, u32 lds) {
    u32 keep;
    asm volatile("s_nop 4\n\ts_mov_b32 %0, m0\n\ts_mov_b32 m0, %4\n\ts_nop 0\n\t"
                 "buffer_load_dwordx4 %1, %2, %3 offen nt lds\n\ts_add_u32 m0, m0, 0x380\n\ts_nop 0\n\t"
                 "buffer_load_dwordx4 %1, %2, %3 offen offset:128 nt lds\n\ts_add_u32 m0, m0, 0x380\n\ts_nop 0\n\t"
                 "buffer_load_dwordx4 %1, %2, %3 offen offset:256 nt lds\n\ts_mov_b32 m0, %0"
                 : "=&s"(keep) : "v"(vo), "s"(srd), "s"(so), "s"(lds) : "memory", "scc");
}
__device__ __forceinline__ float qk_unit(v4u k0, v4u k1, v4u k2, const v4u (&q)[3]) {
    float s = 0.f, t = 0.f;
#pragma unroll
    for (int r = 0; r < 4; r += 2) { s = dot2(k0[r], q[0][r], s); t = dot2(k0[r + 1], q[0][r + 1], t); }
#pragma unroll
    for (int r = 0; r < 4; r += 2) { s = dot2(k1[r], q[1][r], s); t = dot2(k1[r + 1], q[1][r + 1], t); }
#pragma unroll
    for (int r = 0; r < 4; r += 2) { s = dot2(k2[r], q[2][r], s); t = dot2(k2[r + 1], q[2][r + 1], t); }
    return s + t;
}
__device__ __forceinline__ void pv_unit(v4u v0, v4u v1, v4u v2, u32 plo, float (&acc)[24]) {
    const u32 phi = plo << 16;
#pragma unroll
    for (int r = 0; r < 4; ++r) {
        acc[2 * r] = dot2(v0[r], plo, acc[2 * r]); acc[2 * r + 1] = dot2(v0[r], phi, acc[2 * r + 1]);
        acc[8 + 2 * r] = dot2(v1[r], plo, acc[8 + 2 * r]); acc[8 + 2 * r + 1] = dot2(v1[r], phi, acc[8 + 2 * r + 1]);
        acc[16 + 2 * r] = dot2(v2[r], plo, acc[16 + 2 * r]); acc[16 + 2 * r + 1] = dot2(v2[r], phi, acc[16 + 2 * r + 1]);
    }
    asm volatile("" : "+v"(acc[0]), "+v"(acc[1]), "+v"(acc[2]), "+v"(acc[3]), "+v"(acc[4]), "+v"(acc[5]), "+v"(acc[6]), "+v"(acc[7]), "+v"(acc[8]), "+v"(acc[9]), "+v"(acc[10]), "+v"(acc[11]),
                      "+v"(acc[12]), "+v"(acc[13]), "+v"(acc[14]), "+v"(acc[15]), "+v"(acc[16]), "+v"(acc[17]), "+v"(acc[18]), "+v"(acc[19]), "+v"(acc[20]), "+v"(acc[21]), "+v"(acc[22]), "+v"(acc[23]));
}

template <bool B> struct BoolC { static constexpr bool value = B; };
__device__ __forceinline__ void kv_path(const Params& p, LAS unsigned char* L, int j, int w, int l) {
    const rsrc_t rws = __builtin_amdgcn_make_buffer_rsrc((void*)p.ws, 0, WS_END, 0x00020000);
    const rsrc_t rk = __builtin_amdgcn_make_buffer_rsrc((void*)p.kc, 0, 4 * NH * SKV * DH * 2, 0x00020000);
    const rsrc_t rv = __builtin_amdgcn_make_buffer_rsrc((void*)p.vc, 0, 4 * NH * SKV * DH * 2, 0x00020000);
    volatile LAS u32* misc = (volatile LAS u32*)(L + L_MISC);
    const int rk3 = (j >> 3) / 3, t3 = (j & 7) * 8 + rk3, pos = (j >> 3) - 3 * rk3;
    const int slot = pos == 1 ? (w & 1) : 0;
    const int bh = 2 * t3 + (pos == 2 ? 1 : (pos == 1 ? slot : 0));
    const int part = pos == 0 ? 0 : (pos == 2 ? 1 : (slot == 0 ? 1 : 0));
    const int wi = pos == 1 ? (w >> 1) : w;
    const int ub = pos == 0 ? 0 : (pos == 2 ? 43 : (slot == 0 ? 85 : 0));
    const int ng = pos == 1 ? 2 : 4, u1 = ub + (wi >= ng ? 11 * ng + wi - ng : wi);
    const u32 ust = ng * 3072;
    const int nl = (pos == 1 ? wi == 3 : wi >= 5) ? 2 : 3;
    const bool lead = w == 0 || (w == 1 && pos == 1);
    const u32 opb = WS_OP + (bh * 2 + part) * 384, mlb = WS_ML + ((bh & 31) * 4 + (bh >> 5)) * 16 + part * 8;
    const u32 vo = (l >> 3) * 384 + (l & 7) * 16;
    const u32 base = ((u32)bh * SKV + (SKV - WIN)) * 384u + (u32)u1 * 3072u;
    const u32 wl = w * K_W, lb = (u32)(size_t)L + wl;
    const v4u sk = mk_srd(p.kc, 4 * NH * SKV * DH * 2), sv = mk_srd(p.vc, 4 * NH * SKV * DH * 2);
    STAMP(0);
    if (w != 4) { dma_kv(sk, vo, base + 8 * ust, lb); dma_kv(sv, vo, base + 8 * ust, lb + K_V); }
    if (threadIdx.x < 12) misc[threadIdx.x] = 0;
    BAR();
    if (w == 4) {
        __builtin_amdgcn_s_setprio(3);
        const u64 T = __hip_atomic_load((GAS u64*)(p.ws + WS_E), RLX_AGENT) + 1ull;
        st_sc1(rws, l < 24 ? opb + l * 16 : OOB, v4u{SENT, SENT, SENT, SENT});
        st_sc1_2(rws, l == 0 ? mlb : OOB, v2u{SENT, SENT});
        const int bh1 = 2 * t3 + 1;
        st_sc1(rws, (pos == 1 && l < 24) ? WS_OP + bh1 * 2 * 384 + l * 16 : OOB, v4u{SENT, SENT, SENT, SENT});
        st_sc1_2(rws, (pos == 1 && l == 0) ? WS_ML + ((bh1 & 31) * 4 + (bh1 >> 5)) * 16 : OOB, v2u{SENT, SENT});
        VMW(0);
        if (l == 0) {
            __hip_atomic_store((GAS u64*)(p.ws + WS_FLAG) + blockIdx.x, T, RLX_AGENT);
            misc[4] = (u32)T; misc[5] = (u32)(T >> 32); LGKM0(); misc[2] = 1;
        }
        STAMP(9);
        __builtin_amdgcn_s_setprio(0);
        dma_kv(sk, vo, base + 8 * ust, lb); dma_kv(sv, vo, base + 8 * ust, lb + K_V);
    }
    const int c = l & 7;
    float m, lsum, fsc;
    const u32 nslot = pos == 1 ? 4u : 8u;
    auto slot_stats = [&](float& M, float& mi, float& li) {
        const v2u t = *(LAS v2u*)(L + K_ML + c * 8);
        const bool mine = pos != 1 || (l & 1) == slot;
        mi = mine ? asf(t[0]) : -INFINITY; li = mine ? asf(t[1]) : 0.f;
        M = fmaxf(mi, dppf<0xB1>(mi)); M = fmaxf(M, dppf<0x4E>(M)); M = fmaxf(M, dppf<0x141>(M));
    };
    auto post = [&]() {
        u32 old = 0;
        if (l == 0) { *(LAS v2u*)(L + K_ML + w * 8) = v2u{asu(m), asu(lsum)}; LGKM0(); old = __hip_atomic_fetch_add((LAS u32*)(L + L_MISC + 32 + slot * 4), 1u, __ATOMIC_RELAXED, __HIP_MEMORY_SCOPE_WORKGROUP); }
        if (__builtin_amdgcn_readfirstlane(old) == nslot - 1u) {
            MEMBAR();
            float M, mi, li; slot_stats(M, mi, li);
            float e = li * __builtin_amdgcn_exp2f((mi - M) * LOG2E);
            e += dppf<0xB1>(e); e += dppf<0x4E>(e); e += dppf<0x141>(e);
            st_sc1_2(rws, l == 0 ? mlb : OOB, v2u{canon(asu(M)), canon(asu(e))});
            STAMP(5);
        }
    };
    auto scale = [&]() {
        for (int sp = 0; misc[8 + slot] < nslot && sp < SPIN_L; ++sp) __builtin_amdgcn_s_sleep(1);
        MEMBAR();
        float M, mi, li; slot_stats(M, mi, li);
        float e = li * __builtin_amdgcn_exp2f((mi - M) * LOG2E);
        e += dppf<0xB1>(e); e += dppf<0x4E>(e); e += dppf<0x141>(e);
        fsc = fdiv(__builtin_amdgcn_exp2f((m - M) * LOG2E), e);
    };
    dma_kv(sk, vo, base + 9 * ust, lb + 3072); dma_kv(sv, vo, base + 9 * ust, lb + K_V + 3072);
    if (nl == 3) { dma_kv(sk, vo, base + 10 * ust, lb + 6144); dma_kv(sv, vo, base + 10 * ust, lb + K_V + 6144); }
    v4u K[24], V[24];
#pragma unroll
    for (int jj = 0; jj < 8; ++jj)
#pragma unroll
        for (int i = 0; i < 3; ++i) K[jj * 3 + i] = __builtin_amdgcn_raw_buffer_load_b128(rk, vo + i * 128, base + jj * ust, 2);
#pragma unroll
    for (int jj = 0; jj < VSPL; ++jj)
#pragma unroll
        for (int i = 0; i < 3; ++i) V[jj * 3 + i] = __builtin_amdgcn_raw_buffer_load_b128(rv, vo + i * 128, base + jj * ust, 2);
    __builtin_amdgcn_sched_barrier(0);
    const bool early = __builtin_amdgcn_readfirstlane(misc[slot]) != 0u;
    float acc[24];
    auto attend = [&](auto ec) {
        constexpr bool EARLY = decltype(ec)::value;
        if (!EARLY) {
#pragma unroll
            for (int jj = VSPL; jj < 8; ++jj)
#pragma unroll
                for (int i = 0; i < 3; ++i) V[jj * 3 + i] = __builtin_amdgcn_raw_buffer_load_b128(rv, vo + i * 128, base + jj * ust, 2);
            __builtin_amdgcn_sched_barrier(0);
            if (w == 0) STAMP(1);
            if (w == 7) STAMP(2);
            if (w < 4) {
                for (int sp = 0; misc[2] == 0 && sp < SPIN_L; ++sp) __builtin_amdgcn_s_sleep(1);
                const u32 tlo = misc[4], thi = misc[5];
                for (int sp = 0; sp < SPIN_G && misc[6] == 0; ++sp) {
                    MEMBAR();
                    const v2u f = ld_sc1_2(rws, WS_FLAG + l * 8);
                    if (__builtin_amdgcn_ballot_w64(f[0] != tlo || f[1] != thi) == 0ull) misc[6] = 1;
                }
                const u32 qo = WS_QP + (u32)((bh >> 5) * NQ + (bh & 31) * DH + 4 * l) * 4u;
                v4u a[5] = {};
                bool need = l < 48;
                for (int sp = 0; sp < SPIN_G; ++sp) {
                    MEMBAR();
                    if (__builtin_amdgcn_readfirstlane(misc[slot]) != 0u) break;
                    if (need) {
                        u32 mx = 0;
#pragma unroll
                        for (int ks = 0; ks < 5; ++ks) a[ks] = ld_sc1(rws, qo + ks * (4 * NQ * 4));
#pragma unroll
                        for (int ks = 0; ks < 5; ++ks) mx = vmax4(mx, a[ks]);
                        need = mx == SENT;
                    }
                    if (__builtin_amdgcn_ballot_w64(need) == 0ull || sp == SPIN_G - 1) {
                        const f4 qs = (((asf4(a[0]) + asf4(a[1])) + asf4(a[2])) + asf4(a[3])) + asf4(a[4]);
                        if (l < 48) *(LAS v2u*)(L + K_QS + slot * 384 + l * 8) = v2u{pack2(qs[0], qs[1]), pack2(qs[2], qs[3])};
                        LGKM0();
                        misc[slot] = 1;
                        break;
                    }
                }
                if (w == 0) STAMP(3);
            } else {
                for (int sp = 0; misc[slot] == 0 && sp < SPIN_L; ++sp) __builtin_amdgcn_s_sleep(1);
            }
        }
        VMW(EARLY ? 24 + 3 * VSPL : 48);
        v4u p0, p1, p2;
        {
            v4u q[3];
#pragma unroll
            for (int i = 0; i < 3; ++i) q[i] = *(LAS v4u*)(L + K_QS + slot * 384 + (c + 8 * i) * 16);
            float y[4], x[8];
            LAS v4u* KL = (LAS v4u*)(L + wl + l * 16);
#pragma unroll
            for (int jj = 0; jj < 3; ++jj) { y[jj] = qk_unit(KL[jj * 192], KL[jj * 192 + 64], KL[jj * 192 + 128], q); __builtin_amdgcn_sched_barrier(0); }
#pragma unroll
            for (int jj = 0; jj < 8; jj += 2) { x[jj] = qk_unit(K[3 * jj], K[3 * jj + 1], K[3 * jj + 2], q); x[jj + 1] = qk_unit(K[3 * jj + 3], K[3 * jj + 4], K[3 * jj + 5], q); __builtin_amdgcn_sched_barrier(0); }
#pragma unroll
            for (int h = 0; h < 4; ++h) { const float a = x[h] + dppf<0x141>(x[h]), b = x[h + 4] + dppf<0x141>(x[h + 4]); x[h] = (c & 4) ? b : a; }
            { const float a = y[0] + dppf<0x141>(y[0]), b = y[2] + dppf<0x141>(y[2]), a1 = y[1] + dppf<0x141>(y[1]); y[0] = (c & 4) ? b : a; y[1] = a1; }
#pragma unroll
            for (int h = 0; h < 2; ++h) { const float a = x[h] + dppf<0x4E>(x[h]), b = x[h + 2] + dppf<0x4E>(x[h + 2]); x[h] = (c & 2) ? b : a; }
            { const float a = y[0] + dppf<0x4E>(y[0]), b = y[1] + dppf<0x4E>(y[1]); y[0] = (c & 2) ? b : a; }
            { const float a = x[0] + dppf<0xB1>(x[0]), b = x[1] + dppf<0xB1>(x[1]); x[0] = (c & 1) ? b : a; }
            y[0] += dppf<0xB1>(y[0]);
            const float s1 = rbf(rbf(x[0]) * 0.07216878364870322f);
            const float s2 = (c >> 1) < nl ? rbf(rbf(y[0]) * 0.07216878364870322f) : -INFINITY;
            m = wave_max(fmaxf(s1, s2));
            const float e1 = __builtin_amdgcn_exp2f((s1 - m) * LOG2E), e2 = __builtin_amdgcn_exp2f((s2 - m) * LOG2E);
            *(LAS u32*)(L + wl + K_PX + l * 4) = pack2(e1, 0.f);
            *(LAS u32*)(L + wl + K_PX2 + (l >> 1) * 4) = pack2(e2, 0.f);
            lsum = wave_sum(e1 + ((c & 1) ? 0.f : e2));
            post();
            LGKM0();
            p0 = *(LAS v4u*)(L + wl + K_PX + (l >> 3) * 32); p1 = *(LAS v4u*)(L + wl + K_PX + (l >> 3) * 32 + 16); p2 = *(LAS v4u*)(L + wl + K_PX2 + (l >> 3) * 16);
        }
        if (EARLY) {
            __builtin_amdgcn_sched_barrier(0);
#pragma unroll
            for (int jj = VSPL; jj < 8; ++jj)
#pragma unroll
                for (int i = 0; i < 3; ++i) V[jj * 3 + i] = __builtin_amdgcn_raw_buffer_load_b128(rv, vo + i * 128, base + jj * ust, 2);
            __builtin_amdgcn_sched_barrier(0);
        }
        if (w == 0) STAMP(4);
#pragma unroll
        for (int i = 0; i < 24; ++i) acc[i] = 0.f;
        {
            LAS v4u* VL = (LAS v4u*)(L + wl + K_V + l * 16);
            pv_unit(VL[0], VL[64], VL[128], p2[0], acc);
            pv_unit(VL[192], VL[256], VL[320], p2[1], acc);
            if (nl == 3) pv_unit(VL[384], VL[448], VL[512], p2[2], acc);
        }
#pragma unroll
        for (int jj = 0; jj < 6; ++jj) pv_unit(V[3 * jj], V[3 * jj + 1], V[3 * jj + 2], jj < 4 ? p0[jj & 3] : p1[jj & 3], acc);
        scale();
#pragma unroll
        for (int jj = 6; jj < 8; ++jj) pv_unit(V[3 * jj], V[3 * jj + 1], V[3 * jj + 2], p1[jj & 3], acc);
    };
    if (early) attend(BoolC<true>{}); else attend(BoolC<false>{});
    if (w == 7) STAMP(6);
    float y[12], z[6];
#pragma unroll
    for (int n = 0; n < 12; ++n) { auto s = __builtin_amdgcn_permlane32_swap(asu(acc[n]), asu(acc[n + 12]), false, false); y[n] = asf(s[0]) + asf(s[1]); }
#pragma unroll
    for (int n = 0; n < 6; ++n) { auto s = __builtin_amdgcn_permlane16_swap(asu(y[n]), asu(y[n + 6]), false, false); z[n] = asf(s[0]) + asf(s[1]); }
#pragma unroll
    for (int n = 0; n < 3; ++n) {
        const float a = z[n] + dppf<0x128>(z[n]), b = z[n + 3] + dppf<0x128>(z[n + 3]);
        const int ai = 3 * (l >> 3) + n;
        *(LAS float*)(L + wl + (64 * (ai >> 3) + 8 * c + (ai & 7)) * 4) = ((l & 8) ? b : a) * fsc;
    }
    BAR();
    if (w == 0) STAMP(7);
    if (lead) {
        const u32 pb = slot * K_W + (l < 48 ? l : 47) * 16;
        f4 o = (*(LAS f4*)(L + pb) + *(LAS f4*)(L + pb + 2 * K_W)) + (*(LAS f4*)(L + pb + 4 * K_W) + *(LAS f4*)(L + pb + 6 * K_W));
        if (pos != 1) o += (*(LAS f4*)(L + pb + K_W) + *(LAS f4*)(L + pb + 3 * K_W)) + (*(LAS f4*)(L + pb + 5 * K_W) + *(LAS f4*)(L + pb + 7 * K_W));
        st_sc1_2(rws, l < 48 ? opb + l * 8 : OOB, v2u{canon(pack2(o[0], o[1])), canon(pack2(o[2], o[3]))});
        if (w == 0) STAMP(8); else STAMP(10);
    }
}

enum { R_SA = 0, R_SB = 1, R_SC = 2 };
#define UNDEF4(v) asm volatile("" : "=v"(v))
template <int ROLE>
__device__ __forceinline__ void w_path(const Params& p, LAS unsigned char* L, int u, int w, int l) {
    const rsrc_t rws = __builtin_amdgcn_make_buffer_rsrc((void*)p.ws, 0, WS_END, 0x00020000);
    const int tid = threadIdx.x, g = l >> 4, tq = (l & 15) >> 2, b4 = l & 3;
    const u32 lbase = (u32)(size_t)L, stg = lbase + w * 16384;
    const bool hasF1 = u < 50, hasF2 = u < 60;
    const int kf = u / 10, lp = u - 10 * kf, kf2 = u / 12, cs = u - 12 * kf2;
    const int ri = ROLE == R_SA ? u : (ROLE == R_SB ? u - 30 : u - 54);
    const WStream sF1 = mk_stream(mk_srd(p.wm, hasF1 ? DM * 3840 * 2 : 0), (u32)((128 * kf + 32 * (w >> 1)) * 7680 + (128 * lp + 64 * (w & 1)) * 2), 7680, l);
    const WStream sF2 = mk_stream(mk_srd(p.wq, hasF2 ? DM * NQ * 2 : 0), (u32)(128 * kf2 * 12288 + (512 * cs + 64 * w) * 2), 12288, l);
    const int mid = ROLE == R_SC ? (w < 4 ? (ri >= 6 ? ri : -1) : 30 + ri) : (w < 4 ? (ri < 6 ? ri : ri + 4) : (ri == 20 ? 28 : (ri == 21 ? 29 : -1)));
    const bool hasM = ROLE != R_SA && mid >= 0;
    const WStream sM = mk_stream(mk_srd(p.wm, hasM ? DM * 3840 * 2 : 0), (u32)(160 * (w & 3) * 7680 + (1280 + 64 * mid) * 2), 7680, l);
    const int ra = ri / 10, ca = ri - 10 * ra;
    const WStream sT = ROLE == R_SA ? mk_stream(mk_srd(p.wo, NQ * DM * 2), (u32)((2048 * ra + 256 * w) * 1280 + ca * 128), 1280, l)
                     : ROLE == R_SB ? mk_stream(mk_srd((w & 1) ? p.wu : p.wg, DM * FF * 2), (u32)(160 * (w >> 1) * 3072 + ri * 128), 3072, l)
                                    : mk_stream(mk_srd(p.wd, FF * DM * 2), (u32)(192 * w * 1280 + ri * 128), 1280, l);
    constexpr int NM = ROLE == R_SA ? 0 : 5, NT = ROLE == R_SA ? 8 : (ROLE == R_SB ? 5 : 6), NA = NM + NT;
    u32 ta[4];
    { const u32 f = 2u * (tq >> 1) | 4u * (g & 1);
#pragma unroll
      for (int T = 0; T < 4; ++T) ta[T] = w * 16384 + (8 * g + tq) * 128 + ((((u32)(2 * T)) ^ f) + (b4 >> 1)) * 16 + (b4 & 1) * 8; }
    const u32 dcol = 16 * tq + 4 * g;
    const u32 f1o = hasF1 ? WS_F1P + (u32)((kf * 4 + b4) * 1280 + 128 * lp + 64 * w + dcol) * 4u : OOB;
    const u32 qpo = hasF2 ? WS_QP + (u32)((kf2 * 4 + b4) * NQ + 512 * cs + 64 * w + dcol) * 4u : OOB;
    const u32 mfo = hasM ? WS_MODF + (u32)(b4 * 2560 + 64 * mid + dcol) * 2u : OOB;
    const u32 outo = ROLE == R_SA ? WS_AP + (u32)((ra * 4 + b4) * DM + 64 * ca + dcol) * 4u : WS_HID + (u32)(b4 * FF + 64 * ri + dcol) * 2u;
    volatile LAS u32* misc = (volatile LAS u32*)(L + L_MISC);
    STAMP(0);
    if (w < 5) { dma_1k(mk_srd(p.c, 5120), l * 16, w * 1024, lbase + L_CS + w * 1024); dma_1k(mk_srd(p.x, 5120), l * 16, w * 1024, lbase + L_XS + w * 1024); }
    issue(sF1, 0, stg);
    { u32 keep;
      asm volatile("s_nop 4\n\ts_mov_b32 %0, m0\n\ts_mov_b32 m0, %5\n\ts_nop 0\n\tbuffer_load_dword %1, %2, %3 offen nt lds\n\tbuffer_load_dword %1, %4, 0 offen nt lds\n\ts_mov_b32 m0, %0"
                   : "=&s"(keep) : "v"(l == 0 ? 0u : OOB), "s"(sT.srd), "s"(sT.base), "s"(mk_srd(p.out, 5120)), "s"(lbase + L_GB) : "memory"); }
    if (w == 0) {
        u32 keep;
        asm volatile("s_nop 4\n\ts_mov_b32 %0, m0\n\ts_mov_b32 m0, %3\n\ts_nop 0\n\tbuffer_load_dword %1, %2, 0 offen sc1 lds\n\ts_mov_b32 m0, %0"
                     : "=&s"(keep) : "v"(l < 2 ? WS_E + l * 4 : OOB), "s"(mk_srd(p.ws, WS_END)), "s"(lbase + L_GA) : "memory");
    }
    { const v4u s4 = {SENT, SENT, SENT, SENT};
      st_sc1(rws, qpo, s4);
      if (w < 2) st_sc1(rws, f1o, s4);
      if (ROLE != R_SA && (w & 3) == 0) st_sc1_2(rws, mfo, v2u{SENT, SENT});
      if (w == 0) {
          if (ROLE == R_SA) st_sc1(rws, outo, s4);
          if (ROLE == R_SB) st_sc1_2(rws, outo, v2u{SENT, SENT});
      } }
    if (tid == 0) { misc[0] = 0; misc[1] = 0; }
    BAR();
    issue(sF2, 0, stg + 4096);
    auto issueB = [&](int n) { if (n < NM) issue(sM, n, stg + ((n + 1) & 3) * 4096); else issue(sT, n - NM, stg + ((n + 1) & 3) * 4096); };
    {
        v4u ft[NT][4];
        f4 xa, xb, gaa, gab, c1a, c1b, sba, sbb;
        const int col = 128 * w + 4 * (l & 15);
        {
            VMW(4);
            BAR();
            STAMP(1);
            const u64 T = *(LAS u64*)(L + L_GA) + 1ull;
            const u32 tlo = (u32)T, thi = (u32)(T >> 32);
            if (tid == 0) __hip_atomic_store((GAS u64*)(p.ws + WS_FLAG) + blockIdx.x, T, RLX_AGENT);
            auto wait_all_flags = [&]() {
                for (int sp = 0; sp < SPIN_G; ++sp) {
                    MEMBAR();
                    const v4u f0 = ld_sc1(rws, WS_FLAG + l * 16), f1 = ld_sc1(rws, WS_FLAG + 1024 + l * 16);
                    const bool bad = f0[0] != tlo || f0[1] != thi || f0[2] != tlo || f0[3] != thi || f1[0] != tlo || f1[1] != thi || f1[2] != tlo || f1[3] != thi;
                    if (__builtin_amdgcn_ballot_w64(bad) == 0ull) break;
                }
            };
            {
                v4u f[4]; read_unit(L, ta, 0, f);
                const v4u a = silu8(*(LAS v4u*)(L + L_CS + b4 * 1280 + (128 * kf + 32 * (w >> 1) + 8 * g) * 2));
                f4 d[4];
#pragma unroll
                for (int T2 = 0; T2 < 4; ++T2) d[T2] = mfma(f[T2], a, f4{0.f, 0.f, 0.f, 0.f});
                *(LAS f4*)(L + L_RED + w * 1024 + l * 16) = sel_tile(d, tq);
                if (w >= 4) {
                    float xv[10], s = 0.f;
#pragma unroll
                    for (int i = 0; i < 5; ++i) { const u32 t = *(LAS u32*)(L + L_XS + (w - 4) * 1280 + (l + 64 * i) * 4); xv[2 * i] = bflo(t); xv[2 * i + 1] = bfhi(t); s += xv[2 * i] + xv[2 * i + 1]; }
                    const float mean = wave_sum(s) * (1.f / DM);
                    float v = 0.f;
#pragma unroll
                    for (int i = 0; i < 10; ++i) v = fmaf(xv[i] - mean, xv[i] - mean, v);
                    const float var = wave_sum(v) * (1.f / DM);
                    if (l == 0) *(LAS v2u*)(L + L_ST + (w - 4) * 8) = v2u{asu(rbf(mean)), asu(rbf(__builtin_amdgcn_sqrtf(rbf(rbf(var) + 1e-5f))))};
                }
                BAR();
                if (w < 2) {
                    LAS f4* R = (LAS f4*)(L + L_RED + w * 1024 + l * 16);
                    st_sc1(rws, f1o, canon4((R[0] + R[128]) + (R[256] + R[384])));
                }
                if (w == 0) STAMP(2);
                issue(sF2, 1, stg + 8192); issue(sF2, 2, stg + 12288); issue(sF2, 3, stg);
            }
            if (w < 2) {
                for (int sp = 0; sp < SPIN_G; ++sp) {
                    MEMBAR();
                    const v2u f = ld_sc1_2(rws, WS_FLAG + l * 8);
                    if (__builtin_amdgcn_ballot_w64(f[0] != tlo || f[1] != thi) == 0ull) break;
                }
                if (w == 0) STAMP(19);
                const int hb = tid >> 5, hi = tid & 31, col = 128 * kf2 + 4 * hi;
                const u32 o = WS_F1P + (u32)(hb * 1280 + col) * 4u;
                v4u a[5] = {}, c2[5] = {};
                bool need = hasF2;
                for (int sp = 0; sp < SPIN_G; ++sp) {
                    MEMBAR();
                    if (need) {
                        u32 mx = 0;
#pragma unroll
                        for (int ks = 0; ks < 5; ++ks) { a[ks] = ld_sc1(rws, o + ks * 20480); c2[ks] = ld_sc1(rws, o + ks * 20480 + 2560); }
#pragma unroll
                        for (int ks = 0; ks < 5; ++ks) mx = vmax4(vmax4(mx, a[ks]), c2[ks]);
                        need = mx == SENT;
                    }
                    if (__builtin_amdgcn_ballot_w64(need) == 0ull) break;
                }
                const f4 sa = (((asf4(a[0]) + asf4(a[1])) + asf4(a[2])) + asf4(a[3])) + asf4(a[4]);
                const f4 sc = (((asf4(c2[0]) + asf4(c2[1])) + asf4(c2[2])) + asf4(c2[3])) + asf4(c2[4]);
                const v2u st = *(LAS v2u*)(L + L_ST + hb * 8), xr = *(LAS v2u*)(L + L_XS + hb * 1280 + (hasF2 ? col : 0) * 2);
                const float den = asf(st[1]), rc = __builtin_amdgcn_rcpf(den);
                const f4 e = rb4(unpk(xr) - asf(st[0])), q1 = e * rc;
                const f4 h = rb4(rb4(fma4(fma4(-q1, bc4(den), e), bc4(rc), q1)) * rb4(rb4(sc) + 1.f)) + rb4(sa);
                *(LAS v2u*)(L + L_HB + hb * 256 + hi * 8) = v2u{pack2(h[0], h[1]), pack2(h[2], h[3])};
                LGKM0();
                if (l == 0) __hip_atomic_fetch_add((LAS u32*)(L + L_MISC), 1u, __ATOMIC_RELAXED, __HIP_MEMORY_SCOPE_WORKGROUP);
                if (w == 0) STAMP(4);
            }
            {
                v4u fq[4][4];
                VMW(12); read_unit(L, ta, 1, fq[0]); LGKM0();
                VMW(8); read_unit(L, ta, 2, fq[1]);
                VMW(4); read_unit(L, ta, 3, fq[2]);
                issueB(0);
                VMW(4); read_unit(L, ta, 0, fq[3]);
                if (w == 7) STAMP(3);
                for (int sp = 0; misc[0] < 2u && sp < SPIN_L; ++sp) __builtin_amdgcn_s_sleep(1);
                MEMBAR();
                f4 d[4] = {};
#pragma unroll
                for (int n = 0; n < 4; ++n) {
                    const v4u a = *(LAS v4u*)(L + L_HB + b4 * 256 + (32 * n + 8 * g) * 2);
#pragma unroll
                    for (int T2 = 0; T2 < 4; ++T2) d[T2] = mfma(fq[n][T2], a, d[T2]);
                }
                st_sc1(rws, qpo, canon4(sel_tile(d, tq)));
                BAR();
                if (w == 7) STAMP(5);
                issueB(1); issueB(2); issueB(3);
            }
            if (NM) {
                f4 d[4] = {};
#pragma unroll
                for (int n = 0; n < NM; ++n) {
                    if (n < 2) VMW(12); else if (n == 2) VMW(8); else if (n == 3) VMW(4); else VMW(0);
                    v4u f[4]; read_unit(L, ta, (n + 1) & 3, f);
                    const v4u a = silu8(*(LAS v4u*)(L + L_CS + b4 * 1280 + (160 * (w & 3) + 32 * n + 8 * g) * 2));
                    LGKM0();
                    if (n == 0) issueB(4);
#pragma unroll
                    for (int T2 = 0; T2 < 4; ++T2) d[T2] = mfma(f[T2], a, d[T2]);
                }
                *(LAS f4*)(L + L_RED + w * 1024 + l * 16) = sel_tile(d, tq);
                BAR();
                if ((w & 3) == 0) {
                    LAS f4* R = (LAS f4*)(L + L_RED + w * 1024 + l * 16);
                    const f4 s = (R[0] + R[64]) + (R[128] + R[192]);
                    st_sc1_2(rws, mfo, v2u{canon(pack2(s[0], s[1])), canon(pack2(s[2], s[3]))});
                }
                UNDEF4(xa); UNDEF4(xb); UNDEF4(gaa); UNDEF4(gab); UNDEF4(c1a); UNDEF4(c1b); UNDEF4(sba); UNDEF4(sbb);
                if (ROLE == R_SB && w < 5) {
                    v2u gv[2], sv[2], cv[2];
                    const u32 mo = WS_MODF + (u32)(g * 2560 + col) * 2u;
                    for (int sp = 0; sp < SPIN_G; ++sp) {
                        MEMBAR();
                        u32 mx = 0;
#pragma unroll
                        for (int h = 0; h < 2; ++h) { gv[h] = ld_sc1_2(rws, mo + h * 128); sv[h] = ld_sc1_2(rws, mo + 1280 + h * 128); cv[h] = ld_sc1_2(rws, mo + 2560 + h * 128); }
#pragma unroll
                        for (int h = 0; h < 2; ++h) mx = umax3(umax3(umax3(mx, gv[h][0], gv[h][1]), sv[h][0], sv[h][1]), cv[h][0], cv[h][1]);
                        if (all_valid(mx)) break;
                    }
                    xa = unpk(*(LAS v2u*)(L + L_XS + g * 1280 + col * 2)); xb = unpk(*(LAS v2u*)(L + L_XS + g * 1280 + col * 2 + 128));
                    gaa = unpk(gv[0]); gab = unpk(gv[1]);
                    c1a = rb4(unpk(cv[0]) + 1.f); c1b = rb4(unpk(cv[1]) + 1.f);
                    sba = unpk(sv[0]); sbb = unpk(sv[1]);
                    asm volatile("" : "+v"(xa), "+v"(xb), "+v"(gaa), "+v"(gab), "+v"(c1a), "+v"(c1b), "+v"(sba), "+v"(sbb));
                }
                if (ROLE == R_SC && w == 0) {
                    v2u ga, gb;
                    const u32 mo = WS_MODF + (u32)(b4 * 2560 + 64 * ri + dcol) * 2u;
                    for (int sp = 0; sp < SPIN_G; ++sp) {
                        MEMBAR();
                        ga = ld_sc1_2(rws, mo); gb = ld_sc1_2(rws, mo + 3840);
                        if (all_valid(umax3(umax3(ga[0], ga[1], gb[0]), gb[1], 0u))) break;
                    }
                    xa = unpk(*(LAS v2u*)(L + L_XS + b4 * 1280 + (64 * ri + dcol) * 2)); gaa = unpk(ga); gab = unpk(gb);
                    asm volatile("" : "+v"(xa), "+v"(gaa), "+v"(gab));
                }
                if (w == 5) {
                    wait_all_flags();
                    for (int sp = 0; sp < SPIN_G; ++sp) {
                        MEMBAR();
                        const v4u e0 = ld_sc1(rws, WS_ML + l * 16), e1 = ld_sc1(rws, WS_ML + 1024 + l * 16);
                        const int cnt = __builtin_popcountll(__builtin_amdgcn_ballot_w64(e0[0] != SENT)) + __builtin_popcountll(__builtin_amdgcn_ballot_w64(e0[2] != SENT))
                                      + __builtin_popcountll(__builtin_amdgcn_ballot_w64(e1[0] != SENT)) + __builtin_popcountll(__builtin_amdgcn_ballot_w64(e1[2] != SENT));
                        if (cnt >= (ROLE == R_SB ? JIT_B : JIT_C)) break;
                        __builtin_amdgcn_s_sleep(8);
                    }
                }
                BAR();
                if (w == 0) STAMP(29);
                issueB(5); issueB(6); issueB(7); issueB(8);
            }
#pragma unroll
            for (int n = 0; n < NT; ++n) {
                const int a = NM + n, younger = (NA - 1 - a) < 3 ? (NA - 1 - a) : 3;
                if (younger == 3) VMW(12); else if (younger == 2) VMW(8); else if (younger == 1) VMW(4); else VMW(0);
                read_unit(L, ta, (a + 1) & 3, ft[n]);
                if (a + 4 < NA) { LGKM0(); issueB(a + 4); }
                if (ROLE == R_SA && n == 1 && w == 0) {
                    wait_all_flags();
                    if (u == 0 && l == 0) __hip_atomic_store((GAS u64*)(p.ws + WS_E), T, RLX_AGENT);
                }
            }
            LGKM0();
            BAR();
            STAMP(6);
        }
        if (ROLE == R_SA) {
            f4 res[2];
            UNDEF4(res[0]); UNDEF4(res[1]);
            u32 oo[2], mo[2];
#pragma unroll
            for (int r = 0; r < 2; ++r) {
                const int row = 2048 * ra + 256 * w + 32 * (4 * r + tq) + 8 * g, hd = row / DH, dd = row - hd * DH;
                oo[r] = WS_OP + (u32)((b4 * 32 + hd) * 2) * 384u + dd * 2; mo[r] = WS_ML + (u32)(hd * 4 + b4) * 16u;
            }
            bool done[2] = {false, false}, wdone[2] = {false, false}, obad[2] = {true, true}, mbad[2] = {true, true};
            v4u a[2][2], ml[2];
            float w0[2], w1[2];
#pragma unroll
            for (int r = 0; r < 2; ++r) { UNDEF4(ml[r]); UNDEF4(w0[r]); UNDEF4(w1[r]); UNDEF4(a[r][0]); UNDEF4(a[r][1]); }
            for (int sp = 0; sp < SPIN_G && !(done[0] && done[1]); ++sp) {
                MEMBAR();
                bool ldo[2];
#pragma unroll
                for (int r = 0; r < 2; ++r) {
                    ldo[r] = obad[r] && (!mbad[r] || sp == 0);
                    if (mbad[r]) ml[r] = ld_sc1(rws, mo[r]);
                    if (ldo[r]) { a[r][0] = ld_sc1(rws, oo[r]); a[r][1] = ld_sc1(rws, oo[r] + 384); }
                }
                const bool last = sp == SPIN_G - 1;
#pragma unroll
                for (int r = 0; r < 2; ++r) {
                    if (!wdone[r]) {
                        mbad[r] = vmax4(0u, ml[r]) == SENT;
                        if (__builtin_amdgcn_ballot_w64(mbad[r]) == 0ull || last) {
                            const float m0 = asf(ml[r][0]), m1 = asf(ml[r][2]), M = fmaxf(m0, m1);
                            const float e0 = asf(ml[r][1]) * __builtin_amdgcn_exp2f((m0 - M) * LOG2E), e1 = asf(ml[r][3]) * __builtin_amdgcn_exp2f((m1 - M) * LOG2E);
                            const float inv = fdiv(1.f, e0 + e1);
                            w0[r] = e0 * inv; w1[r] = e1 * inv; wdone[r] = true;
                        }
                    }
                    if (!done[r]) {
                        obad[r] = obad[r] && (!ldo[r] || vmax4(vmax4(0u, a[r][0]), a[r][1]) == SENT);
                        if ((__builtin_amdgcn_ballot_w64(obad[r]) == 0ull && wdone[r]) || last) {
                            STAMP(20 + w);
                            const f4 x0 = fma4(unpk(v2u{a[r][0][0], a[r][0][1]}), bc4(w0[r]), unpk(v2u{a[r][1][0], a[r][1][1]}) * w1[r]);
                            const f4 x1 = fma4(unpk(v2u{a[r][0][2], a[r][0][3]}), bc4(w0[r]), unpk(v2u{a[r][1][2], a[r][1][3]}) * w1[r]);
                            const v4u bq = {pack2(x0[0], x0[1]), pack2(x0[2], x0[3]), pack2(x1[0], x1[1]), pack2(x1[2], x1[3])};
                            const v4u bk[4] = {bq, ror4k<1>(bq), ror4k<2>(bq), ror4k<3>(bq)};
                            f4 d[4] = {};
#pragma unroll
                            for (int s = 0; s < 4; ++s)
#pragma unroll
                                for (int T2 = 0; T2 < 4; ++T2) d[(T2 + s) & 3] = mfma(ft[4 * r + s][(T2 + s) & 3], bk[T2], d[(T2 + s) & 3]);
                            res[r] = sel_tile(d, tq);
                            done[r] = true;
                        }
                    }
                }
            }
            if (w == 0) STAMP(7);
            *(LAS f4*)(L + L_RED + w * 1024 + l * 16) = res[0] + res[1];
            BAR();
            if (w == 0) {
                STAMP(8);
                LAS f4* R = (LAS f4*)(L + L_RED + l * 16);
                st_sc1(rws, outo, canon4(((R[0] + R[64]) + (R[128] + R[192])) + ((R[256] + R[320]) + (R[384] + R[448]))));
                STAMP(9);
            }
        } else if (ROLE == R_SB) {
            if (w < 5) {
                v4u a[6];
                if (w == 0) STAMP(10);
                const u32 ao = WS_AP + (u32)(g * DM + col) * 4u;
#pragma unroll
                for (int r = 0; r < 6; ++r) UNDEF4(a[r]);
                bool need = true;
                for (int sp = 0; sp < SPIN_G; ++sp) {
                    MEMBAR();
                    if (need) {
                        u32 mx = 0;
#pragma unroll
                        for (int r = 0; r < 3; ++r) { a[2 * r] = ld_sc1(rws, ao + r * 10240); a[2 * r + 1] = ld_sc1(rws, ao + r * 10240 + 256); }
#pragma unroll
                        for (int r = 0; r < 6; ++r) mx = vmax4(mx, a[r]);
                        need = mx == SENT;
                    }
                    if (__builtin_amdgcn_ballot_w64(need) == 0ull) break;
                }
                if (w == 0) STAMP(11);
                f4 ta2 = (asf4(a[0]) + asf4(a[2])) + asf4(a[4]), tb2 = (asf4(a[1]) + asf4(a[3])) + asf4(a[5]);
                rb8(ta2, tb2); ta2 *= gaa; tb2 *= gab; rb8(ta2, tb2); xa += ta2; xb += tb2; rb8(xa, xb);
                const float sh = dppf<0x150>(xa[0] + 0.f);
                const f4 da = xa - sh, db = xb - sh, t = da + db, q2 = fma4(da, da, db * db);
                float s1 = (t[0] + t[1]) + (t[2] + t[3]), s2 = (q2[0] + q2[1]) + (q2[2] + q2[3]);
                s1 += dppf<0x128>(s1); s2 += dppf<0x128>(s2); s1 += dppf<0x124>(s1); s2 += dppf<0x124>(s2);
                s1 += dppf<0x122>(s1); s2 += dppf<0x122>(s2); s1 += dppf<0x121>(s1); s2 += dppf<0x121>(s2);
                const float dm1 = s1 * (1.f / 128.f), mw = sh + dm1, v = fmaf(-dm1, s1, s2);
                *(LAS v2u*)(L + L_ST + 64 + g * 48 + w * 8) = v2u{asu(mw), asu(v)};
            }
            BAR();
            if (w < 5) {
                const f4 s0 = *(LAS f4*)(L + L_ST + 64 + g * 48), s1 = *(LAS f4*)(L + L_ST + 64 + g * 48 + 16);
                const f2 s2 = *(LAS f2*)(L + L_ST + 64 + g * 48 + 32);
                const float mean = (((s0[0] + s0[2]) + (s1[0] + s1[2])) + s2[0]) * 0.2f;
                const f4 dm = f4{s0[0], s0[2], s1[0], s1[2]} - mean, dq = dm * dm; const float d4 = s2[0] - mean;
                const float M2 = fmaf(128.f, fmaf(d4, d4, (dq[0] + dq[1]) + (dq[2] + dq[3])), ((s0[1] + s0[3]) + (s1[1] + s1[3])) + s2[1]);
                const float mb = rbf(mean), den = rbf(__builtin_amdgcn_sqrtf(rbf(rbf(M2 * (1.f / DM)) + 1e-5f))), rc = __builtin_amdgcn_rcpf(den);
                f4 ea = xa - mb, eb = xb - mb;
                rb8(ea, eb);
                const f4 qa = ea * rc, qb = eb * rc;
                f4 ha = fma4(fma4(-qa, bc4(den), ea), bc4(rc), qa), hb = fma4(fma4(-qb, bc4(den), eb), bc4(rc), qb);
                rb8(ha, hb); ha *= c1a; hb *= c1b; rb8(ha, hb); ha += sba; hb += sbb;
                *(LAS v2u*)(L + L_HB + g * 1280 + col * 2) = v2u{pack2(ha[0], ha[1]), pack2(ha[2], ha[3])};
                *(LAS v2u*)(L + L_HB + g * 1280 + col * 2 + 128) = v2u{pack2(hb[0], hb[1]), pack2(hb[2], hb[3])};
            }
            BAR();
            if (w == 0) STAMP(12);
            f4 d[4] = {};
#pragma unroll
            for (int n = 0; n < 5; ++n) {
                const v4u a = *(LAS v4u*)(L + L_HB + b4 * 1280 + (160 * (w >> 1) + 32 * n + 8 * g) * 2);
#pragma unroll
                for (int T2 = 0; T2 < 4; ++T2) d[T2] = mfma(ft[n][T2], a, d[T2]);
            }
            *(LAS f4*)(L + L_RED + w * 1024 + l * 16) = sel_tile(d, tq);
            BAR();
            if (w == 0) {
                STAMP(13);
                LAS f4* R = (LAS f4*)(L + L_RED + l * 16);
                const f4 gt = rb4((R[0] + R[128]) + (R[256] + R[384])), up = rb4((R[64] + R[192]) + (R[320] + R[448]));
                const f4 mk = {gt[0] > 0.f ? 1.f : 0.f, gt[1] > 0.f ? 1.f : 0.f, gt[2] > 0.f ? 1.f : 0.f, gt[3] > 0.f ? 1.f : 0.f};
                const f4 hh = (gt * mk) * up;
                st_sc1_2(rws, outo, v2u{canon(pack2(hh[0], hh[1])), canon(pack2(hh[2], hh[3]))});
                STAMP(14);
            }
        } else {
            f4 x1;
            UNDEF4(x1);
            if (w == 0) {
                const u32 ao = WS_AP + (u32)(b4 * DM + 64 * ri + dcol) * 4u;
                f4 s;
                for (int sp = 0; sp < SPIN_G; ++sp) {
                    MEMBAR();
                    const v4u p0 = ld_sc1(rws, ao), p1 = ld_sc1(rws, ao + 10240), p2 = ld_sc1(rws, ao + 20480);
                    s = (asf4(p0) + asf4(p1)) + asf4(p2);
                    if (all_valid(vmax4(vmax4(vmax4(0u, p0), p1), p2))) break;
                }
                x1 = rb4(xa + rb4(gaa * rb4(s)));
                asm volatile("" : "+v"(x1));
                STAMP(15);
            }
            v4u a0, a1;
            UNDEF4(a0); UNDEF4(a1);
            const u32 h0 = WS_HID + (u32)(b4 * FF + 192 * w + 32 * tq + 8 * g) * 2u, h1 = tq < 2 ? h0 + 256 : OOB;
            bool need = true;
            for (int sp = 0; sp < SPIN_G; ++sp) {
                MEMBAR();
                if (need) { a0 = ld_sc1(rws, h0); a1 = ld_sc1(rws, h1); need = vmax4(vmax4(0u, a0), a1) == SENT; }
                if (__builtin_amdgcn_ballot_w64(need) == 0ull) break;
            }
            if (w == 0) STAMP(16);
            const v4u bk[4] = {a0, ror4k<1>(a0), ror4k<2>(a0), ror4k<3>(a0)}, bl[4] = {a1, ror4k<1>(a1), ror4k<2>(a1), ror4k<3>(a1)};
            f4 d[4] = {};
#pragma unroll
            for (int k = 0; k < 4; ++k)
#pragma unroll
                for (int s = 0; s < 6; ++s) d[(s + k) & 3] = mfma(ft[s][(s + k) & 3], s < 4 ? bk[k] : bl[k], d[(s + k) & 3]);
            *(LAS f4*)(L + L_RED + w * 1024 + l * 16) = sel_tile(d, tq);
            BAR();
            if (w == 0) {
                STAMP(17);
                LAS f4* R = (LAS f4*)(L + L_RED + l * 16);
                const f4 o = x1 + rb4(gab * rb4(((R[0] + R[64]) + (R[128] + R[192])) + ((R[256] + R[320]) + (R[384] + R[448]))));
                __builtin_amdgcn_raw_buffer_store_b64(v2u{pack2(o[0], o[1]), pack2(o[2], o[3])}, __builtin_amdgcn_make_buffer_rsrc((void*)p.out, 0, 4 * DM * 2, 0x00020000),
                                                      (u32)(b4 * DM + 64 * ri + dcol) * 2u, 0, 16);
                STAMP(18);
            }
        }
    }
}

typedef const __attribute__((address_space(4))) Params* kargp;
#define GET_PARAMS() kargp pp_ = kp; asm volatile("" : "+s"(pp_)); \
    const Params q_ = {pp_->x, pp_->kc, pp_->vc, pp_->wq, pp_->wo, pp_->wg, pp_->wu, pp_->wd, pp_->c, pp_->wm, pp_->out, pp_->ws};
__global__ void __launch_bounds__(512, 2) fused_block(Params p) {
    extern __shared__ __attribute__((aligned(16))) unsigned char smem[];
    LAS unsigned char* L = (LAS unsigned char*)smem;
    const kargp kp = (kargp)__builtin_amdgcn_kernarg_segment_ptr();
    asm volatile("s_load_dword s98, %0, 0x0\n\ts_load_dword s99, %0, 0x40" :: "s"(kp) : "s98", "s99");
    const int l = threadIdx.x & 63, w = __builtin_amdgcn_readfirstlane(threadIdx.x >> 6), c = blockIdx.x;
    const int x = c & 7, y = c >> 3;
    if (__builtin_expect(c >= NW, 1)) { GET_PARAMS(); kv_path(q_, L, c - NW, w, l); }
    else if (y >= 5) { GET_PARAMS(); w_path<R_SB>(q_, L, 30 + 8 * (y - 5) + x, w, l); }
    else if (x < 6) { GET_PARAMS(); w_path<R_SA>(q_, L, 10 * (x >> 1) + 5 * (x & 1) + y, w, l); }
    else { GET_PARAMS(); w_path<R_SC>(q_, L, 54 + 5 * (x - 6) + y, w, l); }
}

extern "C" void kernel_launch(void* const* d_in, const int* in_sizes, int n_in, void* d_out, int out_size, void* d_ws, size_t ws_size, hipStream_t stream) {
    Params p;
    p.x = (const u16*)d_in[0]; p.kc = (const u16*)d_in[1]; p.vc = (const u16*)d_in[2]; p.wq = (const u16*)d_in[3]; p.wo = (const u16*)d_in[4];
    p.wg = (const u16*)d_in[6]; p.wu = (const u16*)d_in[7]; p.wd = (const u16*)d_in[8]; p.c = (const u16*)d_in[9]; p.wm = (const u16*)d_in[10];
    p.out = (u16*)d_out; p.ws = (unsigned char*)d_ws;
    (void)hipFuncSetAttribute((const void*)fused_block, hipFuncAttributeMaxDynamicSharedMemorySize, (int)L_END);
    hipLaunchKernelGGL(fused_block, dim3(256), dim3(512), L_END, stream, p);
}
```

```cpp
#include <hip/hip_runtime.h>
#include <stdint.h>
#pragma clang fp contract(off)

typedef unsigned short u16;
typedef unsigned u32;
typedef unsigned long long u64;
typedef u32 v4u __attribute__((ext_vector_type(4)));
typedef u32 v2u __attribute__((ext_vector_type(2)));
typedef float f4 __attribute__((ext_vector_type(4)));
typedef short v4s __attribute__((ext_vector_type(4)));
typedef __bf16 bf8 __attribute__((ext_vector_type(8)));
typedef __bf16 bf2 __attribute__((ext_vector_type(2)));
#define LAS __attribute__((address_space(3)))
#define GAS __attribute__((address_space(1)))
#define RLX_AGENT __ATOMIC_RELAXED, __HIP_MEMORY_SCOPE_AGENT

constexpr int DM = 640, NH = 32, SKV = 4096, DH = 192, WIN = 1024, FF = 1536, NQ = NH * DH;
constexpr u32 SENT = 0xFFFFFFFFu;
constexpr int NW = 64;
constexpr u32 WS_E = 0, WS_FLAG = 4096, WS_F1P = 8192, WS_MODF = WS_F1P + 5 * 4 * 1280 * 4, WS_QP = WS_MODF + 4 * 2560 * 2,
              WS_OP = WS_QP + 5 * 4 * NQ * 4, WS_ML = WS_OP + 128 * 2 * DH * 2, WS_AP = WS_ML + 32 * 4 * 16, WS_HID = WS_AP + 3 * 4 * DM * 4,
              WS_END = WS_HID + 4 * FF * 2;
constexpr u32 OOB = 0x7FFFFF00u;
constexpr u32 L_XS = 131072, L_CS = L_XS + 5120, L_HB = L_CS + 5120, L_RED = L_HB + 5120, L_ST = L_RED + 8192, L_GA = L_ST + 256, L_GB = L_GA + 1024,
              L_X1 = L_GB + 1024, L_MISC = L_X1 + 1024, L_STAMP = L_MISC + 64, L_END = L_STAMP + 256;
constexpr u32 K_W = 18432, K_PX = 1024, K_PX2 = 1536, K_V = 9216, K_QS = 8 * K_W, K_ML = K_QS + 768, K_END = K_ML + 64;
static_assert(K_END <= L_MISC, "LDS");
constexpr float LOG2E = 1.4426950408889634f;
static_assert(L_END <= 163840, "LDS");
constexpr int SPIN_G = 40000, SPIN_L = 2000000;
constexpr int VSPL = 4;
constexpr int PRE_B = 2, PRE_C = 0, JIT_B = 24, JIT_C = 200;

#define STAMP(i) do {} while (0)

struct Params { const u16 *x, *kc, *vc, *wq, *wo, *wg, *wu, *wd, *c, *wm; u16* out; unsigned char* ws; };

__device__ __forceinline__ v4u mk_srd(const void* p, u32 bytes) { const u64 a = (u64)p; return v4u{(u32)a, (u32)(a >> 32) & 0xffffu, bytes, 0x00020000u}; }
__device__ __forceinline__ float asf(u32 u) { return __builtin_bit_cast(float, u); }
__device__ __forceinline__ u32 asu(float f) { return __builtin_bit_cast(u32, f); }
__device__ __forceinline__ float bflo(u32 u) { return asf(u << 16); }
__device__ __forceinline__ float bfhi(u32 u) { return asf(u & 0xffff0000u); }
__device__ __forceinline__ u32 pack2(float lo, float hi) { bf2 v; v[0] = (__bf16)lo; v[1] = (__bf16)hi; return __builtin_bit_cast(u32, v); }
__device__ __forceinline__ float rbf(float f) { return (float)(__bf16)f; }
typedef float f2 __attribute__((ext_vector_type(2)));
__device__ __forceinline__ f4 rb4(f4 v) {
    float a = v[0], b = v[1], c = v[2], d = v[3];
    asm("v_cvt_pk_bf16_f32 %0, 0, %0\n\tv_cvt_pk_bf16_f32 %1, 0, %1\n\tv_cvt_pk_bf16_f32 %2, 0, %2\n\tv_cvt_pk_bf16_f32 %3, 0, %3" : "+v"(a), "+v"(b), "+v"(c), "+v"(d));
    return f4{a, b, c, d}; }
__device__ __forceinline__ void rb8(f4& u, f4& v) {
    float a = u[0], b = u[1], c = u[2], d = u[3], e = v[0], f = v[1], g = v[2], h = v[3];
    asm("v_cvt_pk_bf16_f32 %0, 0, %0\n\tv_cvt_pk_bf16_f32 %1, 0, %1\n\tv_cvt_pk_bf16_f32 %2, 0, %2\n\tv_cvt_pk_bf16_f32 %3, 0, %3\n\t"
        "v_cvt_pk_bf16_f32 %4, 0, %4\n\tv_cvt_pk_bf16_f32 %5, 0, %5\n\tv_cvt_pk_bf16_f32 %6, 0, %6\n\tv_cvt_pk_bf16_f32 %7, 0, %7"
        : "+v"(a), "+v"(b), "+v"(c), "+v"(d), "+v"(e), "+v"(f), "+v"(g), "+v"(h));
    u = f4{a, b, c, d}; v = f4{e, f, g, h}; }
__device__ __forceinline__ f4 unpk(v2u v) { return f4{bflo(v[0]), bfhi(v[0]), bflo(v[1]), bfhi(v[1])}; }
__device__ __forceinline__ f4 fma4(f4 a, f4 b, f4 c) { return __builtin_elementwise_fma(a, b, c); }
__device__ __forceinline__ f4 bc4(float x) { return f4{x, x, x, x}; }
__device__ __forceinline__ u32 canon(u32 u) { return u < 0xFFFFFFFEu ? u : 0xFFFFFFFEu; }
__device__ __forceinline__ v4u canon4(f4 v) { return v4u{canon(asu(v[0])), canon(asu(v[1])), canon(asu(v[2])), canon(asu(v[3]))}; }
__device__ __forceinline__ u32 umax3(u32 a, u32 b, u32 c) { const u32 m = a > b ? a : b; return m > c ? m : c; }
__device__ __forceinline__ u32 vmax4(u32 m, v4u v) { return umax3(umax3(m, v[0], v[1]), v[2], v[3]); }
__device__ __forceinline__ bool all_valid(u32 m) { return __builtin_amdgcn_ballot_w64(m == SENT) == 0ull; }
__device__ __forceinline__ float dot2(u32 a, u32 b, float c) { return __builtin_amdgcn_fdot2_f32_bf16(__builtin_bit_cast(bf2, a), __builtin_bit_cast(bf2, b), c, false); }
__device__ __forceinline__ f4 mfma(v4u a, v4u b, f4 c) { return __builtin_amdgcn_mfma_f32_16x16x32_bf16(__builtin_bit_cast(bf8, a), __builtin_bit_cast(bf8, b), c, 0, 0, 0); }
template <int CTRL> __device__ __forceinline__ u32 dppu(u32 x) { return (u32)__builtin_amdgcn_mov_dpp((int)x, CTRL, 0xf, 0xf, true); }
template <int CTRL> __device__ __forceinline__ float dppf(float x) { return asf(dppu<CTRL>(asu(x))); }
template <int K> __device__ __forceinline__ v4u ror4k(v4u v) {
    if constexpr (K == 0) return v; else return v4u{dppu<0x120 + 4 * K>(v[0]), dppu<0x120 + 4 * K>(v[1]), dppu<0x120 + 4 * K>(v[2]), dppu<0x120 + 4 * K>(v[3])}; }
__device__ __forceinline__ float row_sum(float x) { x += dppf<0x128>(x); x += dppf<0x124>(x); x += dppf<0x122>(x); x += dppf<0x121>(x); return x; }
__device__ __forceinline__ float row_max(float x) { x = fmaxf(x, dppf<0x128>(x)); x = fmaxf(x, dppf<0x124>(x)); x = fmaxf(x, dppf<0x122>(x)); x = fmaxf(x, dppf<0x121>(x)); return x; }
__device__ __forceinline__ float xrow_sum(float x) {
    auto s = __builtin_amdgcn_permlane16_swap(asu(x), asu(x), false, false); x = asf(s[0]) + asf(s[1]);
    auto t = __builtin_amdgcn_permlane32_swap(asu(x), asu(x), false, false); return asf(t[0]) + asf(t[1]); }
__device__ __forceinline__ float xrow_max(float x) {
    auto s = __builtin_amdgcn_permlane16_swap(asu(x), asu(x), false, false); x = fmaxf(asf(s[0]), asf(s[1]));
    auto t = __builtin_amdgcn_permlane32_swap(asu(x), asu(x), false, false); return fmaxf(asf(t[0]), asf(t[1])); }
__device__ __forceinline__ float wave_sum(float x) { return xrow_sum(row_sum(x)); }
__device__ __forceinline__ float wave_max(float x) { return xrow_max(row_max(x)); }
__device__ __forceinline__ float fdiv(float a, float b) { const float r = __builtin_amdgcn_rcpf(b); const float q = a * r; return fmaf(fmaf(-q, b, a), r, q); }
__device__ __forceinline__ float silu_bf(float v) { return fdiv(v, 1.f + __builtin_amdgcn_exp2f(fminf(-v * 1.4426950408889634f, 126.f))); }
__device__ __forceinline__ v4u silu8(v4u c) {
    v4u r;
#pragma unroll
    for (int i = 0; i < 4; ++i) r[i] = pack2(silu_bf(bflo(c[i])), silu_bf(bfhi(c[i])));
    return r; }
#define BAR() asm volatile("s_waitcnt lgkmcnt(0)\n\ts_barrier" ::: "memory")
#define LGKM0() asm volatile("s_waitcnt lgkmcnt(0)" ::: "memory")
#define VMW(n) asm volatile("s_waitcnt vmcnt(%0)" :: "i"(n) : "memory")
#define MEMBAR() asm volatile("" ::: "memory")
typedef __amdgpu_buffer_rsrc_t rsrc_t;
__device__ __forceinline__ v4u ld_sc1(rsrc_t r, u32 off) { return __builtin_amdgcn_raw_buffer_load_b128(r, off, 0, 16); }
__device__ __forceinline__ v2u ld_sc1_2(rsrc_t r, u32 off) { return __builtin_amdgcn_raw_buffer_load_b64(r, off, 0, 16); }
__device__ __forceinline__ void st_sc1(rsrc_t r, u32 off, v4u v) { __builtin_amdgcn_raw_buffer_store_b128(v, r, off, 0, 16); }
__device__ __forceinline__ void st_sc1_2(rsrc_t r, u32 off, v2u v) { __builtin_amdgcn_raw_buffer_store_b64(v, r, off, 0, 16); }
__device__ __forceinline__ f4 asf4(v4u v) { return __builtin_bit_cast(f4, v); }

__device__ __forceinline__ void dma_unit(v4u srd, u32 v0, u32 v1, u32 sA, u32 sB, u32 lds) {
    u32 keep;
    asm volatile("s_nop 4\n\ts_mov_b32 %0, m0\n\ts_mov_b32 m0, %6\n\ts_nop 0\n\t"
                 "buffer_load_dwordx4 %1, %3, %4 offen nt lds\n\ts_add_u32 m0, m0, 0x400\n\ts_nop 0\n\t"
                 "buffer_load_dwordx4 %2, %3, %4 offen nt lds\n\ts_add_u32 m0, m0, 0x400\n\ts_nop 0\n\t"
                 "buffer_load_dwordx4 %1, %3, %5 offen nt lds\n\ts_add_u32 m0, m0, 0x400\n\ts_nop 0\n\t"
                 "buffer_load_dwordx4 %2, %3, %5 offen nt lds\n\ts_mov_b32 m0, %0"
                 : "=&s"(keep) : "v"(v0), "v"(v1), "s"(srd), "s"(sA), "s"(sB), "s"(lds) : "memory", "scc");
}
__device__ __forceinline__ void dma_1k(v4u srd, u32 voff, u32 soff, u32 lds) {
    u32 keep;
    asm volatile("s_nop 4\n\ts_mov_b32 %0, m0\n\ts_mov_b32 m0, %4\n\ts_nop 0\n\tbuffer_load_dwordx4 %1, %2, %3 offen lds\n\ts_mov_b32 m0, %0"
                 : "=&s"(keep) : "v"(voff), "s"(srd), "s"(soff), "s"(lds) : "memory");
}
struct WStream { v4u srd; u32 base, pitch, v0, v1; };
__device__ __forceinline__ WStream mk_stream(v4u srd, u32 base, u32 pitch, int l) {
    WStream s; s.srd = srd; s.base = base; s.pitch = pitch;
    const u32 sw = 2u * ((l >> 4) & 1);
    s.v0 = (l >> 3) * pitch + (((l & 7) ^ sw) << 4);
    s.v1 = (8 + (l >> 3)) * pitch + (((l & 7) ^ (sw | 4u)) << 4);
    return s; }
__device__ __forceinline__ void issue(const WStream& s, int n, u32 lds) { const u32 a = s.base + (u32)n * 32u * s.pitch; dma_unit(s.srd, s.v0, s.v1, a, a + 16u * s.pitch, lds); }
__device__ __forceinline__ void read_unit(LAS unsigned char* L, const u32 (&ta)[4], int slot, v4u (&f)[4]) {
#pragma unroll
    for (int T = 0; T < 4; ++T) {
        const v2u lo = __builtin_bit_cast(v2u, __builtin_amdgcn_ds_read_tr16_b64_v4i16((LAS v4s*)(L + ta[T] + slot * 4096)));
        const v2u hi = __builtin_bit_cast(v2u, __builtin_amdgcn_ds_read_tr16_b64_v4i16((LAS v4s*)(L + ta[T] + slot * 4096 + 512)));
        f[T] = v4u{lo[0], lo[1], hi[0], hi[1]};
    } }
__device__ __forceinline__ f4 sel_tile(const f4 (&a)[4], int t) {
    const bool b0 = t & 1, b1 = t & 2;
    f4 r;
#pragma unroll
    for (int i = 0; i < 4; ++i) { const float lo = b0 ? a[1][i] : a[0][i], hi = b0 ? a[3][i] : a[2][i]; r[i] = b1 ? hi : lo; }
    return r; }

__device__ __forceinline__ void dma_kv(v4u srd, u32 vo, u32 so, u32 lds) {
    u32 keep;
    asm volatile("s_nop 4\n\ts_mov_b32 %0, m0\n\ts_mov_b32 m0, %4\n\ts_nop 0\n\t"
                 "buffer_load_dwordx4 %1, %2, %3 offen nt lds\n\ts_add_u32 m0, m0, 0x380\n\ts_nop 0\n\t"
                 "buffer_load_dwordx4 %1, %2, %3 offen offset:128 nt lds\n\ts_add_u32 m0, m0, 0x380\n\ts_nop 0\n\t"
                 "buffer_load_dwordx4 %1, %2, %3 offen offset:256 nt lds\n\ts_mov_b32 m0, %0"
                 : "=&s"(keep) : "v"(vo), "s"(srd), "s"(so), "s"(lds) : "memory", "scc");
}
__device__ __forceinline__ float qk_unit(v4u k0, v4u k1, v4u k2, const v4u (&q)[3]) {
    float s = 0.f, t = 0.f;
#pragma unroll
    for (int r = 0; r < 4; r += 2) { s = dot2(k0[r], q[0][r], s); t = dot2(k0[r + 1], q[0][r + 1], t); }
#pragma unroll
    for (int r = 0; r < 4; r += 2) { s = dot2(k1[r], q[1][r], s); t = dot2(k1[r + 1], q[1][r + 1], t); }
#pragma unroll
    for (int r = 0; r < 4; r += 2) { s = dot2(k2[r], q[2][r], s); t = dot2(k2[r + 1], q[2][r + 1], t); }
    return s + t;
}
__device__ __forceinline__ void pv_unit(v4u v0, v4u v1, v4u v2, u32 plo, float (&acc)[24]) {
    const u32 phi = plo << 16;
#pragma unroll
    for (int r = 0; r < 4; ++r) {
        acc[2 * r] = dot2(v0[r], plo, acc[2 * r]); acc[2 * r + 1] = dot2(v0[r], phi, acc[2 * r + 1]);
        acc[8 + 2 * r] = dot2(v1[r], plo, acc[8 + 2 * r]); acc[8 + 2 * r + 1] = dot2(v1[r], phi, acc[8 + 2 * r + 1]);
        acc[16 + 2 * r] = dot2(v2[r], plo, acc[16 + 2 * r]); acc[16 + 2 * r + 1] = dot2(v2[r], phi, acc[16 + 2 * r + 1]);
    }
    asm volatile("" : "+v"(acc[0]), "+v"(acc[1]), "+v"(acc[2]), "+v"(acc[3]), "+v"(acc[4]), "+v"(acc[5]), "+v"(acc[6]), "+v"(acc[7]), "+v"(acc[8]), "+v"(acc[9]), "+v"(acc[10]), "+v"(acc[11]),
                      "+v"(acc[12]), "+v"(acc[13]), "+v"(acc[14]), "+v"(acc[15]), "+v"(acc[16]), "+v"(acc[17]), "+v"(acc[18]), "+v"(acc[19]), "+v"(acc[20]), "+v"(acc[21]), "+v"(acc[22]), "+v"(acc[23]));
}

template <bool B> struct BoolC { static constexpr bool value = B; };
__device__ __forceinline__ void kv_path(const Params& p, LAS unsigned char* L, int j, int w, int l) {
    const rsrc_t rws = __builtin_amdgcn_make_buffer_rsrc((void*)p.ws, 0, WS_END, 0x00020000);
    const rsrc_t rk = __builtin_amdgcn_make_buffer_rsrc((void*)p.kc, 0, 4 * NH * SKV * DH * 2, 0x00020000);
    const rsrc_t rv = __builtin_amdgcn_make_buffer_rsrc((void*)p.vc, 0, 4 * NH * SKV * DH * 2, 0x00020000);
    volatile LAS u32* misc = (volatile LAS u32*)(L + L_MISC);
    const int rk3 = (j >> 3) / 3, t3 = (j & 7) * 8 + rk3, pos = (j >> 3) - 3 * rk3;
    const int slot = pos == 1 ? (w & 1) : 0;
    const int bh = 2 * t3 + (pos == 2 ? 1 : (pos == 1 ? slot : 0));
    const int part = pos == 0 ? 0 : (pos == 2 ? 1 : (slot == 0 ? 1 : 0));
    const int wi = pos == 1 ? (w >> 1) : w;
    const int ub = pos == 0 ? 0 : (pos == 2 ? 43 : (slot == 0 ? 85 : 0));
    const int ng = pos == 1 ? 2 : 4, u1 = ub + (wi >= ng ? 11 * ng + wi - ng : wi);
    const u32 ust = ng * 3072;
    const int nl = (pos == 1 ? wi == 3 : wi >= 5) ? 2 : 3;
    const bool lead = w == 0 || (w == 1 && pos == 1);
    const u32 opb = WS_OP + (bh * 2 + part) * 384, mlb = WS_ML + ((bh & 31) * 4 + (bh >> 5)) * 16 + part * 8;
    const u32 vo = (l >> 3) * 384 + (l & 7) * 16;
    const u32 base = ((u32)bh * SKV + (SKV - WIN)) * 384u + (u32)u1 * 3072u;
    const u32 wl = w * K_W, lb = (u32)(size_t)L + wl;
    const v4u sk = mk_srd(p.kc, 4 * NH * SKV * DH * 2), sv = mk_srd(p.vc, 4 * NH * SKV * DH * 2);
    STAMP(0);
    if (w != 4) { dma_kv(sk, vo, base + 8 * ust, lb); dma_kv(sv, vo, base + 8 * ust, lb + K_V); }
    if (threadIdx.x < 12) misc[threadIdx.x] = 0;
    BAR();
    if (w == 4) {
        __builtin_amdgcn_s_setprio(3);
        const u64 T = __hip_atomic_load((GAS u64*)(p.ws + WS_E), RLX_AGENT) + 1ull;
        st_sc1(rws, l < 24 ? opb + l * 16 : OOB, v4u{SENT, SENT, SENT, SENT});
        st_sc1_2(rws, l == 0 ? mlb : OOB, v2u{SENT, SENT});
        const int bh1 = 2 * t3 + 1;
        st_sc1(rws, (pos == 1 && l < 24) ? WS_OP + bh1 * 2 * 384 + l * 16 : OOB, v4u{SENT, SENT, SENT, SENT});
        st_sc1_2(rws, (pos == 1 && l == 0) ? WS_ML + ((bh1 & 31) * 4 + (bh1 >> 5)) * 16 : OOB, v2u{SENT, SENT});
        VMW(0);
        if (l == 0) {
            __hip_atomic_store((GAS u64*)(p.ws + WS_FLAG) + blockIdx.x, T, RLX_AGENT);
            misc[4] = (u32)T; misc[5] = (u32)(T >> 32); LGKM0(); misc[2] = 1;
        }
        STAMP(9);
        __builtin_amdgcn_s_setprio(0);
        dma_kv(sk, vo, base + 8 * ust, lb); dma_kv(sv, vo, base + 8 * ust, lb + K_V);
    }
    const int c = l & 7;
    float m, lsum, fsc;
    const u32 nslot = pos == 1 ? 4u : 8u;
    auto slot_stats = [&](float& M, float& mi, float& li) {
        const v2u t = *(LAS v2u*)(L + K_ML + c * 8);
        const bool mine = pos != 1 || (l & 1) == slot;
        mi = mine ? asf(t[0]) : -INFINITY; li = mine ? asf(t[1]) : 0.f;
        M = fmaxf(mi, dppf<0xB1>(mi)); M = fmaxf(M, dppf<0x4E>(M)); M = fmaxf(M, dppf<0x141>(M));
    };
    auto post = [&]() {
        u32 old = 0;
        if (l == 0) { *(LAS v2u*)(L + K_ML + w * 8) = v2u{asu(m), asu(lsum)}; LGKM0(); old = __hip_atomic_fetch_add((LAS u32*)(L + L_MISC + 32 + slot * 4), 1u, __ATOMIC_RELAXED, __HIP_MEMORY_SCOPE_WORKGROUP); }
        if (__builtin_amdgcn_readfirstlane(old) == nslot - 1u) {
            MEMBAR();
            float M, mi, li; slot_stats(M, mi, li);
            float e = li * __builtin_amdgcn_exp2f((mi - M) * LOG2E);
            e += dppf<0xB1>(e); e += dppf<0x4E>(e); e += dppf<0x141>(e);
            st_sc1_2(rws, l == 0 ? mlb : OOB, v2u{canon(asu(M)), canon(asu(e))});
            STAMP(5);
        }
    };
    auto scale = [&]() {
        for (int sp = 0; misc[8 + slot] < nslot && sp < SPIN_L; ++sp) __builtin_amdgcn_s_sleep(1);
        MEMBAR();
        float M, mi, li; slot_stats(M, mi, li);
        float e = li * __builtin_amdgcn_exp2f((mi - M) * LOG2E);
        e += dppf<0xB1>(e); e += dppf<0x4E>(e); e += dppf<0x141>(e);
        fsc = fdiv(__builtin_amdgcn_exp2f((m - M) * LOG2E), e);
    };
    dma_kv(sk, vo, base + 9 * ust, lb + 3072); dma_kv(sv, vo, base + 9 * ust, lb + K_V + 3072);
    if (nl == 3) { dma_kv(sk, vo, base + 10 * ust, lb + 6144); dma_kv(sv, vo, base + 10 * ust, lb + K_V + 6144); }
    v4u K[24], V[24];
#pragma unroll
    for (int jj = 0; jj < 8; ++jj)
#pragma unroll
        for (int i = 0; i < 3; ++i) K[jj * 3 + i] = __builtin_amdgcn_raw_buffer_load_b128(rk, vo + i * 128, base + jj * ust, 2);
#pragma unroll
    for (int jj = 0; jj < VSPL; ++jj)
#pragma unroll
        for (int i = 0; i < 3; ++i) V[jj * 3 + i] = __builtin_amdgcn_raw_buffer_load_b128(rv, vo + i * 128, base + jj * ust, 2);
    __builtin_amdgcn_sched_barrier(0);
    const bool early = __builtin_amdgcn_readfirstlane(misc[slot]) != 0u;
    float acc[24];
    auto attend = [&](auto ec) {
        constexpr bool EARLY = decltype(ec)::value;
        if (!EARLY) {
#pragma unroll
            for (int jj = VSPL; jj < 8; ++jj)
#pragma unroll
                for (int i = 0; i < 3; ++i) V[jj * 3 + i] = __builtin_amdgcn_raw_buffer_load_b128(rv, vo + i * 128, base + jj * ust, 2);
            __builtin_amdgcn_sched_barrier(0);
            if (w == 0) STAMP(1);
            if (w == 7) STAMP(2);
            if (w < 4) {
                for (int sp = 0; misc[2] == 0 && sp < SPIN_L; ++sp) __builtin_amdgcn_s_sleep(1);
                const u32 tlo = misc[4], thi = misc[5];
                for (int sp = 0; sp < SPIN_G && misc[6] == 0; ++sp) {
                    MEMBAR();
                    const v2u f = ld_sc1_2(rws, WS_FLAG + l * 8);
                    if (__builtin_amdgcn_ballot_w64(f[0] != tlo || f[1] != thi) == 0ull) misc[6] = 1;
                }
                const u32 qo = WS_QP + (u32)((bh >> 5) * NQ + (bh & 31) * DH + 4 * l) * 4u;
                v4u a[5] = {};
                bool need = l < 48;
                for (int sp = 0; sp < SPIN_G; ++sp) {
                    MEMBAR();
                    if (__builtin_amdgcn_readfirstlane(misc[slot]) != 0u) break;
                    if (need) {
                        u32 mx = 0;
#pragma unroll
                        for (int ks = 0; ks < 5; ++ks) a[ks] = ld_sc1(rws, qo + ks * (4 * NQ * 4));
#pragma unroll
                        for (int ks = 0; ks < 5; ++ks) mx = vmax4(mx, a[ks]);
                        need = mx == SENT;
                    }
                    if (__builtin_amdgcn_ballot_w64(need) == 0ull || sp == SPIN_G - 1) {
                        const f4 qs = (((asf4(a[0]) + asf4(a[1])) + asf4(a[2])) + asf4(a[3])) + asf4(a[4]);
                        if (l < 48) *(LAS v2u*)(L + K_QS + slot * 384 + l * 8) = v2u{pack2(qs[0], qs[1]), pack2(qs[2], qs[3])};
                        LGKM0();
                        misc[slot] = 1;
                        break;
                    }
                }
                if (w == 0) STAMP(3);
            } else {
                for (int sp = 0; misc[slot] == 0 && sp < SPIN_L; ++sp) __builtin_amdgcn_s_sleep(1);
            }
        }
        VMW(EARLY ? 24 + 3 * VSPL : 48);
        v4u p0, p1, p2;
        {
            v4u q[3];
#pragma unroll
            for (int i = 0; i < 3; ++i) q[i] = *(LAS v4u*)(L + K_QS + slot * 384 + (c + 8 * i) * 16);
            float y[4], x[8];
            LAS v4u* KL = (LAS v4u*)(L + wl + l * 16);
#pragma unroll
            for (int jj = 0; jj < 3; ++jj) { y[jj] = qk_unit(KL[jj * 192], KL[jj * 192 + 64], KL[jj * 192 + 128], q); __builtin_amdgcn_sched_barrier(0); }
#pragma unroll
            for (int jj = 0; jj < 8; jj += 2) { x[jj] = qk_unit(K[3 * jj], K[3 * jj + 1], K[3 * jj + 2], q); x[jj + 1] = qk_unit(K[3 * jj + 3], K[3 * jj + 4], K[3 * jj + 5], q); __builtin_amdgcn_sched_barrier(0); }
#pragma unroll
            for (int h = 0; h < 4; ++h) { const float a = x[h] + dppf<0x141>(x[h]), b = x[h + 4] + dppf<0x141>(x[h + 4]); x[h] = (c & 4) ? b : a; }
            { const float a = y[0] + dppf<0x141>(y[0]), b = y[2] + dppf<0x141>(y[2]), a1 = y[1] + dppf<0x141>(y[1]); y[0] = (c & 4) ? b : a; y[1] = a1; }
#pragma unroll
            for (int h = 0; h < 2; ++h) { const float a = x[h] + dppf<0x4E>(x[h]), b = x[h + 2] + dppf<0x4E>(x[h + 2]); x[h] = (c & 2) ? b : a; }
            { const float a = y[0] + dppf<0x4E>(y[0]), b = y[1] + dppf<0x4E>(y[1]); y[0] = (c & 2) ? b : a; }
            { const float a = x[0] + dppf<0xB1>(x[0]), b = x[1] + dppf<0xB1>(x[1]); x[0] = (c & 1) ? b : a; }
            y[0] += dppf<0xB1>(y[0]);
            const float s1 = rbf(rbf(x[0]) * 0.07216878364870322f);
            const float s2 = (c >> 1) < nl ? rbf(rbf(y[0]) * 0.07216878364870322f) : -INFINITY;
            m = wave_max(fmaxf(s1, s2));
            const float e1 = __builtin_amdgcn_exp2f((s1 - m) * LOG2E), e2 = __builtin_amdgcn_exp2f((s2 - m) * LOG2E);
            *(LAS u32*)(L + wl + K_PX + l * 4) = pack2(e1, 0.f);
            *(LAS u32*)(L + wl + K_PX2 + (l >> 1) * 4) = pack2(e2, 0.f);
            lsum = wave_sum(e1 + ((c & 1) ? 0.f : e2));
            post();
            LGKM0();
            p0 = *(LAS v4u*)(L + wl + K_PX + (l >> 3) * 32); p1 = *(LAS v4u*)(L + wl + K_PX + (l >> 3) * 32 + 16); p2 = *(LAS v4u*)(L + wl + K_PX2 + (l >> 3) * 16);
        }
        if (EARLY) {
            __builtin_amdgcn_sched_barrier(0);
#pragma unroll
            for (int jj = VSPL; jj < 8; ++jj)
#pragma unroll
                for (int i = 0; i < 3; ++i) V[jj * 3 + i] = __builtin_amdgcn_raw_buffer_load_b128(rv, vo + i * 128, base + jj * ust, 2);
            __builtin_amdgcn_sched_barrier(0);
        }
        if (w == 0) STAMP(4);
#pragma unroll
        for (int i = 0; i < 24; ++i) acc[i] = 0.f;
        {
            LAS v4u* VL = (LAS v4u*)(L + wl + K_V + l * 16);
            pv_unit(VL[0], VL[64], VL[128], p2[0], acc);
            pv_unit(VL[192], VL[256], VL[320], p2[1], acc);
            if (nl == 3) pv_unit(VL[384], VL[448], VL[512], p2[2], acc);
        }
#pragma unroll
        for (int jj = 0; jj < 6; ++jj) pv_unit(V[3 * jj], V[3 * jj + 1], V[3 * jj + 2], jj < 4 ? p0[jj & 3] : p1[jj & 3], acc);
        scale();
#pragma unroll
        for (int jj = 6; jj < 8; ++jj) pv_unit(V[3 * jj], V[3 * jj + 1], V[3 * jj + 2], p1[jj & 3], acc);
    };
    if (early) attend(BoolC<true>{}); else attend(BoolC<false>{});
    if (w == 7) STAMP(6);
    float y[12], z[6];
#pragma unroll
    for (int n = 0; n < 12; ++n) { auto s = __builtin_amdgcn_permlane32_swap(asu(acc[n]), asu(acc[n + 12]), false, false); y[n] = asf(s[0]) + asf(s[1]); }
#pragma unroll
    for (int n = 0; n < 6; ++n) { auto s = __builtin_amdgcn_permlane16_swap(asu(y[n]), asu(y[n + 6]), false, false); z[n] = asf(s[0]) + asf(s[1]); }
#pragma unroll
    for (int n = 0; n < 3; ++n) {
        const float a = z[n] + dppf<0x128>(z[n]), b = z[n + 3] + dppf<0x128>(z[n + 3]);
        const int ai = 3 * (l >> 3) + n;
        *(LAS float*)(L + wl + (64 * (ai >> 3) + 8 * c + (ai & 7)) * 4) = ((l & 8) ? b : a) * fsc;
    }
    BAR();
    if (w == 0) STAMP(7);
    if (lead) {
        const u32 pb = slot * K_W + (l < 48 ? l : 47) * 16;
        f4 o = (*(LAS f4*)(L + pb) + *(LAS f4*)(L + pb + 2 * K_W)) + (*(LAS f4*)(L + pb + 4 * K_W) + *(LAS f4*)(L + pb + 6 * K_W));
        if (pos != 1) o += (*(LAS f4*)(L + pb + K_W) + *(LAS f4*)(L + pb + 3 * K_W)) + (*(LAS f4*)(L + pb + 5 * K_W) + *(LAS f4*)(L + pb + 7 * K_W));
        st_sc1_2(rws, l < 48 ? opb + l * 8 : OOB, v2u{canon(pack2(o[0], o[1])), canon(pack2(o[2], o[3]))});
        if (w == 0) STAMP(8); else STAMP(10);
    }
}

enum { R_SA = 0, R_SB = 1, R_SC = 2 };
#define UNDEF4(v) asm volatile("" : "=v"(v))
template <int ROLE>
__device__ __forceinline__ void w_path(const Params& p, LAS unsigned char* L, int u, int w, int l) {
    const rsrc_t rws = __builtin_amdgcn_make_buffer_rsrc((void*)p.ws, 0, WS_END, 0x00020000);
    const int tid = threadIdx.x, g = l >> 4, tq = (l & 15) >> 2, b4 = l & 3;
    const u32 lbase = (u32)(size_t)L, stg = lbase + w * 16384;
    const bool hasF1 = u < 50, hasF2 = u < 60;
    const int kf = u / 10, lp = u - 10 * kf, kf2 = u / 12, cs = u - 12 * kf2;
    const int ri = ROLE == R_SA ? u : (ROLE == R_SB ? u - 30 : u - 54);
    const WStream sF1 = mk_stream(mk_srd(p.wm, hasF1 ? DM * 3840 * 2 : 0), (u32)((128 * kf + 32 * (w >> 1)) * 7680 + (128 * lp + 64 * (w & 1)) * 2), 7680, l);
    const WStream sF2 = mk_stream(mk_srd(p.wq, hasF2 ? DM * NQ * 2 : 0), (u32)(128 * kf2 * 12288 + (512 * cs + 64 * w) * 2), 12288, l);
    const int mid = ROLE == R_SC ? (w < 4 ? (ri >= 6 ? ri : -1) : 30 + ri) : (w < 4 ? (ri < 6 ? ri : ri + 4) : (ri == 20 ? 28 : (ri == 21 ? 29 : -1)));
    const bool hasM = ROLE != R_SA && mid >= 0;
    const WStream sM = mk_stream(mk_srd(p.wm, hasM ? DM * 3840 * 2 : 0), (u32)(160 * (w & 3) * 7680 + (1280 + 64 * mid) * 2), 7680, l);
    const int ra = ri / 10, ca = ri - 10 * ra;
    const WStream sT = ROLE == R_SA ? mk_stream(mk_srd(p.wo, NQ * DM * 2), (u32)((2048 * ra + 256 * w) * 1280 + ca * 128), 1280, l)
                     : ROLE == R_SB ? mk_stream(mk_srd((w & 1) ? p.wu : p.wg, DM * FF * 2), (u32)(160 * (w >> 1) * 3072 + ri * 128), 3072, l)
                                    : mk_stream(mk_srd(p.wd, FF * DM * 2), (u32)(192 * w * 1280 + ri * 128), 1280, l);
    constexpr int NM = ROLE == R_SA ? 0 : 5, NT = ROLE == R_SA ? 8 : (ROLE == R_SB ? 5 : 6), NA = NM + NT;
    u32 ta[4];
    { const u32 f = 2u * (tq >> 1) | 4u * (g & 1);
#pragma unroll
      for (int T = 0; T < 4; ++T) ta[T] = w * 16384 + (8 * g + tq) * 128 + ((((u32)(2 * T)) ^ f) + (b4 >> 1)) * 16 + (b4 & 1) * 8; }
    const u32 dcol = 16 * tq + 4 * g;
    const u32 f1o = hasF1 ? WS_F1P + (u32)((kf * 4 + b4) * 1280 + 128 * lp + 64 * w + dcol) * 4u : OOB;
    const u32 qpo = hasF2 ? WS_QP + (u32)((kf2 * 4 + b4) * NQ + 512 * cs + 64 * w + dcol) * 4u : OOB;
    const u32 mfo = hasM ? WS_MODF + (u32)(b4 * 2560 + 64 * mid + dcol) * 2u : OOB;
    const u32 outo = ROLE == R_SA ? WS_AP + (u32)((ra * 4 + b4) * DM + 64 * ca + dcol) * 4u : WS_HID + (u32)(b4 * FF + 64 * ri + dcol) * 2u;
    volatile LAS u32* misc = (volatile LAS u32*)(L + L_MISC);
    STAMP(0);
    if (w < 5) { dma_1k(mk_srd(p.c, 5120), l * 16, w * 1024, lbase + L_CS + w * 1024); dma_1k(mk_srd(p.x, 5120), l * 16, w * 1024, lbase + L_XS + w * 1024); }
    issue(sF1, 0, stg);
    { u32 keep;
      asm volatile("s_nop 4\n\ts_mov_b32 %0, m0\n\ts_mov_b32 m0, %5\n\ts_nop 0\n\tbuffer_load_dword %1, %2, %3 offen nt lds\n\tbuffer_load_dword %1, %4, 0 offen nt lds\n\ts_mov_b32 m0, %0"
                   : "=&s"(keep) : "v"(l == 0 ? 0u : OOB), "s"(sT.srd), "s"(sT.base), "s"(mk_srd(p.out, 5120)), "s"(lbase + L_GB) : "memory"); }
    if (w == 0) {
        u32 keep;
        asm volatile("s_nop 4\n\ts_mov_b32 %0, m0\n\ts_mov_b32 m0, %3\n\ts_nop 0\n\tbuffer_load_dword %1, %2, 0 offen sc1 lds\n\ts_mov_b32 m0, %0"
                     : "=&s"(keep) : "v"(l < 2 ? WS_E + l * 4 : OOB), "s"(mk_srd(p.ws, WS_END)), "s"(lbase + L_GA) : "memory");
    }
    { const v4u s4 = {SENT, SENT, SENT, SENT};
      st_sc1(rws, qpo, s4);
      if (w < 2) st_sc1(rws, f1o, s4);
      if (ROLE != R_SA && (w & 3) == 0) st_sc1_2(rws, mfo, v2u{SENT, SENT});
      if (w == 0) {
          if (ROLE == R_SA) st_sc1(rws, outo, s4);
          if (ROLE == R_SB) st_sc1_2(rws, outo, v2u{SENT, SENT});
      } }
    if (tid == 0) { misc[0] = 0; misc[1] = 0; }
    BAR();
    issue(sF2, 0, stg + 4096);
    auto issueB = [&](int n) { if (n < NM) issue(sM, n, stg + ((n + 1) & 3) * 4096); else issue(sT, n - NM, stg + ((n + 1) & 3) * 4096); };
    {
        v4u ft[NT][4];
        f4 xa, xb, gaa, gab, c1a, c1b, sba, sbb;
        const int col = 128 * w + 4 * (l & 15);
        {
            VMW(4);
            BAR();
            STAMP(1);
            const u64 T = *(LAS u64*)(L + L_GA) + 1ull;
            const u32 tlo = (u32)T, thi = (u32)(T >> 32);
            if (tid == 0) __hip_atomic_store((GAS u64*)(p.ws + WS_FLAG) + blockIdx.x, T, RLX_AGENT);
            auto wait_all_flags = [&]() {
                for (int sp = 0; sp < SPIN_G; ++sp) {
                    MEMBAR();
                    const v4u f0 = ld_sc1(rws, WS_FLAG + l * 16), f1 = ld_sc1(rws, WS_FLAG + 1024 + l * 16);
                    const bool bad = f0[0] != tlo || f0[1] != thi || f0[2] != tlo || f0[3] != thi || f1[0] != tlo || f1[1] != thi || f1[2] != tlo || f1[3] != thi;
                    if (__builtin_amdgcn_ballot_w64(bad) == 0ull) break;
                }
            };
            {
                v4u f[4]; read_unit(L, ta, 0, f);
                const v4u a = silu8(*(LAS v4u*)(L + L_CS + b4 * 1280 + (128 * kf + 32 * (w >> 1) + 8 * g) * 2));
                f4 d[4];
#pragma unroll
                for (int T2 = 0; T2 < 4; ++T2) d[T2] = mfma(f[T2], a, f4{0.f, 0.f, 0.f, 0.f});
                *(LAS f4*)(L + L_RED + w * 1024 + l * 16) = sel_tile(d, tq);
                if (w >= 4) {
                    float xv[10], s = 0.f;
#pragma unroll
                    for (int i = 0; i < 5; ++i) { const u32 t = *(LAS u32*)(L + L_XS + (w - 4) * 1280 + (l + 64 * i) * 4); xv[2 * i] = bflo(t); xv[2 * i + 1] = bfhi(t); s += xv[2 * i] + xv[2 * i + 1]; }
                    const float mean = wave_sum(s) * (1.f / DM);
                    float v = 0.f;
#pragma unroll
                    for (int i = 0; i < 10; ++i) v = fmaf(xv[i] - mean, xv[i] - mean, v);
                    const float var = wave_sum(v) * (1.f / DM);
                    if (l == 0) *(LAS v2u*)(L + L_ST + (w - 4) * 8) = v2u{asu(rbf(mean)), asu(rbf(__builtin_amdgcn_sqrtf(rbf(rbf(var) + 1e-5f))))};
                }
                BAR();
                if (w < 2) {
                    LAS f4* R = (LAS f4*)(L + L_RED + w * 1024 + l * 16);
                    st_sc1(rws, f1o, canon4((R[0] + R[128]) + (R[256] + R[384])));
                }
                if (w == 0) STAMP(2);
                issue(sF2, 1, stg + 8192); issue(sF2, 2, stg + 12288); issue(sF2, 3, stg);
            }
            if (w < 2) {
                for (int sp = 0; sp < SPIN_G; ++sp) {
                    MEMBAR();
                    const v2u f = ld_sc1_2(rws, WS_FLAG + l * 8);
                    if (__builtin_amdgcn_ballot_w64(f[0] != tlo || f[1] != thi) == 0ull) break;
                }
                if (w == 0) STAMP(19);
                const int hb = tid >> 5, hi = tid & 31, col = 128 * kf2 + 4 * hi;
                const u32 o = WS_F1P + (u32)(hb * 1280 + col) * 4u;
                v4u a[5] = {}, c2[5] = {};
                bool need = hasF2;
                for (int sp = 0; sp < SPIN_G; ++sp) {
                    MEMBAR();
                    if (need) {
                        u32 mx = 0;
#pragma unroll
                        for (int ks = 0; ks < 5; ++ks) { a[ks] = ld_sc1(rws, o + ks * 20480); c2[ks] = ld_sc1(rws, o + ks * 20480 + 2560); }
#pragma unroll
                        for (int ks = 0; ks < 5; ++ks) mx = vmax4(vmax4(mx, a[ks]), c2[ks]);
                        need = mx == SENT;
                    }
                    if (__builtin_amdgcn_ballot_w64(need) == 0ull) break;
                }
                const f4 sa = (((asf4(a[0]) + asf4(a[1])) + asf4(a[2])) + asf4(a[3])) + asf4(a[4]);
                const f4 sc = (((asf4(c2[0]) + asf4(c2[1])) + asf4(c2[2])) + asf4(c2[3])) + asf4(c2[4]);
                const v2u st = *(LAS v2u*)(L + L_ST + hb * 8), xr = *(LAS v2u*)(L + L_XS + hb * 1280 + (hasF2 ? col : 0) * 2);
                const float den = asf(st[1]), rc = __builtin_amdgcn_rcpf(den);
                const f4 e = rb4(unpk(xr) - asf(st[0])), q1 = e * rc;
                const f4 h = rb4(rb4(fma4(fma4(-q1, bc4(den), e), bc4(rc), q1)) * rb4(rb4(sc) + 1.f)) + rb4(sa);
                *(LAS v2u*)(L + L_HB + hb * 256 + hi * 8) = v2u{pack2(h[0], h[1]), pack2(h[2], h[3])};
                LGKM0();
                if (l == 0) __hip_atomic_fetch_add((LAS u32*)(L + L_MISC), 1u, __ATOMIC_RELAXED, __HIP_MEMORY_SCOPE_WORKGROUP);
                if (w == 0) STAMP(4);
            }
            {
                v4u fq[4][4];
                VMW(12); read_unit(L, ta, 1, fq[0]); LGKM0();
                VMW(8); read_unit(L, ta, 2, fq[1]);
                VMW(4); read_unit(L, ta, 3, fq[2]);
                issueB(0);
                VMW(4); read_unit(L, ta, 0, fq[3]);
                if (w == 7) STAMP(3);
                for (int sp = 0; misc[0] < 2u && sp < SPIN_L; ++sp) __builtin_amdgcn_s_sleep(1);
                MEMBAR();
                f4 d[4] = {};
#pragma unroll
                for (int n = 0; n < 4; ++n) {
                    const v4u a = *(LAS v4u*)(L + L_HB + b4 * 256 + (32 * n + 8 * g) * 2);
#pragma unroll
                    for (int T2 = 0; T2 < 4; ++T2) d[T2] = mfma(fq[n][T2], a, d[T2]);
                }
                st_sc1(rws, qpo, canon4(sel_tile(d, tq)));
                BAR();
                if (w == 7) STAMP(5);
                issueB(1); issueB(2); issueB(3);
            }
            if (NM) {
                f4 d[4] = {};
#pragma unroll
                for (int n = 0; n < NM; ++n) {
                    if (n < 2) VMW(12); else if (n == 2) VMW(8); else if (n == 3) VMW(4); else VMW(0);
                    v4u f[4]; read_unit(L, ta, (n + 1) & 3, f);
                    const v4u a = silu8(*(LAS v4u*)(L + L_CS + b4 * 1280 + (160 * (w & 3) + 32 * n + 8 * g) * 2));
                    LGKM0();
                    if (n == 0) issueB(4);
#pragma unroll
                    for (int T2 = 0; T2 < 4; ++T2) d[T2] = mfma(f[T2], a, d[T2]);
                }
                *(LAS f4*)(L + L_RED + w * 1024 + l * 16) = sel_tile(d, tq);
                BAR();
                if ((w & 3) == 0) {
                    LAS f4* R = (LAS f4*)(L + L_RED + w * 1024 + l * 16);
                    const f4 s = (R[0] + R[64]) + (R[128] + R[192]);
                    st_sc1_2(rws, mfo, v2u{canon(pack2(s[0], s[1])), canon(pack2(s[2], s[3]))});
                }
                constexpr int PRE = ROLE == R_SB ? PRE_B : PRE_C;
#pragma unroll
                for (int n = 0; n < PRE; ++n) issueB(5 + n);
                UNDEF4(xa); UNDEF4(xb); UNDEF4(gaa); UNDEF4(gab); UNDEF4(c1a); UNDEF4(c1b); UNDEF4(sba); UNDEF4(sbb);
                if (ROLE == R_SB && w < 5) {
                    v2u gv[2], sv[2], cv[2];
                    const u32 mo = WS_MODF + (u32)(g * 2560 + col) * 2u;
                    for (int sp = 0; sp < SPIN_G; ++sp) {
                        MEMBAR();
                        u32 mx = 0;
#pragma unroll
                        for (int h = 0; h < 2; ++h) { gv[h] = ld_sc1_2(rws, mo + h * 128); sv[h] = ld_sc1_2(rws, mo + 1280 + h * 128); cv[h] = ld_sc1_2(rws, mo + 2560 + h * 128); }
#pragma unroll
                        for (int h = 0; h < 2; ++h) mx = umax3(umax3(umax3(mx, gv[h][0], gv[h][1]), sv[h][0], sv[h][1]), cv[h][0], cv[h][1]);
                        if (all_valid(mx)) break;
                    }
                    xa = unpk(*(LAS v2u*)(L + L_XS + g * 1280 + col * 2)); xb = unpk(*(LAS v2u*)(L + L_XS + g * 1280 + col * 2 + 128));
                    gaa = unpk(gv[0]); gab = unpk(gv[1]);
                    c1a = rb4(unpk(cv[0]) + 1.f); c1b = rb4(unpk(cv[1]) + 1.f);
                    sba = unpk(sv[0]); sbb = unpk(sv[1]);
                    asm volatile("" : "+v"(xa), "+v"(xb), "+v"(gaa), "+v"(gab), "+v"(c1a), "+v"(c1b), "+v"(sba), "+v"(sbb));
                }
                if (ROLE == R_SC && w == 0) {
                    v2u ga, gb;
                    const u32 mo = WS_MODF + (u32)(b4 * 2560 + 64 * ri + dcol) * 2u;
                    for (int sp = 0; sp < SPIN_G; ++sp) {
                        MEMBAR();
                        ga = ld_sc1_2(rws, mo); gb = ld_sc1_2(rws, mo + 3840);
                        if (all_valid(umax3(umax3(ga[0], ga[1], gb[0]), gb[1], 0u))) break;
                    }
                    xa = unpk(*(LAS v2u*)(L + L_XS + b4 * 1280 + (64 * ri + dcol) * 2)); gaa = unpk(ga); gab = unpk(gb);
                    asm volatile("" : "+v"(xa), "+v"(gaa), "+v"(gab));
                }
                if (w == 5) {
                    wait_all_flags();
                    for (int sp = 0; sp < SPIN_G; ++sp) {
                        MEMBAR();
                        const v4u e0 = ld_sc1(rws, WS_ML + l * 16), e1 = ld_sc1(rws, WS_ML + 1024 + l * 16);
                        const int cnt = __builtin_popcountll(__builtin_amdgcn_ballot_w64(e0[0] != SENT)) + __builtin_popcountll(__builtin_amdgcn_ballot_w64(e0[2] != SENT))
                                      + __builtin_popcountll(__builtin_amdgcn_ballot_w64(e1[0] != SENT)) + __builtin_popcountll(__builtin_amdgcn_ballot_w64(e1[2] != SENT));
                        if (cnt >= (ROLE == R_SB ? JIT_B : JIT_C)) break;
                        __builtin_amdgcn_s_sleep(8);
                    }
                }
                BAR();
                if (w == 0) STAMP(29);
#pragma unroll
                for (int n = PRE; n < 4; ++n) issueB(5 + n);
            }
#pragma unroll
            for (int n = 0; n < NT; ++n) {
                const int a = NM + n, younger = (NA - 1 - a) < 3 ? (NA - 1 - a) : 3;
                if (younger == 3) VMW(12); else if (younger == 2) VMW(8); else if (younger == 1) VMW(4); else VMW(0);
                read_unit(L, ta, (a + 1) & 3, ft[n]);
                if (a + 4 < NA) { LGKM0(); issueB(a + 4); }
                if (ROLE == R_SA && n == 1 && w == 0) {
                    wait_all_flags();
                    if (u == 0 && l == 0) __hip_atomic_store((GAS u64*)(p.ws + WS_E), T, RLX_AGENT);
                }
            }
            LGKM0();
            BAR();
            STAMP(6);
        }
        if (ROLE == R_SA) {
            f4 res[2];
            UNDEF4(res[0]); UNDEF4(res[1]);
            u32 oo[2], mo[2];
#pragma unroll
            for (int r = 0; r < 2; ++r) {
                const int row = 2048 * ra + 256 * w + 32 * (4 * r + tq) + 8 * g, hd = row / DH, dd = row - hd * DH;
                oo[r] = WS_OP + (u32)((b4 * 32 + hd) * 2) * 384u + dd * 2; mo[r] = WS_ML + (u32)(hd * 4 + b4) * 16u;
            }
            bool done[2] = {false, false}, wdone[2] = {false, false}, obad[2] = {true, true}, mbad[2] = {true, true};
            v4u a[2][2], ml[2];
            float w0[2], w1[2];
#pragma unroll
            for (int r = 0; r < 2; ++r) { UNDEF4(ml[r]); UNDEF4(w0[r]); UNDEF4(w1[r]); UNDEF4(a[r][0]); UNDEF4(a[r][1]); }
            for (int sp = 0; sp < SPIN_G && !(done[0] && done[1]); ++sp) {
                MEMBAR();
                bool ldo[2];
#pragma unroll
                for (int r = 0; r < 2; ++r) {
                    ldo[r] = obad[r] && (!mbad[r] || sp == 0);
                    if (mbad[r]) ml[r] = ld_sc1(rws, mo[r]);
                    if (ldo[r]) { a[r][0] = ld_sc1(rws, oo[r]); a[r][1] = ld_sc1(rws, oo[r] + 384); }
                }
                const bool last = sp == SPIN_G - 1;
#pragma unroll
                for (int r = 0; r < 2; ++r) {
                    if (!wdone[r]) {
                        mbad[r] = vmax4(0u, ml[r]) == SENT;
                        if (__builtin_amdgcn_ballot_w64(mbad[r]) == 0ull || last) {
                            const float m0 = asf(ml[r][0]), m1 = asf(ml[r][2]), M = fmaxf(m0, m1);
                            const float e0 = asf(ml[r][1]) * __builtin_amdgcn_exp2f((m0 - M) * LOG2E), e1 = asf(ml[r][3]) * __builtin_amdgcn_exp2f((m1 - M) * LOG2E);
                            const float inv = fdiv(1.f, e0 + e1);
                            w0[r] = e0 * inv; w1[r] = e1 * inv; wdone[r] = true;
                        }
                    }
                    if (!done[r]) {
                        obad[r] = obad[r] && (!ldo[r] || vmax4(vmax4(0u, a[r][0]), a[r][1]) == SENT);
                        if ((__builtin_amdgcn_ballot_w64(obad[r]) == 0ull && wdone[r]) || last) {
                            STAMP(20 + w);
                            const f4 x0 = fma4(unpk(v2u{a[r][0][0], a[r][0][1]}), bc4(w0[r]), unpk(v2u{a[r][1][0], a[r][1][1]}) * w1[r]);
                            const f4 x1 = fma4(unpk(v2u{a[r][0][2], a[r][0][3]}), bc4(w0[r]), unpk(v2u{a[r][1][2], a[r][1][3]}) * w1[r]);
                            const v4u bq = {pack2(x0[0], x0[1]), pack2(x0[2], x0[3]), pack2(x1[0], x1[1]), pack2(x1[2], x1[3])};
                            const v4u bk[4] = {bq, ror4k<1>(bq), ror4k<2>(bq), ror4k<3>(bq)};
                            f4 d[4] = {};
#pragma unroll
                            for (int s = 0; s < 4; ++s)
#pragma unroll
                                for (int T2 = 0; T2 < 4; ++T2) d[(T2 + s) & 3] = mfma(ft[4 * r + s][(T2 + s) & 3], bk[T2], d[(T2 + s) & 3]);
                            res[r] = sel_tile(d, tq);
                            done[r] = true;
                        }
                    }
                }
            }
            if (w == 0) STAMP(7);
            *(LAS f4*)(L + L_RED + w * 1024 + l * 16) = res[0] + res[1];
            BAR();
            if (w == 0) {
                STAMP(8);
                LAS f4* R = (LAS f4*)(L + L_RED + l * 16);
                st_sc1(rws, outo, canon4(((R[0] + R[64]) + (R[128] + R[192])) + ((R[256] + R[320]) + (R[384] + R[448]))));
                STAMP(9);
            }
        } else if (ROLE == R_SB) {
            if (w < 5) {
                v4u a[6];
                if (w == 0) STAMP(10);
                const u32 ao = WS_AP + (u32)(g * DM + col) * 4u;
#pragma unroll
                for (int r = 0; r < 6; ++r) UNDEF4(a[r]);
                bool need = true;
                for (int sp = 0; sp < SPIN_G; ++sp) {
                    MEMBAR();
                    if (need) {
                        u32 mx = 0;
#pragma unroll
                        for (int r = 0; r < 3; ++r) { a[2 * r] = ld_sc1(rws, ao + r * 10240); a[2 * r + 1] = ld_sc1(rws, ao + r * 10240 + 256); }
#pragma unroll
                        for (int r = 0; r < 6; ++r) mx = vmax4(mx, a[r]);
                        need = mx == SENT;
                    }
                    if (__builtin_amdgcn_ballot_w64(need) == 0ull) break;
                }
                if (w == 0) STAMP(11);
                f4 ta2 = (asf4(a[0]) + asf4(a[2])) + asf4(a[4]), tb2 = (asf4(a[1]) + asf4(a[3])) + asf4(a[5]);
                rb8(ta2, tb2); ta2 *= gaa; tb2 *= gab; rb8(ta2, tb2); xa += ta2; xb += tb2; rb8(xa, xb);
                const float sh = dppf<0x150>(xa[0] + 0.f);
                const f4 da = xa - sh, db = xb - sh, t = da + db, q2 = fma4(da, da, db * db);
                float s1 = (t[0] + t[1]) + (t[2] + t[3]), s2 = (q2[0] + q2[1]) + (q2[2] + q2[3]);
                s1 += dppf<0x128>(s1); s2 += dppf<0x128>(s2); s1 += dppf<0x124>(s1); s2 += dppf<0x124>(s2);
                s1 += dppf<0x122>(s1); s2 += dppf<0x122>(s2); s1 += dppf<0x121>(s1); s2 += dppf<0x121>(s2);
                const float dm1 = s1 * (1.f / 128.f), mw = sh + dm1, v = fmaf(-dm1, s1, s2);
                *(LAS v2u*)(L + L_ST + 64 + g * 48 + w * 8) = v2u{asu(mw), asu(v)};
            }
            BAR();
            if (w < 5) {
                const f4 s0 = *(LAS f4*)(L + L_ST + 64 + g * 48), s1 = *(LAS f4*)(L + L_ST + 64 + g * 48 + 16);
                const f2 s2 = *(LAS f2*)(L + L_ST + 64 + g * 48 + 32);
                const float mean = (((s0[0] + s0[2]) + (s1[0] + s1[2])) + s2[0]) * 0.2f;
                const f4 dm = f4{s0[0], s0[2], s1[0], s1[2]} - mean, dq = dm * dm; const float d4 = s2[0] - mean;
                const float M2 = fmaf(128.f, fmaf(d4, d4, (dq[0] + dq[1]) + (dq[2] + dq[3])), ((s0[1] + s0[3]) + (s1[1] + s1[3])) + s2[1]);
                const float mb = rbf(mean), den = rbf(__builtin_amdgcn_sqrtf(rbf(rbf(M2 * (1.f / DM)) + 1e-5f))), rc = __builtin_amdgcn_rcpf(den);
                f4 ea = xa - mb, eb = xb - mb;
                rb8(ea, eb);
                const f4 qa = ea * rc, qb = eb * rc;
                f4 ha = fma4(fma4(-qa, bc4(den), ea), bc4(rc), qa), hb = fma4(fma4(-qb, bc4(den), eb), bc4(rc), qb);
                rb8(ha, hb); ha *= c1a; hb *= c1b; rb8(ha, hb); ha += sba; hb += sbb;
                *(LAS v2u*)(L + L_HB + g * 1280 + col * 2) = v2u{pack2(ha[0], ha[1]), pack2(ha[2], ha[3])};
                *(LAS v2u*)(L + L_HB + g * 1280 + col * 2 + 128) = v2u{pack2(hb[0], hb[1]), pack2(hb[2], hb[3])};
            }
            BAR();
            if (w == 0) STAMP(12);
            f4 d[4] = {};
#pragma unroll
            for (int n = 0; n < 5; ++n) {
                const v4u a = *(LAS v4u*)(L + L_HB + b4 * 1280 + (160 * (w >> 1) + 32 * n + 8 * g) * 2);
#pragma unroll
                for (int T2 = 0; T2 < 4; ++T2) d[T2] = mfma(ft[n][T2], a, d[T2]);
            }
            *(LAS f4*)(L + L_RED + w * 1024 + l * 16) = sel_tile(d, tq);
            BAR();
            if (w == 0) {
                STAMP(13);
                LAS f4* R = (LAS f4*)(L + L_RED + l * 16);
                const f4 gt = rb4((R[0] + R[128]) + (R[256] + R[384])), up = rb4((R[64] + R[192]) + (R[320] + R[448]));
                const f4 mk = {gt[0] > 0.f ? 1.f : 0.f, gt[1] > 0.f ? 1.f : 0.f, gt[2] > 0.f ? 1.f : 0.f, gt[3] > 0.f ? 1.f : 0.f};
                const f4 hh = (gt * mk) * up;
                st_sc1_2(rws, outo, v2u{canon(pack2(hh[0], hh[1])), canon(pack2(hh[2], hh[3]))});
                STAMP(14);
            }
        } else {
            f4 x1;
            UNDEF4(x1);
            if (w == 0) {
                const u32 ao = WS_AP + (u32)(b4 * DM + 64 * ri + dcol) * 4u;
                f4 s;
                for (int sp = 0; sp < SPIN_G; ++sp) {
                    MEMBAR();
                    const v4u p0 = ld_sc1(rws, ao), p1 = ld_sc1(rws, ao + 10240), p2 = ld_sc1(rws, ao + 20480);
                    s = (asf4(p0) + asf4(p1)) + asf4(p2);
                    if (all_valid(vmax4(vmax4(vmax4(0u, p0), p1), p2))) break;
                }
                x1 = rb4(xa + rb4(gaa * rb4(s)));
                asm volatile("" : "+v"(x1));
                STAMP(15);
            }
            v4u a0, a1;
            UNDEF4(a0); UNDEF4(a1);
            const u32 h0 = WS_HID + (u32)(b4 * FF + 192 * w + 32 * tq + 8 * g) * 2u, h1 = tq < 2 ? h0 + 256 : OOB;
            bool need = true;
            for (int sp = 0; sp < SPIN_G; ++sp) {
                MEMBAR();
                if (need) { a0 = ld_sc1(rws, h0); a1 = ld_sc1(rws, h1); need = vmax4(vmax4(0u, a0), a1) == SENT; }
                if (__builtin_amdgcn_ballot_w64(need) == 0ull) break;
            }
            if (w == 0) STAMP(16);
            const v4u bk[4] = {a0, ror4k<1>(a0), ror4k<2>(a0), ror4k<3>(a0)}, bl[4] = {a1, ror4k<1>(a1), ror4k<2>(a1), ror4k<3>(a1)};
            f4 d[4] = {};
#pragma unroll
            for (int k = 0; k < 4; ++k)
#pragma unroll
                for (int s = 0; s < 6; ++s) d[(s + k) & 3] = mfma(ft[s][(s + k) & 3], s < 4 ? bk[k] : bl[k], d[(s + k) & 3]);
            *(LAS f4*)(L + L_RED + w * 1024 + l * 16) = sel_tile(d, tq);
            BAR();
            if (w == 0) {
                STAMP(17);
                LAS f4* R = (LAS f4*)(L + L_RED + l * 16);
                const f4 o = x1 + rb4(gab * rb4(((R[0] + R[64]) + (R[128] + R[192])) + ((R[256] + R[320]) + (R[384] + R[448]))));
                __builtin_amdgcn_raw_buffer_store_b64(v2u{pack2(o[0], o[1]), pack2(o[2], o[3])}, __builtin_amdgcn_make_buffer_rsrc((void*)p.out, 0, 4 * DM * 2, 0x00020000),
                                                      (u32)(b4 * DM + 64 * ri + dcol) * 2u, 0, 16);
                STAMP(18);
            }
        }
    }
}

typedef const __attribute__((address_space(4))) Params* kargp;
#define GET_PARAMS() kargp pp_ = kp; asm volatile("" : "+s"(pp_)); \
    const Params q_ = {pp_->x, pp_->kc, pp_->vc, pp_->wq, pp_->wo, pp_->wg, pp_->wu, pp_->wd, pp_->c, pp_->wm, pp_->out, pp_->ws};
__global__ void __launch_bounds__(512, 2) fused_block(Params p) {
    extern __shared__ __attribute__((aligned(16))) unsigned char smem[];
    LAS unsigned char* L = (LAS unsigned char*)smem;
    const kargp kp = (kargp)__builtin_amdgcn_kernarg_segment_ptr();
    asm volatile("s_load_dword s98, %0, 0x0\n\ts_load_dword s99, %0, 0x40" :: "s"(kp) : "s98", "s99");
    const int l = threadIdx.x & 63, w = __builtin_amdgcn_readfirstlane(threadIdx.x >> 6), c = blockIdx.x;
    const int x = c & 7, y = c >> 3;
    if (__builtin_expect(c >= NW, 1)) { GET_PARAMS(); kv_path(q_, L, c - NW, w, l); }
    else if (y >= 5) { GET_PARAMS(); w_path<R_SB>(q_, L, 30 + 8 * (y - 5) + x, w, l); }
    else if (x < 6) { GET_PARAMS(); w_path<R_SA>(q_, L, 10 * (x >> 1) + 5 * (x & 1) + y, w, l); }
    else { GET_PARAMS(); w_path<R_SC>(q_, L, 54 + 5 * (x - 6) + y, w, l); }
}

extern "C" void kernel_launch(void* const* d_in, const int* in_sizes, int n_in, void* d_out, int out_size, void* d_ws, size_t ws_size, hipStream_t stream) {
    Params p;
    p.x = (const u16*)d_in[0]; p.kc = (const u16*)d_in[1]; p.vc = (const u16*)d_in[2]; p.wq = (const u16*)d_in[3]; p.wo = (const u16*)d_in[4];
    p.wg = (const u16*)d_in[6]; p.wu = (const u16*)d_in[7]; p.wd = (const u16*)d_in[8]; p.c = (const u16*)d_in[9]; p.wm = (const u16*)d_in[10];
    p.out = (u16*)d_out; p.ws = (unsigned char*)d_ws;
    (void)hipFuncSetAttribute((const void*)fused_block, hipFuncAttributeMaxDynamicSharedMemorySize, (int)L_END);
    hipLaunchKernelGGL(fused_block, dim3(256), dim3(512), L_END, stream, p);
}
```

```cpp
#include <hip/hip_runtime.h>
#include <stdint.h>
#pragma clang fp contract(off)

typedef unsigned short u16;
typedef unsigned u32;
typedef unsigned long long u64;
typedef u32 v4u __attribute__((ext_vector_type(4)));
typedef u32 v2u __attribute__((ext_vector_type(2)));
typedef float f4 __attribute__((ext_vector_type(4)));
typedef short v4s __attribute__((ext_vector_type(4)));
typedef __bf16 bf8 __attribute__((ext_vector_type(8)));
typedef __bf16 bf2 __attribute__((ext_vector_type(2)));
#define LAS __attribute__((address_space(3)))
#define GAS __attribute__((address_space(1)))
#define RLX_AGENT __ATOMIC_RELAXED, __HIP_MEMORY_SCOPE_AGENT

constexpr int DM = 640, NH = 32, SKV = 4096, DH = 192, WIN = 1024, FF = 1536, NQ = NH * DH;
constexpr u32 SENT = 0xFFFFFFFFu;
constexpr int NW = 64;
constexpr u32 WS_E = 0, WS_FLAG = 4096, WS_F1P = 8192, WS_MODF = WS_F1P + 5 * 4 * 1280 * 4, WS_QP = WS_MODF + 4 * 2560 * 2,
              WS_OP = WS_QP + 5 * 4 * NQ * 4, WS_ML = WS_OP + 128 * 2 * DH * 2, WS_AP = WS_ML + 32 * 4 * 16, WS_HID = WS_AP + 3 * 4 * DM * 4,
              WS_END = WS_HID + 4 * FF * 2;
constexpr u32 OOB = 0x7FFFFF00u;
constexpr u32 L_XS = 131072, L_CS = L_XS + 5120, L_HB = L_CS + 5120, L_RED = L_HB + 5120, L_ST = L_RED + 8192, L_GA = L_ST + 256, L_GB = L_GA + 1024,
              L_X1 = L_GB + 1024, L_MISC = L_X1 + 1024, L_STAMP = L_MISC + 64, L_END = L_STAMP + 256;
constexpr u32 K_W = 18432, K_PX = 1024, K_PX2 = 1536, K_V = 9216, K_QS = 8 * K_W, K_ML = K_QS + 768, K_END = K_ML + 64;
static_assert(K_END <= L_MISC, "LDS");
constexpr float LOG2E = 1.4426950408889634f;
static_assert(L_END <= 163840, "LDS");
constexpr int SPIN_G = 40000, SPIN_L = 2000000;
constexpr int VSPL = 4;
constexpr int PRE_B = 2, PRE_C = 0, JIT_B = 24, JIT_C = 200;

#define STAMP(i) do {} while (0)

struct Params { const u16 *x, *kc, *vc, *wq, *wo, *wg, *wu, *wd, *c, *wm; u16* out; unsigned char* ws; };

__device__ __forceinline__ v4u mk_srd(const void* p, u32 bytes) { const u64 a = (u64)p; return v4u{(u32)a, (u32)(a >> 32) & 0xffffu, bytes, 0x00020000u}; }
__device__ __forceinline__ float asf(u32 u) { return __builtin_bit_cast(float, u); }
__device__ __forceinline__ u32 asu(float f) { return __builtin_bit_cast(u32, f); }
__device__ __forceinline__ float bflo(u32 u) { return asf(u << 16); }
__device__ __forceinline__ float bfhi(u32 u) { return asf(u & 0xffff0000u); }
__device__ __forceinline__ u32 pack2(float lo, float hi) { bf2 v; v[0] = (__bf16)lo; v[1] = (__bf16)hi; return __builtin_bit_cast(u32, v); }
__device__ __forceinline__ float rbf(float f) { return (float)(__bf16)f; }
typedef float f2 __attribute__((ext_vector_type(2)));
__device__ __forceinline__ f4 rb4(f4 v) {
    float a = v[0], b = v[1], c = v[2], d = v[3];
    asm("v_cvt_pk_bf16_f32 %0, 0, %0\n\tv_cvt_pk_bf16_f32 %1, 0, %1\n\tv_cvt_pk_bf16_f32 %2, 0, %2\n\tv_cvt_pk_bf16_f32 %3, 0, %3" : "+v"(a), "+v"(b), "+v"(c), "+v"(d));
    return f4{a, b, c, d}; }
__device__ __forceinline__ void rb8(f4& u, f4& v) {
    float a = u[0], b = u[1], c = u[2], d = u[3], e = v[0], f = v[1], g = v[2], h = v[3];
    asm("v_cvt_pk_bf16_f32 %0, 0, %0\n\tv_cvt_pk_bf16_f32 %1, 0, %1\n\tv_cvt_pk_bf16_f32 %2, 0, %2\n\tv_cvt_pk_bf16_f32 %3, 0, %3\n\t"
        "v_cvt_pk_bf16_f32 %4, 0, %4\n\tv_cvt_pk_bf16_f32 %5, 0, %5\n\tv_cvt_pk_bf16_f32 %6, 0, %6\n\tv_cvt_pk_bf16_f32 %7, 0, %7"
        : "+v"(a), "+v"(b), "+v"(c), "+v"(d), "+v"(e), "+v"(f), "+v"(g), "+v"(h));
    u = f4{a, b, c, d}; v = f4{e, f, g, h}; }
__device__ __forceinline__ f4 unpk(v2u v) { return f4{bflo(v[0]), bfhi(v[0]), bflo(v[1]), bfhi(v[1])}; }
__device__ __forceinline__ f4 fma4(f4 a, f4 b, f4 c) { return __builtin_elementwise_fma(a, b, c); }
__device__ __forceinline__ f4 bc4(float x) { return f4{x, x, x, x}; }
__device__ __forceinline__ u32 canon(u32 u) { return u < 0xFFFFFFFEu ? u : 0xFFFFFFFEu; }
__device__ __forceinline__ v4u canon4(f4 v) { return v4u{canon(asu(v[0])), canon(asu(v[1])), canon(asu(v[2])), canon(asu(v[3]))}; }
__device__ __forceinline__ u32 umax3(u32 a, u32 b, u32 c) { const u32 m = a > b ? a : b; return m > c ? m : c; }
__device__ __forceinline__ u32 vmax4(u32 m, v4u v) { return umax3(umax3(m, v[0], v[1]), v[2], v[3]); }
__device__ __forceinline__ bool all_valid(u32 m) { return __builtin_amdgcn_ballot_w64(m == SENT) == 0ull; }
__device__ __forceinline__ float dot2(u32 a, u32 b, float c) { return __builtin_amdgcn_fdot2_f32_bf16(__builtin_bit_cast(bf2, a), __builtin_bit_cast(bf2, b), c, false); }
__device__ __forceinline__ f4 mfma(v4u a, v4u b, f4 c) { return __builtin_amdgcn_mfma_f32_16x16x32_bf16(__builtin_bit_cast(bf8, a), __builtin_bit_cast(bf8, b), c, 0, 0, 0); }
template <int CTRL> __device__ __forceinline__ u32 dppu(u32 x) { return (u32)__builtin_amdgcn_mov_dpp((int)x, CTRL, 0xf, 0xf, true); }
template <int CTRL> __device__ __forceinline__ float dppf(float x) { return asf(dppu<CTRL>(asu(x))); }
template <int K> __device__ __forceinline__ v4u ror4k(v4u v) {
    if constexpr (K == 0) return v; else return v4u{dppu<0x120 + 4 * K>(v[0]), dppu<0x120 + 4 * K>(v[1]), dppu<0x120 + 4 * K>(v[2]), dppu<0x120 + 4 * K>(v[3])}; }
__device__ __forceinline__ float row_sum(float x) { x += dppf<0x128>(x); x += dppf<0x124>(x); x += dppf<0x122>(x); x += dppf<0x121>(x); return x; }
__device__ __forceinline__ float row_max(float x) { x = fmaxf(x, dppf<0x128>(x)); x = fmaxf(x, dppf<0x124>(x)); x = fmaxf(x, dppf<0x122>(x)); x = fmaxf(x, dppf<0x121>(x)); return x; }
__device__ __forceinline__ float xrow_sum(float x) {
    auto s = __builtin_amdgcn_permlane16_swap(asu(x), asu(x), false, false); x = asf(s[0]) + asf(s[1]);
    auto t = __builtin_amdgcn_permlane32_swap(asu(x), asu(x), false, false); return asf(t[0]) + asf(t[1]); }
__device__ __forceinline__ float xrow_max(float x) {
    auto s = __builtin_amdgcn_permlane16_swap(asu(x), asu(x), false, false); x = fmaxf(asf(s[0]), asf(s[1]));
    auto t = __builtin_amdgcn_permlane32_swap(asu(x), asu(x), false, false); return fmaxf(asf(t[0]), asf(t[1])); }
__device__ __forceinline__ float wave_sum(float x) { return xrow_sum(row_sum(x)); }
__device__ __forceinline__ float wave_max(float x) { return xrow_max(row_max(x)); }
__device__ __forceinline__ float fdiv(float a, float b) { const float r = __builtin_amdgcn_rcpf(b); const float q = a * r; return fmaf(fmaf(-q, b, a), r, q); }
__device__ __forceinline__ float silu_bf(float v) { return fdiv(v, 1.f + __builtin_amdgcn_exp2f(fminf(-v * 1.4426950408889634f, 126.f))); }
__device__ __forceinline__ v4u silu8(v4u c) {
    v4u r;
#pragma unroll
    for (int i = 0; i < 4; ++i) r[i] = pack2(silu_bf(bflo(c[i])), silu_bf(bfhi(c[i])));
    return r; }
#define BAR() asm volatile("s_waitcnt lgkmcnt(0)\n\ts_barrier" ::: "memory")
#define LGKM0() asm volatile("s_waitcnt lgkmcnt(0)" ::: "memory")
#define VMW(n) asm volatile("s_waitcnt vmcnt(%0)" :: "i"(n) : "memory")
#define MEMBAR() asm volatile("" ::: "memory")
typedef __amdgpu_buffer_rsrc_t rsrc_t;
__device__ __forceinline__ v4u ld_sc1(rsrc_t r, u32 off) { return __builtin_amdgcn_raw_buffer_load_b128(r, off, 0, 16); }
__device__ __forceinline__ v2u ld_sc1_2(rsrc_t r, u32 off) { return __builtin_amdgcn_raw_buffer_load_b64(r, off, 0, 16); }
__device__ __forceinline__ void st_sc1(rsrc_t r, u32 off, v4u v) { __builtin_amdgcn_raw_buffer_store_b128(v, r, off, 0, 16); }
__device__ __forceinline__ void st_sc1_2(rsrc_t r, u32 off, v2u v) { __builtin_amdgcn_raw_buffer_store_b64(v, r, off, 0, 16); }
__device__ __forceinline__ f4 asf4(v4u v) { return __builtin_bit_cast(f4, v); }

__device__ __forceinline__ void dma_unit(v4u srd, u32 v0, u32 v1, u32 sA, u32 sB, u32 lds) {
    u32 keep;
    asm volatile("s_nop 4\n\ts_mov_b32 %0, m0\n\ts_mov_b32 m0, %6\n\ts_nop 0\n\t"
                 "buffer_load_dwordx4 %1, %3, %4 offen nt lds\n\ts_add_u32 m0, m0, 0x400\n\ts_nop 0\n\t"
                 "buffer_load_dwordx4 %2, %3, %4 offen nt lds\n\ts_add_u32 m0, m0, 0x400\n\ts_nop 0\n\t"
                 "buffer_load_dwordx4 %1, %3, %5 offen nt lds\n\ts_add_u32 m0, m0, 0x400\n\ts_nop 0\n\t"
                 "buffer_load_dwordx4 %2, %3, %5 offen nt lds\n\ts_mov_b32 m0, %0"
                 : "=&s"(keep) : "v"(v0), "v"(v1), "s"(srd), "s"(sA), "s"(sB), "s"(lds) : "memory", "scc");
}
__device__ __forceinline__ void dma_1k(v4u srd, u32 voff, u32 soff, u32 lds) {
    u32 keep;
    asm volatile("s_nop 4\n\ts_mov_b32 %0, m0\n\ts_mov_b32 m0, %4\n\ts_nop 0\n\tbuffer_load_dwordx4 %1, %2, %3 offen lds\n\ts_mov_b32 m0, %0"
                 : "=&s"(keep) : "v"(voff), "s"(srd), "s"(soff), "s"(lds) : "memory");
}
struct WStream { v4u srd; u32 base, pitch, v0, v1; };
__device__ __forceinline__ WStream mk_stream(v4u srd, u32 base, u32 pitch, int l) {
    WStream s; s.srd = srd; s.base = base; s.pitch = pitch;
    const u32 sw = 2u * ((l >> 4) & 1);
    s.v0 = (l >> 3) * pitch + (((l & 7) ^ sw) << 4);
    s.v1 = (8 + (l >> 3)) * pitch + (((l & 7) ^ (sw | 4u)) << 4);
    return s; }
__device__ __forceinline__ void issue(const WStream& s, int n, u32 lds) { const u32 a = s.base + (u32)n * 32u * s.pitch; dma_unit(s.srd, s.v0, s.v1, a, a + 16u * s.pitch, lds); }
__device__ __forceinline__ void read_unit(LAS unsigned char* L, const u32 (&ta)[4], int slot, v4u (&f)[4]) {
#pragma unroll
    for (int T = 0; T < 4; ++T) {
        const v2u lo = __builtin_bit_cast(v2u, __builtin_amdgcn_ds_read_tr16_b64_v4i16((LAS v4s*)(L + ta[T] + slot * 4096)));
        const v2u hi = __builtin_bit_cast(v2u, __builtin_amdgcn_ds_read_tr16_b64_v4i16((LAS v4s*)(L + ta[T] + slot * 4096 + 512)));
        f[T] = v4u{lo[0], lo[1], hi[0], hi[1]};
    } }
__device__ __forceinline__ f4 sel_tile(const f4 (&a)[4], int t) {
    const bool b0 = t & 1, b1 = t & 2;
    f4 r;
#pragma unroll
    for (int i = 0; i < 4; ++i) { const float lo = b0 ? a[1][i] : a[0][i], hi = b0 ? a[3][i] : a[2][i]; r[i] = b1 ? hi : lo; }
    return r; }

__device__ __forceinline__ void dma_kv(v4u srd, u32 vo, u32 so, u32 lds) {
    u32 keep;
    asm volatile("s_nop 4\n\ts_mov_b32 %0, m0\n\ts_mov_b32 m0, %4\n\ts_nop 0\n\t"
                 "buffer_load_dwordx4 %1, %2, %3 offen nt lds\n\ts_add_u32 m0, m0, 0x380\n\ts_nop 0\n\t"
                 "buffer_load_dwordx4 %1, %2, %3 offen offset:128 nt lds\n\ts_add_u32 m0, m0, 0x380\n\ts_nop 0\n\t"
                 "buffer_load_dwordx4 %1, %2, %3 offen offset:256 nt lds\n\ts_mov_b32 m0, %0"
                 : "=&s"(keep) : "v"(vo), "s"(srd), "s"(so), "s"(lds) : "memory", "scc");
}
__device__ __forceinline__ float qk_unit(v4u k0, v4u k1, v4u k2, const v4u (&q)[3]) {
    float s = 0.f, t = 0.f;
#pragma unroll
    for (int r = 0; r < 4; r += 2) { s = dot2(k0[r], q[0][r], s); t = dot2(k0[r + 1], q[0][r + 1], t); }
#pragma unroll
    for (int r = 0; r < 4; r += 2) { s = dot2(k1[r], q[1][r], s); t = dot2(k1[r + 1], q[1][r + 1], t); }
#pragma unroll
    for (int r = 0; r < 4; r += 2) { s = dot2(k2[r], q[2][r], s); t = dot2(k2[r + 1], q[2][r + 1], t); }
    return s + t;
}
__device__ __forceinline__ void pv_unit(v4u v0, v4u v1, v4u v2, u32 plo, float (&acc)[24]) {
    const u32 phi = plo << 16;
#pragma unroll
    for (int r = 0; r < 4; ++r) {
        acc[2 * r] = dot2(v0[r], plo, acc[2 * r]); acc[2 * r + 1] = dot2(v0[r], phi, acc[2 * r + 1]);
        acc[8 + 2 * r] = dot2(v1[r], plo, acc[8 + 2 * r]); acc[8 + 2 * r + 1] = dot2(v1[r], phi, acc[8 + 2 * r + 1]);
        acc[16 + 2 * r] = dot2(v2[r], plo, acc[16 + 2 * r]); acc[16 + 2 * r + 1] = dot2(v2[r], phi, acc[16 + 2 * r + 1]);
    }
    asm volatile("" : "+v"(acc[0]), "+v"(acc[1]), "+v"(acc[2]), "+v"(acc[3]), "+v"(acc[4]), "+v"(acc[5]), "+v"(acc[6]), "+v"(acc[7]), "+v"(acc[8]), "+v"(acc[9]), "+v"(acc[10]), "+v"(acc[11]),
                      "+v"(acc[12]), "+v"(acc[13]), "+v"(acc[14]), "+v"(acc[15]), "+v"(acc[16]), "+v"(acc[17]), "+v"(acc[18]), "+v"(acc[19]), "+v"(acc[20]), "+v"(acc[21]), "+v"(acc[22]), "+v"(acc[23]));
}

template <bool B> struct BoolC { static constexpr bool value = B; };
__device__ __forceinline__ void kv_path(const Params& p, LAS unsigned char* L, int j, int w, int l) {
    const rsrc_t rws = __builtin_amdgcn_make_buffer_rsrc((void*)p.ws, 0, WS_END, 0x00020000);
    const rsrc_t rk = __builtin_amdgcn_make_buffer_rsrc((void*)p.kc, 0, 4 * NH * SKV * DH * 2, 0x00020000);
    const rsrc_t rv = __builtin_amdgcn_make_buffer_rsrc((void*)p.vc, 0, 4 * NH * SKV * DH * 2, 0x00020000);
    volatile LAS u32* misc = (volatile LAS u32*)(L + L_MISC);
    const int r8 = j >> 3, rk3 = (r8 * 171) >> 9, pos = r8 - 3 * rk3, t3 = (j & 7) * 8 + rk3;
    const int is1 = pos & 1, is2 = pos >> 1;
    const int slot = w & is1, bh = 2 * t3 + (is2 | slot), wi = w >> is1, ng = 4 >> is1;
    const int u1 = 43 * is2 + 85 * (is1 & ~slot) + wi + (wi >= ng ? 10 * ng : 0);
    const u32 ust = 12288u >> is1;
    const u32 vo = (l >> 3) * 384 + (l & 7) * 16;
    const u32 base = ((u32)bh * SKV + (SKV - WIN)) * 384u + (u32)u1 * 3072u;
    const u32 wl = w * K_W, lb = (u32)(size_t)L + wl;
    const v4u sk = mk_srd(p.kc, 4 * NH * SKV * DH * 2), sv = mk_srd(p.vc, 4 * NH * SKV * DH * 2);
    STAMP(0);
    dma_kv(sk, vo, base + 8 * ust, lb); dma_kv(sv, vo, base + 8 * ust, lb + K_V);
    const int part = is2 | (is1 & ~slot);
    const int nl = wi >= 5 - 2 * is1 ? 2 : 3;
    const bool lead = w == 0 || (w == 1 && is1);
    const u32 opb = WS_OP + (bh * 2 + part) * 384, mlb = WS_ML + ((bh & 31) * 4 + (bh >> 5)) * 16 + part * 8;
    if (threadIdx.x < 12) misc[threadIdx.x] = 0;
    BAR();
    if (w == 4) {
        __builtin_amdgcn_s_setprio(3);
        const u64 T = __hip_atomic_load((GAS u64*)(p.ws + WS_E), RLX_AGENT) + 1ull;
        st_sc1(rws, l < 24 ? opb + l * 16 : OOB, v4u{SENT, SENT, SENT, SENT});
        st_sc1_2(rws, l == 0 ? mlb : OOB, v2u{SENT, SENT});
        const int bh1 = 2 * t3 + 1;
        st_sc1(rws, (pos == 1 && l < 24) ? WS_OP + bh1 * 2 * 384 + l * 16 : OOB, v4u{SENT, SENT, SENT, SENT});
        st_sc1_2(rws, (pos == 1 && l == 0) ? WS_ML + ((bh1 & 31) * 4 + (bh1 >> 5)) * 16 : OOB, v2u{SENT, SENT});
        VMW(0);
        if (l == 0) {
            __hip_atomic_store((GAS u64*)(p.ws + WS_FLAG) + blockIdx.x, T, RLX_AGENT);
            misc[4] = (u32)T; misc[5] = (u32)(T >> 32); LGKM0(); misc[2] = 1;
        }
        STAMP(9);
        __builtin_amdgcn_s_setprio(0);
    }
    const int c = l & 7;
    float m, lsum, fsc;
    const u32 nslot = pos == 1 ? 4u : 8u;
    auto slot_stats = [&](float& M, float& mi, float& li) {
        const v2u t = *(LAS v2u*)(L + K_ML + c * 8);
        const bool mine = pos != 1 || (l & 1) == slot;
        mi = mine ? asf(t[0]) : -INFINITY; li = mine ? asf(t[1]) : 0.f;
        M = fmaxf(mi, dppf<0xB1>(mi)); M = fmaxf(M, dppf<0x4E>(M)); M = fmaxf(M, dppf<0x141>(M));
    };
    auto post = [&]() {
        u32 old = 0;
        if (l == 0) { *(LAS v2u*)(L + K_ML + w * 8) = v2u{asu(m), asu(lsum)}; LGKM0(); old = __hip_atomic_fetch_add((LAS u32*)(L + L_MISC + 32 + slot * 4), 1u, __ATOMIC_RELAXED, __HIP_MEMORY_SCOPE_WORKGROUP); }
        if (__builtin_amdgcn_readfirstlane(old) == nslot - 1u) {
            MEMBAR();
            float M, mi, li; slot_stats(M, mi, li);
            float e = li * __builtin_amdgcn_exp2f((mi - M) * LOG2E);
            e += dppf<0xB1>(e); e += dppf<0x4E>(e); e += dppf<0x141>(e);
            st_sc1_2(rws, l == 0 ? mlb : OOB, v2u{canon(asu(M)), canon(asu(e))});
            STAMP(5);
        }
    };
    auto scale = [&]() {
        for (int sp = 0; misc[8 + slot] < nslot && sp < SPIN_L; ++sp) __builtin_amdgcn_s_sleep(1);
        MEMBAR();
        float M, mi, li; slot_stats(M, mi, li);
        float e = li * __builtin_amdgcn_exp2f((mi - M) * LOG2E);
        e += dppf<0xB1>(e); e += dppf<0x4E>(e); e += dppf<0x141>(e);
        fsc = fdiv(__builtin_amdgcn_exp2f((m - M) * LOG2E), e);
    };
    dma_kv(sk, vo, base + 9 * ust, lb + 3072); dma_kv(sv, vo, base + 9 * ust, lb + K_V + 3072);
    if (nl == 3) { dma_kv(sk, vo, base + 10 * ust, lb + 6144); dma_kv(sv, vo, base + 10 * ust, lb + K_V + 6144); }
    v4u K[24], V[24];
#pragma unroll
    for (int jj = 0; jj < 8; ++jj)
#pragma unroll
        for (int i = 0; i < 3; ++i) K[jj * 3 + i] = __builtin_amdgcn_raw_buffer_load_b128(rk, vo + i * 128, base + jj * ust, 2);
#pragma unroll
    for (int jj = 0; jj < VSPL; ++jj)
#pragma unroll
        for (int i = 0; i < 3; ++i) V[jj * 3 + i] = __builtin_amdgcn_raw_buffer_load_b128(rv, vo + i * 128, base + jj * ust, 2);
    __builtin_amdgcn_sched_barrier(0);
    const bool early = __builtin_amdgcn_readfirstlane(misc[slot]) != 0u;
    float acc[24];
    auto attend = [&](auto ec) {
        constexpr bool EARLY = decltype(ec)::value;
        if (!EARLY) {
#pragma unroll
            for (int jj = VSPL; jj < 8; ++jj)
#pragma unroll
                for (int i = 0; i < 3; ++i) V[jj * 3 + i] = __builtin_amdgcn_raw_buffer_load_b128(rv, vo + i * 128, base + jj * ust, 2);
            __builtin_amdgcn_sched_barrier(0);
            if (w == 0) STAMP(1);
            if (w == 7) STAMP(2);
            if (w < 4) {
                for (int sp = 0; misc[2] == 0 && sp < SPIN_L; ++sp) __builtin_amdgcn_s_sleep(1);
                const u32 tlo = misc[4], thi = misc[5];
                for (int sp = 0; sp < SPIN_G && misc[6] == 0; ++sp) {
                    MEMBAR();
                    const v2u f = ld_sc1_2(rws, WS_FLAG + l * 8);
                    if (__builtin_amdgcn_ballot_w64(f[0] != tlo || f[1] != thi) == 0ull) misc[6] = 1;
                }
                const u32 qo = WS_QP + (u32)((bh >> 5) * NQ + (bh & 31) * DH + 4 * l) * 4u;
                v4u a[5] = {};
                bool need = l < 48;
                for (int sp = 0; sp < SPIN_G; ++sp) {
                    MEMBAR();
                    if (__builtin_amdgcn_readfirstlane(misc[slot]) != 0u) break;
                    if (need) {
                        u32 mx = 0;
#pragma unroll
                        for (int ks = 0; ks < 5; ++ks) a[ks] = ld_sc1(rws, qo + ks * (4 * NQ * 4));
#pragma unroll
                        for (int ks = 0; ks < 5; ++ks) mx = vmax4(mx, a[ks]);
                        need = mx == SENT;
                    }
                    if (__builtin_amdgcn_ballot_w64(need) == 0ull || sp == SPIN_G - 1) {
                        const f4 qs = (((asf4(a[0]) + asf4(a[1])) + asf4(a[2])) + asf4(a[3])) + asf4(a[4]);
                        if (l < 48) *(LAS v2u*)(L + K_QS + slot * 384 + l * 8) = v2u{pack2(qs[0], qs[1]), pack2(qs[2], qs[3])};
                        LGKM0();
                        misc[slot] = 1;
                        break;
                    }
                }
                if (w == 0) STAMP(3);
            } else {
                for (int sp = 0; misc[slot] == 0 && sp < SPIN_L; ++sp) __builtin_amdgcn_s_sleep(1);
            }
        }
        VMW(EARLY ? 24 + 3 * VSPL : 48);
        v4u p0, p1, p2;
        {
            v4u q[3];
#pragma unroll
            for (int i = 0; i < 3; ++i) q[i] = *(LAS v4u*)(L + K_QS + slot * 384 + (c + 8 * i) * 16);
            float y[4], x[8];
            LAS v4u* KL = (LAS v4u*)(L + wl + l * 16);
#pragma unroll
            for (int jj = 0; jj < 3; ++jj) { y[jj] = qk_unit(KL[jj * 192], KL[jj * 192 + 64], KL[jj * 192 + 128], q); __builtin_amdgcn_sched_barrier(0); }
#pragma unroll
            for (int jj = 0; jj < 8; jj += 2) { x[jj] = qk_unit(K[3 * jj], K[3 * jj + 1], K[3 * jj + 2], q); x[jj + 1] = qk_unit(K[3 * jj + 3], K[3 * jj + 4], K[3 * jj + 5], q); __builtin_amdgcn_sched_barrier(0); }
#pragma unroll
            for (int h = 0; h < 4; ++h) { const float a = x[h] + dppf<0x141>(x[h]), b = x[h + 4] + dppf<0x141>(x[h + 4]); x[h] = (c & 4) ? b : a; }
            { const float a = y[0] + dppf<0x141>(y[0]), b = y[2] + dppf<0x141>(y[2]), a1 = y[1] + dppf<0x141>(y[1]); y[0] = (c & 4) ? b : a; y[1] = a1; }
#pragma unroll
            for (int h = 0; h < 2; ++h) { const float a = x[h] + dppf<0x4E>(x[h]), b = x[h + 2] + dppf<0x4E>(x[h + 2]); x[h] = (c & 2) ? b : a; }
            { const float a = y[0] + dppf<0x4E>(y[0]), b = y[1] + dppf<0x4E>(y[1]); y[0] = (c & 2) ? b : a; }
            { const float a = x[0] + dppf<0xB1>(x[0]), b = x[1] + dppf<0xB1>(x[1]); x[0] = (c & 1) ? b : a; }
            y[0] += dppf<0xB1>(y[0]);
            const float s1 = rbf(rbf(x[0]) * 0.07216878364870322f);
            const float s2 = (c >> 1) < nl ? rbf(rbf(y[0]) * 0.07216878364870322f) : -INFINITY;
            m = wave_max(fmaxf(s1, s2));
            const float e1 = __builtin_amdgcn_exp2f((s1 - m) * LOG2E), e2 = __builtin_amdgcn_exp2f((s2 - m) * LOG2E);
            *(LAS u32*)(L + wl + K_PX + l * 4) = pack2(e1, 0.f);
            *(LAS u32*)(L + wl + K_PX2 + (l >> 1) * 4) = pack2(e2, 0.f);
            lsum = wave_sum(e1 + ((c & 1) ? 0.f : e2));
            post();
            LGKM0();
            p0 = *(LAS v4u*)(L + wl + K_PX + (l >> 3) * 32); p1 = *(LAS v4u*)(L + wl + K_PX + (l >> 3) * 32 + 16); p2 = *(LAS v4u*)(L + wl + K_PX2 + (l >> 3) * 16);
        }
        if (EARLY) {
            __builtin_amdgcn_sched_barrier(0);
#pragma unroll
            for (int jj = VSPL; jj < 8; ++jj)
#pragma unroll
                for (int i = 0; i < 3; ++i) V[jj * 3 + i] = __builtin_amdgcn_raw_buffer_load_b128(rv, vo + i * 128, base + jj * ust, 2);
            __builtin_amdgcn_sched_barrier(0);
        }
        if (w == 0) STAMP(4);
#pragma unroll
        for (int i = 0; i < 24; ++i) acc[i] = 0.f;
        {
            LAS v4u* VL = (LAS v4u*)(L + wl + K_V + l * 16);
            pv_unit(VL[0], VL[64], VL[128], p2[0], acc);
            pv_unit(VL[192], VL[256], VL[320], p2[1], acc);
            if (nl == 3) pv_unit(VL[384], VL[448], VL[512], p2[2], acc);
        }
#pragma unroll
        for (int jj = 0; jj < 6; ++jj) pv_unit(V[3 * jj], V[3 * jj + 1], V[3 * jj + 2], jj < 4 ? p0[jj & 3] : p1[jj & 3], acc);
        scale();
#pragma unroll
        for (int jj = 6; jj < 8; ++jj) pv_unit(V[3 * jj], V[3 * jj + 1], V[3 * jj + 2], p1[jj & 3], acc);
    };
    if (early) attend(BoolC<true>{}); else attend(BoolC<false>{});
    if (w == 7) STAMP(6);
    float y[12], z[6];
#pragma unroll
    for (int n = 0; n < 12; ++n) { auto s = __builtin_amdgcn_permlane32_swap(asu(acc[n]), asu(acc[n + 12]), false, false); y[n] = asf(s[0]) + asf(s[1]); }
#pragma unroll
    for (int n = 0; n < 6; ++n) { auto s = __builtin_amdgcn_permlane16_swap(asu(y[n]), asu(y[n + 6]), false, false); z[n] = asf(s[0]) + asf(s[1]); }
#pragma unroll
    for (int n = 0; n < 3; ++n) {
        const float a = z[n] + dppf<0x128>(z[n]), b = z[n + 3] + dppf<0x128>(z[n + 3]);
        const int ai = 3 * (l >> 3) + n;
        *(LAS float*)(L + wl + (64 * (ai >> 3) + 8 * c + (ai & 7)) * 4) = ((l & 8) ? b : a) * fsc;
    }
    BAR();
    if (w == 0) STAMP(7);
    if (lead) {
        const u32 pb = slot * K_W + (l < 48 ? l : 47) * 16;
        f4 o = (*(LAS f4*)(L + pb) + *(LAS f4*)(L + pb + 2 * K_W)) + (*(LAS f4*)(L + pb + 4 * K_W) + *(LAS f4*)(L + pb + 6 * K_W));
        if (pos != 1) o += (*(LAS f4*)(L + pb + K_W) + *(LAS f4*)(L + pb + 3 * K_W)) + (*(LAS f4*)(L + pb + 5 * K_W) + *(LAS f4*)(L + pb + 7 * K_W));
        st_sc1_2(rws, l < 48 ? opb + l * 8 : OOB, v2u{canon(pack2(o[0], o[1])), canon(pack2(o[2], o[3]))});
        if (w == 0) STAMP(8); else STAMP(10);
    }
}

enum { R_SA = 0, R_SB = 1, R_SC = 2 };
#define UNDEF4(v) asm volatile("" : "=v"(v))
template <int ROLE>
__device__ __forceinline__ void w_path(const Params& p, LAS unsigned char* L, int u, int w, int l) {
    const rsrc_t rws = __builtin_amdgcn_make_buffer_rsrc((void*)p.ws, 0, WS_END, 0x00020000);
    const int tid = threadIdx.x, g = l >> 4, tq = (l & 15) >> 2, b4 = l & 3;
    const u32 lbase = (u32)(size_t)L, stg = lbase + w * 16384;
    const bool hasF1 = u < 50, hasF2 = u < 60;
    const int kf = u / 10, lp = u - 10 * kf, kf2 = u / 12, cs = u - 12 * kf2;
    const int ri = ROLE == R_SA ? u : (ROLE == R_SB ? u - 30 : u - 54);
    const WStream sF1 = mk_stream(mk_srd(p.wm, hasF1 ? DM * 3840 * 2 : 0), (u32)((128 * kf + 32 * (w >> 1)) * 7680 + (128 * lp + 64 * (w & 1)) * 2), 7680, l);
    const WStream sF2 = mk_stream(mk_srd(p.wq, hasF2 ? DM * NQ * 2 : 0), (u32)(128 * kf2 * 12288 + (512 * cs + 64 * w) * 2), 12288, l);
    const int mid = ROLE == R_SC ? (w < 4 ? (ri >= 6 ? ri : -1) : 30 + ri) : (w < 4 ? (ri < 6 ? ri : ri + 4) : (ri == 20 ? 28 : (ri == 21 ? 29 : -1)));
    const bool hasM = ROLE != R_SA && mid >= 0;
    const WStream sM = mk_stream(mk_srd(p.wm, hasM ? DM * 3840 * 2 : 0), (u32)(160 * (w & 3) * 7680 + (1280 + 64 * mid) * 2), 7680, l);
    const int ra = ri / 10, ca = ri - 10 * ra;
    const WStream sT = ROLE == R_SA ? mk_stream(mk_srd(p.wo, NQ * DM * 2), (u32)((2048 * ra + 256 * w) * 1280 + ca * 128), 1280, l)
                     : ROLE == R_SB ? mk_stream(mk_srd((w & 1) ? p.wu : p.wg, DM * FF * 2), (u32)(160 * (w >> 1) * 3072 + ri * 128), 3072, l)
                                    : mk_stream(mk_srd(p.wd, FF * DM * 2), (u32)(192 * w * 1280 + ri * 128), 1280, l);
    constexpr int NM = ROLE == R_SA ? 0 : 5, NT = ROLE == R_SA ? 8 : (ROLE == R_SB ? 5 : 6), NA = NM + NT;
    u32 ta[4];
    { const u32 f = 2u * (tq >> 1) | 4u * (g & 1);
#pragma unroll
      for (int T = 0; T < 4; ++T) ta[T] = w * 16384 + (8 * g + tq) * 128 + ((((u32)(2 * T)) ^ f) + (b4 >> 1)) * 16 + (b4 & 1) * 8; }
    const u32 dcol = 16 * tq + 4 * g;
    const u32 f1o = hasF1 ? WS_F1P + (u32)((kf * 4 + b4) * 1280 + 128 * lp + 64 * w + dcol) * 4u : OOB;
    const u32 qpo = hasF2 ? WS_QP + (u32)((kf2 * 4 + b4) * NQ + 512 * cs + 64 * w + dcol) * 4u : OOB;
    const u32 mfo = hasM ? WS_MODF + (u32)(b4 * 2560 + 64 * mid + dcol) * 2u : OOB;
    const u32 outo = ROLE == R_SA ? WS_AP + (u32)((ra * 4 + b4) * DM + 64 * ca + dcol) * 4u : WS_HID + (u32)(b4 * FF + 64 * ri + dcol) * 2u;
    volatile LAS u32* misc = (volatile LAS u32*)(L + L_MISC);
    STAMP(0);
    if (w < 5) { dma_1k(mk_srd(p.c, 5120), l * 16, w * 1024, lbase + L_CS + w * 1024); dma_1k(mk_srd(p.x, 5120), l * 16, w * 1024, lbase + L_XS + w * 1024); }
    issue(sF1, 0, stg);
    { u32 keep;
      asm volatile("s_nop 4\n\ts_mov_b32 %0, m0\n\ts_mov_b32 m0, %5\n\ts_nop 0\n\tbuffer_load_dword %1, %2, %3 offen nt lds\n\tbuffer_load_dword %1, %4, 0 offen nt lds\n\ts_mov_b32 m0, %0"
                   : "=&s"(keep) : "v"(l == 0 ? 0u : OOB), "s"(sT.srd), "s"(sT.base), "s"(mk_srd(p.out, 5120)), "s"(lbase + L_GB) : "memory"); }
    if (w == 0) {
        u32 keep;
        asm volatile("s_nop 4\n\ts_mov_b32 %0, m0\n\ts_mov_b32 m0, %3\n\ts_nop 0\n\tbuffer_load_dword %1, %2, 0 offen sc1 lds\n\ts_mov_b32 m0, %0"
                     : "=&s"(keep) : "v"(l < 2 ? WS_E + l * 4 : OOB), "s"(mk_srd(p.ws, WS_END)), "s"(lbase + L_GA) : "memory");
    }
    { const v4u s4 = {SENT, SENT, SENT, SENT};
      st_sc1(rws, qpo, s4);
      if (w < 2) st_sc1(rws, f1o, s4);
      if (ROLE != R_SA && (w & 3) == 0) st_sc1_2(rws, mfo, v2u{SENT, SENT});
      if (w == 0) {
          if (ROLE == R_SA) st_sc1(rws, outo, s4);
          if (ROLE == R_SB) st_sc1_2(rws, outo, v2u{SENT, SENT});
      } }
    if (tid == 0) { misc[0] = 0; misc[1] = 0; }
    BAR();
    issue(sF2, 0, stg + 4096);
    auto issueB = [&](int n) { if (n < NM) issue(sM, n, stg + ((n + 1) & 3) * 4096); else issue(sT, n - NM, stg + ((n + 1) & 3) * 4096); };
    {
        v4u ft[NT][4];
        f4 xa, xb, gaa, gab, c1a, c1b, sba, sbb;
        const int col = 128 * w + 4 * (l & 15);
        {
            VMW(4);
            BAR();
            STAMP(1);
            const u64 T = *(LAS u64*)(L + L_GA) + 1ull;
            const u32 tlo = (u32)T, thi = (u32)(T >> 32);
            if (tid == 0) __hip_atomic_store((GAS u64*)(p.ws + WS_FLAG) + blockIdx.x, T, RLX_AGENT);
            auto wait_all_flags = [&]() {
                for (int sp = 0; sp < SPIN_G; ++sp) {
                    MEMBAR();
                    const v4u f0 = ld_sc1(rws, WS_FLAG + l * 16), f1 = ld_sc1(rws, WS_FLAG + 1024 + l * 16);
                    const bool bad = f0[0] != tlo || f0[1] != thi || f0[2] != tlo || f0[3] != thi || f1[0] != tlo || f1[1] != thi || f1[2] != tlo || f1[3] != thi;
                    if (__builtin_amdgcn_ballot_w64(bad) == 0ull) break;
                }
            };
            {
                v4u f[4]; read_unit(L, ta, 0, f);
                const v4u a = silu8(*(LAS v4u*)(L + L_CS + b4 * 1280 + (128 * kf + 32 * (w >> 1) + 8 * g) * 2));
                f4 d[4];
#pragma unroll
                for (int T2 = 0; T2 < 4; ++T2) d[T2] = mfma(f[T2], a, f4{0.f, 0.f, 0.f, 0.f});
                *(LAS f4*)(L + L_RED + w * 1024 + l * 16) = sel_tile(d, tq);
                if (w >= 4) {
                    float xv[10], s = 0.f;
#pragma unroll
                    for (int i = 0; i < 5; ++i) { const u32 t = *(LAS u32*)(L + L_XS + (w - 4) * 1280 + (l + 64 * i) * 4); xv[2 * i] = bflo(t); xv[2 * i + 1] = bfhi(t); s += xv[2 * i] + xv[2 * i + 1]; }
                    const float mean = wave_sum(s) * (1.f / DM);
                    float v = 0.f;
#pragma unroll
                    for (int i = 0; i < 10; ++i) v = fmaf(xv[i] - mean, xv[i] - mean, v);
                    const float var = wave_sum(v) * (1.f / DM);
                    if (l == 0) *(LAS v2u*)(L + L_ST + (w - 4) * 8) = v2u{asu(rbf(mean)), asu(rbf(__builtin_amdgcn_sqrtf(rbf(rbf(var) + 1e-5f))))};
                }
                BAR();
                if (w < 2) {
                    LAS f4* R = (LAS f4*)(L + L_RED + w * 1024 + l * 16);
                    st_sc1(rws, f1o, canon4((R[0] + R[128]) + (R[256] + R[384])));
                }
                if (w == 0) STAMP(2);
                issue(sF2, 1, stg + 8192); issue(sF2, 2, stg + 12288); issue(sF2, 3, stg);
            }
            if (w < 2) {
                for (int sp = 0; sp < SPIN_G; ++sp) {
                    MEMBAR();
                    const v2u f = ld_sc1_2(rws, WS_FLAG + l * 8);
                    if (__builtin_amdgcn_ballot_w64(f[0] != tlo || f[1] != thi) == 0ull) break;
                }
                if (w == 0) STAMP(19);
                const int hb = tid >> 5, hi = tid & 31, col = 128 * kf2 + 4 * hi;
                const u32 o = WS_F1P + (u32)(hb * 1280 + col) * 4u;
                v4u a[5] = {}, c2[5] = {};
                bool need = hasF2;
                for (int sp = 0; sp < SPIN_G; ++sp) {
                    MEMBAR();
                    if (need) {
                        u32 mx = 0;
#pragma unroll
                        for (int ks = 0; ks < 5; ++ks) { a[ks] = ld_sc1(rws, o + ks * 20480); c2[ks] = ld_sc1(rws, o + ks * 20480 + 2560); }
#pragma unroll
                        for (int ks = 0; ks < 5; ++ks) mx = vmax4(vmax4(mx, a[ks]), c2[ks]);
                        need = mx == SENT;
                    }
                    if (__builtin_amdgcn_ballot_w64(need) == 0ull) break;
                }
                const f4 sa = (((asf4(a[0]) + asf4(a[1])) + asf4(a[2])) + asf4(a[3])) + asf4(a[4]);
                const f4 sc = (((asf4(c2[0]) + asf4(c2[1])) + asf4(c2[2])) + asf4(c2[3])) + asf4(c2[4]);
                const v2u st = *(LAS v2u*)(L + L_ST + hb * 8), xr = *(LAS v2u*)(L + L_XS + hb * 1280 + (hasF2 ? col : 0) * 2);
                const float den = asf(st[1]), rc = __builtin_amdgcn_rcpf(den);
                const f4 e = rb4(unpk(xr) - asf(st[0])), q1 = e * rc;
                const f4 h = rb4(rb4(fma4(fma4(-q1, bc4(den), e), bc4(rc), q1)) * rb4(rb4(sc) + 1.f)) + rb4(sa);
                *(LAS v2u*)(L + L_HB + hb * 256 + hi * 8) = v2u{pack2(h[0], h[1]), pack2(h[2], h[3])};
                LGKM0();
                if (l == 0) __hip_atomic_fetch_add((LAS u32*)(L + L_MISC), 1u, __ATOMIC_RELAXED, __HIP_MEMORY_SCOPE_WORKGROUP);
                if (w == 0) STAMP(4);
            }
            {
                v4u fq[4][4];
                VMW(12); read_unit(L, ta, 1, fq[0]); LGKM0();
                VMW(8); read_unit(L, ta, 2, fq[1]);
                VMW(4); read_unit(L, ta, 3, fq[2]);
                issueB(0);
                VMW(4); read_unit(L, ta, 0, fq[3]);
                if (w == 7) STAMP(3);
                for (int sp = 0; misc[0] < 2u && sp < SPIN_L; ++sp) __builtin_amdgcn_s_sleep(1);
                MEMBAR();
                f4 d[4] = {};
#pragma unroll
                for (int n = 0; n < 4; ++n) {
                    const v4u a = *(LAS v4u*)(L + L_HB + b4 * 256 + (32 * n + 8 * g) * 2);
#pragma unroll
                    for (int T2 = 0; T2 < 4; ++T2) d[T2] = mfma(fq[n][T2], a, d[T2]);
                }
                st_sc1(rws, qpo, canon4(sel_tile(d, tq)));
                BAR();
                if (w == 7) STAMP(5);
                issueB(1); issueB(2); issueB(3);
            }
            if (NM) {
                f4 d[4] = {};
#pragma unroll
                for (int n = 0; n < NM; ++n) {
                    if (n < 2) VMW(12); else if (n == 2) VMW(8); else if (n == 3) VMW(4); else VMW(0);
                    v4u f[4]; read_unit(L, ta, (n + 1) & 3, f);
                    const v4u a = silu8(*(LAS v4u*)(L + L_CS + b4 * 1280 + (160 * (w & 3) + 32 * n + 8 * g) * 2));
                    LGKM0();
                    if (n == 0) issueB(4);
#pragma unroll
                    for (int T2 = 0; T2 < 4; ++T2) d[T2] = mfma(f[T2], a, d[T2]);
                }
                *(LAS f4*)(L + L_RED + w * 1024 + l * 16) = sel_tile(d, tq);
                BAR();
                if ((w & 3) == 0) {
                    LAS f4* R = (LAS f4*)(L + L_RED + w * 1024 + l * 16);
                    const f4 s = (R[0] + R[64]) + (R[128] + R[192]);
                    st_sc1_2(rws, mfo, v2u{canon(pack2(s[0], s[1])), canon(pack2(s[2], s[3]))});
                }
                constexpr int PRE = ROLE == R_SB ? PRE_B : PRE_C;
#pragma unroll
                for (int n = 0; n < PRE; ++n) issueB(5 + n);
                UNDEF4(xa); UNDEF4(xb); UNDEF4(gaa); UNDEF4(gab); UNDEF4(c1a); UNDEF4(c1b); UNDEF4(sba); UNDEF4(sbb);
                if (ROLE == R_SB && w < 5) {
                    v2u gv[2], sv[2], cv[2];
                    const u32 mo = WS_MODF + (u32)(g * 2560 + col) * 2u;
                    for (int sp = 0; sp < SPIN_G; ++sp) {
                        MEMBAR();
                        u32 mx = 0;
#pragma unroll
                        for (int h = 0; h < 2; ++h) { gv[h] = ld_sc1_2(rws, mo + h * 128); sv[h] = ld_sc1_2(rws, mo + 1280 + h * 128); cv[h] = ld_sc1_2(rws, mo + 2560 + h * 128); }
#pragma unroll
                        for (int h = 0; h < 2; ++h) mx = umax3(umax3(umax3(mx, gv[h][0], gv[h][1]), sv[h][0], sv[h][1]), cv[h][0], cv[h][1]);
                        if (all_valid(mx)) break;
                    }
                    xa = unpk(*(LAS v2u*)(L + L_XS + g * 1280 + col * 2)); xb = unpk(*(LAS v2u*)(L + L_XS + g * 1280 + col * 2 + 128));
                    gaa = unpk(gv[0]); gab = unpk(gv[1]);
                    c1a = rb4(unpk(cv[0]) + 1.f); c1b = rb4(unpk(cv[1]) + 1.f);
                    sba = unpk(sv[0]); sbb = unpk(sv[1]);
                    asm volatile("" : "+v"(xa), "+v"(xb), "+v"(gaa), "+v"(gab), "+v"(c1a), "+v"(c1b), "+v"(sba), "+v"(sbb));
                }
                if (ROLE == R_SC && w == 0) {
                    v2u ga, gb;
                    const u32 mo = WS_MODF + (u32)(b4 * 2560 + 64 * ri + dcol) * 2u;
                    for (int sp = 0; sp < SPIN_G; ++sp) {
                        MEMBAR();
                        ga = ld_sc1_2(rws, mo); gb = ld_sc1_2(rws, mo + 3840);
                        if (all_valid(umax3(umax3(ga[0], ga[1], gb[0]), gb[1], 0u))) break;
                    }
                    xa = unpk(*(LAS v2u*)(L + L_XS + b4 * 1280 + (64 * ri + dcol) * 2)); gaa = unpk(ga); gab = unpk(gb);
                    asm volatile("" : "+v"(xa), "+v"(gaa), "+v"(gab));
                }
                if (w == 5) {
                    wait_all_flags();
                    for (int sp = 0; sp < SPIN_G; ++sp) {
                        MEMBAR();
                        const v4u e0 = ld_sc1(rws, WS_ML + l * 16), e1 = ld_sc1(rws, WS_ML + 1024 + l * 16);
                        const int cnt = __builtin_popcountll(__builtin_amdgcn_ballot_w64(e0[0] != SENT)) + __builtin_popcountll(__builtin_amdgcn_ballot_w64(e0[2] != SENT))
                                      + __builtin_popcountll(__builtin_amdgcn_ballot_w64(e1[0] != SENT)) + __builtin_popcountll(__builtin_amdgcn_ballot_w64(e1[2] != SENT));
                        if (cnt >= (ROLE == R_SB ? JIT_B : JIT_C)) break;
                        __builtin_amdgcn_s_sleep(8);
                    }
                }
                BAR();
                if (w == 0) STAMP(29);
#pragma unroll
                for (int n = PRE; n < 4; ++n) issueB(5 + n);
            }
#pragma unroll
            for (int n = 0; n < NT; ++n) {
                const int a = NM + n, younger = (NA - 1 - a) < 3 ? (NA - 1 - a) : 3;
                if (younger == 3) VMW(12); else if (younger == 2) VMW(8); else if (younger == 1) VMW(4); else VMW(0);
                read_unit(L, ta, (a + 1) & 3, ft[n]);
                if (a + 4 < NA) { LGKM0(); issueB(a + 4); }
                if (ROLE == R_SA && n == 1 && w == 0) {
                    wait_all_flags();
                    if (u == 0 && l == 0) __hip_atomic_store((GAS u64*)(p.ws + WS_E), T, RLX_AGENT);
                }
            }
            LGKM0();
            BAR();
            STAMP(6);
        }
        if (ROLE == R_SA) {
            f4 res[2];
            UNDEF4(res[0]); UNDEF4(res[1]);
            u32 oo[2], mo[2];
#pragma unroll
            for (int r = 0; r < 2; ++r) {
                const int row = 2048 * ra + 256 * w + 32 * (4 * r + tq) + 8 * g, hd = row / DH, dd = row - hd * DH;
                oo[r] = WS_OP + (u32)((b4 * 32 + hd) * 2) * 384u + dd * 2; mo[r] = WS_ML + (u32)(hd * 4 + b4) * 16u;
            }
            bool done[2] = {false, false}, wdone[2] = {false, false}, obad[2] = {true, true}, mbad[2] = {true, true};
            v4u a[2][2], ml[2];
            float w0[2], w1[2];
#pragma unroll
            for (int r = 0; r < 2; ++r) { UNDEF4(ml[r]); UNDEF4(w0[r]); UNDEF4(w1[r]); UNDEF4(a[r][0]); UNDEF4(a[r][1]); }
            for (int sp = 0; sp < SPIN_G && !(done[0] && done[1]); ++sp) {
                MEMBAR();
                bool ldo[2];
#pragma unroll
                for (int r = 0; r < 2; ++r) {
                    ldo[r] = obad[r] && (!mbad[r] || sp == 0);
                    if (mbad[r]) ml[r] = ld_sc1(rws, mo[r]);
                    if (ldo[r]) { a[r][0] = ld_sc1(rws, oo[r]); a[r][1] = ld_sc1(rws, oo[r] + 384); }
                }
                const bool last = sp == SPIN_G - 1;
#pragma unroll
                for (int r = 0; r < 2; ++r) {
                    if (!wdone[r]) {
                        mbad[r] = vmax4(0u, ml[r]) == SENT;
                        if (__builtin_amdgcn_ballot_w64(mbad[r]) == 0ull || last) {
                            const float m0 = asf(ml[r][0]), m1 = asf(ml[r][2]), M = fmaxf(m0, m1);
                            const float e0 = asf(ml[r][1]) * __builtin_amdgcn_exp2f((m0 - M) * LOG2E), e1 = asf(ml[r][3]) * __builtin_amdgcn_exp2f((m1 - M) * LOG2E);
                            const float inv = fdiv(1.f, e0 + e1);
                            w0[r] = e0 * inv; w1[r] = e1 * inv; wdone[r] = true;
                        }
                    }
                    if (!done[r]) {
                        obad[r] = obad[r] && (!ldo[r] || vmax4(vmax4(0u, a[r][0]), a[r][1]) == SENT);
                        if ((__builtin_amdgcn_ballot_w64(obad[r]) == 0ull && wdone[r]) || last) {
                            STAMP(20 + w);
                            const f4 x0 = fma4(unpk(v2u{a[r][0][0], a[r][0][1]}), bc4(w0[r]), unpk(v2u{a[r][1][0], a[r][1][1]}) * w1[r]);
                            const f4 x1 = fma4(unpk(v2u{a[r][0][2], a[r][0][3]}), bc4(w0[r]), unpk(v2u{a[r][1][2], a[r][1][3]}) * w1[r]);
                            const v4u bq = {pack2(x0[0], x0[1]), pack2(x0[2], x0[3]), pack2(x1[0], x1[1]), pack2(x1[2], x1[3])};
                            const v4u bk[4] = {bq, ror4k<1>(bq), ror4k<2>(bq), ror4k<3>(bq)};
                            f4 d[4] = {};
#pragma unroll
                            for (int s = 0; s < 4; ++s)
#pragma unroll
                                for (int T2 = 0; T2 < 4; ++T2) d[(T2 + s) & 3] = mfma(ft[4 * r + s][(T2 + s) & 3], bk[T2], d[(T2 + s) & 3]);
                            res[r] = sel_tile(d, tq);
                            done[r] = true;
                        }
                    }
                }
            }
            if (w == 0) STAMP(7);
            *(LAS f4*)(L + L_RED + w * 1024 + l * 16) = res[0] + res[1];
            BAR();
            if (w == 0) {
                STAMP(8);
                LAS f4* R = (LAS f4*)(L + L_RED + l * 16);
                st_sc1(rws, outo, canon4(((R[0] + R[64]) + (R[128] + R[192])) + ((R[256] + R[320]) + (R[384] + R[448]))));
                STAMP(9);
            }
        } else if (ROLE == R_SB) {
            if (w < 5) {
                v4u a[6];
                if (w == 0) STAMP(10);
                const u32 ao = WS_AP + (u32)(g * DM + col) * 4u;
#pragma unroll
                for (int r = 0; r < 6; ++r) UNDEF4(a[r]);
                bool need = true;
                for (int sp = 0; sp < SPIN_G; ++sp) {
                    MEMBAR();
                    if (need) {
                        u32 mx = 0;
#pragma unroll
                        for (int r = 0; r < 3; ++r) { a[2 * r] = ld_sc1(rws, ao + r * 10240); a[2 * r + 1] = ld_sc1(rws, ao + r * 10240 + 256); }
#pragma unroll
                        for (int r = 0; r < 6; ++r) mx = vmax4(mx, a[r]);
                        need = mx == SENT;
                    }
                    if (__builtin_amdgcn_ballot_w64(need) == 0ull) break;
                }
                if (w == 0) STAMP(11);
                f4 ta2 = (asf4(a[0]) + asf4(a[2])) + asf4(a[4]), tb2 = (asf4(a[1]) + asf4(a[3])) + asf4(a[5]);
                rb8(ta2, tb2); ta2 *= gaa; tb2 *= gab; rb8(ta2, tb2); xa += ta2; xb += tb2; rb8(xa, xb);
                const float sh = dppf<0x150>(xa[0] + 0.f);
                const f4 da = xa - sh, db = xb - sh, t = da + db, q2 = fma4(da, da, db * db);
                float s1 = (t[0] + t[1]) + (t[2] + t[3]), s2 = (q2[0] + q2[1]) + (q2[2] + q2[3]);
                s1 += dppf<0x128>(s1); s2 += dppf<0x128>(s2); s1 += dppf<0x124>(s1); s2 += dppf<0x124>(s2);
                s1 += dppf<0x122>(s1); s2 += dppf<0x122>(s2); s1 += dppf<0x121>(s1); s2 += dppf<0x121>(s2);
                const float dm1 = s1 * (1.f / 128.f), mw = sh + dm1, v = fmaf(-dm1, s1, s2);
                *(LAS v2u*)(L + L_ST + 64 + g * 48 + w * 8) = v2u{asu(mw), asu(v)};
            }
            BAR();
            if (w < 5) {
                const f4 s0 = *(LAS f4*)(L + L_ST + 64 + g * 48), s1 = *(LAS f4*)(L + L_ST + 64 + g * 48 + 16);
                const f2 s2 = *(LAS f2*)(L + L_ST + 64 + g * 48 + 32);
                const float mean = (((s0[0] + s0[2]) + (s1[0] + s1[2])) + s2[0]) * 0.2f;
                const f4 dm = f4{s0[0], s0[2], s1[0], s1[2]} - mean, dq = dm * dm; const float d4 = s2[0] - mean;
                const float M2 = fmaf(128.f, fmaf(d4, d4, (dq[0] + dq[1]) + (dq[2] + dq[3])), ((s0[1] + s0[3]) + (s1[1] + s1[3])) + s2[1]);
                const float mb = rbf(mean), den = rbf(__builtin_amdgcn_sqrtf(rbf(rbf(M2 * (1.f / DM)) + 1e-5f))), rc = __builtin_amdgcn_rcpf(den);
                f4 ea = xa - mb, eb = xb - mb;
                rb8(ea, eb);
                const f4 qa = ea * rc, qb = eb * rc;
                f4 ha = fma4(fma4(-qa, bc4(den), ea), bc4(rc), qa), hb = fma4(fma4(-qb, bc4(den), eb), bc4(rc), qb);
                rb8(ha, hb); ha *= c1a; hb *= c1b; rb8(ha, hb); ha += sba; hb += sbb;
                *(LAS v2u*)(L + L_HB + g * 1280 + col * 2) = v2u{pack2(ha[0], ha[1]), pack2(ha[2], ha[3])};
                *(LAS v2u*)(L + L_HB + g * 1280 + col * 2 + 128) = v2u{pack2(hb[0], hb[1]), pack2(hb[2], hb[3])};
            }
            BAR();
            if (w == 0) STAMP(12);
            f4 d[4] = {};
#pragma unroll
            for (int n = 0; n < 5; ++n) {
                const v4u a = *(LAS v4u*)(L + L_HB + b4 * 1280 + (160 * (w >> 1) + 32 * n + 8 * g) * 2);
#pragma unroll
                for (int T2 = 0; T2 < 4; ++T2) d[T2] = mfma(ft[n][T2], a, d[T2]);
            }
            *(LAS f4*)(L + L_RED + w * 1024 + l * 16) = sel_tile(d, tq);
            BAR();
            if (w == 0) {
                STAMP(13);
                LAS f4* R = (LAS f4*)(L + L_RED + l * 16);
                const f4 gt = rb4((R[0] + R[128]) + (R[256] + R[384])), up = rb4((R[64] + R[192]) + (R[320] + R[448]));
                const f4 mk = {gt[0] > 0.f ? 1.f : 0.f, gt[1] > 0.f ? 1.f : 0.f, gt[2] > 0.f ? 1.f : 0.f, gt[3] > 0.f ? 1.f : 0.f};
                const f4 hh = (gt * mk) * up;
                st_sc1_2(rws, outo, v2u{canon(pack2(hh[0], hh[1])), canon(pack2(hh[2], hh[3]))});
                STAMP(14);
            }
        } else {
            f4 x1;
            UNDEF4(x1);
            if (w == 0) {
                const u32 ao = WS_AP + (u32)(b4 * DM + 64 * ri + dcol) * 4u;
                f4 s;
                for (int sp = 0; sp < SPIN_G; ++sp) {
                    MEMBAR();
                    const v4u p0 = ld_sc1(rws, ao), p1 = ld_sc1(rws, ao + 10240), p2 = ld_sc1(rws, ao + 20480);
                    s = (asf4(p0) + asf4(p1)) + asf4(p2);
                    if (all_valid(vmax4(vmax4(vmax4(0u, p0), p1), p2))) break;
                }
                x1 = rb4(xa + rb4(gaa * rb4(s)));
                asm volatile("" : "+v"(x1));
                STAMP(15);
            }
            v4u a0, a1;
            UNDEF4(a0); UNDEF4(a1);
            const u32 h0 = WS_HID + (u32)(b4 * FF + 192 * w + 32 * tq + 8 * g) * 2u, h1 = tq < 2 ? h0 + 256 : OOB;
            bool need = true;
            for (int sp = 0; sp < SPIN_G; ++sp) {
                MEMBAR();
                if (need) { a0 = ld_sc1(rws, h0); a1 = ld_sc1(rws, h1); need = vmax4(vmax4(0u, a0), a1) == SENT; }
                if (__builtin_amdgcn_ballot_w64(need) == 0ull) break;
            }
            if (w == 0) STAMP(16);
            const v4u bk[4] = {a0, ror4k<1>(a0), ror4k<2>(a0), ror4k<3>(a0)}, bl[4] = {a1, ror4k<1>(a1), ror4k<2>(a1), ror4k<3>(a1)};
            f4 d[4] = {};
#pragma unroll
            for (int k = 0; k < 4; ++k)
#pragma unroll
                for (int s = 0; s < 6; ++s) d[(s + k) & 3] = mfma(ft[s][(s + k) & 3], s < 4 ? bk[k] : bl[k], d[(s + k) & 3]);
            *(LAS f4*)(L + L_RED + w * 1024 + l * 16) = sel_tile(d, tq);
            BAR();
            if (w == 0) {
                STAMP(17);
                LAS f4* R = (LAS f4*)(L + L_RED + l * 16);
                const f4 o = x1 + rb4(gab * rb4(((R[0] + R[64]) + (R[128] + R[192])) + ((R[256] + R[320]) + (R[384] + R[448]))));
                __builtin_amdgcn_raw_buffer_store_b64(v2u{pack2(o[0], o[1]), pack2(o[2], o[3])}, __builtin_amdgcn_make_buffer_rsrc((void*)p.out, 0, 4 * DM * 2, 0x00020000),
                                                      (u32)(b4 * DM + 64 * ri + dcol) * 2u, 0, 16);
                STAMP(18);
            }
        }
    }
}

typedef const __attribute__((address_space(4))) Params* kargp;
#define GET_PARAMS() kargp pp_ = kp; asm volatile("" : "+s"(pp_)); \
    const Params q_ = {pp_->x, pp_->kc, pp_->vc, pp_->wq, pp_->wo, pp_->wg, pp_->wu, pp_->wd, pp_->c, pp_->wm, pp_->out, pp_->ws};
__global__ void __launch_bounds__(512, 2) fused_block(Params p) {
    extern __shared__ __attribute__((aligned(16))) unsigned char smem[];
    LAS unsigned char* L = (LAS unsigned char*)smem;
    const kargp kp = (kargp)__builtin_amdgcn_kernarg_segment_ptr();
    asm volatile("s_load_dword s98, %0, 0x0\n\ts_load_dword s99, %0, 0x40" :: "s"(kp) : "s98", "s99");
    const int l = threadIdx.x & 63, w = __builtin_amdgcn_readfirstlane(threadIdx.x >> 6), c = blockIdx.x;
    const int x = c & 7, y = c >> 3;
    if (__builtin_expect(c >= NW, 1)) { GET_PARAMS(); kv_path(q_, L, c - NW, w, l); }
    else if (y >= 5) { GET_PARAMS(); w_path<R_SB>(q_, L, 30 + 8 * (y - 5) + x, w, l); }
    else if (x < 6) { GET_PARAMS(); w_path<R_SA>(q_, L, 10 * (x >> 1) + 5 * (x & 1) + y, w, l); }
    else { GET_PARAMS(); w_path<R_SC>(q_, L, 54 + 5 * (x - 6) + y, w, l); }
}

extern "C" void kernel_launch(void* const* d_in, const int* in_sizes, int n_in, void* d_out, int out_size, void* d_ws, size_t ws_size, hipStream_t stream) {
    Params p;
    p.x = (const u16*)d_in[0]; p.kc = (const u16*)d_in[1]; p.vc = (const u16*)d_in[2]; p.wq = (const u16*)d_in[3]; p.wo = (const u16*)d_in[4];
    p.wg = (const u16*)d_in[6]; p.wu = (const u16*)d_in[7]; p.wd = (const u16*)d_in[8]; p.c = (const u16*)d_in[9]; p.wm = (const u16*)d_in[10];
    p.out = (u16*)d_out; p.ws = (unsigned char*)d_ws;
    (void)hipFuncSetAttribute((const void*)fused_block, hipFuncAttributeMaxDynamicSharedMemorySize, (int)L_END);
    hipLaunchKernelGGL(fused_block, dim3(256), dim3(512), L_END, stream, p);
}
```

```cpp
#include <hip/hip_runtime.h>
#include <stdint.h>
#pragma clang fp contract(off)

#define KSYM "_Z11fused_block6Params"
typedef unsigned short u16;
typedef unsigned u32;
typedef unsigned long long u64;
typedef u32 v4u __attribute__((ext_vector_type(4)));
typedef u32 v2u __attribute__((ext_vector_type(2)));
typedef float f4 __attribute__((ext_vector_type(4)));
typedef short v4s __attribute__((ext_vector_type(4)));
typedef __bf16 bf8 __attribute__((ext_vector_type(8)));
typedef __bf16 bf2 __attribute__((ext_vector_type(2)));
#define LAS __attribute__((address_space(3)))
#define GAS __attribute__((address_space(1)))
#define RLX_AGENT __ATOMIC_RELAXED, __HIP_MEMORY_SCOPE_AGENT

constexpr int DM = 640, NH = 32, SKV = 4096, DH = 192, WIN = 1024, FF = 1536, NQ = NH * DH;
constexpr u32 SENT = 0xFFFFFFFFu;
constexpr int NW = 64;
constexpr u32 WS_E = 0, WS_FLAG = 4096, WS_F1P = 8192, WS_MODF = WS_F1P + 5 * 4 * 1280 * 4, WS_QP = WS_MODF + 4 * 2560 * 2,
              WS_OP = WS_QP + 5 * 4 * NQ * 4, WS_ML = WS_OP + 128 * 2 * DH * 2, WS_AP = WS_ML + 32 * 4 * 16, WS_HID = WS_AP + 3 * 4 * DM * 4,
              WS_END = WS_HID + 4 * FF * 2;
constexpr u32 OOB = 0x7FFFFF00u;
constexpr u32 L_XS = 131072, L_CS = L_XS + 5120, L_HB = L_CS + 5120, L_RED = L_HB + 5120, L_ST = L_RED + 8192, L_GA = L_ST + 256, L_GB = L_GA + 1024,
              L_X1 = L_GB + 1024, L_MISC = L_X1 + 1024, L_STAMP = L_MISC + 64, L_END = L_STAMP + 256;
constexpr u32 K_W = 18432, K_PX = 1024, K_PX2 = 1536, K_V = 9216, K_QS = 8 * K_W, K_ML = K_QS + 768, K_END = K_ML + 64;
static_assert(K_END <= L_MISC, "LDS");
constexpr float LOG2E = 1.4426950408889634f;
static_assert(L_END <= 163840, "LDS");
constexpr int SPIN_G = 40000, SPIN_L = 2000000;
constexpr u32 CODE_BYTES = 47104;
constexpr int VSPL = 4;
constexpr int PRE_B = 2, PRE_C = 0, JIT_B = 24, JIT_C = 200;

#define STAMP(i) do {} while (0)

struct Params { const u16 *x, *kc, *vc, *wq, *wo, *wg, *wu, *wd, *c, *wm; u16* out; unsigned char* ws; };

__device__ __forceinline__ v4u mk_srd(const void* p, u32 bytes) { const u64 a = (u64)p; return v4u{(u32)a, (u32)(a >> 32) & 0xffffu, bytes, 0x00020000u}; }
__device__ __forceinline__ float asf(u32 u) { return __builtin_bit_cast(float, u); }
__device__ __forceinline__ u32 asu(float f) { return __builtin_bit_cast(u32, f); }
__device__ __forceinline__ float bflo(u32 u) { return asf(u << 16); }
__device__ __forceinline__ float bfhi(u32 u) { return asf(u & 0xffff0000u); }
__device__ __forceinline__ u32 pack2(float lo, float hi) { bf2 v; v[0] = (__bf16)lo; v[1] = (__bf16)hi; return __builtin_bit_cast(u32, v); }
__device__ __forceinline__ float rbf(float f) { return (float)(__bf16)f; }
typedef float f2 __attribute__((ext_vector_type(2)));
__device__ __forceinline__ f4 rb4(f4 v) {
    float a = v[0], b = v[1], c = v[2], d = v[3];
    asm("v_cvt_pk_bf16_f32 %0, 0, %0\n\tv_cvt_pk_bf16_f32 %1, 0, %1\n\tv_cvt_pk_bf16_f32 %2, 0, %2\n\tv_cvt_pk_bf16_f32 %3, 0, %3" : "+v"(a), "+v"(b), "+v"(c), "+v"(d));
    return f4{a, b, c, d}; }
__device__ __forceinline__ void rb8(f4& u, f4& v) {
    float a = u[0], b = u[1], c = u[2], d = u[3], e = v[0], f = v[1], g = v[2], h = v[3];
    asm("v_cvt_pk_bf16_f32 %0, 0, %0\n\tv_cvt_pk_bf16_f32 %1, 0, %1\n\tv_cvt_pk_bf16_f32 %2, 0, %2\n\tv_cvt_pk_bf16_f32 %3, 0, %3\n\t"
        "v_cvt_pk_bf16_f32 %4, 0, %4\n\tv_cvt_pk_bf16_f32 %5, 0, %5\n\tv_cvt_pk_bf16_f32 %6, 0, %6\n\tv_cvt_pk_bf16_f32 %7, 0, %7"
        : "+v"(a), "+v"(b), "+v"(c), "+v"(d), "+v"(e), "+v"(f), "+v"(g), "+v"(h));
    u = f4{a, b, c, d}; v = f4{e, f, g, h}; }
__device__ __forceinline__ f4 unpk(v2u v) { return f4{bflo(v[0]), bfhi(v[0]), bflo(v[1]), bfhi(v[1])}; }
__device__ __forceinline__ f4 fma4(f4 a, f4 b, f4 c) { return __builtin_elementwise_fma(a, b, c); }
__device__ __forceinline__ f4 bc4(float x) { return f4{x, x, x, x}; }
__device__ __forceinline__ u32 canon(u32 u) { return u < 0xFFFFFFFEu ? u : 0xFFFFFFFEu; }
__device__ __forceinline__ v4u canon4(f4 v) { return v4u{canon(asu(v[0])), canon(asu(v[1])), canon(asu(v[2])), canon(asu(v[3]))}; }
__device__ __forceinline__ u32 umax3(u32 a, u32 b, u32 c) { const u32 m = a > b ? a : b; return m > c ? m : c; }
__device__ __forceinline__ u32 vmax4(u32 m, v4u v) { return umax3(umax3(m, v[0], v[1]), v[2], v[3]); }
__device__ __forceinline__ bool all_valid(u32 m) { return __builtin_amdgcn_ballot_w64(m == SENT) == 0ull; }
__device__ __forceinline__ float dot2(u32 a, u32 b, float c) { return __builtin_amdgcn_fdot2_f32_bf16(__builtin_bit_cast(bf2, a), __builtin_bit_cast(bf2, b), c, false); }
__device__ __forceinline__ f4 mfma(v4u a, v4u b, f4 c) { return __builtin_amdgcn_mfma_f32_16x16x32_bf16(__builtin_bit_cast(bf8, a), __builtin_bit_cast(bf8, b), c, 0, 0, 0); }
template <int CTRL> __device__ __forceinline__ u32 dppu(u32 x) { return (u32)__builtin_amdgcn_mov_dpp((int)x, CTRL, 0xf, 0xf, true); }
template <int CTRL> __device__ __forceinline__ float dppf(float x) { return asf(dppu<CTRL>(asu(x))); }
template <int K> __device__ __forceinline__ v4u ror4k(v4u v) {
    if constexpr (K == 0) return v; else return v4u{dppu<0x120 + 4 * K>(v[0]), dppu<0x120 + 4 * K>(v[1]), dppu<0x120 + 4 * K>(v[2]), dppu<0x120 + 4 * K>(v[3])}; }
__device__ __forceinline__ float row_sum(float x) { x += dppf<0x128>(x); x += dppf<0x124>(x); x += dppf<0x122>(x); x += dppf<0x121>(x); return x; }
__device__ __forceinline__ float row_max(float x) { x = fmaxf(x, dppf<0x128>(x)); x = fmaxf(x, dppf<0x124>(x)); x = fmaxf(x, dppf<0x122>(x)); x = fmaxf(x, dppf<0x121>(x)); return x; }
__device__ __forceinline__ float xrow_sum(float x) {
    auto s = __builtin_amdgcn_permlane16_swap(asu(x), asu(x), false, false); x = asf(s[0]) + asf(s[1]);
    auto t = __builtin_amdgcn_permlane32_swap(asu(x), asu(x), false, false); return asf(t[0]) + asf(t[1]); }
__device__ __forceinline__ float xrow_max(float x) {
    auto s = __builtin_amdgcn_permlane16_swap(asu(x), asu(x), false, false); x = fmaxf(asf(s[0]), asf(s[1]));
    auto t = __builtin_amdgcn_permlane32_swap(asu(x), asu(x), false, false); return fmaxf(asf(t[0]), asf(t[1])); }
__device__ __forceinline__ float wave_sum(float x) { return xrow_sum(row_sum(x)); }
__device__ __forceinline__ float wave_max(float x) { return xrow_max(row_max(x)); }
__device__ __forceinline__ float fdiv(float a, float b) { const float r = __builtin_amdgcn_rcpf(b); const float q = a * r; return fmaf(fmaf(-q, b, a), r, q); }
__device__ __forceinline__ float silu_bf(float v) { return fdiv(v, 1.f + __builtin_amdgcn_exp2f(fminf(-v * 1.4426950408889634f, 126.f))); }
__device__ __forceinline__ v4u silu8(v4u c) {
    v4u r;
#pragma unroll
    for (int i = 0; i < 4; ++i) r[i] = pack2(silu_bf(bflo(c[i])), silu_bf(bfhi(c[i])));
    return r; }
#define BAR() asm volatile("s_waitcnt lgkmcnt(0)\n\ts_barrier" ::: "memory")
#define LGKM0() asm volatile("s_waitcnt lgkmcnt(0)" ::: "memory")
#define VMW(n) asm volatile("s_waitcnt vmcnt(%0)" :: "i"(n) : "memory")
#define MEMBAR() asm volatile("" ::: "memory")
typedef __amdgpu_buffer_rsrc_t rsrc_t;
__device__ __forceinline__ v4u ld_sc1(rsrc_t r, u32 off) { return __builtin_amdgcn_raw_buffer_load_b128(r, off, 0, 16); }
__device__ __forceinline__ v2u ld_sc1_2(rsrc_t r, u32 off) { return __builtin_amdgcn_raw_buffer_load_b64(r, off, 0, 16); }
__device__ __forceinline__ void st_sc1(rsrc_t r, u32 off, v4u v) { __builtin_amdgcn_raw_buffer_store_b128(v, r, off, 0, 16); }
__device__ __forceinline__ void st_sc1_2(rsrc_t r, u32 off, v2u v) { __builtin_amdgcn_raw_buffer_store_b64(v, r, off, 0, 16); }
__device__ __forceinline__ f4 asf4(v4u v) { return __builtin_bit_cast(f4, v); }

__device__ __forceinline__ void dma_unit(v4u srd, u32 v0, u32 v1, u32 sA, u32 sB, u32 lds) {
    u32 keep;
    asm volatile("s_nop 4\n\ts_mov_b32 %0, m0\n\ts_mov_b32 m0, %6\n\ts_nop 0\n\t"
                 "buffer_load_dwordx4 %1, %3, %4 offen nt lds\n\ts_add_u32 m0, m0, 0x400\n\ts_nop 0\n\t"
                 "buffer_load_dwordx4 %2, %3, %4 offen nt lds\n\ts_add_u32 m0, m0, 0x400\n\ts_nop 0\n\t"
                 "buffer_load_dwordx4 %1, %3, %5 offen nt lds\n\ts_add_u32 m0, m0, 0x400\n\ts_nop 0\n\t"
                 "buffer_load_dwordx4 %2, %3, %5 offen nt lds\n\ts_mov_b32 m0, %0"
                 : "=&s"(keep) : "v"(v0), "v"(v1), "s"(srd), "s"(sA), "s"(sB), "s"(lds) : "memory", "scc");
}
__device__ __forceinline__ void dma_1k(v4u srd, u32 voff, u32 soff, u32 lds) {
    u32 keep;
    asm volatile("s_nop 4\n\ts_mov_b32 %0, m0\n\ts_mov_b32 m0, %4\n\ts_nop 0\n\tbuffer_load_dwordx4 %1, %2, %3 offen lds\n\ts_mov_b32 m0, %0"
                 : "=&s"(keep) : "v"(voff), "s"(srd), "s"(soff), "s"(lds) : "memory");
}
struct WStream { v4u srd; u32 base, pitch, v0, v1; };
__device__ __forceinline__ WStream mk_stream(v4u srd, u32 base, u32 pitch, int l) {
    WStream s; s.srd = srd; s.base = base; s.pitch = pitch;
    const u32 sw = 2u * ((l >> 4) & 1);
    s.v0 = (l >> 3) * pitch + (((l & 7) ^ sw) << 4);
    s.v1 = (8 + (l >> 3)) * pitch + (((l & 7) ^ (sw | 4u)) << 4);
    return s; }
__device__ __forceinline__ void issue(const WStream& s, int n, u32 lds) { const u32 a = s.base + (u32)n * 32u * s.pitch; dma_unit(s.srd, s.v0, s.v1, a, a + 16u * s.pitch, lds); }
__device__ __forceinline__ void read_unit(LAS unsigned char* L, const u32 (&ta)[4], int slot, v4u (&f)[4]) {
#pragma unroll
    for (int T = 0; T < 4; ++T) {
        const v2u lo = __builtin_bit_cast(v2u, __builtin_amdgcn_ds_read_tr16_b64_v4i16((LAS v4s*)(L + ta[T] + slot * 4096)));
        const v2u hi = __builtin_bit_cast(v2u, __builtin_amdgcn_ds_read_tr16_b64_v4i16((LAS v4s*)(L + ta[T] + slot * 4096 + 512)));
        f[T] = v4u{lo[0], lo[1], hi[0], hi[1]};
    } }
__device__ __forceinline__ f4 sel_tile(const f4 (&a)[4], int t) {
    const bool b0 = t & 1, b1 = t & 2;
    f4 r;
#pragma unroll
    for (int i = 0; i < 4; ++i) { const float lo = b0 ? a[1][i] : a[0][i], hi = b0 ? a[3][i] : a[2][i]; r[i] = b1 ? hi : lo; }
    return r; }

__device__ __forceinline__ void dma_kv(v4u srd, u32 vo, u32 so, u32 lds) {
    u32 keep;
    asm volatile("s_nop 4\n\ts_mov_b32 %0, m0\n\ts_mov_b32 m0, %4\n\ts_nop 0\n\t"
                 "buffer_load_dwordx4 %1, %2, %3 offen nt lds\n\ts_add_u32 m0, m0, 0x380\n\ts_nop 0\n\t"
                 "buffer_load_dwordx4 %1, %2, %3 offen offset:128 nt lds\n\ts_add_u32 m0, m0, 0x380\n\ts_nop 0\n\t"
                 "buffer_load_dwordx4 %1, %2, %3 offen offset:256 nt lds\n\ts_mov_b32 m0, %0"
                 : "=&s"(keep) : "v"(vo), "s"(srd), "s"(so), "s"(lds) : "memory", "scc");
}
__device__ __forceinline__ float qk_unit(v4u k0, v4u k1, v4u k2, const v4u (&q)[3]) {
    float s = 0.f, t = 0.f;
#pragma unroll
    for (int r = 0; r < 4; r += 2) { s = dot2(k0[r], q[0][r], s); t = dot2(k0[r + 1], q[0][r + 1], t); }
#pragma unroll
    for (int r = 0; r < 4; r += 2) { s = dot2(k1[r], q[1][r], s); t = dot2(k1[r + 1], q[1][r + 1], t); }
#pragma unroll
    for (int r = 0; r < 4; r += 2) { s = dot2(k2[r], q[2][r], s); t = dot2(k2[r + 1], q[2][r + 1], t); }
    return s + t;
}
__device__ __forceinline__ void pv_unit(v4u v0, v4u v1, v4u v2, u32 plo, float (&acc)[24]) {
    const u32 phi = plo << 16;
#pragma unroll
    for (int r = 0; r < 4; ++r) {
        acc[2 * r] = dot2(v0[r], plo, acc[2 * r]); acc[2 * r + 1] = dot2(v0[r], phi, acc[2 * r + 1]);
        acc[8 + 2 * r] = dot2(v1[r], plo, acc[8 + 2 * r]); acc[8 + 2 * r + 1] = dot2(v1[r], phi, acc[8 + 2 * r + 1]);
        acc[16 + 2 * r] = dot2(v2[r], plo, acc[16 + 2 * r]); acc[16 + 2 * r + 1] = dot2(v2[r], phi, acc[16 + 2 * r + 1]);
    }
    asm volatile("" : "+v"(acc[0]), "+v"(acc[1]), "+v"(acc[2]), "+v"(acc[3]), "+v"(acc[4]), "+v"(acc[5]), "+v"(acc[6]), "+v"(acc[7]), "+v"(acc[8]), "+v"(acc[9]), "+v"(acc[10]), "+v"(acc[11]),
                      "+v"(acc[12]), "+v"(acc[13]), "+v"(acc[14]), "+v"(acc[15]), "+v"(acc[16]), "+v"(acc[17]), "+v"(acc[18]), "+v"(acc[19]), "+v"(acc[20]), "+v"(acc[21]), "+v"(acc[22]), "+v"(acc[23]));
}

template <bool B> struct BoolC { static constexpr bool value = B; };
__device__ __forceinline__ void kv_path(const Params& p, LAS unsigned char* L, int j, int w, int l) {
    const rsrc_t rws = __builtin_amdgcn_make_buffer_rsrc((void*)p.ws, 0, WS_END, 0x00020000);
    const rsrc_t rk = __builtin_amdgcn_make_buffer_rsrc((void*)p.kc, 0, 4 * NH * SKV * DH * 2, 0x00020000);
    const rsrc_t rv = __builtin_amdgcn_make_buffer_rsrc((void*)p.vc, 0, 4 * NH * SKV * DH * 2, 0x00020000);
    volatile LAS u32* misc = (volatile LAS u32*)(L + L_MISC);
    const int r8 = j >> 3, rk3 = (r8 * 171) >> 9, pos = r8 - 3 * rk3, t3 = (j & 7) * 8 + rk3;
    const int is1 = pos & 1, is2 = pos >> 1;
    const int slot = w & is1, bh = 2 * t3 + (is2 | slot), wi = w >> is1, ng = 4 >> is1;
    const int u1 = 43 * is2 + 85 * (is1 & ~slot) + wi + (wi >= ng ? 10 * ng : 0);
    const u32 ust = 12288u >> is1;
    const u32 vo = (l >> 3) * 384 + (l & 7) * 16;
    const u32 base = ((u32)bh * SKV + (SKV - WIN)) * 384u + (u32)u1 * 3072u;
    const u32 wl = w * K_W, lb = (u32)(size_t)L + wl;
    const v4u sk = mk_srd(p.kc, 4 * NH * SKV * DH * 2), sv = mk_srd(p.vc, 4 * NH * SKV * DH * 2);
    STAMP(0);
    dma_kv(sk, vo, base + 8 * ust, lb); dma_kv(sv, vo, base + 8 * ust, lb + K_V);
    const int part = is2 | (is1 & ~slot);
    const int nl = wi >= 5 - 2 * is1 ? 2 : 3;
    const bool lead = w == 0 || (w == 1 && is1);
    const u32 opb = WS_OP + (bh * 2 + part) * 384, mlb = WS_ML + ((bh & 31) * 4 + (bh >> 5)) * 16 + part * 8;
    if (threadIdx.x < 12) misc[threadIdx.x] = 0;
    BAR();
    if (__builtin_expect(w == 4, 0)) {
        __builtin_amdgcn_s_setprio(3);
        const u64 T = __hip_atomic_load((GAS u64*)(p.ws + WS_E), RLX_AGENT) + 1ull;
        st_sc1(rws, l < 24 ? opb + l * 16 : OOB, v4u{SENT, SENT, SENT, SENT});
        st_sc1_2(rws, l == 0 ? mlb : OOB, v2u{SENT, SENT});
        const int bh1 = 2 * t3 + 1;
        st_sc1(rws, (pos == 1 && l < 24) ? WS_OP + bh1 * 2 * 384 + l * 16 : OOB, v4u{SENT, SENT, SENT, SENT});
        st_sc1_2(rws, (pos == 1 && l == 0) ? WS_ML + ((bh1 & 31) * 4 + (bh1 >> 5)) * 16 : OOB, v2u{SENT, SENT});
        VMW(0);
        if (l == 0) {
            __hip_atomic_store((GAS u64*)(p.ws + WS_FLAG) + blockIdx.x, T, RLX_AGENT);
            misc[4] = (u32)T; misc[5] = (u32)(T >> 32); LGKM0(); misc[2] = 1;
        }
        STAMP(9);
        __builtin_amdgcn_s_setprio(0);
    }
    const int c = l & 7;
    float m, lsum, fsc;
    const u32 nslot = pos == 1 ? 4u : 8u;
    auto slot_stats = [&](float& M, float& mi, float& li) {
        const v2u t = *(LAS v2u*)(L + K_ML + c * 8);
        const bool mine = pos != 1 || (l & 1) == slot;
        mi = mine ? asf(t[0]) : -INFINITY; li = mine ? asf(t[1]) : 0.f;
        M = fmaxf(mi, dppf<0xB1>(mi)); M = fmaxf(M, dppf<0x4E>(M)); M = fmaxf(M, dppf<0x141>(M));
    };
    auto post = [&]() {
        u32 old = 0;
        if (l == 0) { *(LAS v2u*)(L + K_ML + w * 8) = v2u{asu(m), asu(lsum)}; LGKM0(); old = __hip_atomic_fetch_add((LAS u32*)(L + L_MISC + 32 + slot * 4), 1u, __ATOMIC_RELAXED, __HIP_MEMORY_SCOPE_WORKGROUP); }
        if (__builtin_amdgcn_readfirstlane(old) == nslot - 1u) {
            MEMBAR();
            float M, mi, li; slot_stats(M, mi, li);
            float e = li * __builtin_amdgcn_exp2f((mi - M) * LOG2E);
            e += dppf<0xB1>(e); e += dppf<0x4E>(e); e += dppf<0x141>(e);
            st_sc1_2(rws, l == 0 ? mlb : OOB, v2u{canon(asu(M)), canon(asu(e))});
            STAMP(5);
        }
    };
    auto scale = [&]() {
        for (int sp = 0; misc[8 + slot] < nslot && sp < SPIN_L; ++sp) __builtin_amdgcn_s_sleep(1);
        MEMBAR();
        float M, mi, li; slot_stats(M, mi, li);
        float e = li * __builtin_amdgcn_exp2f((mi - M) * LOG2E);
        e += dppf<0xB1>(e); e += dppf<0x4E>(e); e += dppf<0x141>(e);
        fsc = fdiv(__builtin_amdgcn_exp2f((m - M) * LOG2E), e);
    };
    dma_kv(sk, vo, base + 9 * ust, lb + 3072); dma_kv(sv, vo, base + 9 * ust, lb + K_V + 3072);
    if (nl == 3) { dma_kv(sk, vo, base + 10 * ust, lb + 6144); dma_kv(sv, vo, base + 10 * ust, lb + K_V + 6144); }
    v4u K[24], V[24];
#pragma unroll
    for (int jj = 0; jj < 8; ++jj)
#pragma unroll
        for (int i = 0; i < 3; ++i) K[jj * 3 + i] = __builtin_amdgcn_raw_buffer_load_b128(rk, vo + i * 128, base + jj * ust, 2);
#pragma unroll
    for (int jj = 0; jj < VSPL; ++jj)
#pragma unroll
        for (int i = 0; i < 3; ++i) V[jj * 3 + i] = __builtin_amdgcn_raw_buffer_load_b128(rv, vo + i * 128, base + jj * ust, 2);
    __builtin_amdgcn_sched_barrier(0);
    const bool early = __builtin_amdgcn_readfirstlane(misc[slot]) != 0u;
    float acc[24];
    auto attend = [&](auto ec) {
        constexpr bool EARLY = decltype(ec)::value;
        if (!EARLY) {
#pragma unroll
            for (int jj = VSPL; jj < 8; ++jj)
#pragma unroll
                for (int i = 0; i < 3; ++i) V[jj * 3 + i] = __builtin_amdgcn_raw_buffer_load_b128(rv, vo + i * 128, base + jj * ust, 2);
            __builtin_amdgcn_sched_barrier(0);
            if (w == 0) STAMP(1);
            if (w == 7) STAMP(2);
            if (w < 4) {
                for (int sp = 0; misc[2] == 0 && sp < SPIN_L; ++sp) __builtin_amdgcn_s_sleep(1);
                const u32 tlo = misc[4], thi = misc[5];
                for (int sp = 0; sp < SPIN_G && misc[6] == 0; ++sp) {
                    MEMBAR();
                    const v2u f = ld_sc1_2(rws, WS_FLAG + l * 8);
                    if (__builtin_amdgcn_ballot_w64(f[0] != tlo || f[1] != thi) == 0ull) misc[6] = 1;
                }
                const u32 qo = WS_QP + (u32)((bh >> 5) * NQ + (bh & 31) * DH + 4 * l) * 4u;
                v4u a[5] = {};
                bool need = l < 48;
                for (int sp = 0; sp < SPIN_G; ++sp) {
                    MEMBAR();
                    if (__builtin_amdgcn_readfirstlane(misc[slot]) != 0u) break;
                    if (need) {
                        u32 mx = 0;
#pragma unroll
                        for (int ks = 0; ks < 5; ++ks) a[ks] = ld_sc1(rws, qo + ks * (4 * NQ * 4));
#pragma unroll
                        for (int ks = 0; ks < 5; ++ks) mx = vmax4(mx, a[ks]);
                        need = mx == SENT;
                    }
                    if (__builtin_amdgcn_ballot_w64(need) == 0ull || sp == SPIN_G - 1) {
                        const f4 qs = (((asf4(a[0]) + asf4(a[1])) + asf4(a[2])) + asf4(a[3])) + asf4(a[4]);
                        if (l < 48) *(LAS v2u*)(L + K_QS + slot * 384 + l * 8) = v2u{pack2(qs[0], qs[1]), pack2(qs[2], qs[3])};
                        LGKM0();
                        misc[slot] = 1;
                        break;
                    }
                }
                if (w == 0) STAMP(3);
            } else {
                for (int sp = 0; misc[slot] == 0 && sp < SPIN_L; ++sp) __builtin_amdgcn_s_sleep(1);
            }
        }
        VMW(EARLY ? 24 + 3 * VSPL : 48);
        v4u p0, p1, p2;
        {
            v4u q[3];
#pragma unroll
            for (int i = 0; i < 3; ++i) q[i] = *(LAS v4u*)(L + K_QS + slot * 384 + (c + 8 * i) * 16);
            float y[4], x[8];
            LAS v4u* KL = (LAS v4u*)(L + wl + l * 16);
#pragma unroll
            for (int jj = 0; jj < 3; ++jj) { y[jj] = qk_unit(KL[jj * 192], KL[jj * 192 + 64], KL[jj * 192 + 128], q); __builtin_amdgcn_sched_barrier(0); }
#pragma unroll
            for (int jj = 0; jj < 8; jj += 2) { x[jj] = qk_unit(K[3 * jj], K[3 * jj + 1], K[3 * jj + 2], q); x[jj + 1] = qk_unit(K[3 * jj + 3], K[3 * jj + 4], K[3 * jj + 5], q); __builtin_amdgcn_sched_barrier(0); }
#pragma unroll
            for (int h = 0; h < 4; ++h) { const float a = x[h] + dppf<0x141>(x[h]), b = x[h + 4] + dppf<0x141>(x[h + 4]); x[h] = (c & 4) ? b : a; }
            { const float a = y[0] + dppf<0x141>(y[0]), b = y[2] + dppf<0x141>(y[2]), a1 = y[1] + dppf<0x141>(y[1]); y[0] = (c & 4) ? b : a; y[1] = a1; }
#pragma unroll
            for (int h = 0; h < 2; ++h) { const float a = x[h] + dppf<0x4E>(x[h]), b = x[h + 2] + dppf<0x4E>(x[h + 2]); x[h] = (c & 2) ? b : a; }
            { const float a = y[0] + dppf<0x4E>(y[0]), b = y[1] + dppf<0x4E>(y[1]); y[0] = (c & 2) ? b : a; }
            { const float a = x[0] + dppf<0xB1>(x[0]), b = x[1] + dppf<0xB1>(x[1]); x[0] = (c & 1) ? b : a; }
            y[0] += dppf<0xB1>(y[0]);
            const float s1 = rbf(rbf(x[0]) * 0.07216878364870322f);
            const float s2 = (c >> 1) < nl ? rbf(rbf(y[0]) * 0.07216878364870322f) : -INFINITY;
            m = wave_max(fmaxf(s1, s2));
            const float e1 = __builtin_amdgcn_exp2f((s1 - m) * LOG2E), e2 = __builtin_amdgcn_exp2f((s2 - m) * LOG2E);
            *(LAS u32*)(L + wl + K_PX + l * 4) = pack2(e1, 0.f);
            *(LAS u32*)(L + wl + K_PX2 + (l >> 1) * 4) = pack2(e2, 0.f);
            lsum = wave_sum(e1 + ((c & 1) ? 0.f : e2));
            post();
            LGKM0();
            p0 = *(LAS v4u*)(L + wl + K_PX + (l >> 3) * 32); p1 = *(LAS v4u*)(L + wl + K_PX + (l >> 3) * 32 + 16); p2 = *(LAS v4u*)(L + wl + K_PX2 + (l >> 3) * 16);
        }
        if (EARLY) {
            __builtin_amdgcn_sched_barrier(0);
#pragma unroll
            for (int jj = VSPL; jj < 8; ++jj)
#pragma unroll
                for (int i = 0; i < 3; ++i) V[jj * 3 + i] = __builtin_amdgcn_raw_buffer_load_b128(rv, vo + i * 128, base + jj * ust, 2);
            __builtin_amdgcn_sched_barrier(0);
        }
        if (w == 0) STAMP(4);
#pragma unroll
        for (int i = 0; i < 24; ++i) acc[i] = 0.f;
        {
            LAS v4u* VL = (LAS v4u*)(L + wl + K_V + l * 16);
            pv_unit(VL[0], VL[64], VL[128], p2[0], acc);
            pv_unit(VL[192], VL[256], VL[320], p2[1], acc);
            if (nl == 3) pv_unit(VL[384], VL[448], VL[512], p2[2], acc);
        }
#pragma unroll
        for (int jj = 0; jj < 6; ++jj) pv_unit(V[3 * jj], V[3 * jj + 1], V[3 * jj + 2], jj < 4 ? p0[jj & 3] : p1[jj & 3], acc);
        scale();
#pragma unroll
        for (int jj = 6; jj < 8; ++jj) pv_unit(V[3 * jj], V[3 * jj + 1], V[3 * jj + 2], p1[jj & 3], acc);
    };
    if (early) attend(BoolC<true>{}); else attend(BoolC<false>{});
    if (w == 7) STAMP(6);
    float y[12], z[6];
#pragma unroll
    for (int n = 0; n < 12; ++n) { auto s = __builtin_amdgcn_permlane32_swap(asu(acc[n]), asu(acc[n + 12]), false, false); y[n] = asf(s[0]) + asf(s[1]); }
#pragma unroll
    for (int n = 0; n < 6; ++n) { auto s = __builtin_amdgcn_permlane16_swap(asu(y[n]), asu(y[n + 6]), false, false); z[n] = asf(s[0]) + asf(s[1]); }
#pragma unroll
    for (int n = 0; n < 3; ++n) {
        const float a = z[n] + dppf<0x128>(z[n]), b = z[n + 3] + dppf<0x128>(z[n + 3]);
        const int ai = 3 * (l >> 3) + n;
        *(LAS float*)(L + wl + (64 * (ai >> 3) + 8 * c + (ai & 7)) * 4) = ((l & 8) ? b : a) * fsc;
    }
    BAR();
    if (w == 0) STAMP(7);
    if (lead) {
        const u32 pb = slot * K_W + (l < 48 ? l : 47) * 16;
        f4 o = (*(LAS f4*)(L + pb) + *(LAS f4*)(L + pb + 2 * K_W)) + (*(LAS f4*)(L + pb + 4 * K_W) + *(LAS f4*)(L + pb + 6 * K_W));
        if (pos != 1) o += (*(LAS f4*)(L + pb + K_W) + *(LAS f4*)(L + pb + 3 * K_W)) + (*(LAS f4*)(L + pb + 5 * K_W) + *(LAS f4*)(L + pb + 7 * K_W));
        st_sc1_2(rws, l < 48 ? opb + l * 8 : OOB, v2u{canon(pack2(o[0], o[1])), canon(pack2(o[2], o[3]))});
        if (w == 0) STAMP(8); else STAMP(10);
    }
}

enum { R_SA = 0, R_SB = 1, R_SC = 2 };
#define UNDEF4(v) asm volatile("" : "=v"(v))
template <int ROLE>
__device__ __forceinline__ void w_path(const Params& p, LAS unsigned char* L, int u, int w, int l) {
    const rsrc_t rws = __builtin_amdgcn_make_buffer_rsrc((void*)p.ws, 0, WS_END, 0x00020000);
    const int tid = threadIdx.x, g = l >> 4, tq = (l & 15) >> 2, b4 = l & 3;
    const u32 lbase = (u32)(size_t)L, stg = lbase + w * 16384;
    const bool hasF1 = u < 50, hasF2 = u < 60;
    const int kf = u / 10, lp = u - 10 * kf, kf2 = u / 12, cs = u - 12 * kf2;
    const int ri = ROLE == R_SA ? u : (ROLE == R_SB ? u - 30 : u - 54);
    const WStream sF1 = mk_stream(mk_srd(p.wm, hasF1 ? DM * 3840 * 2 : 0), (u32)((128 * kf + 32 * (w >> 1)) * 7680 + (128 * lp + 64 * (w & 1)) * 2), 7680, l);
    const WStream sF2 = mk_stream(mk_srd(p.wq, hasF2 ? DM * NQ * 2 : 0), (u32)(128 * kf2 * 12288 + (512 * cs + 64 * w) * 2), 12288, l);
    const int mid = ROLE == R_SC ? (w < 4 ? (ri >= 6 ? ri : -1) : 30 + ri) : (w < 4 ? (ri < 6 ? ri : ri + 4) : (ri == 20 ? 28 : (ri == 21 ? 29 : -1)));
    const bool hasM = ROLE != R_SA && mid >= 0;
    const WStream sM = mk_stream(mk_srd(p.wm, hasM ? DM * 3840 * 2 : 0), (u32)(160 * (w & 3) * 7680 + (1280 + 64 * mid) * 2), 7680, l);
    const int ra = ri / 10, ca = ri - 10 * ra;
    const WStream sT = ROLE == R_SA ? mk_stream(mk_srd(p.wo, NQ * DM * 2), (u32)((2048 * ra + 256 * w) * 1280 + ca * 128), 1280, l)
                     : ROLE == R_SB ? mk_stream(mk_srd((w & 1) ? p.wu : p.wg, DM * FF * 2), (u32)(160 * (w >> 1) * 3072 + ri * 128), 3072, l)
                                    : mk_stream(mk_srd(p.wd, FF * DM * 2), (u32)(192 * w * 1280 + ri * 128), 1280, l);
    constexpr int NM = ROLE == R_SA ? 0 : 5, NT = ROLE == R_SA ? 8 : (ROLE == R_SB ? 5 : 6), NA = NM + NT;
    u32 ta[4];
    { const u32 f = 2u * (tq >> 1) | 4u * (g & 1);
#pragma unroll
      for (int T = 0; T < 4; ++T) ta[T] = w * 16384 + (8 * g + tq) * 128 + ((((u32)(2 * T)) ^ f) + (b4 >> 1)) * 16 + (b4 & 1) * 8; }
    const u32 dcol = 16 * tq + 4 * g;
    const u32 f1o = hasF1 ? WS_F1P + (u32)((kf * 4 + b4) * 1280 + 128 * lp + 64 * w + dcol) * 4u : OOB;
    const u32 qpo = hasF2 ? WS_QP + (u32)((kf2 * 4 + b4) * NQ + 512 * cs + 64 * w + dcol) * 4u : OOB;
    const u32 mfo = hasM ? WS_MODF + (u32)(b4 * 2560 + 64 * mid + dcol) * 2u : OOB;
    const u32 outo = ROLE == R_SA ? WS_AP + (u32)((ra * 4 + b4) * DM + 64 * ca + dcol) * 4u : WS_HID + (u32)(b4 * FF + 64 * ri + dcol) * 2u;
    volatile LAS u32* misc = (volatile LAS u32*)(L + L_MISC);
    STAMP(0);
    if (w < 5) { dma_1k(mk_srd(p.c, 5120), l * 16, w * 1024, lbase + L_CS + w * 1024); dma_1k(mk_srd(p.x, 5120), l * 16, w * 1024, lbase + L_XS + w * 1024); }
    issue(sF1, 0, stg);
    {
        u64 pc; u32 rel;
        asm volatile("s_getpc_b64 %0\n.Lpcw%=:\n\ts_mov_b32 %1, .Lpcw%=-" KSYM : "=&s"(pc), "=&s"(rel));
        pc -= rel;
        constexpr u32 len = CODE_BYTES;
        const u32 piece = (blockIdx.x >> 3) * 8 + w;
        dma_1k(mk_srd((const void*)pc, len), l * 16, piece * 1024, lbase + L_RED + w * 1024);
    }
    { u32 keep;
      asm volatile("s_nop 4\n\ts_mov_b32 %0, m0\n\ts_mov_b32 m0, %5\n\ts_nop 0\n\tbuffer_load_dword %1, %2, %3 offen nt lds\n\tbuffer_load_dword %1, %4, 0 offen nt lds\n\ts_mov_b32 m0, %0"
                   : "=&s"(keep) : "v"(l == 0 ? 0u : OOB), "s"(sT.srd), "s"(sT.base), "s"(mk_srd(p.out, 5120)), "s"(lbase + L_GB) : "memory"); }
    if (w == 0) {
        u32 keep;
        asm volatile("s_nop 4\n\ts_mov_b32 %0, m0\n\ts_mov_b32 m0, %3\n\ts_nop 0\n\tbuffer_load_dword %1, %2, 0 offen sc1 lds\n\ts_mov_b32 m0, %0"
                     : "=&s"(keep) : "v"(l < 2 ? WS_E + l * 4 : OOB), "s"(mk_srd(p.ws, WS_END)), "s"(lbase + L_GA) : "memory");
    }
    { const v4u s4 = {SENT, SENT, SENT, SENT};
      st_sc1(rws, qpo, s4);
      if (w < 2) st_sc1(rws, f1o, s4);
      if (ROLE != R_SA && (w & 3) == 0) st_sc1_2(rws, mfo, v2u{SENT, SENT});
      if (w == 0) {
          if (ROLE == R_SA) st_sc1(rws, outo, s4);
          if (ROLE == R_SB) st_sc1_2(rws, outo, v2u{SENT, SENT});
      } }
    if (tid == 0) { misc[0] = 0; misc[1] = 0; }
    BAR();
    issue(sF2, 0, stg + 4096);
    auto issueB = [&](int n) { if (n < NM) issue(sM, n, stg + ((n + 1) & 3) * 4096); else issue(sT, n - NM, stg + ((n + 1) & 3) * 4096); };
    {
        v4u ft[NT][4];
        f4 xa, xb, gaa, gab, c1a, c1b, sba, sbb;
        const int col = 128 * w + 4 * (l & 15);
        {
            VMW(4);
            BAR();
            STAMP(1);
            const u64 T = *(LAS u64*)(L + L_GA) + 1ull;
            const u32 tlo = (u32)T, thi = (u32)(T >> 32);
            if (tid == 0) __hip_atomic_store((GAS u64*)(p.ws + WS_FLAG) + blockIdx.x, T, RLX_AGENT);
            auto wait_all_flags = [&]() {
                for (int sp = 0; sp < SPIN_G; ++sp) {
                    MEMBAR();
                    const v4u f0 = ld_sc1(rws, WS_FLAG + l * 16), f1 = ld_sc1(rws, WS_FLAG + 1024 + l * 16);
                    const bool bad = f0[0] != tlo || f0[1] != thi || f0[2] != tlo || f0[3] != thi || f1[0] != tlo || f1[1] != thi || f1[2] != tlo || f1[3] != thi;
                    if (__builtin_amdgcn_ballot_w64(bad) == 0ull) break;
                }
            };
            {
                v4u f[4]; read_unit(L, ta, 0, f);
                const v4u a = silu8(*(LAS v4u*)(L + L_CS + b4 * 1280 + (128 * kf + 32 * (w >> 1) + 8 * g) * 2));
                f4 d[4];
#pragma unroll
                for (int T2 = 0; T2 < 4; ++T2) d[T2] = mfma(f[T2], a, f4{0.f, 0.f, 0.f, 0.f});
                *(LAS f4*)(L + L_RED + w * 1024 + l * 16) = sel_tile(d, tq);
                if (w >= 4) {
                    float xv[10], s = 0.f;
#pragma unroll
                    for (int i = 0; i < 5; ++i) { const u32 t = *(LAS u32*)(L + L_XS + (w - 4) * 1280 + (l + 64 * i) * 4); xv[2 * i] = bflo(t); xv[2 * i + 1] = bfhi(t); s += xv[2 * i] + xv[2 * i + 1]; }
                    const float mean = wave_sum(s) * (1.f / DM);
                    float v = 0.f;
#pragma unroll
                    for (int i = 0; i < 10; ++i) v = fmaf(xv[i] - mean, xv[i] - mean, v);
                    const float var = wave_sum(v) * (1.f / DM);
                    if (l == 0) *(LAS v2u*)(L + L_ST + (w - 4) * 8) = v2u{asu(rbf(mean)), asu(rbf(__builtin_amdgcn_sqrtf(rbf(rbf(var) + 1e-5f))))};
                }
                BAR();
                if (w < 2) {
                    LAS f4* R = (LAS f4*)(L + L_RED + w * 1024 + l * 16);
                    st_sc1(rws, f1o, canon4((R[0] + R[128]) + (R[256] + R[384])));
                }
                if (w == 0) STAMP(2);
                issue(sF2, 1, stg + 8192); issue(sF2, 2, stg + 12288); issue(sF2, 3, stg);
            }
            if (w < 2) {
                for (int sp = 0; sp < SPIN_G; ++sp) {
                    MEMBAR();
                    const v2u f = ld_sc1_2(rws, WS_FLAG + l * 8);
                    if (__builtin_amdgcn_ballot_w64(f[0] != tlo || f[1] != thi) == 0ull) break;
                }
                if (w == 0) STAMP(19);
                const int hb = tid >> 5, hi = tid & 31, col = 128 * kf2 + 4 * hi;
                const u32 o = WS_F1P + (u32)(hb * 1280 + col) * 4u;
                v4u a[5] = {}, c2[5] = {};
                bool need = hasF2;
                for (int sp = 0; sp < SPIN_G; ++sp) {
                    MEMBAR();
                    if (need) {
                        u32 mx = 0;
#pragma unroll
                        for (int ks = 0; ks < 5; ++ks) { a[ks] = ld_sc1(rws, o + ks * 20480); c2[ks] = ld_sc1(rws, o + ks * 20480 + 2560); }
#pragma unroll
                        for (int ks = 0; ks < 5; ++ks) mx = vmax4(vmax4(mx, a[ks]), c2[ks]);
                        need = mx == SENT;
                    }
                    if (__builtin_amdgcn_ballot_w64(need) == 0ull) break;
                }
                const f4 sa = (((asf4(a[0]) + asf4(a[1])) + asf4(a[2])) + asf4(a[3])) + asf4(a[4]);
                const f4 sc = (((asf4(c2[0]) + asf4(c2[1])) + asf4(c2[2])) + asf4(c2[3])) + asf4(c2[4]);
                const v2u st = *(LAS v2u*)(L + L_ST + hb * 8), xr = *(LAS v2u*)(L + L_XS + hb * 1280 + (hasF2 ? col : 0) * 2);
                const float den = asf(st[1]), rc = __builtin_amdgcn_rcpf(den);
                const f4 e = rb4(unpk(xr) - asf(st[0])), q1 = e * rc;
                const f4 h = rb4(rb4(fma4(fma4(-q1, bc4(den), e), bc4(rc), q1)) * rb4(rb4(sc) + 1.f)) + rb4(sa);
                *(LAS v2u*)(L + L_HB + hb * 256 + hi * 8) = v2u{pack2(h[0], h[1]), pack2(h[2], h[3])};
                LGKM0();
                if (l == 0) __hip_atomic_fetch_add((LAS u32*)(L + L_MISC), 1u, __ATOMIC_RELAXED, __HIP_MEMORY_SCOPE_WORKGROUP);
                if (w == 0) STAMP(4);
            }
            {
                v4u fq[4][4];
                VMW(12); read_unit(L, ta, 1, fq[0]); LGKM0();
                VMW(8); read_unit(L, ta, 2, fq[1]);
                VMW(4); read_unit(L, ta, 3, fq[2]);
                issueB(0);
                VMW(4); read_unit(L, ta, 0, fq[3]);
                if (w == 7) STAMP(3);
                for (int sp = 0; misc[0] < 2u && sp < SPIN_L; ++sp) __builtin_amdgcn_s_sleep(1);
                MEMBAR();
                f4 d[4] = {};
#pragma unroll
                for (int n = 0; n < 4; ++n) {
                    const v4u a = *(LAS v4u*)(L + L_HB + b4 * 256 + (32 * n + 8 * g) * 2);
#pragma unroll
                    for (int T2 = 0; T2 < 4; ++T2) d[T2] = mfma(fq[n][T2], a, d[T2]);
                }
                st_sc1(rws, qpo, canon4(sel_tile(d, tq)));
                BAR();
                if (w == 7) STAMP(5);
                issueB(1); issueB(2); issueB(3);
            }
            if (NM) {
                f4 d[4] = {};
#pragma unroll
                for (int n = 0; n < NM; ++n) {
                    if (n < 2) VMW(12); else if (n == 2) VMW(8); else if (n == 3) VMW(4); else VMW(0);
                    v4u f[4]; read_unit(L, ta, (n + 1) & 3, f);
                    const v4u a = silu8(*(LAS v4u*)(L + L_CS + b4 * 1280 + (160 * (w & 3) + 32 * n + 8 * g) * 2));
                    LGKM0();
                    if (n == 0) issueB(4);
#pragma unroll
                    for (int T2 = 0; T2 < 4; ++T2) d[T2] = mfma(f[T2], a, d[T2]);
                }
                *(LAS f4*)(L + L_RED + w * 1024 + l * 16) = sel_tile(d, tq);
                BAR();
                if ((w & 3) == 0) {
                    LAS f4* R = (LAS f4*)(L + L_RED + w * 1024 + l * 16);
                    const f4 s = (R[0] + R[64]) + (R[128] + R[192]);
                    st_sc1_2(rws, mfo, v2u{canon(pack2(s[0], s[1])), canon(pack2(s[2], s[3]))});
                }
                constexpr int PRE = ROLE == R_SB ? PRE_B : PRE_C;
#pragma unroll
                for (int n = 0; n < PRE; ++n) issueB(5 + n);
                UNDEF4(xa); UNDEF4(xb); UNDEF4(gaa); UNDEF4(gab); UNDEF4(c1a); UNDEF4(c1b); UNDEF4(sba); UNDEF4(sbb);
                if (ROLE == R_SB && w < 5) {
                    v2u gv[2], sv[2], cv[2];
                    const u32 mo = WS_MODF + (u32)(g * 2560 + col) * 2u;
                    for (int sp = 0; sp < SPIN_G; ++sp) {
                        MEMBAR();
                        u32 mx = 0;
#pragma unroll
                        for (int h = 0; h < 2; ++h) { gv[h] = ld_sc1_2(rws, mo + h * 128); sv[h] = ld_sc1_2(rws, mo + 1280 + h * 128); cv[h] = ld_sc1_2(rws, mo + 2560 + h * 128); }
#pragma unroll
                        for (int h = 0; h < 2; ++h) mx = umax3(umax3(umax3(mx, gv[h][0], gv[h][1]), sv[h][0], sv[h][1]), cv[h][0], cv[h][1]);
                        if (all_valid(mx)) break;
                    }
                    xa = unpk(*(LAS v2u*)(L + L_XS + g * 1280 + col * 2)); xb = unpk(*(LAS v2u*)(L + L_XS + g * 1280 + col * 2 + 128));
                    gaa = unpk(gv[0]); gab = unpk(gv[1]);
                    c1a = rb4(unpk(cv[0]) + 1.f); c1b = rb4(unpk(cv[1]) + 1.f);
                    sba = unpk(sv[0]); sbb = unpk(sv[1]);
                    asm volatile("" : "+v"(xa), "+v"(xb), "+v"(gaa), "+v"(gab), "+v"(c1a), "+v"(c1b), "+v"(sba), "+v"(sbb));
                }
                if (ROLE == R_SC && w == 0) {
                    v2u ga, gb;
                    const u32 mo = WS_MODF + (u32)(b4 * 2560 + 64 * ri + dcol) * 2u;
                    for (int sp = 0; sp < SPIN_G; ++sp) {
                        MEMBAR();
                        ga = ld_sc1_2(rws, mo); gb = ld_sc1_2(rws, mo + 3840);
                        if (all_valid(umax3(umax3(ga[0], ga[1], gb[0]), gb[1], 0u))) break;
                    }
                    xa = unpk(*(LAS v2u*)(L + L_XS + b4 * 1280 + (64 * ri + dcol) * 2)); gaa = unpk(ga); gab = unpk(gb);
                    asm volatile("" : "+v"(xa), "+v"(gaa), "+v"(gab));
                }
                if (w == 5) {
                    wait_all_flags();
                    for (int sp = 0; sp < SPIN_G; ++sp) {
                        MEMBAR();
                        const v4u e0 = ld_sc1(rws, WS_ML + l * 16), e1 = ld_sc1(rws, WS_ML + 1024 + l * 16);
                        const int cnt = __builtin_popcountll(__builtin_amdgcn_ballot_w64(e0[0] != SENT)) + __builtin_popcountll(__builtin_amdgcn_ballot_w64(e0[2] != SENT))
                                      + __builtin_popcountll(__builtin_amdgcn_ballot_w64(e1[0] != SENT)) + __builtin_popcountll(__builtin_amdgcn_ballot_w64(e1[2] != SENT));
                        if (cnt >= (ROLE == R_SB ? JIT_B : JIT_C)) break;
                        __builtin_amdgcn_s_sleep(8);
                    }
                }
                BAR();
                if (w == 0) STAMP(29);
#pragma unroll
                for (int n = PRE; n < 4; ++n) issueB(5 + n);
            }
#pragma unroll
            for (int n = 0; n < NT; ++n) {
                const int a = NM + n, younger = (NA - 1 - a) < 3 ? (NA - 1 - a) : 3;
                if (younger == 3) VMW(12); else if (younger == 2) VMW(8); else if (younger == 1) VMW(4); else VMW(0);
                read_unit(L, ta, (a + 1) & 3, ft[n]);
                if (a + 4 < NA) { LGKM0(); issueB(a + 4); }
                if (ROLE == R_SA && n == 1 && w == 0) {
                    wait_all_flags();
                    if (u == 0 && l == 0) __hip_atomic_store((GAS u64*)(p.ws + WS_E), T, RLX_AGENT);
                }
            }
            LGKM0();
            BAR();
            STAMP(6);
        }
        if (ROLE == R_SA) {
            f4 res[2];
            UNDEF4(res[0]); UNDEF4(res[1]);
            u32 oo[2], mo[2];
#pragma unroll
            for (int r = 0; r < 2; ++r) {
                const int row = 2048 * ra + 256 * w + 32 * (4 * r + tq) + 8 * g, hd = row / DH, dd = row - hd * DH;
                oo[r] = WS_OP + (u32)((b4 * 32 + hd) * 2) * 384u + dd * 2; mo[r] = WS_ML + (u32)(hd * 4 + b4) * 16u;
            }
            bool done[2] = {false, false}, wdone[2] = {false, false}, obad[2] = {true, true}, mbad[2] = {true, true};
            v4u a[2][2], ml[2];
            float w0[2], w1[2];
#pragma unroll
            for (int r = 0; r < 2; ++r) { UNDEF4(ml[r]); UNDEF4(w0[r]); UNDEF4(w1[r]); UNDEF4(a[r][0]); UNDEF4(a[r][1]); }
            for (int sp = 0; sp < SPIN_G && !(done[0] && done[1]); ++sp) {
                MEMBAR();
                bool ldo[2];
#pragma unroll
                for (int r = 0; r < 2; ++r) {
                    ldo[r] = obad[r] && (!mbad[r] || sp == 0);
                    if (mbad[r]) ml[r] = ld_sc1(rws, mo[r]);
                    if (ldo[r]) { a[r][0] = ld_sc1(rws, oo[r]); a[r][1] = ld_sc1(rws, oo[r] + 384); }
                }
                const bool last = sp == SPIN_G - 1;
#pragma unroll
                for (int r = 0; r < 2; ++r) {
                    if (!wdone[r]) {
                        mbad[r] = vmax4(0u, ml[r]) == SENT;
                        if (__builtin_amdgcn_ballot_w64(mbad[r]) == 0ull || last) {
                            const float m0 = asf(ml[r][0]), m1 = asf(ml[r][2]), M = fmaxf(m0, m1);
                            const float e0 = asf(ml[r][1]) * __builtin_amdgcn_exp2f((m0 - M) * LOG2E), e1 = asf(ml[r][3]) * __builtin_amdgcn_exp2f((m1 - M) * LOG2E);
                            const float inv = fdiv(1.f, e0 + e1);
                            w0[r] = e0 * inv; w1[r] = e1 * inv; wdone[r] = true;
                        }
                    }
                    if (!done[r]) {
                        obad[r] = obad[r] && (!ldo[r] || vmax4(vmax4(0u, a[r][0]), a[r][1]) == SENT);
                        if ((__builtin_amdgcn_ballot_w64(obad[r]) == 0ull && wdone[r]) || last) {
                            STAMP(20 + w);
                            const f4 x0 = fma4(unpk(v2u{a[r][0][0], a[r][0][1]}), bc4(w0[r]), unpk(v2u{a[r][1][0], a[r][1][1]}) * w1[r]);
                            const f4 x1 = fma4(unpk(v2u{a[r][0][2], a[r][0][3]}), bc4(w0[r]), unpk(v2u{a[r][1][2], a[r][1][3]}) * w1[r]);
                            const v4u bq = {pack2(x0[0], x0[1]), pack2(x0[2], x0[3]), pack2(x1[0], x1[1]), pack2(x1[2], x1[3])};
                            const v4u bk[4] = {bq, ror4k<1>(bq), ror4k<2>(bq), ror4k<3>(bq)};
                            f4 d[4] = {};
#pragma unroll
                            for (int s = 0; s < 4; ++s)
#pragma unroll
                                for (int T2 = 0; T2 < 4; ++T2) d[(T2 + s) & 3] = mfma(ft[4 * r + s][(T2 + s) & 3], bk[T2], d[(T2 + s) & 3]);
                            res[r] = sel_tile(d, tq);
                            done[r] = true;
                        }
                    }
                }
            }
            if (w == 0) STAMP(7);
            *(LAS f4*)(L + L_RED + w * 1024 + l * 16) = res[0] + res[1];
            BAR();
            if (w == 0) {
                STAMP(8);
                LAS f4* R = (LAS f4*)(L + L_RED + l * 16);
                st_sc1(rws, outo, canon4(((R[0] + R[64]) + (R[128] + R[192])) + ((R[256] + R[320]) + (R[384] + R[448]))));
                STAMP(9);
            }
        } else if (ROLE == R_SB) {
            if (w < 5) {
                v4u a[6];
                if (w == 0) STAMP(10);
                const u32 ao = WS_AP + (u32)(g * DM + col) * 4u;
#pragma unroll
                for (int r = 0; r < 6; ++r) UNDEF4(a[r]);
                bool need = true;
                for (int sp = 0; sp < SPIN_G; ++sp) {
                    MEMBAR();
                    if (need) {
                        u32 mx = 0;
#pragma unroll
                        for (int r = 0; r < 3; ++r) { a[2 * r] = ld_sc1(rws, ao + r * 10240); a[2 * r + 1] = ld_sc1(rws, ao + r * 10240 + 256); }
#pragma unroll
                        for (int r = 0; r < 6; ++r) mx = vmax4(mx, a[r]);
                        need = mx == SENT;
                    }
                    if (__builtin_amdgcn_ballot_w64(need) == 0ull) break;
                }
                if (w == 0) STAMP(11);
                f4 ta2 = (asf4(a[0]) + asf4(a[2])) + asf4(a[4]), tb2 = (asf4(a[1]) + asf4(a[3])) + asf4(a[5]);
                rb8(ta2, tb2); ta2 *= gaa; tb2 *= gab; rb8(ta2, tb2); xa += ta2; xb += tb2; rb8(xa, xb);
                const float sh = dppf<0x150>(xa[0] + 0.f);
                const f4 da = xa - sh, db = xb - sh, t = da + db, q2 = fma4(da, da, db * db);
                float s1 = (t[0] + t[1]) + (t[2] + t[3]), s2 = (q2[0] + q2[1]) + (q2[2] + q2[3]);
                s1 += dppf<0x128>(s1); s2 += dppf<0x128>(s2); s1 += dppf<0x124>(s1); s2 += dppf<0x124>(s2);
                s1 += dppf<0x122>(s1); s2 += dppf<0x122>(s2); s1 += dppf<0x121>(s1); s2 += dppf<0x121>(s2);
                const float dm1 = s1 * (1.f / 128.f), mw = sh + dm1, v = fmaf(-dm1, s1, s2);
                *(LAS v2u*)(L + L_ST + 64 + g * 48 + w * 8) = v2u{asu(mw), asu(v)};
            }
            BAR();
            if (w < 5) {
                const f4 s0 = *(LAS f4*)(L + L_ST + 64 + g * 48), s1 = *(LAS f4*)(L + L_ST + 64 + g * 48 + 16);
                const f2 s2 = *(LAS f2*)(L + L_ST + 64 + g * 48 + 32);
                const float mean = (((s0[0] + s0[2]) + (s1[0] + s1[2])) + s2[0]) * 0.2f;
                const f4 dm = f4{s0[0], s0[2], s1[0], s1[2]} - mean, dq = dm * dm; const float d4 = s2[0] - mean;
                const float M2 = fmaf(128.f, fmaf(d4, d4, (dq[0] + dq[1]) + (dq[2] + dq[3])), ((s0[1] + s0[3]) + (s1[1] + s1[3])) + s2[1]);
                const float mb = rbf(mean), den = rbf(__builtin_amdgcn_sqrtf(rbf(rbf(M2 * (1.f / DM)) + 1e-5f))), rc = __builtin_amdgcn_rcpf(den);
                f4 ea = xa - mb, eb = xb - mb;
                rb8(ea, eb);
                const f4 qa = ea * rc, qb = eb * rc;
                f4 ha = fma4(fma4(-qa, bc4(den), ea), bc4(rc), qa), hb = fma4(fma4(-qb, bc4(den), eb), bc4(rc), qb);
                rb8(ha, hb); ha *= c1a; hb *= c1b; rb8(ha, hb); ha += sba; hb += sbb;
                *(LAS v2u*)(L + L_HB + g * 1280 + col * 2) = v2u{pack2(ha[0], ha[1]), pack2(ha[2], ha[3])};
                *(LAS v2u*)(L + L_HB + g * 1280 + col * 2 + 128) = v2u{pack2(hb[0], hb[1]), pack2(hb[2], hb[3])};
            }
            BAR();
            if (w == 0) STAMP(12);
            f4 d[4] = {};
#pragma unroll
            for (int n = 0; n < 5; ++n) {
                const v4u a = *(LAS v4u*)(L + L_HB + b4 * 1280 + (160 * (w >> 1) + 32 * n + 8 * g) * 2);
#pragma unroll
                for (int T2 = 0; T2 < 4; ++T2) d[T2] = mfma(ft[n][T2], a, d[T2]);
            }
            *(LAS f4*)(L + L_RED + w * 1024 + l * 16) = sel_tile(d, tq);
            BAR();
            if (w == 0) {
                STAMP(13);
                LAS f4* R = (LAS f4*)(L + L_RED + l * 16);
                const f4 gt = rb4((R[0] + R[128]) + (R[256] + R[384])), up = rb4((R[64] + R[192]) + (R[320] + R[448]));
                const f4 mk = {gt[0] > 0.f ? 1.f : 0.f, gt[1] > 0.f ? 1.f : 0.f, gt[2] > 0.f ? 1.f : 0.f, gt[3] > 0.f ? 1.f : 0.f};
                const f4 hh = (gt * mk) * up;
                st_sc1_2(rws, outo, v2u{canon(pack2(hh[0], hh[1])), canon(pack2(hh[2], hh[3]))});
                STAMP(14);
            }
        } else {
            f4 x1;
            UNDEF4(x1);
            if (w == 0) {
                const u32 ao = WS_AP + (u32)(b4 * DM + 64 * ri + dcol) * 4u;
                f4 s;
                for (int sp = 0; sp < SPIN_G; ++sp) {
                    MEMBAR();
                    const v4u p0 = ld_sc1(rws, ao), p1 = ld_sc1(rws, ao + 10240), p2 = ld_sc1(rws, ao + 20480);
                    s = (asf4(p0) + asf4(p1)) + asf4(p2);
                    if (all_valid(vmax4(vmax4(vmax4(0u, p0), p1), p2))) break;
                }
                x1 = rb4(xa + rb4(gaa * rb4(s)));
                asm volatile("" : "+v"(x1));
                STAMP(15);
            }
            v4u a0, a1;
            UNDEF4(a0); UNDEF4(a1);
            const u32 h0 = WS_HID + (u32)(b4 * FF + 192 * w + 32 * tq + 8 * g) * 2u, h1 = tq < 2 ? h0 + 256 : OOB;
            bool need = true;
            for (int sp = 0; sp < SPIN_G; ++sp) {
                MEMBAR();
                if (need) { a0 = ld_sc1(rws, h0); a1 = ld_sc1(rws, h1); need = vmax4(vmax4(0u, a0), a1) == SENT; }
                if (__builtin_amdgcn_ballot_w64(need) == 0ull) break;
            }
            if (w == 0) STAMP(16);
            const v4u bk[4] = {a0, ror4k<1>(a0), ror4k<2>(a0), ror4k<3>(a0)}, bl[4] = {a1, ror4k<1>(a1), ror4k<2>(a1), ror4k<3>(a1)};
            f4 d[4] = {};
#pragma unroll
            for (int k = 0; k < 4; ++k)
#pragma unroll
                for (int s = 0; s < 6; ++s) d[(s + k) & 3] = mfma(ft[s][(s + k) & 3], s < 4 ? bk[k] : bl[k], d[(s + k) & 3]);
            *(LAS f4*)(L + L_RED + w * 1024 + l * 16) = sel_tile(d, tq);
            BAR();
            if (w == 0) {
                STAMP(17);
                LAS f4* R = (LAS f4*)(L + L_RED + l * 16);
                const f4 o = x1 + rb4(gab * rb4(((R[0] + R[64]) + (R[128] + R[192])) + ((R[256] + R[320]) + (R[384] + R[448]))));
                __builtin_amdgcn_raw_buffer_store_b64(v2u{pack2(o[0], o[1]), pack2(o[2], o[3])}, __builtin_amdgcn_make_buffer_rsrc((void*)p.out, 0, 4 * DM * 2, 0x00020000),
                                                      (u32)(b4 * DM + 64 * ri + dcol) * 2u, 0, 16);
                STAMP(18);
            }
        }
    }
}

typedef const __attribute__((address_space(4))) Params* kargp;
#define GET_PARAMS() kargp pp_ = kp; asm volatile("" : "+s"(pp_)); \
    const Params q_ = {pp_->x, pp_->kc, pp_->vc, pp_->wq, pp_->wo, pp_->wg, pp_->wu, pp_->wd, pp_->c, pp_->wm, pp_->out, pp_->ws};
__global__ void __launch_bounds__(512, 2) fused_block(Params p) {
    extern __shared__ __attribute__((aligned(16))) unsigned char smem[];
    LAS unsigned char* L = (LAS unsigned char*)smem;
    const kargp kp = (kargp)__builtin_amdgcn_kernarg_segment_ptr();
    asm volatile("s_load_dword s98, %0, 0x0\n\ts_load_dword s99, %0, 0x40" :: "s"(kp) : "s98", "s99");
    const int l = threadIdx.x & 63, w = __builtin_amdgcn_readfirstlane(threadIdx.x >> 6), c = blockIdx.x;
    const int x = c & 7, y = c >> 3;
    if (__builtin_expect(c >= NW, 1)) { GET_PARAMS(); kv_path(q_, L, c - NW, w, l); }
    else if (y >= 5) { GET_PARAMS(); w_path<R_SB>(q_, L, 30 + 8 * (y - 5) + x, w, l); }
    else if (x < 6) { GET_PARAMS(); w_path<R_SA>(q_, L, 10 * (x >> 1) + 5 * (x & 1) + y, w, l); }
    else { GET_PARAMS(); w_path<R_SC>(q_, L, 54 + 5 * (x - 6) + y, w, l); }
}

extern "C" void kernel_launch(void* const* d_in, const int* in_sizes, int n_in, void* d_out, int out_size, void* d_ws, size_t ws_size, hipStream_t stream) {
    Params p;
    p.x = (const u16*)d_in[0]; p.kc = (const u16*)d_in[1]; p.vc = (const u16*)d_in[2]; p.wq = (const u16*)d_in[3]; p.wo = (const u16*)d_in[4];
    p.wg = (const u16*)d_in[6]; p.wu = (const u16*)d_in[7]; p.wd = (const u16*)d_in[8]; p.c = (const u16*)d_in[9]; p.wm = (const u16*)d_in[10];
    p.out = (u16*)d_out; p.ws = (unsigned char*)d_ws;
    (void)hipFuncSetAttribute((const void*)fused_block, hipFuncAttributeMaxDynamicSharedMemorySize, (int)L_END);
    hipLaunchKernelGGL(fused_block, dim3(256), dim3(512), L_END, stream, p);
}
```

```cpp
#include <hip/hip_runtime.h>
#include <stdint.h>
#pragma clang fp contract(off)

typedef unsigned short u16;
typedef unsigned u32;
typedef unsigned long long u64;
typedef u32 v4u __attribute__((ext_vector_type(4)));
typedef u32 v2u __attribute__((ext_vector_type(2)));
typedef float f4 __attribute__((ext_vector_type(4)));
typedef short v4s __attribute__((ext_vector_type(4)));
typedef __bf16 bf8 __attribute__((ext_vector_type(8)));
typedef __bf16 bf2 __attribute__((ext_vector_type(2)));
#define LAS __attribute__((address_space(3)))
#define GAS __attribute__((address_space(1)))
#define RLX_AGENT __ATOMIC_RELAXED, __HIP_MEMORY_SCOPE_AGENT

constexpr int DM = 640, NH = 32, SKV = 4096, DH = 192, WIN = 1024, FF = 1536, NQ = NH * DH;
constexpr u32 SENT = 0xFFFFFFFFu;
constexpr int NW = 64;
constexpr u32 WS_E = 0, WS_FLAG = 4096, WS_F1P = 8192, WS_MODF = WS_F1P + 5 * 4 * 1280 * 4, WS_QP = WS_MODF + 4 * 2560 * 2,
              WS_OP = WS_QP + 5 * 4 * NQ * 4, WS_ML = WS_OP + 128 * 2 * DH * 2, WS_AP = WS_ML + 32 * 4 * 16, WS_HID = WS_AP + 3 * 4 * DM * 4,
              WS_END = WS_HID + 4 * FF * 2;
constexpr u32 OOB = 0x7FFFFF00u;
constexpr u32 L_XS = 131072, L_CS = L_XS + 5120, L_HB = L_CS + 5120, L_RED = L_HB + 5120, L_ST = L_RED + 8192, L_GA = L_ST + 256, L_GB = L_GA + 1024,
              L_X1 = L_GB + 1024, L_MISC = L_X1 + 1024, L_STAMP = L_MISC + 64, L_END = L_STAMP + 256;
constexpr u32 K_W = 18432, K_PX = 1024, K_PX2 = 1536, K_V = 9216, K_QS = 8 * K_W, K_ML = K_QS + 768, K_END = K_ML + 64;
static_assert(K_END <= L_MISC, "LDS");
constexpr float LOG2E = 1.4426950408889634f;
static_assert(L_END <= 163840, "LDS");
constexpr int SPIN_G = 40000, SPIN_L = 2000000;
constexpr int VSPL = 4;
constexpr int PRE_B = 2, PRE_C = 0, JIT_B = 24, JIT_C = 200;

#define STAMP(i) do {} while (0)

struct Params { const u16 *x, *kc, *vc, *wq, *wo, *wg, *wu, *wd, *c, *wm; u16* out; unsigned char* ws; };

__device__ __forceinline__ v4u mk_srd(const void* p, u32 bytes) { const u64 a = (u64)p; return v4u{(u32)a, (u32)(a >> 32) & 0xffffu, bytes, 0x00020000u}; }
__device__ __forceinline__ float asf(u32 u) { return __builtin_bit_cast(float, u); }
__device__ __forceinline__ u32 asu(float f) { return __builtin_bit_cast(u32, f); }
__device__ __forceinline__ float bflo(u32 u) { return asf(u << 16); }
__device__ __forceinline__ float bfhi(u32 u) { return asf(u & 0xffff0000u); }
__device__ __forceinline__ u32 pack2(float lo, float hi) { bf2 v; v[0] = (__bf16)lo; v[1] = (__bf16)hi; return __builtin_bit_cast(u32, v); }
__device__ __forceinline__ float rbf(float f) { return (float)(__bf16)f; }
typedef float f2 __attribute__((ext_vector_type(2)));
__device__ __forceinline__ f4 rb4(f4 v) {
    float a = v[0], b = v[1], c = v[2], d = v[3];
    asm("v_cvt_pk_bf16_f32 %0, 0, %0\n\tv_cvt_pk_bf16_f32 %1, 0, %1\n\tv_cvt_pk_bf16_f32 %2, 0, %2\n\tv_cvt_pk_bf16_f32 %3, 0, %3" : "+v"(a), "+v"(b), "+v"(c), "+v"(d));
    return f4{a, b, c, d}; }
__device__ __forceinline__ void rb8(f4& u, f4& v) {
    float a = u[0], b = u[1], c = u[2], d = u[3], e = v[0], f = v[1], g = v[2], h = v[3];
    asm("v_cvt_pk_bf16_f32 %0, 0, %0\n\tv_cvt_pk_bf16_f32 %1, 0, %1\n\tv_cvt_pk_bf16_f32 %2, 0, %2\n\tv_cvt_pk_bf16_f32 %3, 0, %3\n\t"
        "v_cvt_pk_bf16_f32 %4, 0, %4\n\tv_cvt_pk_bf16_f32 %5, 0, %5\n\tv_cvt_pk_bf16_f32 %6, 0, %6\n\tv_cvt_pk_bf16_f32 %7, 0, %7"
        : "+v"(a), "+v"(b), "+v"(c), "+v"(d), "+v"(e), "+v"(f), "+v"(g), "+v"(h));
    u = f4{a, b, c, d}; v = f4{e, f, g, h}; }
__device__ __forceinline__ f4 unpk(v2u v) { return f4{bflo(v[0]), bfhi(v[0]), bflo(v[1]), bfhi(v[1])}; }
__device__ __forceinline__ f4 fma4(f4 a, f4 b, f4 c) { return __builtin_elementwise_fma(a, b, c); }
__device__ __forceinline__ f4 bc4(float x) { return f4{x, x, x, x}; }
__device__ __forceinline__ u32 canon(u32 u) { return u < 0xFFFFFFFEu ? u : 0xFFFFFFFEu; }
__device__ __forceinline__ v4u canon4(f4 v) { return v4u{canon(asu(v[0])), canon(asu(v[1])), canon(asu(v[2])), canon(asu(v[3]))}; }
__device__ __forceinline__ u32 umax3(u32 a, u32 b, u32 c) { const u32 m = a > b ? a : b; return m > c ? m : c; }
__device__ __forceinline__ u32 vmax4(u32 m, v4u v) { return umax3(umax3(m, v[0], v[1]), v[2], v[3]); }
__device__ __forceinline__ bool all_valid(u32 m) { return __builtin_amdgcn_ballot_w64(m == SENT) == 0ull; }
__device__ __forceinline__ float dot2(u32 a, u32 b, float c) { return __builtin_amdgcn_fdot2_f32_bf16(__builtin_bit_cast(bf2, a), __builtin_bit_cast(bf2, b), c, false); }
__device__ __forceinline__ f4 mfma(v4u a, v4u b, f4 c) { return __builtin_amdgcn_mfma_f32_16x16x32_bf16(__builtin_bit_cast(bf8, a), __builtin_bit_cast(bf8, b), c, 0, 0, 0); }
template <int CTRL> __device__ __forceinline__ u32 dppu(u32 x) { return (u32)__builtin_amdgcn_mov_dpp((int)x, CTRL, 0xf, 0xf, true); }
template <int CTRL> __device__ __forceinline__ float dppf(float x) { return asf(dppu<CTRL>(asu(x))); }
template <int K> __device__ __forceinline__ v4u ror4k(v4u v) {
    if constexpr (K == 0) return v; else return v4u{dppu<0x120 + 4 * K>(v[0]), dppu<0x120 + 4 * K>(v[1]), dppu<0x120 + 4 * K>(v[2]), dppu<0x120 + 4 * K>(v[3])}; }
__device__ __forceinline__ float row_sum(float x) { x += dppf<0x128>(x); x += dppf<0x124>(x); x += dppf<0x122>(x); x += dppf<0x121>(x); return x; }
__device__ __forceinline__ float row_max(float x) { x = fmaxf(x, dppf<0x128>(x)); x = fmaxf(x, dppf<0x124>(x)); x = fmaxf(x, dppf<0x122>(x)); x = fmaxf(x, dppf<0x121>(x)); return x; }
__device__ __forceinline__ float xrow_sum(float x) {
    auto s = __builtin_amdgcn_permlane16_swap(asu(x), asu(x), false, false); x = asf(s[0]) + asf(s[1]);
    auto t = __builtin_amdgcn_permlane32_swap(asu(x), asu(x), false, false); return asf(t[0]) + asf(t[1]); }
__device__ __forceinline__ float xrow_max(float x) {
    auto s = __builtin_amdgcn_permlane16_swap(asu(x), asu(x), false, false); x = fmaxf(asf(s[0]), asf(s[1]));
    auto t = __builtin_amdgcn_permlane32_swap(asu(x), asu(x), false, false); return fmaxf(asf(t[0]), asf(t[1])); }
__device__ __forceinline__ float wave_sum(float x) { return xrow_sum(row_sum(x)); }
__device__ __forceinline__ float wave_max(float x) { return xrow_max(row_max(x)); }
__device__ __forceinline__ float fdiv(float a, float b) { const float r = __builtin_amdgcn_rcpf(b); const float q = a * r; return fmaf(fmaf(-q, b, a), r, q); }
__device__ __forceinline__ float silu_bf(float v) { return fdiv(v, 1.f + __builtin_amdgcn_exp2f(fminf(-v * 1.4426950408889634f, 126.f))); }
__device__ __forceinline__ v4u silu8(v4u c) {
    v4u r;
#pragma unroll
    for (int i = 0; i < 4; ++i) r[i] = pack2(silu_bf(bflo(c[i])), silu_bf(bfhi(c[i])));
    return r; }
#define BAR() asm volatile("s_waitcnt lgkmcnt(0)\n\ts_barrier" ::: "memory")
#define LGKM0() asm volatile("s_waitcnt lgkmcnt(0)" ::: "memory")
#define VMW(n) asm volatile("s_waitcnt vmcnt(%0)" :: "i"(n) : "memory")
#define MEMBAR() asm volatile("" ::: "memory")
typedef __amdgpu_buffer_rsrc_t rsrc_t;
__device__ __forceinline__ v4u ld_sc1(rsrc_t r, u32 off) { return __builtin_amdgcn_raw_buffer_load_b128(r, off, 0, 16); }
__device__ __forceinline__ v2u ld_sc1_2(rsrc_t r, u32 off) { return __builtin_amdgcn_raw_buffer_load_b64(r, off, 0, 16); }
__device__ __forceinline__ void st_sc1(rsrc_t r, u32 off, v4u v) { __builtin_amdgcn_raw_buffer_store_b128(v, r, off, 0, 16); }
__device__ __forceinline__ void st_sc1_2(rsrc_t r, u32 off, v2u v) { __builtin_amdgcn_raw_buffer_store_b64(v, r, off, 0, 16); }
__device__ __forceinline__ f4 asf4(v4u v) { return __builtin_bit_cast(f4, v); }

__device__ __forceinline__ void dma_unit(v4u srd, u32 v0, u32 v1, u32 sA, u32 sB, u32 lds) {
    u32 keep;
    asm volatile("s_nop 4\n\ts_mov_b32 %0, m0\n\ts_mov_b32 m0, %6\n\ts_nop 0\n\t"
                 "buffer_load_dwordx4 %1, %3, %4 offen nt lds\n\ts_add_u32 m0, m0, 0x400\n\ts_nop 0\n\t"
                 "buffer_load_dwordx4 %2, %3, %4 offen nt lds\n\ts_add_u32 m0, m0, 0x400\n\ts_nop 0\n\t"
                 "buffer_load_dwordx4 %1, %3, %5 offen nt lds\n\ts_add_u32 m0, m0, 0x400\n\ts_nop 0\n\t"
                 "buffer_load_dwordx4 %2, %3, %5 offen nt lds\n\ts_mov_b32 m0, %0"
                 : "=&s"(keep) : "v"(v0), "v"(v1), "s"(srd), "s"(sA), "s"(sB), "s"(lds) : "memory", "scc");
}
__device__ __forceinline__ void dma_1k(v4u srd, u32 voff, u32 soff, u32 lds) {
    u32 keep;
    asm volatile("s_nop 4\n\ts_mov_b32 %0, m0\n\ts_mov_b32 m0, %4\n\ts_nop 0\n\tbuffer_load_dwordx4 %1, %2, %3 offen lds\n\ts_mov_b32 m0, %0"
                 : "=&s"(keep) : "v"(voff), "s"(srd), "s"(soff), "s"(lds) : "memory");
}
struct WStream { v4u srd; u32 base, pitch, v0, v1; };
__device__ __forceinline__ WStream mk_stream(v4u srd, u32 base, u32 pitch, int l) {
    WStream s; s.srd = srd; s.base = base; s.pitch = pitch;
    const u32 sw = 2u * ((l >> 4) & 1);
    s.v0 = (l >> 3) * pitch + (((l & 7) ^ sw) << 4);
    s.v1 = (8 + (l >> 3)) * pitch + (((l & 7) ^ (sw | 4u)) << 4);
    return s; }
__device__ __forceinline__ void issue(const WStream& s, int n, u32 lds) { const u32 a = s.base + (u32)n * 32u * s.pitch; dma_unit(s.srd, s.v0, s.v1, a, a + 16u * s.pitch, lds); }
__device__ __forceinline__ void read_unit(LAS unsigned char* L, const u32 (&ta)[4], int slot, v4u (&f)[4]) {
#pragma unroll
    for (int T = 0; T < 4; ++T) {
        const v2u lo = __builtin_bit_cast(v2u, __builtin_amdgcn_ds_read_tr16_b64_v4i16((LAS v4s*)(L + ta[T] + slot * 4096)));
        const v2u hi = __builtin_bit_cast(v2u, __builtin_amdgcn_ds_read_tr16_b64_v4i16((LAS v4s*)(L + ta[T] + slot * 4096 + 512)));
        f[T] = v4u{lo[0], lo[1], hi[0], hi[1]};
    } }
__device__ __forceinline__ f4 sel_tile(const f4 (&a)[4], int t) {
    const bool b0 = t & 1, b1 = t & 2;
    f4 r;
#pragma unroll
    for (int i = 0; i < 4; ++i) { const float lo = b0 ? a[1][i] : a[0][i], hi = b0 ? a[3][i] : a[2][i]; r[i] = b1 ? hi : lo; }
    return r; }

__device__ __forceinline__ void dma_kv(v4u srd, u32 vo, u32 so, u32 lds) {
    u32 keep;
    asm volatile("s_nop 4\n\ts_mov_b32 %0, m0\n\ts_mov_b32 m0, %4\n\ts_nop 0\n\t"
                 "buffer_load_dwordx4 %1, %2, %3 offen nt lds\n\ts_add_u32 m0, m0, 0x380\n\ts_nop 0\n\t"
                 "buffer_load_dwordx4 %1, %2, %3 offen offset:128 nt lds\n\ts_add_u32 m0, m0, 0x380\n\ts_nop 0\n\t"
                 "buffer_load_dwordx4 %1, %2, %3 offen offset:256 nt lds\n\ts_mov_b32 m0, %0"
                 : "=&s"(keep) : "v"(vo), "s"(srd), "s"(so), "s"(lds) : "memory", "scc");
}
__device__ __forceinline__ float qk_unit(v4u k0, v4u k1, v4u k2, const v4u (&q)[3]) {
    float s = 0.f, t = 0.f;
#pragma unroll
    for (int r = 0; r < 4; r += 2) { s = dot2(k0[r], q[0][r], s); t = dot2(k0[r + 1], q[0][r + 1], t); }
#pragma unroll
    for (int r = 0; r < 4; r += 2) { s = dot2(k1[r], q[1][r], s); t = dot2(k1[r + 1], q[1][r + 1], t); }
#pragma unroll
    for (int r = 0; r < 4; r += 2) { s = dot2(k2[r], q[2][r], s); t = dot2(k2[r + 1], q[2][r + 1], t); }
    return s + t;
}
__device__ __forceinline__ void pv_unit(v4u v0, v4u v1, v4u v2, u32 plo, float (&acc)[24]) {
    const u32 phi = plo << 16;
#pragma unroll
    for (int r = 0; r < 4; ++r) {
        acc[2 * r] = dot2(v0[r], plo, acc[2 * r]); acc[2 * r + 1] = dot2(v0[r], phi, acc[2 * r + 1]);
        acc[8 + 2 * r] = dot2(v1[r], plo, acc[8 + 2 * r]); acc[8 + 2 * r + 1] = dot2(v1[r], phi, acc[8 + 2 * r + 1]);
        acc[16 + 2 * r] = dot2(v2[r], plo, acc[16 + 2 * r]); acc[16 + 2 * r + 1] = dot2(v2[r], phi, acc[16 + 2 * r + 1]);
    }
    asm volatile("" : "+v"(acc[0]), "+v"(acc[1]), "+v"(acc[2]), "+v"(acc[3]), "+v"(acc[4]), "+v"(acc[5]), "+v"(acc[6]), "+v"(acc[7]), "+v"(acc[8]), "+v"(acc[9]), "+v"(acc[10]), "+v"(acc[11]),
                      "+v"(acc[12]), "+v"(acc[13]), "+v"(acc[14]), "+v"(acc[15]), "+v"(acc[16]), "+v"(acc[17]), "+v"(acc[18]), "+v"(acc[19]), "+v"(acc[20]), "+v"(acc[21]), "+v"(acc[22]), "+v"(acc[23]));
}

template <bool B> struct BoolC { static constexpr bool value = B; };
__device__ __forceinline__ void kv_path(const Params& p, LAS unsigned char* L, int j, int w, int l) {
    const rsrc_t rws = __builtin_amdgcn_make_buffer_rsrc((void*)p.ws, 0, WS_END, 0x00020000);
    const rsrc_t rk = __builtin_amdgcn_make_buffer_rsrc((void*)p.kc, 0, 4 * NH * SKV * DH * 2, 0x00020000);
    const rsrc_t rv = __builtin_amdgcn_make_buffer_rsrc((void*)p.vc, 0, 4 * NH * SKV * DH * 2, 0x00020000);
    volatile LAS u32* misc = (volatile LAS u32*)(L + L_MISC);
    const int r8 = j >> 3, rk3 = (r8 * 171) >> 9, pos = r8 - 3 * rk3, t3 = (j & 7) * 8 + rk3;
    const int is1 = pos & 1, is2 = pos >> 1;
    const int slot = w & is1, bh = 2 * t3 + (is2 | slot), wi = w >> is1, ng = 4 >> is1;
    const int u1 = 43 * is2 + 85 * (is1 & ~slot) + wi + (wi >= ng ? 10 * ng : 0);
    const u32 ust = 12288u >> is1;
    const u32 vo = (l >> 3) * 384 + (l & 7) * 16;
    const u32 base = ((u32)bh * SKV + (SKV - WIN)) * 384u + (u32)u1 * 3072u;
    const u32 wl = w * K_W, lb = (u32)(size_t)L + wl;
    const v4u sk = mk_srd(p.kc, 4 * NH * SKV * DH * 2), sv = mk_srd(p.vc, 4 * NH * SKV * DH * 2);
    STAMP(0);
    dma_kv(sk, vo, base + 8 * ust, lb); dma_kv(sv, vo, base + 8 * ust, lb + K_V);
    const int part = is2 | (is1 & ~slot);
    const int nl = wi >= 5 - 2 * is1 ? 2 : 3;
    const bool lead = w == 0 || (w == 1 && is1);
    const u32 opb = WS_OP + (bh * 2 + part) * 384, mlb = WS_ML + ((bh & 31) * 4 + (bh >> 5)) * 16 + part * 8;
    if (threadIdx.x < 12) misc[threadIdx.x] = 0;
    BAR();
    if (__builtin_expect(w == 4, 0)) {
        __builtin_amdgcn_s_setprio(3);
        const u64 T = __hip_atomic_load((GAS u64*)(p.ws + WS_E), RLX_AGENT) + 1ull;
        st_sc1(rws, l < 24 ? opb + l * 16 : OOB, v4u{SENT, SENT, SENT, SENT});
        st_sc1_2(rws, l == 0 ? mlb : OOB, v2u{SENT, SENT});
        const int bh1 = 2 * t3 + 1;
        st_sc1(rws, (pos == 1 && l < 24) ? WS_OP + bh1 * 2 * 384 + l * 16 : OOB, v4u{SENT, SENT, SENT, SENT});
        st_sc1_2(rws, (pos == 1 && l == 0) ? WS_ML + ((bh1 & 31) * 4 + (bh1 >> 5)) * 16 : OOB, v2u{SENT, SENT});
        VMW(0);
        if (l == 0) {
            __hip_atomic_store((GAS u64*)(p.ws + WS_FLAG) + blockIdx.x, T, RLX_AGENT);
            misc[4] = (u32)T; misc[5] = (u32)(T >> 32); LGKM0(); misc[2] = 1;
        }
        STAMP(9);
        __builtin_amdgcn_s_setprio(0);
    }
    const int c = l & 7;
    float m, lsum, fsc;
    const u32 nslot = pos == 1 ? 4u : 8u;
    auto slot_stats = [&](float& M, float& mi, float& li) {
        const v2u t = *(LAS v2u*)(L + K_ML + c * 8);
        const bool mine = pos != 1 || (l & 1) == slot;
        mi = mine ? asf(t[0]) : -INFINITY; li = mine ? asf(t[1]) : 0.f;
        M = fmaxf(mi, dppf<0xB1>(mi)); M = fmaxf(M, dppf<0x4E>(M)); M = fmaxf(M, dppf<0x141>(M));
    };
    auto post = [&]() {
        u32 old = 0;
        if (l == 0) { *(LAS v2u*)(L + K_ML + w * 8) = v2u{asu(m), asu(lsum)}; LGKM0(); old = __hip_atomic_fetch_add((LAS u32*)(L + L_MISC + 32 + slot * 4), 1u, __ATOMIC_RELAXED, __HIP_MEMORY_SCOPE_WORKGROUP); }
        if (__builtin_amdgcn_readfirstlane(old) == nslot - 1u) {
            MEMBAR();
            float M, mi, li; slot_stats(M, mi, li);
            float e = li * __builtin_amdgcn_exp2f((mi - M) * LOG2E);
            e += dppf<0xB1>(e); e += dppf<0x4E>(e); e += dppf<0x141>(e);
            st_sc1_2(rws, l == 0 ? mlb : OOB, v2u{canon(asu(M)), canon(asu(e))});
            STAMP(5);
        }
    };
    auto scale = [&]() {
        for (int sp = 0; misc[8 + slot] < nslot && sp < SPIN_L; ++sp) __builtin_amdgcn_s_sleep(1);
        MEMBAR();
        float M, mi, li; slot_stats(M, mi, li);
        float e = li * __builtin_amdgcn_exp2f((mi - M) * LOG2E);
        e += dppf<0xB1>(e); e += dppf<0x4E>(e); e += dppf<0x141>(e);
        fsc = fdiv(__builtin_amdgcn_exp2f((m - M) * LOG2E), e);
    };
    dma_kv(sk, vo, base + 9 * ust, lb + 3072); dma_kv(sv, vo, base + 9 * ust, lb + K_V + 3072);
    if (nl == 3) { dma_kv(sk, vo, base + 10 * ust, lb + 6144); dma_kv(sv, vo, base + 10 * ust, lb + K_V + 6144); }
    v4u K[24], V[24];
#pragma unroll
    for (int jj = 0; jj < 8; ++jj)
#pragma unroll
        for (int i = 0; i < 3; ++i) K[jj * 3 + i] = __builtin_amdgcn_raw_buffer_load_b128(rk, vo + i * 128, base + jj * ust, 2);
#pragma unroll
    for (int jj = 0; jj < VSPL; ++jj)
#pragma unroll
        for (int i = 0; i < 3; ++i) V[jj * 3 + i] = __builtin_amdgcn_raw_buffer_load_b128(rv, vo + i * 128, base + jj * ust, 2);
    __builtin_amdgcn_sched_barrier(0);
    const bool early = __builtin_amdgcn_readfirstlane(misc[slot]) != 0u;
    float acc[24];
    auto attend = [&](auto ec) {
        constexpr bool EARLY = decltype(ec)::value;
        if (!EARLY) {
#pragma unroll
            for (int jj = VSPL; jj < 8; ++jj)
#pragma unroll
                for (int i = 0; i < 3; ++i) V[jj * 3 + i] = __builtin_amdgcn_raw_buffer_load_b128(rv, vo + i * 128, base + jj * ust, 2);
            __builtin_amdgcn_sched_barrier(0);
            if (w == 0) STAMP(1);
            if (w == 7) STAMP(2);
            if (w < 4) {
                for (int sp = 0; misc[2] == 0 && sp < SPIN_L; ++sp) __builtin_amdgcn_s_sleep(1);
                const u32 tlo = misc[4], thi = misc[5];
                for (int sp = 0; sp < SPIN_G && misc[6] == 0; ++sp) {
                    MEMBAR();
                    const v2u f = ld_sc1_2(rws, WS_FLAG + l * 8);
                    if (__builtin_amdgcn_ballot_w64(f[0] != tlo || f[1] != thi) == 0ull) misc[6] = 1;
                }
                const u32 qo = WS_QP + (u32)((bh >> 5) * NQ + (bh & 31) * DH + 4 * l) * 4u;
                v4u a[5] = {};
                bool need = l < 48;
                for (int sp = 0; sp < SPIN_G; ++sp) {
                    MEMBAR();
                    if (__builtin_amdgcn_readfirstlane(misc[slot]) != 0u) break;
                    if (need) {
                        u32 mx = 0;
#pragma unroll
                        for (int ks = 0; ks < 5; ++ks) a[ks] = ld_sc1(rws, qo + ks * (4 * NQ * 4));
#pragma unroll
                        for (int ks = 0; ks < 5; ++ks) mx = vmax4(mx, a[ks]);
                        need = mx == SENT;
                    }
                    if (__builtin_amdgcn_ballot_w64(need) == 0ull || sp == SPIN_G - 1) {
                        const f4 qs = (((asf4(a[0]) + asf4(a[1])) + asf4(a[2])) + asf4(a[3])) + asf4(a[4]);
                        if (l < 48) *(LAS v2u*)(L + K_QS + slot * 384 + l * 8) = v2u{pack2(qs[0], qs[1]), pack2(qs[2], qs[3])};
                        LGKM0();
                        misc[slot] = 1;
                        break;
                    }
                }
                if (w == 0) STAMP(3);
            } else {
                for (int sp = 0; misc[slot] == 0 && sp < SPIN_L; ++sp) __builtin_amdgcn_s_sleep(1);
            }
        }
        VMW(EARLY ? 24 + 3 * VSPL : 48);
        v4u p0, p1, p2;
        {
            v4u q[3];
#pragma unroll
            for (int i = 0; i < 3; ++i) q[i] = *(LAS v4u*)(L + K_QS + slot * 384 + (c + 8 * i) * 16);
            float y[4], x[8];
            LAS v4u* KL = (LAS v4u*)(L + wl + l * 16);
#pragma unroll
            for (int jj = 0; jj < 3; ++jj) { y[jj] = qk_unit(KL[jj * 192], KL[jj * 192 + 64], KL[jj * 192 + 128], q); __builtin_amdgcn_sched_barrier(0); }
#pragma unroll
            for (int jj = 0; jj < 8; jj += 2) { x[jj] = qk_unit(K[3 * jj], K[3 * jj + 1], K[3 * jj + 2], q); x[jj + 1] = qk_unit(K[3 * jj + 3], K[3 * jj + 4], K[3 * jj + 5], q); __builtin_amdgcn_sched_barrier(0); }
#pragma unroll
            for (int h = 0; h < 4; ++h) { const float a = x[h] + dppf<0x141>(x[h]), b = x[h + 4] + dppf<0x141>(x[h + 4]); x[h] = (c & 4) ? b : a; }
            { const float a = y[0] + dppf<0x141>(y[0]), b = y[2] + dppf<0x141>(y[2]), a1 = y[1] + dppf<0x141>(y[1]); y[0] = (c & 4) ? b : a; y[1] = a1; }
#pragma unroll
            for (int h = 0; h < 2; ++h) { const float a = x[h] + dppf<0x4E>(x[h]), b = x[h + 2] + dppf<0x4E>(x[h + 2]); x[h] = (c & 2) ? b : a; }
            { const float a = y[0] + dppf<0x4E>(y[0]), b = y[1] + dppf<0x4E>(y[1]); y[0] = (c & 2) ? b : a; }
            { const float a = x[0] + dppf<0xB1>(x[0]), b = x[1] + dppf<0xB1>(x[1]); x[0] = (c & 1) ? b : a; }
            y[0] += dppf<0xB1>(y[0]);
            const float s1 = rbf(rbf(x[0]) * 0.07216878364870322f);
            const float s2 = (c >> 1) < nl ? rbf(rbf(y[0]) * 0.07216878364870322f) : -INFINITY;
            m = wave_max(fmaxf(s1, s2));
            const float e1 = __builtin_amdgcn_exp2f((s1 - m) * LOG2E), e2 = __builtin_amdgcn_exp2f((s2 - m) * LOG2E);
            *(LAS u32*)(L + wl + K_PX + l * 4) = pack2(e1, 0.f);
            *(LAS u32*)(L + wl + K_PX2 + (l >> 1) * 4) = pack2(e2, 0.f);
            lsum = wave_sum(e1 + ((c & 1) ? 0.f : e2));
            post();
            LGKM0();
            p0 = *(LAS v4u*)(L + wl + K_PX + (l >> 3) * 32); p1 = *(LAS v4u*)(L + wl + K_PX + (l >> 3) * 32 + 16); p2 = *(LAS v4u*)(L + wl + K_PX2 + (l >> 3) * 16);
        }
        if (EARLY) {
            __builtin_amdgcn_sched_barrier(0);
#pragma unroll
            for (int jj = VSPL; jj < 8; ++jj)
#pragma unroll
                for (int i = 0; i < 3; ++i) V[jj * 3 + i] = __builtin_amdgcn_raw_buffer_load_b128(rv, vo + i * 128, base + jj * ust, 2);
            __builtin_amdgcn_sched_barrier(0);
        }
        if (w == 0) STAMP(4);
#pragma unroll
        for (int i = 0; i < 24; ++i) acc[i] = 0.f;
        {
            LAS v4u* VL = (LAS v4u*)(L + wl + K_V + l * 16);
            pv_unit(VL[0], VL[64], VL[128], p2[0], acc);
            pv_unit(VL[192], VL[256], VL[320], p2[1], acc);
            if (nl == 3) pv_unit(VL[384], VL[448], VL[512], p2[2], acc);
        }
#pragma unroll
        for (int jj = 0; jj < 6; ++jj) pv_unit(V[3 * jj], V[3 * jj + 1], V[3 * jj + 2], jj < 4 ? p0[jj & 3] : p1[jj & 3], acc);
        scale();
#pragma unroll
        for (int jj = 6; jj < 8; ++jj) pv_unit(V[3 * jj], V[3 * jj + 1], V[3 * jj + 2], p1[jj & 3], acc);
    };
    if (early) attend(BoolC<true>{}); else attend(BoolC<false>{});
    if (w == 7) STAMP(6);
    float y[12], z[6];
#pragma unroll
    for (int n = 0; n < 12; ++n) { auto s = __builtin_amdgcn_permlane32_swap(asu(acc[n]), asu(acc[n + 12]), false, false); y[n] = asf(s[0]) + asf(s[1]); }
#pragma unroll
    for (int n = 0; n < 6; ++n) { auto s = __builtin_amdgcn_permlane16_swap(asu(y[n]), asu(y[n + 6]), false, false); z[n] = asf(s[0]) + asf(s[1]); }
#pragma unroll
    for (int n = 0; n < 3; ++n) {
        const float a = z[n] + dppf<0x128>(z[n]), b = z[n + 3] + dppf<0x128>(z[n + 3]);
        const int ai = 3 * (l >> 3) + n;
        *(LAS float*)(L + wl + (64 * (ai >> 3) + 8 * c + (ai & 7)) * 4) = ((l & 8) ? b : a) * fsc;
    }
    BAR();
    if (w == 0) STAMP(7);
    if (lead) {
        const u32 pb = slot * K_W + (l < 48 ? l : 47) * 16;
        f4 o = (*(LAS f4*)(L + pb) + *(LAS f4*)(L + pb + 2 * K_W)) + (*(LAS f4*)(L + pb + 4 * K_W) + *(LAS f4*)(L + pb + 6 * K_W));
        if (pos != 1) o += (*(LAS f4*)(L + pb + K_W) + *(LAS f4*)(L + pb + 3 * K_W)) + (*(LAS f4*)(L + pb + 5 * K_W) + *(LAS f4*)(L + pb + 7 * K_W));
        st_sc1_2(rws, l < 48 ? opb + l * 8 : OOB, v2u{canon(pack2(o[0], o[1])), canon(pack2(o[2], o[3]))});
        if (w == 0) STAMP(8); else STAMP(10);
    }
}

enum { R_SA = 0, R_SB = 1, R_SC = 2 };
#define UNDEF4(v) asm volatile("" : "=v"(v))
template <int ROLE>
__device__ __forceinline__ void w_path(const Params& p, LAS unsigned char* L, int u, int w, int l) {
    const rsrc_t rws = __builtin_amdgcn_make_buffer_rsrc((void*)p.ws, 0, WS_END, 0x00020000);
    const int tid = threadIdx.x, g = l >> 4, tq = (l & 15) >> 2, b4 = l & 3;
    const u32 lbase = (u32)(size_t)L, stg = lbase + w * 16384;
    const bool hasF1 = u < 50, hasF2 = u < 60;
    const int kf = u / 10, lp = u - 10 * kf, kf2 = u / 12, cs = u - 12 * kf2;
    const int ri = ROLE == R_SA ? u : (ROLE == R_SB ? u - 30 : u - 54);
    const WStream sF1 = mk_stream(mk_srd(p.wm, hasF1 ? DM * 3840 * 2 : 0), (u32)((128 * kf + 32 * (w >> 1)) * 7680 + (128 * lp + 64 * (w & 1)) * 2), 7680, l);
    const WStream sF2 = mk_stream(mk_srd(p.wq, hasF2 ? DM * NQ * 2 : 0), (u32)(128 * kf2 * 12288 + (512 * cs + 64 * w) * 2), 12288, l);
    const int mid = ROLE == R_SC ? (w < 4 ? (ri >= 6 ? ri : -1) : 30 + ri) : (w < 4 ? (ri < 6 ? ri : ri + 4) : (ri == 20 ? 28 : (ri == 21 ? 29 : -1)));
    const bool hasM = ROLE != R_SA && mid >= 0;
    const WStream sM = mk_stream(mk_srd(p.wm, hasM ? DM * 3840 * 2 : 0), (u32)(160 * (w & 3) * 7680 + (1280 + 64 * mid) * 2), 7680, l);
    const int ra = ri / 10, ca = ri - 10 * ra;
    const WStream sT = ROLE == R_SA ? mk_stream(mk_srd(p.wo, NQ * DM * 2), (u32)((2048 * ra + 256 * w) * 1280 + ca * 128), 1280, l)
                     : ROLE == R_SB ? mk_stream(mk_srd((w & 1) ? p.wu : p.wg, DM * FF * 2), (u32)(160 * (w >> 1) * 3072 + ri * 128), 3072, l)
                                    : mk_stream(mk_srd(p.wd, FF * DM * 2), (u32)(192 * w * 1280 + ri * 128), 1280, l);
    constexpr int NM = ROLE == R_SA ? 0 : 5, NT = ROLE == R_SA ? 8 : (ROLE == R_SB ? 5 : 6), NA = NM + NT;
    u32 ta[4];
    { const u32 f = 2u * (tq >> 1) | 4u * (g & 1);
#pragma unroll
      for (int T = 0; T < 4; ++T) ta[T] = w * 16384 + (8 * g + tq) * 128 + ((((u32)(2 * T)) ^ f) + (b4 >> 1)) * 16 + (b4 & 1) * 8; }
    const u32 dcol = 16 * tq + 4 * g;
    const u32 f1o = hasF1 ? WS_F1P + (u32)((kf * 4 + b4) * 1280 + 128 * lp + 64 * w + dcol) * 4u : OOB;
    const u32 qpo = hasF2 ? WS_QP + (u32)((kf2 * 4 + b4) * NQ + 512 * cs + 64 * w + dcol) * 4u : OOB;
    const u32 mfo = hasM ? WS_MODF + (u32)(b4 * 2560 + 64 * mid + dcol) * 2u : OOB;
    const u32 outo = ROLE == R_SA ? WS_AP + (u32)((ra * 4 + b4) * DM + 64 * ca + dcol) * 4u : WS_HID + (u32)(b4 * FF + 64 * ri + dcol) * 2u;
    volatile LAS u32* misc = (volatile LAS u32*)(L + L_MISC);
    STAMP(0);
    if (w < 5) { dma_1k(mk_srd(p.c, 5120), l * 16, w * 1024, lbase + L_CS + w * 1024); dma_1k(mk_srd(p.x, 5120), l * 16, w * 1024, lbase + L_XS + w * 1024); }
    issue(sF1, 0, stg);
    { u32 keep;
      asm volatile("s_nop 4\n\ts_mov_b32 %0, m0\n\ts_mov_b32 m0, %5\n\ts_nop 0\n\tbuffer_load_dword %1, %2, %3 offen nt lds\n\tbuffer_load_dword %1, %4, 0 offen nt lds\n\ts_mov_b32 m0, %0"
                   : "=&s"(keep) : "v"(l == 0 ? 0u : OOB), "s"(sT.srd), "s"(sT.base), "s"(mk_srd(p.out, 5120)), "s"(lbase + L_GB) : "memory"); }
    if (w == 0) {
        u32 keep;
        asm volatile("s_nop 4\n\ts_mov_b32 %0, m0\n\ts_mov_b32 m0, %3\n\ts_nop 0\n\tbuffer_load_dword %1, %2, 0 offen sc1 lds\n\ts_mov_b32 m0, %0"
                     : "=&s"(keep) : "v"(l < 2 ? WS_E + l * 4 : OOB), "s"(mk_srd(p.ws, WS_END)), "s"(lbase + L_GA) : "memory");
    }
    { const v4u s4 = {SENT, SENT, SENT, SENT};
      st_sc1(rws, qpo, s4);
      if (w < 2) st_sc1(rws, f1o, s4);
      if (ROLE != R_SA && (w & 3) == 0) st_sc1_2(rws, mfo, v2u{SENT, SENT});
      if (w == 0) {
          if (ROLE == R_SA) st_sc1(rws, outo, s4);
          if (ROLE == R_SB) st_sc1_2(rws, outo, v2u{SENT, SENT});
      } }
    if (tid == 0) { misc[0] = 0; misc[1] = 0; }
    BAR();
    issue(sF2, 0, stg + 4096);
    auto issueB = [&](int n) { if (n < NM) issue(sM, n, stg + ((n + 1) & 3) * 4096); else issue(sT, n - NM, stg + ((n + 1) & 3) * 4096); };
    {
        v4u ft[NT][4];
        f4 xa, xb, gaa, gab, c1a, c1b, sba, sbb;
        const int col = 128 * w + 4 * (l & 15);
        {
            VMW(4);
            BAR();
            STAMP(1);
            const u64 T = *(LAS u64*)(L + L_GA) + 1ull;
            const u32 tlo = (u32)T, thi = (u32)(T >> 32);
            if (tid == 0) __hip_atomic_store((GAS u64*)(p.ws + WS_FLAG) + blockIdx.x, T, RLX_AGENT);
            auto wait_all_flags = [&]() {
                for (int sp = 0; sp < SPIN_G; ++sp) {
                    MEMBAR();
                    const v4u f0 = ld_sc1(rws, WS_FLAG + l * 16), f1 = ld_sc1(rws, WS_FLAG + 1024 + l * 16);
                    const bool bad = f0[0] != tlo || f0[1] != thi || f0[2] != tlo || f0[3] != thi || f1[0] != tlo || f1[1] != thi || f1[2] != tlo || f1[3] != thi;
                    if (__builtin_amdgcn_ballot_w64(bad) == 0ull) break;
                }
            };
            {
                v4u f[4]; read_unit(L, ta, 0, f);
                const v4u a = silu8(*(LAS v4u*)(L + L_CS + b4 * 1280 + (128 * kf + 32 * (w >> 1) + 8 * g) * 2));
                f4 d[4];
#pragma unroll
                for (int T2 = 0; T2 < 4; ++T2) d[T2] = mfma(f[T2], a, f4{0.f, 0.f, 0.f, 0.f});
                *(LAS f4*)(L + L_RED + w * 1024 + l * 16) = sel_tile(d, tq);
                if (w >= 4) {
                    float xv[10], s = 0.f;
#pragma unroll
                    for (int i = 0; i < 5; ++i) { const u32 t = *(LAS u32*)(L + L_XS + (w - 4) * 1280 + (l + 64 * i) * 4); xv[2 * i] = bflo(t); xv[2 * i + 1] = bfhi(t); s += xv[2 * i] + xv[2 * i + 1]; }
                    const float mean = wave_sum(s) * (1.f / DM);
                    float v = 0.f;
#pragma unroll
                    for (int i = 0; i < 10; ++i) v = fmaf(xv[i] - mean, xv[i] - mean, v);
                    const float var = wave_sum(v) * (1.f / DM);
                    if (l == 0) *(LAS v2u*)(L + L_ST + (w - 4) * 8) = v2u{asu(rbf(mean)), asu(rbf(__builtin_amdgcn_sqrtf(rbf(rbf(var) + 1e-5f))))};
                }
                BAR();
                if (w < 2) {
                    LAS f4* R = (LAS f4*)(L + L_RED + w * 1024 + l * 16);
                    st_sc1(rws, f1o, canon4((R[0] + R[128]) + (R[256] + R[384])));
                }
                if (w == 0) STAMP(2);
                issue(sF2, 1, stg + 8192); issue(sF2, 2, stg + 12288); issue(sF2, 3, stg);
            }
            if (w < 2) {
                for (int sp = 0; sp < SPIN_G; ++sp) {
                    MEMBAR();
                    const v2u f = ld_sc1_2(rws, WS_FLAG + l * 8);
                    if (__builtin_amdgcn_ballot_w64(f[0] != tlo || f[1] != thi) == 0ull) break;
                }
                if (w == 0) STAMP(19);
                const int hb = tid >> 5, hi = tid & 31, col = 128 * kf2 + 4 * hi;
                const u32 o = WS_F1P + (u32)(hb * 1280 + col) * 4u;
                v4u a[5] = {}, c2[5] = {};
                bool need = hasF2;
                for (int sp = 0; sp < SPIN_G; ++sp) {
                    MEMBAR();
                    if (need) {
                        u32 mx = 0;
#pragma unroll
                        for (int ks = 0; ks < 5; ++ks) { a[ks] = ld_sc1(rws, o + ks * 20480); c2[ks] = ld_sc1(rws, o + ks * 20480 + 2560); }
#pragma unroll
                        for (int ks = 0; ks < 5; ++ks) mx = vmax4(vmax4(mx, a[ks]), c2[ks]);
                        need = mx == SENT;
                    }
                    if (__builtin_amdgcn_ballot_w64(need) == 0ull) break;
                }
                const f4 sa = (((asf4(a[0]) + asf4(a[1])) + asf4(a[2])) + asf4(a[3])) + asf4(a[4]);
                const f4 sc = (((asf4(c2[0]) + asf4(c2[1])) + asf4(c2[2])) + asf4(c2[3])) + asf4(c2[4]);
                const v2u st = *(LAS v2u*)(L + L_ST + hb * 8), xr = *(LAS v2u*)(L + L_XS + hb * 1280 + (hasF2 ? col : 0) * 2);
                const float den = asf(st[1]), rc = __builtin_amdgcn_rcpf(den);
                const f4 e = rb4(unpk(xr) - asf(st[0])), q1 = e * rc;
                const f4 h = rb4(rb4(fma4(fma4(-q1, bc4(den), e), bc4(rc), q1)) * rb4(rb4(sc) + 1.f)) + rb4(sa);
                *(LAS v2u*)(L + L_HB + hb * 256 + hi * 8) = v2u{pack2(h[0], h[1]), pack2(h[2], h[3])};
                LGKM0();
                if (l == 0) __hip_atomic_fetch_add((LAS u32*)(L + L_MISC), 1u, __ATOMIC_RELAXED, __HIP_MEMORY_SCOPE_WORKGROUP);
                if (w == 0) STAMP(4);
            }
            {
                v4u fq[4][4];
                VMW(12); read_unit(L, ta, 1, fq[0]); LGKM0();
                VMW(8); read_unit(L, ta, 2, fq[1]);
                VMW(4); read_unit(L, ta, 3, fq[2]);
                issueB(0);
                VMW(4); read_unit(L, ta, 0, fq[3]);
                if (w == 7) STAMP(3);
                for (int sp = 0; misc[0] < 2u && sp < SPIN_L; ++sp) __builtin_amdgcn_s_sleep(1);
                MEMBAR();
                f4 d[4] = {};
#pragma unroll
                for (int n = 0; n < 4; ++n) {
                    const v4u a = *(LAS v4u*)(L + L_HB + b4 * 256 + (32 * n + 8 * g) * 2);
#pragma unroll
                    for (int T2 = 0; T2 < 4; ++T2) d[T2] = mfma(fq[n][T2], a, d[T2]);
                }
                st_sc1(rws, qpo, canon4(sel_tile(d, tq)));
                BAR();
                if (w == 7) STAMP(5);
                issueB(1); issueB(2); issueB(3);
            }
            if (NM) {
                f4 d[4] = {};
#pragma unroll
                for (int n = 0; n < NM; ++n) {
                    if (n < 2) VMW(12); else if (n == 2) VMW(8); else if (n == 3) VMW(4); else VMW(0);
                    v4u f[4]; read_unit(L, ta, (n + 1) & 3, f);
                    const v4u a = silu8(*(LAS v4u*)(L + L_CS + b4 * 1280 + (160 * (w & 3) + 32 * n + 8 * g) * 2));
                    LGKM0();
                    if (n == 0) issueB(4);
#pragma unroll
                    for (int T2 = 0; T2 < 4; ++T2) d[T2] = mfma(f[T2], a, d[T2]);
                }
                *(LAS f4*)(L + L_RED + w * 1024 + l * 16) = sel_tile(d, tq);
                BAR();
                if ((w & 3) == 0) {
                    LAS f4* R = (LAS f4*)(L + L_RED + w * 1024 + l * 16);
                    const f4 s = (R[0] + R[64]) + (R[128] + R[192]);
                    st_sc1_2(rws, mfo, v2u{canon(pack2(s[0], s[1])), canon(pack2(s[2], s[3]))});
                }
                constexpr int PRE = ROLE == R_SB ? PRE_B : PRE_C;
#pragma unroll
                for (int n = 0; n < PRE; ++n) issueB(5 + n);
                UNDEF4(xa); UNDEF4(xb); UNDEF4(gaa); UNDEF4(gab); UNDEF4(c1a); UNDEF4(c1b); UNDEF4(sba); UNDEF4(sbb);
                if (ROLE == R_SB && w < 5) {
                    v2u gv[2], sv[2], cv[2];
                    const u32 mo = WS_MODF + (u32)(g * 2560 + col) * 2u;
                    for (int sp = 0; sp < SPIN_G; ++sp) {
                        MEMBAR();
                        u32 mx = 0;
#pragma unroll
                        for (int h = 0; h < 2; ++h) { gv[h] = ld_sc1_2(rws, mo + h * 128); sv[h] = ld_sc1_2(rws, mo + 1280 + h * 128); cv[h] = ld_sc1_2(rws, mo + 2560 + h * 128); }
#pragma unroll
                        for (int h = 0; h < 2; ++h) mx = umax3(umax3(umax3(mx, gv[h][0], gv[h][1]), sv[h][0], sv[h][1]), cv[h][0], cv[h][1]);
                        if (all_valid(mx)) break;
                    }
                    xa = unpk(*(LAS v2u*)(L + L_XS + g * 1280 + col * 2)); xb = unpk(*(LAS v2u*)(L + L_XS + g * 1280 + col * 2 + 128));
                    gaa = unpk(gv[0]); gab = unpk(gv[1]);
                    c1a = rb4(unpk(cv[0]) + 1.f); c1b = rb4(unpk(cv[1]) + 1.f);
                    sba = unpk(sv[0]); sbb = unpk(sv[1]);
                    asm volatile("" : "+v"(xa), "+v"(xb), "+v"(gaa), "+v"(gab), "+v"(c1a), "+v"(c1b), "+v"(sba), "+v"(sbb));
                }
                if (ROLE == R_SC && w == 0) {
                    v2u ga, gb;
                    const u32 mo = WS_MODF + (u32)(b4 * 2560 + 64 * ri + dcol) * 2u;
                    for (int sp = 0; sp < SPIN_G; ++sp) {
                        MEMBAR();
                        ga = ld_sc1_2(rws, mo); gb = ld_sc1_2(rws, mo + 3840);
                        if (all_valid(umax3(umax3(ga[0], ga[1], gb[0]), gb[1], 0u))) break;
                    }
                    xa = unpk(*(LAS v2u*)(L + L_XS + b4 * 1280 + (64 * ri + dcol) * 2)); gaa = unpk(ga); gab = unpk(gb);
                    asm volatile("" : "+v"(xa), "+v"(gaa), "+v"(gab));
                }
                if (w == 5) {
                    wait_all_flags();
                    for (int sp = 0; sp < SPIN_G; ++sp) {
                        MEMBAR();
                        const v4u e0 = ld_sc1(rws, WS_ML + l * 16), e1 = ld_sc1(rws, WS_ML + 1024 + l * 16);
                        const int cnt = __builtin_popcountll(__builtin_amdgcn_ballot_w64(e0[0] != SENT)) + __builtin_popcountll(__builtin_amdgcn_ballot_w64(e0[2] != SENT))
                                      + __builtin_popcountll(__builtin_amdgcn_ballot_w64(e1[0] != SENT)) + __builtin_popcountll(__builtin_amdgcn_ballot_w64(e1[2] != SENT));
                        if (cnt >= (ROLE == R_SB ? JIT_B : JIT_C)) break;
                        __builtin_amdgcn_s_sleep(8);
                    }
                }
                BAR();
                if (w == 0) STAMP(29);
#pragma unroll
                for (int n = PRE; n < 4; ++n) issueB(5 + n);
            }
#pragma unroll
            for (int n = 0; n < NT; ++n) {
                const int a = NM + n, younger = (NA - 1 - a) < 3 ? (NA - 1 - a) : 3;
                if (younger == 3) VMW(12); else if (younger == 2) VMW(8); else if (younger == 1) VMW(4); else VMW(0);
                read_unit(L, ta, (a + 1) & 3, ft[n]);
                if (a + 4 < NA) { LGKM0(); issueB(a + 4); }
                if (ROLE == R_SA && n == 1 && w == 0) {
                    wait_all_flags();
                    if (u == 0 && l == 0) __hip_atomic_store((GAS u64*)(p.ws + WS_E), T, RLX_AGENT);
                }
            }
            LGKM0();
            BAR();
            STAMP(6);
        }
        if (ROLE == R_SA) {
            f4 res[2];
            UNDEF4(res[0]); UNDEF4(res[1]);
            u32 oo[2], mo[2];
#pragma unroll
            for (int r = 0; r < 2; ++r) {
                const int row = 2048 * ra + 256 * w + 32 * (4 * r + tq) + 8 * g, hd = row / DH, dd = row - hd * DH;
                oo[r] = WS_OP + (u32)((b4 * 32 + hd) * 2) * 384u + dd * 2; mo[r] = WS_ML + (u32)(hd * 4 + b4) * 16u;
            }
            bool done[2] = {false, false}, wdone[2] = {false, false}, obad[2] = {true, true}, mbad[2] = {true, true};
            v4u a[2][2], ml[2];
            float w0[2], w1[2];
#pragma unroll
            for (int r = 0; r < 2; ++r) { UNDEF4(ml[r]); UNDEF4(w0[r]); UNDEF4(w1[r]); UNDEF4(a[r][0]); UNDEF4(a[r][1]); }
            for (int sp = 0; sp < SPIN_G && !(done[0] && done[1]); ++sp) {
                MEMBAR();
                bool ldo[2];
#pragma unroll
                for (int r = 0; r < 2; ++r) {
                    ldo[r] = obad[r] && (!mbad[r] || sp == 0);
                    if (mbad[r]) ml[r] = ld_sc1(rws, mo[r]);
                    if (ldo[r]) { a[r][0] = ld_sc1(rws, oo[r]); a[r][1] = ld_sc1(rws, oo[r] + 384); }
                }
                const bool last = sp == SPIN_G - 1;
#pragma unroll
                for (int r = 0; r < 2; ++r) {
                    if (!wdone[r]) {
                        mbad[r] = vmax4(0u, ml[r]) == SENT;
                        if (__builtin_amdgcn_ballot_w64(mbad[r]) == 0ull || last) {
                            const float m0 = asf(ml[r][0]), m1 = asf(ml[r][2]), M = fmaxf(m0, m1);
                            const float e0 = asf(ml[r][1]) * __builtin_amdgcn_exp2f((m0 - M) * LOG2E), e1 = asf(ml[r][3]) * __builtin_amdgcn_exp2f((m1 - M) * LOG2E);
                            const float inv = fdiv(1.f, e0 + e1);
                            w0[r] = e0 * inv; w1[r] = e1 * inv; wdone[r] = true;
                        }
                    }
                    if (!done[r]) {
                        obad[r] = obad[r] && (!ldo[r] || vmax4(vmax4(0u, a[r][0]), a[r][1]) == SENT);
                        if ((__builtin_amdgcn_ballot_w64(obad[r]) == 0ull && wdone[r]) || last) {
                            STAMP(20 + w);
                            const f4 x0 = fma4(unpk(v2u{a[r][0][0], a[r][0][1]}), bc4(w0[r]), unpk(v2u{a[r][1][0], a[r][1][1]}) * w1[r]);
                            const f4 x1 = fma4(unpk(v2u{a[r][0][2], a[r][0][3]}), bc4(w0[r]), unpk(v2u{a[r][1][2], a[r][1][3]}) * w1[r]);
                            const v4u bq = {pack2(x0[0], x0[1]), pack2(x0[2], x0[3]), pack2(x1[0], x1[1]), pack2(x1[2], x1[3])};
                            const v4u bk[4] = {bq, ror4k<1>(bq), ror4k<2>(bq), ror4k<3>(bq)};
                            f4 d[4] = {};
#pragma unroll
                            for (int s = 0; s < 4; ++s)
#pragma unroll
                                for (int T2 = 0; T2 < 4; ++T2) d[(T2 + s) & 3] = mfma(ft[4 * r + s][(T2 + s) & 3], bk[T2], d[(T2 + s) & 3]);
                            res[r] = sel_tile(d, tq);
                            done[r] = true;
                        }
                    }
                }
            }
            if (w == 0) STAMP(7);
            *(LAS f4*)(L + L_RED + w * 1024 + l * 16) = res[0] + res[1];
            BAR();
            if (w == 0) {
                STAMP(8);
                LAS f4* R = (LAS f4*)(L + L_RED + l * 16);
                st_sc1(rws, outo, canon4(((R[0] + R[64]) + (R[128] + R[192])) + ((R[256] + R[320]) + (R[384] + R[448]))));
                STAMP(9);
            }
        } else if (ROLE == R_SB) {
            if (w < 5) {
                v4u a[6];
                if (w == 0) STAMP(10);
                const u32 ao = WS_AP + (u32)(g * DM + col) * 4u;
#pragma unroll
                for (int r = 0; r < 6; ++r) UNDEF4(a[r]);
                bool need = true;
                for (int sp = 0; sp < SPIN_G; ++sp) {
                    MEMBAR();
                    if (need) {
                        u32 mx = 0;
#pragma unroll
                        for (int r = 0; r < 3; ++r) { a[2 * r] = ld_sc1(rws, ao + r * 10240); a[2 * r + 1] = ld_sc1(rws, ao + r * 10240 + 256); }
#pragma unroll
                        for (int r = 0; r < 6; ++r) mx = vmax4(mx, a[r]);
                        need = mx == SENT;
                    }
                    if (__builtin_amdgcn_ballot_w64(need) == 0ull) break;
                }
                if (w == 0) STAMP(11);
                f4 ta2 = (asf4(a[0]) + asf4(a[2])) + asf4(a[4]), tb2 = (asf4(a[1]) + asf4(a[3])) + asf4(a[5]);
                rb8(ta2, tb2); ta2 *= gaa; tb2 *= gab; rb8(ta2, tb2); xa += ta2; xb += tb2; rb8(xa, xb);
                const float sh = dppf<0x150>(xa[0] + 0.f);
                const f4 da = xa - sh, db = xb - sh, t = da + db, q2 = fma4(da, da, db * db);
                float s1 = (t[0] + t[1]) + (t[2] + t[3]), s2 = (q2[0] + q2[1]) + (q2[2] + q2[3]);
                s1 += dppf<0x128>(s1); s2 += dppf<0x128>(s2); s1 += dppf<0x124>(s1); s2 += dppf<0x124>(s2);
                s1 += dppf<0x122>(s1); s2 += dppf<0x122>(s2); s1 += dppf<0x121>(s1); s2 += dppf<0x121>(s2);
                const float dm1 = s1 * (1.f / 128.f), mw = sh + dm1, v = fmaf(-dm1, s1, s2);
                *(LAS v2u*)(L + L_ST + 64 + g * 48 + w * 8) = v2u{asu(mw), asu(v)};
            }
            BAR();
            if (w < 5) {
                const f4 s0 = *(LAS f4*)(L + L_ST + 64 + g * 48), s1 = *(LAS f4*)(L + L_ST + 64 + g * 48 + 16);
                const f2 s2 = *(LAS f2*)(L + L_ST + 64 + g * 48 + 32);
                const float mean = (((s0[0] + s0[2]) + (s1[0] + s1[2])) + s2[0]) * 0.2f;
                const f4 dm = f4{s0[0], s0[2], s1[0], s1[2]} - mean, dq = dm * dm; const float d4 = s2[0] - mean;
                const float M2 = fmaf(128.f, fmaf(d4, d4, (dq[0] + dq[1]) + (dq[2] + dq[3])), ((s0[1] + s0[3]) + (s1[1] + s1[3])) + s2[1]);
                const float mb = rbf(mean), den = rbf(__builtin_amdgcn_sqrtf(rbf(rbf(M2 * (1.f / DM)) + 1e-5f))), rc = __builtin_amdgcn_rcpf(den);
                f4 ea = xa - mb, eb = xb - mb;
                rb8(ea, eb);
                const f4 qa = ea * rc, qb = eb * rc;
                f4 ha = fma4(fma4(-qa, bc4(den), ea), bc4(rc), qa), hb = fma4(fma4(-qb, bc4(den), eb), bc4(rc), qb);
                rb8(ha, hb); ha *= c1a; hb *= c1b; rb8(ha, hb); ha += sba; hb += sbb;
                *(LAS v2u*)(L + L_HB + g * 1280 + col * 2) = v2u{pack2(ha[0], ha[1]), pack2(ha[2], ha[3])};
                *(LAS v2u*)(L + L_HB + g * 1280 + col * 2 + 128) = v2u{pack2(hb[0], hb[1]), pack2(hb[2], hb[3])};
            }
            BAR();
            if (w == 0) STAMP(12);
            f4 d[4] = {};
#pragma unroll
            for (int n = 0; n < 5; ++n) {
                const v4u a = *(LAS v4u*)(L + L_HB + b4 * 1280 + (160 * (w >> 1) + 32 * n + 8 * g) * 2);
#pragma unroll
                for (int T2 = 0; T2 < 4; ++T2) d[T2] = mfma(ft[n][T2], a, d[T2]);
            }
            *(LAS f4*)(L + L_RED + w * 1024 + l * 16) = sel_tile(d, tq);
            BAR();
            if (w == 0) {
                STAMP(13);
                LAS f4* R = (LAS f4*)(L + L_RED + l * 16);
                const f4 gt = rb4((R[0] + R[128]) + (R[256] + R[384])), up = rb4((R[64] + R[192]) + (R[320] + R[448]));
                const f4 mk = {gt[0] > 0.f ? 1.f : 0.f, gt[1] > 0.f ? 1.f : 0.f, gt[2] > 0.f ? 1.f : 0.f, gt[3] > 0.f ? 1.f : 0.f};
                const f4 hh = (gt * mk) * up;
                st_sc1_2(rws, outo, v2u{canon(pack2(hh[0], hh[1])), canon(pack2(hh[2], hh[3]))});
                STAMP(14);
            }
        } else {
            f4 x1;
            UNDEF4(x1);
            if (w == 0) {
                const u32 ao = WS_AP + (u32)(b4 * DM + 64 * ri + dcol) * 4u;
                f4 s;
                for (int sp = 0; sp < SPIN_G; ++sp) {
                    MEMBAR();
                    const v4u p0 = ld_sc1(rws, ao), p1 = ld_sc1(rws, ao + 10240), p2 = ld_sc1(rws, ao + 20480);
                    s = (asf4(p0) + asf4(p1)) + asf4(p2);
                    if (all_valid(vmax4(vmax4(vmax4(0u, p0), p1), p2))) break;
                }
                x1 = rb4(xa + rb4(gaa * rb4(s)));
                asm volatile("" : "+v"(x1));
                STAMP(15);
            }
            v4u a0, a1;
            UNDEF4(a0); UNDEF4(a1);
            const u32 h0 = WS_HID + (u32)(b4 * FF + 192 * w + 32 * tq + 8 * g) * 2u, h1 = tq < 2 ? h0 + 256 : OOB;
            bool need = true;
            for (int sp = 0; sp < SPIN_G; ++sp) {
                MEMBAR();
                if (need) { a0 = ld_sc1(rws, h0); a1 = ld_sc1(rws, h1); need = vmax4(vmax4(0u, a0), a1) == SENT; }
                if (__builtin_amdgcn_ballot_w64(need) == 0ull) break;
            }
            if (w == 0) STAMP(16);
            const v4u bk[4] = {a0, ror4k<1>(a0), ror4k<2>(a0), ror4k<3>(a0)}, bl[4] = {a1, ror4k<1>(a1), ror4k<2>(a1), ror4k<3>(a1)};
            f4 d[4] = {};
#pragma unroll
            for (int k = 0; k < 4; ++k)
#pragma unroll
                for (int s = 0; s < 6; ++s) d[(s + k) & 3] = mfma(ft[s][(s + k) & 3], s < 4 ? bk[k] : bl[k], d[(s + k) & 3]);
            *(LAS f4*)(L + L_RED + w * 1024 + l * 16) = sel_tile(d, tq);
            BAR();
            if (w == 0) {
                STAMP(17);
                LAS f4* R = (LAS f4*)(L + L_RED + l * 16);
                const f4 o = x1 + rb4(gab * rb4(((R[0] + R[64]) + (R[128] + R[192])) + ((R[256] + R[320]) + (R[384] + R[448]))));
                __builtin_amdgcn_raw_buffer_store_b64(v2u{pack2(o[0], o[1]), pack2(o[2], o[3])}, __builtin_amdgcn_make_buffer_rsrc((void*)p.out, 0, 4 * DM * 2, 0x00020000),
                                                      (u32)(b4 * DM + 64 * ri + dcol) * 2u, 0, 16);
                STAMP(18);
            }
        }
    }
}

typedef const __attribute__((address_space(4))) Params* kargp;
#define GET_PARAMS() kargp pp_ = kp; asm volatile("" : "+s"(pp_)); \
    const Params q_ = {pp_->x, pp_->kc, pp_->vc, pp_->wq, pp_->wo, pp_->wg, pp_->wu, pp_->wd, pp_->c, pp_->wm, pp_->out, pp_->ws};
__global__ void __launch_bounds__(512, 2) fused_block(Params p) {
    extern __shared__ __attribute__((aligned(16))) unsigned char smem[];
    LAS unsigned char* L = (LAS unsigned char*)smem;
    const kargp kp = (kargp)__builtin_amdgcn_kernarg_segment_ptr();
    asm volatile("s_load_dword s98, %0, 0x0\n\ts_load_dword s99, %0, 0x40" :: "s"(kp) : "s98", "s99");
    const int l = threadIdx.x & 63, w = __builtin_amdgcn_readfirstlane(threadIdx.x >> 6), c = blockIdx.x;
    const int x = c & 7, y = c >> 3;
    if (__builtin_expect(c >= NW, 1)) { GET_PARAMS(); kv_path(q_, L, c - NW, w, l); }
    else if (y >= 5) { GET_PARAMS(); w_path<R_SB>(q_, L, 30 + 8 * (y - 5) + x, w, l); }
    else if (x < 6) { GET_PARAMS(); w_path<R_SA>(q_, L, 10 * (x >> 1) + 5 * (x & 1) + y, w, l); }
    else { GET_PARAMS(); w_path<R_SC>(q_, L, 54 + 5 * (x - 6) + y, w, l); }
}

extern "C" void kernel_launch(void* const* d_in, const int* in_sizes, int n_in, void* d_out, int out_size, void* d_ws, size_t ws_size, hipStream_t stream) {
    Params p;
    p.x = (const u16*)d_in[0]; p.kc = (const u16*)d_in[1]; p.vc = (const u16*)d_in[2]; p.wq = (const u16*)d_in[3]; p.wo = (const u16*)d_in[4];
    p.wg = (const u16*)d_in[6]; p.wu = (const u16*)d_in[7]; p.wd = (const u16*)d_in[8]; p.c = (const u16*)d_in[9]; p.wm = (const u16*)d_in[10];
    p.out = (u16*)d_out; p.ws = (unsigned char*)d_ws;
    (void)hipFuncSetAttribute((const void*)fused_block, hipFuncAttributeMaxDynamicSharedMemorySize, (int)L_END);
    hipLaunchKernelGGL(fused_block, dim3(256), dim3(512), L_END, stream, p);
}
```
